```python
import torch

def _softmax(x, axis=-1):
    return torch.softmax(x, dim=axis)
def _gelu(x):
    return torch.nn.functional.gelu(x, approximate="tanh")
def _silu(x):
    return torch.nn.functional.silu(x)
def _sigmoid(x):
    return torch.sigmoid(x)
def _layernorm(x, axis=-1, eps=1e-5):
    m = x.mean(dim=axis, keepdim=True)
    v = x.var(dim=axis, keepdim=True, unbiased=False)
    return (x - m) / torch.sqrt(v + eps)
def _scatter_add(tgt, idx, src, axis=0):
    return tgt.clone().index_add_(axis, idx, src)
def _segment_sum(src, seg, n):
    out = torch.zeros((n,) + src.shape[1:], dtype=src.dtype)
    return out.index_add_(0, seg, src)
def _pad(x, pw):
    flat = tuple(v for pair in reversed(pw) for v in pair)
    return torch.nn.functional.pad(x, flat)

kernel_name = 'standalone__mha_block_cached__near_ridge'

def setup_inputs():

    _w = (lambda a: (lambda t: t.to(torch.bfloat16) if t.is_floating_point() else t)(torch.as_tensor(a)))
    B, Sq, D = 8, 192, 640
    H, Sk, Dh = 32, 2048, 128
    return {
        "x": _w((lambda *s: torch.randn(*s, dtype=torch.bfloat16) * 0.8)(B, Sq, D)),
        "k_cache": _w((lambda *s: torch.randn(*s, dtype=torch.bfloat16) * 0.8)(B, H, Sk, Dh)),
        "v_cache": _w((lambda *s: torch.randn(*s, dtype=torch.bfloat16) * 0.05)(B, H, Sk, Dh)),
        "wq": _w((lambda *s: torch.randn(*s, dtype=torch.bfloat16) * 0.8)(D, H * Dh) * (D ** -0.5)),
        "wo": _w((lambda *s: torch.randn(*s, dtype=torch.bfloat16) * 0.05)(H * Dh, D) * ((H * Dh) ** -0.5)),
    }

def reference(x, k_cache, v_cache, wq, wo):
    B, Sq, D = x.shape
    H, Dh = 32, 128
    q = (lambda x, *d: x.permute(*d))((x @ wq).reshape(B, Sq, H, Dh), 0, 2, 1, 3)
    scores = torch.einsum("bhqd,bhkd->bhqk", q, k_cache) * (Dh ** -0.5)
    p = _softmax(scores, axis=-1)
    o = torch.einsum("bhqk,bhkd->bhqd", p, v_cache)
    return (lambda x, *d: x.permute(*d))(o, 0, 2, 1, 3).reshape(B, Sq, H * Dh) @ wo
```

```cpp
#include <hip/hip_runtime.h>
#include <hip/hip_bf16.h>
typedef unsigned short u16;
static __device__ __forceinline__ float bf2f(u16 x){ return __uint_as_float(((unsigned)x)<<16); }
static __device__ __forceinline__ u16 f2bf(float f){ unsigned u=__float_as_uint(f); u += 0x7fffu + ((u>>16)&1u); return (u16)(u>>16); }
constexpr int B=8,SQ=192,DM=640,H=32,SK=2048,DH=128;
__global__ void gemm_naive(const u16* A,const u16* W,u16* C,int M,int N,int K){
  int n=blockIdx.x*64+threadIdx.x%64; int m=blockIdx.y*4+threadIdx.x/64;
  if(n>=N||m>=M) return; float acc=0;
  for(int k=0;k<K;k++) acc+=bf2f(A[(size_t)m*K+k])*bf2f(W[(size_t)k*N+n]);
  C[(size_t)m*N+n]=f2bf(acc);
}
__global__ void attn_naive(const u16* Q,const u16* Kc,const u16* Vc,u16* O){
  __shared__ float s[SK]; __shared__ float qs[DH]; __shared__ float red[256];
  int q=blockIdx.x, h=blockIdx.y, b=blockIdx.z, t=threadIdx.x;
  if(t<DH) qs[t]=bf2f(Q[((size_t)(b*SQ+q))*(H*DH)+h*DH+t]);
  __syncthreads();
  const u16* Kp=Kc+((size_t)(b*H+h))*SK*DH; const u16* Vp=Vc+((size_t)(b*H+h))*SK*DH;
  float mx=-1e30f;
  for(int k=t;k<SK;k+=256){ float a=0; for(int d=0;d<DH;d++) a+=qs[d]*bf2f(Kp[(size_t)k*DH+d]); a*=0.08838834764831845f; s[k]=a; mx=fmaxf(mx,a);}
  red[t]=mx; __syncthreads();
  for(int o=128;o>0;o>>=1){ if(t<o) red[t]=fmaxf(red[t],red[t+o]); __syncthreads(); }
  mx=red[0]; __syncthreads();
  float sum=0; for(int k=t;k<SK;k+=256){ float e=__expf(s[k]-mx); s[k]=e; sum+=e; }
  red[t]=sum; __syncthreads();
  for(int o=128;o>0;o>>=1){ if(t<o) red[t]+=red[t+o]; __syncthreads(); }
  sum=red[0]; __syncthreads();
  int d=t%DH, half=t/DH; float acc=0;
  for(int k=half*1024;k<half*1024+1024;k++) acc+=s[k]*bf2f(Vp[(size_t)k*DH+d]);
  red[t]=acc; __syncthreads();
  if(t<DH) O[((size_t)(b*SQ+q))*(H*DH)+h*DH+t]=f2bf((red[t]+red[t+DH])/sum);
}
extern "C" void kernel_launch(void* const* d_in, const int* in_sizes, int n_in, void* d_out, int out_size, void* d_ws, size_t ws_size, hipStream_t stream){
  const u16* x=(const u16*)d_in[0]; const u16* kc=(const u16*)d_in[1]; const u16* vc=(const u16*)d_in[2]; const u16* wq=(const u16*)d_in[3]; const u16* wo=(const u16*)d_in[4];
  u16* q=(u16*)d_ws; u16* o=q+(size_t)B*SQ*H*DH;
  gemm_naive<<<dim3(H*DH/64,B*SQ/4),256,0,stream>>>(x,wq,q,B*SQ,H*DH,DM);
  attn_naive<<<dim3(SQ,H,B),256,0,stream>>>(q,kc,vc,o);
  gemm_naive<<<dim3(DM/64,B*SQ/4),256,0,stream>>>(o,wo,(u16*)d_out,B*SQ,DM,H*DH);
}
```

```cpp
#include <hip/hip_runtime.h>

#define CLOB "v0","v1","v2","v3","v4","v5","v6","v7","v8","v9","v10","v11","v12","v13","v14","v15","v16","v17","v18","v19","v20","v21","v22","v23","v24","v25","v26","v27","v28","v29","v30","v31","v32","v33","v34","v35","v36","v37","v38","v39","v40","v41","v42","v43","v44","v45","v46","v47","v48","v49","v50","v51","v52","v53","v54","v55","v56","v57","v58","v59","v60","v61","v62","v63","v64","v65","v66","v67","v68","v69","v70","v71","v72","v73","v74","v75","v76","v77","v78","v79","v80","v81","v82","v83","v84","v85","v86","v87","v88","v89","v90","v91","v92","v93","v94","v95","v96","v97","v98","v99","v100","v101","v102","v103","v104","v105","v106","v107","v108","v109","v110","v111","v112","v113","v114","v115","v116","v117","v118","v119","v120","v121","v122","v123","v124","v125","v126","v127","v128","v129","v130","v131","v132","v133","v134","v135","v136","v137","v138","v139","v140","v141","v142","v143","v144","v145","v146","v147","v148","v149","v150","v151","v152","v153","v154","v155","v156","v157","v158","v159","v160","v161","v162","v163","v164","v165","v166","v167","v168","v169","v170","v171","v172","v173","v174","v175","v176","v177","v178","v179","v180","v181","v182","v183","v184","v185","v186","v187","v188","v189","v190","v191","v192","v193","v194","v195","v196","v197","v198","v199","v200","v201","v202","v203","v204","v205","v206","v207","v208","v209","v210","v211","v212","v213","v214","v215","v216","v217","v218","v219","v220","v221","v222","v223","v224","v225","v226","v227","v228","v229","v230","v231","v232","v233","v234","v235","v236","v237","v238","v239","v240","v241","v242","v243","v244","v245","v246","v247","v248","v249","v250","v251","v252","v253","v254","v255","a0","a1","a2","a3","a4","a5","a6","a7","a8","a9","a10","a11","a12","a13","a14","a15","a16","a17","a18","a19","a20","a21","a22","a23","a24","a25","a26","a27","a28","a29","a30","a31","a32","a33","a34","a35","a36","a37","a38","a39","a40","a41","a42","a43","a44","a45","a46","a47","a48","a49","a50","a51","a52","a53","a54","a55","a56","a57","a58","a59","a60","a61","a62","a63","a64","a65","a66","a67","a68","a69","a70","a71","a72","a73","a74","a75","a76","a77","a78","a79","a80","a81","a82","a83","a84","a85","a86","a87","a88","a89","a90","a91","a92","a93","a94","a95","a96","a97","a98","a99","a100","a101","a102","a103","a104","a105","a106","a107","a108","a109","a110","a111","a112","a113","a114","a115","a116","a117","a118","a119","a120","a121","a122","a123","a124","a125","a126","a127","a128","a129","a130","a131","a132","a133","a134","a135","a136","a137","a138","a139","a140","a141","a142","a143","a144","a145","a146","a147","a148","a149","a150","a151","a152","a153","a154","a155","a156","a157","a158","a159","a160","a161","a162","a163","a164","a165","a166","a167","a168","a169","a170","a171","a172","a173","a174","a175","a176","a177","a178","a179","a180","a181","a182","a183","a184","a185","a186","a187","a188","a189","a190","a191","a192","a193","a194","a195","a196","a197","a198","a199","a200","a201","a202","a203","a204","a205","a206","a207","a208","a209","a210","a211","a212","a213","a214","a215","a216","a217","a218","a219","a220","a221","a222","a223","a224","a225","a226","a227","a228","a229","a230","a231","a232","a233","a234","a235","a236","a237","a238","a239","a240","a241","a242","a243","a244","a245","a246","a247","a248","a249","a250","a251","a252","a253","a254","a255","s36","s37","s38","s39","s40","s41","s42","s43","s44","s45","s46","s47","s48","s49","s50","s51","s52","s53","s54","s55","s56","s57","s58","s59","s60","s61","s62","s63","s64","s65","s66","s67","s68","s69","s70","s71","s72","s73","s74","s75","s76","s77","s78","s79","s80","s81","s82","s83","s84","s85","s86","s87","s88","s89","s90","s91","s92","s93","s94","s95","s96","s97","s98","s99","vcc","scc","memory","s16","s17","s18","s19","s20","s21","s22","s23","s24","s25","s26","s27","s28","s29","s30","s31","s34","s35"
__global__ __launch_bounds__(256, 1) void mha_k(const char* x, const char* kc, const char* vc, const char* wq, const char* wo, char* ws, char* out) {
  const unsigned wid = __builtin_amdgcn_readfirstlane(threadIdx.x >> 6);
  asm volatile(R"ASM(
  s_mov_b32 s67, 0
  s_memrealtime s[82:83]
RESTART%=:
  s_load_dwordx2 s[52:53], %0, 0x18
  s_load_dwordx2 s[48:49], %0, 0x0
  s_load_dwordx4 s[16:19], %0, 0x8
  s_load_dwordx4 s[20:23], %0, 0x20
  s_load_dwordx2 s[24:25], %0, 0x30
  s_mov_b32 s64, %2
  s_and_b32 s27, %1, 7
  s_lshr_b32 s28, %1, 3
  s_and_b32 s68, s27, 1
  s_lshl_b32 s68, s68, 2
  s_and_b32 s29, s28, 3
  s_or_b32 s29, s29, s68
  s_lshr_b32 s68, s27, 1
  s_lshl_b32 s68, s68, 3
  s_lshr_b32 s30, s28, 2
  s_or_b32 s30, s30, s68
  s_lshr_b32 s68, s27, 2
  s_add_u32 s88, s27, s68
  s_add_u32 s68, s30, s29
  s_and_b32 s68, s68, 1
  s_mul_i32 s68, s68, 5
  s_add_u32 s88, s88, s68
  s_cmp_ge_u32 s88, 10
  s_cselect_b32 s68, 10, 0
  s_sub_u32 s88, s88, s68
  s_mov_b32 s93, s88
  v_mbcnt_lo_u32_b32 v246, -1, 0
  v_mbcnt_hi_u32_b32 v246, -1, v246
  v_lshrrev_b32 v160, 4, v246
  v_and_b32 v161, 15, v246
  v_lshlrev_b32 v162, 1, v160
  v_xor_b32 v162, v162, v161
  v_lshlrev_b32 v163, 13, v160
  v_lshl_add_u32 v150, v162, 4, v163
  v_xor_b32 v152, 128, v150
  v_add_u32 v151, 31744, v150
  v_add_u32 v153, 95232, v152
  v_add_u32 v152, 63488, v152
  s_lshl_b32 s73, s64, 17
  s_lshl_b32 s75, s64, 12
  s_lshl_b32 s68, s88, 19
  s_add_u32 s73, s73, s68
  s_lshl_b32 s68, s30, 8
  s_add_u32 s73, s73, s68
  s_mov_b32 s54, 5242880
  s_mov_b32 s55, 0x20000
  s_add_u32 m0, s75, 106496
  s_waitcnt lgkmcnt(0)
  buffer_load_dwordx4 v150, s[52:55], s73 offen offset:0 lds
  buffer_load_dwordx4 v151, s[52:55], s73 offen offset:1024 lds
  buffer_load_dwordx4 v152, s[52:55], s73 offen offset:2048 lds
  buffer_load_dwordx4 v153, s[52:55], s73 offen offset:3072 lds
  s_add_u32 s88, s88, 1
  s_cmp_eq_u32 s88, 10
  s_cselect_b32 s71, -9, 1
  s_lshl_b32 s71, s71, 19
  s_add_u32 s73, s73, s71
  s_mov_b32 s50, 1966080
  s_mov_b32 s51, 0x20000
  s_mul_i32 s72, s64, 61440
  s_mul_i32 s74, s64, 6144
  s_lshl_b32 s68, s93, 7
  s_add_u32 s72, s72, s68
  s_mul_i32 s68, s29, 245760
  s_add_u32 s72, s72, s68
  v_lshrrev_b32 v162, 3, v246
  v_mul_u32_u24 v162, 1280, v162
  v_and_b32 v163, 7, v246
  v_xor_b32 v163, v163, v160
  v_lshl_add_u32 v144, v163, 4, v162
  v_xor_b32 v145, 64, v144
  v_add_u32 v149, 50176, v145
  v_add_u32 v147, 27648, v145
  v_add_u32 v145, 9216, v145
  v_add_u32 v148, 40960, v144
  v_add_u32 v146, 18432, v144
  s_add_u32 m0, s74, 32768
  s_nop 0
  buffer_load_dwordx4 v144, s[48:51], s72 offen offset:0 lds
  buffer_load_dwordx4 v145, s[48:51], s72 offen offset:1024 lds
  buffer_load_dwordx4 v146, s[48:51], s72 offen offset:2048 lds
  buffer_load_dwordx4 v147, s[48:51], s72 offen offset:3072 lds
  s_add_u32 m0, s74, 36864
  s_nop 0
  buffer_load_dwordx4 v148, s[48:51], s72 offen offset:0 lds
  buffer_load_dwordx4 v149, s[48:51], s72 offen offset:1024 lds
  s_add_u32 s93, s93, 1
  s_cmp_eq_u32 s93, 10
  s_cselect_b32 s71, -9, 1
  s_lshl_b32 s71, s71, 7
  s_add_u32 s72, s72, s71
  s_add_u32 m0, s75, 122880
  s_nop 0
  buffer_load_dwordx4 v150, s[52:55], s73 offen offset:0 lds
  buffer_load_dwordx4 v151, s[52:55], s73 offen offset:1024 lds
  buffer_load_dwordx4 v152, s[52:55], s73 offen offset:2048 lds
  buffer_load_dwordx4 v153, s[52:55], s73 offen offset:3072 lds
  s_add_u32 s88, s88, 1
  s_cmp_eq_u32 s88, 10
  s_cselect_b32 s71, -9, 1
  s_lshl_b32 s71, s71, 19
  s_add_u32 s73, s73, s71
  s_and_b32 s65, s64, 1
  s_lshr_b32 s66, s64, 1
  s_mul_i32 s68, s65, 12288
  s_add_u32 s68, s68, 32768
  v_lshrrev_b32 v162, 1, v161
  v_xor_b32 v162, v162, v160
  v_lshlrev_b32 v163, 7, v161
  v_add_u32 v163, s68, v163
  v_lshl_add_u32 v132, v162, 4, v163
  v_xor_b32 v133, 64, v132
  v_add_u32 v134, 106496, v132
  v_add_u32 v135, 106496, v133
  v_bfe_u32 v164, v246, 2, 2
  v_and_b32 v165, 3, v246
  v_bfe_u32 v162, v246, 2, 3
  s_lshl_b32 s69, s66, 2
  v_xor_b32 v162, s69, v162
  v_lshl_add_u32 v163, v160, 3, v164
  v_lshlrev_b32 v163, 8, v163
  v_lshl_add_u32 v163, v165, 3, v163
  v_lshl_add_u32 v136, v162, 5, v163
  v_xor_b32 v137, 32, v136
  v_xor_b32 v138, 64, v136
  v_xor_b32 v139, 96, v136
  v_add_u32 v140, 106496, v136
  v_add_u32 v141, 106496, v137
  v_add_u32 v142, 106496, v138
  v_add_u32 v143, 106496, v139
  s_lshl_b32 s89, s29, 5
  s_or_b32 s89, s89, s30
  s_cmp_ge_u32 s28, 30
  s_cselect_b32 s63, 1, 0
  s_cselect_b32 s68, 0, s28
  s_mul_i32 s69, s68, 43
  s_lshr_b32 s69, s69, 8
  s_mul_i32 s70, s69, 6
  s_sub_u32 s68, s68, s70
  s_and_b32 s79, s27, 1
  s_mul_i32 s79, s79, 5
  s_add_u32 s79, s79, s69
  s_lshr_b32 s69, s27, 1
  s_mul_i32 s31, s69, 6
  s_add_u32 s31, s31, s68
  s_lshl_b32 s92, s69, 3
  s_add_u32 s92, s92, s68
  s_cmp_ge_u32 s68, 3
  s_addc_u32 s92, s92, 0
  s_mov_b64 s[56:57], s[22:23]
  s_mov_b32 s58, 12582912
  s_add_u32 s56, s56, s58
  s_addc_u32 s57, s57, 0
  s_and_b32 s57, s57, 0xffff
  s_mov_b32 s58, 263168
  s_mov_b32 s59, 0x20000
  v_and_b32 v162, 7, v246
  v_lshrrev_b32 v163, 3, v246
  s_lshl_b32 s70, s92, 2
  s_add_u32 s70, s70, s64
  s_lshl_b32 s70, s70, 7
  s_and_b32 s68, s27, 6
  v_add_u32 v167, s68, v162
  v_and_b32 v167, 7, v167
  s_lshl_b32 s68, s70, 3
  v_lshlrev_b32 v166, 7, v167
  v_lshl_add_u32 v166, v163, 12, v166
  v_add_u32 v249, s68, v166
  s_lshl_b32 s68, s70, 3
  s_lshl_b32 s69, s79, 5
  s_add_u32 s68, s68, s69
  s_add_u32 s68, s68, 131072
  s_lshl_b32 s69, s63, 30
  s_add_u32 s68, s68, s69
  v_mul_u32_u24 v166, 4608, v163
  v_lshl_add_u32 v166, v167, 2, v166
  v_add_u32 v216, s68, v166
  v_mov_b32 v250, 0x40000000
  v_cmp_le_u32 vcc, 16, v246
  s_nop 1
  v_cndmask_b32 v249, v249, v250, vcc
  v_cndmask_b32 v216, v216, v250, vcc
  s_lshr_b32 s68, s89, 5
  s_lshl_b32 s68, s68, 2
  s_add_u32 s68, s68, s64
  s_lshl_b32 s68, s68, 2
  s_bfe_u32 s69, s89, 0x20003
  s_add_u32 s68, s68, s69
  s_lshl_b32 s68, s68, 7
  s_and_b32 s70, s89, 7
  s_lshl_b32 s70, s70, 2
  s_add_u32 s68, s68, s70
  s_sub_u32 s69, s68, s70
  s_lshl_b32 s69, s69, 3
  s_lshl_b32 s71, s70, 5
  s_add_u32 s69, s69, s71
  v_writelane_b32 v249, s69, 16
  v_lshlrev_b32 v166, 2, v246
  v_add_u32 v166, s69, v166
  v_cmp_gt_u32 vcc, 32, v246
  s_nop 1
  v_cndmask_b32 v250, v250, v166, vcc
  s_sub_u32 s68, s68, s70
  s_lshl_b32 s68, s68, 3
  s_add_u32 s68, s68, s70
  s_add_u32 s68, s68, 131072
  v_and_b32 v166, 31, v246
  v_lshlrev_b32 v166, 5, v166
  v_add_u32 v217, s68, v166
  s_cmp_lg_u32 s64, 3
  s_cselect_b32 s68, 0x3ff, 0
  s_cmp_lg_u32 s64, 0
  s_cselect_b32 s69, 0x3ff0000, 0
  s_or_b32 s90, s68, s69
  s_mov_b32 s91, 0
  v_lshlrev_b32 v214, 7, v246
  v_add_u32 v214, 262144, v214
  v_mov_b32 v215, 0x40000000
  s_xor_b32 s68, s28, 13
  s_or_b32 s68, s68, s64
  s_lshl_b32 s69, s27, 7
  s_add_u32 s69, s69, 262144
  s_cmp_eq_u32 s68, 0
  s_cselect_b32 s69, s69, 1073741824
  v_writelane_b32 v215, s69, 0
  v_mov_b32 v204, s82
  v_lshlrev_b32 v166, 1, v160
  v_xor_b32 v166, v166, v162
  v_mul_u32_u24 v180, 1280, v163
  v_lshl_add_u32 v180, v166, 4, v180
  v_add_u32 v181, 9216, v180
  v_add_u32 v182, 18432, v180
  v_add_u32 v183, 27648, v180
  v_and_b32 v163, 3, v163
  v_lshrrev_b32 v166, 2, v246
  v_lshlrev_b32 v166, 7, v166
  v_lshl_add_u32 v166, v165, 3, v166
  v_lshl_add_u32 v166, v163, 5, v166
  s_add_u32 s68, s75, 49152
  v_add_u32 v184, s68, v166
  v_xor_b32 v185, 32, v184
  v_xor_b32 v186, 64, v184
  v_xor_b32 v187, 96, v184
  v_lshlrev_b32 v248, 4, v246
  s_mov_b64 s[40:41], s[16:17]
  s_mov_b32 s42, 0
  s_mul_i32 s43, s89, 524288
  s_add_u32 s42, s42, s43
  s_add_u32 s40, s40, s42
  s_addc_u32 s41, s41, 0
  s_and_b32 s41, s41, 0xffff
  s_mov_b32 s42, 524288
  s_mov_b32 s43, 0x20000
  s_mov_b64 s[44:45], s[18:19]
  s_mov_b32 s46, 0
  s_mul_i32 s47, s89, 524288
  s_add_u32 s46, s46, s47
  s_add_u32 s44, s44, s46
  s_addc_u32 s45, s45, 0
  s_and_b32 s45, s45, 0xffff
  s_mov_b32 s46, 524288
  s_mov_b32 s47, 0x20000
  s_lshl_b32 s62, s64, 12
  s_add_u32 s61, s62, 90112
  s_mov_b32 s76, 1040356334
  s_lshl_b32 s60, s64, 12
  v_bfe_u32 v162, v246, 2, 3
  v_lshrrev_b32 v163, 2, v246
  v_lshlrev_b32 v163, 8, v163
  v_lshl_add_u32 v163, v165, 3, v163
  v_lshl_add_u32 v232, v162, 5, v163
  v_xor_b32 v233, 32, v232
  v_xor_b32 v234, 64, v232
  v_xor_b32 v235, 96, v232
  v_xor_b32 v236, 128, v232
  v_xor_b32 v237, 160, v232
  v_xor_b32 v238, 192, v232
  v_xor_b32 v239, 224, v232
  v_xor_b32 v162, v160, v161
  v_lshlrev_b32 v163, 8, v161
  v_lshl_add_u32 v166, v162, 4, v163
  s_mul_i32 s68, s64, 12288
  s_add_u32 s68, s68, 32768
  v_add_u32 v228, 90112, v166
  v_add_u32 v120, s68, v166
  v_xor_b32 v229, 64, v228
  v_xor_b32 v121, 64, v120
  v_xor_b32 v230, 128, v228
  v_xor_b32 v122, 128, v120
  v_xor_b32 v231, 192, v228
  v_xor_b32 v123, 192, v120
  s_mul_i32 s68, s65, 24576
  s_add_u32 s68, s68, 32768
  v_lshlrev_b32 v163, 8, v160
  v_lshl_add_u32 v240, v162, 4, v163
  v_xor_b32 v241, 64, v240
  v_xor_b32 v242, 128, v240
  v_xor_b32 v243, 192, v240
  v_lshlrev_b32 v162, 1, v160
  v_xor_b32 v162, v162, v161
  v_lshl_add_u32 v244, v162, 4, v163
  v_xor_b32 v245, 128, v244
  v_mov_b32 v166, 0x3f803f80
  v_accvgpr_write_b32 a240, v166
  v_accvgpr_write_b32 a241, v166
  v_accvgpr_write_b32 a242, v166
  v_accvgpr_write_b32 a243, v166
  s_lshl_b32 s69, s66, 3
  v_lshrrev_b32 v162, 1, v160
  v_xor_b32 v162, v162, v161
  v_xor_b32 v162, s69, v162
  v_and_b32 v163, 1, v160
  v_lshlrev_b32 v163, 3, v163
  v_lshl_add_u32 v163, v161, 8, v163
  v_add_u32 v163, s68, v163
  v_lshl_add_u32 v154, v162, 4, v163
  v_xor_b32 v155, 32, v154
  v_xor_b32 v156, 64, v154
  v_xor_b32 v157, 96, v154
  v_mov_b32 v208, 0xff800000
  v_mov_b32 v209, 0xff800000
  v_mov_b32 v210, 0xff800000
  s_mov_b32 s85, s82
  s_mov_b32 s94, 0
  s_bfe_u32 s84, s30, 0x20001
  s_lshr_b32 s34, s29, 1
  s_sub_u32 s84, s84, s34
  s_and_b32 s84, s84, 3
  s_mul_i32 s34, s84, 56
  s_add_u32 s34, s34, 4365
  s_mov_b32 s96, 115
  s_mov_b32 s81, 122
  s_lshl_b32 s95, s96, 4
  s_sub_u32 s96, s96, 4
  s_getpc_b64 s[36:37]
CSTART%=:
  s_and_b32 s37, s37, 0xffff
  s_mov_b32 s38, CEND%=-CSTART%=
  s_mov_b32 s39, 0x20000
  s_lshl_b32 s68, s28, 2
  s_add_u32 s68, s68, s64
  s_lshl_b32 s70, s68, 10
  s_add_u32 m0, s74, 57344
  s_nop 0
  buffer_load_dwordx4 v144, s[48:51], s72 offen offset:0 lds
  buffer_load_dwordx4 v145, s[48:51], s72 offen offset:1024 lds
  buffer_load_dwordx4 v146, s[48:51], s72 offen offset:2048 lds
  buffer_load_dwordx4 v147, s[48:51], s72 offen offset:3072 lds
  s_add_u32 m0, s74, 61440
  s_nop 0
  buffer_load_dwordx4 v148, s[48:51], s72 offen offset:0 lds
  buffer_load_dwordx4 v149, s[48:51], s72 offen offset:1024 lds
  s_add_u32 s93, s93, 1
  s_cmp_eq_u32 s93, 10
  s_cselect_b32 s71, -9, 1
  s_lshl_b32 s71, s71, 7
  s_add_u32 s72, s72, s71
  s_waitcnt vmcnt(10)
  s_barrier
  ds_read_b128 a[112:115], v132 offset:0
  buffer_load_dwordx4 v[220:223], v248, s[36:39], s70 offen offset:0
  ds_read_b128 a[116:119], v133 offset:0
  ds_read_b64_tr_b16 a[32:33], v140 offset:0
  ds_read_b64_tr_b16 a[34:35], v140 offset:1024
  ds_read_b64_tr_b16 a[36:37], v140 offset:8192
  s_add_u32 m0, s75, 0
  ds_read_b64_tr_b16 a[38:39], v140 offset:9216
  ds_read_b64_tr_b16 a[40:41], v141 offset:0
  ds_read_b64_tr_b16 a[42:43], v141 offset:1024
  ds_read_b64_tr_b16 a[44:45], v141 offset:8192
  buffer_load_dwordx4 v150, s[52:55], s73 offen offset:0 lds
  ds_read_b64_tr_b16 a[46:47], v141 offset:9216
  ds_read_b64_tr_b16 a[48:49], v142 offset:0
  ds_read_b64_tr_b16 a[50:51], v142 offset:1024
  ds_read_b64_tr_b16 a[52:53], v142 offset:8192
  buffer_load_dwordx4 v151, s[52:55], s73 offen offset:1024 lds
  ds_read_b64_tr_b16 a[54:55], v142 offset:9216
  ds_read_b64_tr_b16 a[56:57], v143 offset:0
  ds_read_b64_tr_b16 a[58:59], v143 offset:1024
  ds_read_b64_tr_b16 a[60:61], v143 offset:8192
  buffer_load_dwordx4 v152, s[52:55], s73 offen offset:2048 lds
  ds_read_b64_tr_b16 a[62:63], v143 offset:9216
  ds_read_b128 a[120:123], v132 offset:2048
  ds_read_b128 a[124:127], v133 offset:2048
  ds_read_b128 a[128:131], v132 offset:4096
  buffer_load_dwordx4 v153, s[52:55], s73 offen offset:3072 lds
  ds_read_b128 a[132:135], v133 offset:4096
  ds_read_b128 a[136:139], v132 offset:6144
  ds_read_b128 a[140:143], v133 offset:6144
  ds_read_b128 a[144:147], v132 offset:8192
  s_add_u32 s88, s88, 1
  s_cmp_eq_u32 s88, 10
  s_cselect_b32 s71, -9, 1
  s_lshl_b32 s71, s71, 19
  s_add_u32 s73, s73, s71
  ds_read_b128 a[148:151], v133 offset:8192
  ds_read_b128 a[152:155], v132 offset:10240
  ds_read_b128 a[156:159], v133 offset:10240
  s_waitcnt lgkmcnt(0)
  v_mfma_f32_16x16x32_bf16 v[0:3], a[32:35], a[112:115], 0
  s_add_u32 m0, s74, 81920
  v_mfma_f32_16x16x32_bf16 v[0:3], a[36:39], a[116:119], v[0:3]
  buffer_load_dwordx4 v144, s[48:51], s72 offen offset:0 lds
  v_mfma_f32_16x16x32_bf16 v[4:7], a[40:43], a[112:115], 0
  v_mfma_f32_16x16x32_bf16 v[4:7], a[44:47], a[116:119], v[4:7]
  v_mfma_f32_16x16x32_bf16 v[8:11], a[48:51], a[112:115], 0
  buffer_load_dwordx4 v145, s[48:51], s72 offen offset:1024 lds
  v_mfma_f32_16x16x32_bf16 v[8:11], a[52:55], a[116:119], v[8:11]
  v_mfma_f32_16x16x32_bf16 v[12:15], a[56:59], a[112:115], 0
  v_mfma_f32_16x16x32_bf16 v[12:15], a[60:63], a[116:119], v[12:15]
  v_mfma_f32_16x16x32_bf16 v[16:19], a[32:35], a[120:123], 0
  buffer_load_dwordx4 v146, s[48:51], s72 offen offset:2048 lds
  v_mfma_f32_16x16x32_bf16 v[16:19], a[36:39], a[124:127], v[16:19]
  v_mfma_f32_16x16x32_bf16 v[20:23], a[40:43], a[120:123], 0
  v_mfma_f32_16x16x32_bf16 v[20:23], a[44:47], a[124:127], v[20:23]
  v_mfma_f32_16x16x32_bf16 v[24:27], a[48:51], a[120:123], 0
  buffer_load_dwordx4 v147, s[48:51], s72 offen offset:3072 lds
  v_mfma_f32_16x16x32_bf16 v[24:27], a[52:55], a[124:127], v[24:27]
  v_mfma_f32_16x16x32_bf16 v[28:31], a[56:59], a[120:123], 0
  v_mfma_f32_16x16x32_bf16 v[28:31], a[60:63], a[124:127], v[28:31]
  s_add_u32 m0, s74, 86016
  v_mfma_f32_16x16x32_bf16 v[32:35], a[32:35], a[128:131], 0
  buffer_load_dwordx4 v148, s[48:51], s72 offen offset:0 lds
  v_mfma_f32_16x16x32_bf16 v[32:35], a[36:39], a[132:135], v[32:35]
  s_waitcnt vmcnt(10)
  s_barrier
  v_mfma_f32_16x16x32_bf16 v[36:39], a[40:43], a[128:131], 0
  ds_read_b128 a[64:67], v132 offset:24576
  ds_read_b128 a[68:71], v133 offset:24576
  v_mfma_f32_16x16x32_bf16 v[36:39], a[44:47], a[132:135], v[36:39]
  ds_read_b64_tr_b16 a[0:1], v140 offset:16384
  ds_read_b64_tr_b16 a[2:3], v140 offset:17408
  v_mfma_f32_16x16x32_bf16 v[40:43], a[48:51], a[128:131], 0
  buffer_load_dwordx4 v149, s[48:51], s72 offen offset:1024 lds
  v_mfma_f32_16x16x32_bf16 v[40:43], a[52:55], a[132:135], v[40:43]
  s_add_u32 s93, s93, 1
  s_cmp_eq_u32 s93, 10
  s_cselect_b32 s71, -9, 1
  s_lshl_b32 s71, s71, 7
  s_add_u32 s72, s72, s71
  v_mfma_f32_16x16x32_bf16 v[44:47], a[56:59], a[128:131], 0
  ds_read_b64_tr_b16 a[4:5], v140 offset:24576
  ds_read_b64_tr_b16 a[6:7], v140 offset:25600
  v_mfma_f32_16x16x32_bf16 v[44:47], a[60:63], a[132:135], v[44:47]
  s_add_u32 m0, s75, 16384
  v_mfma_f32_16x16x32_bf16 v[48:51], a[32:35], a[136:139], 0
  buffer_load_dwordx4 v150, s[52:55], s73 offen offset:0 lds
  v_mfma_f32_16x16x32_bf16 v[48:51], a[36:39], a[140:143], v[48:51]
  ds_read_b64_tr_b16 a[8:9], v141 offset:16384
  ds_read_b64_tr_b16 a[10:11], v141 offset:17408
  v_mfma_f32_16x16x32_bf16 v[52:55], a[40:43], a[136:139], 0
  ds_read_b64_tr_b16 a[12:13], v141 offset:24576
  ds_read_b64_tr_b16 a[14:15], v141 offset:25600
  v_mfma_f32_16x16x32_bf16 v[52:55], a[44:47], a[140:143], v[52:55]
  buffer_load_dwordx4 v151, s[52:55], s73 offen offset:1024 lds
  v_mfma_f32_16x16x32_bf16 v[56:59], a[48:51], a[136:139], 0
  ds_read_b64_tr_b16 a[16:17], v142 offset:16384
  ds_read_b64_tr_b16 a[18:19], v142 offset:17408
  v_mfma_f32_16x16x32_bf16 v[56:59], a[52:55], a[140:143], v[56:59]
  ds_read_b64_tr_b16 a[20:21], v142 offset:24576
  ds_read_b64_tr_b16 a[22:23], v142 offset:25600
  v_mfma_f32_16x16x32_bf16 v[60:63], a[56:59], a[136:139], 0
  ds_read_b64_tr_b16 a[24:25], v143 offset:16384
  ds_read_b64_tr_b16 a[26:27], v143 offset:17408
  v_mfma_f32_16x16x32_bf16 v[60:63], a[60:63], a[140:143], v[60:63]
  buffer_load_dwordx4 v152, s[52:55], s73 offen offset:2048 lds
  v_mfma_f32_16x16x32_bf16 v[64:67], a[32:35], a[144:147], 0
  ds_read_b64_tr_b16 a[28:29], v143 offset:24576
  ds_read_b64_tr_b16 a[30:31], v143 offset:25600
  v_mfma_f32_16x16x32_bf16 v[64:67], a[36:39], a[148:151], v[64:67]
  ds_read_b128 a[72:75], v132 offset:26624
  ds_read_b128 a[76:79], v133 offset:26624
  v_mfma_f32_16x16x32_bf16 v[68:71], a[40:43], a[144:147], 0
  ds_read_b128 a[80:83], v132 offset:28672
  ds_read_b128 a[84:87], v133 offset:28672
  v_mfma_f32_16x16x32_bf16 v[68:71], a[44:47], a[148:151], v[68:71]
  buffer_load_dwordx4 v153, s[52:55], s73 offen offset:3072 lds
  v_mfma_f32_16x16x32_bf16 v[72:75], a[48:51], a[144:147], 0
  s_add_u32 s88, s88, 1
  s_cmp_eq_u32 s88, 10
  s_cselect_b32 s71, -9, 1
  s_lshl_b32 s71, s71, 19
  s_add_u32 s73, s73, s71
  v_mfma_f32_16x16x32_bf16 v[72:75], a[52:55], a[148:151], v[72:75]
  ds_read_b128 a[88:91], v132 offset:30720
  ds_read_b128 a[92:95], v133 offset:30720
  v_mfma_f32_16x16x32_bf16 v[76:79], a[56:59], a[144:147], 0
  s_add_u32 m0, s74, 139264
  v_mfma_f32_16x16x32_bf16 v[76:79], a[60:63], a[148:151], v[76:79]
  buffer_load_dwordx4 v144, s[48:51], s72 offen offset:0 lds
  v_mfma_f32_16x16x32_bf16 v[80:83], a[32:35], a[152:155], 0
  ds_read_b128 a[96:99], v132 offset:32768
  ds_read_b128 a[100:103], v133 offset:32768
  v_mfma_f32_16x16x32_bf16 v[80:83], a[36:39], a[156:159], v[80:83]
  ds_read_b128 a[104:107], v132 offset:34816
  v_mfma_f32_16x16x32_bf16 v[84:87], a[40:43], a[152:155], 0
  ds_read_b128 a[108:111], v133 offset:34816
  v_mfma_f32_16x16x32_bf16 v[84:87], a[44:47], a[156:159], v[84:87]
  buffer_load_dwordx4 v145, s[48:51], s72 offen offset:1024 lds
  v_mfma_f32_16x16x32_bf16 v[88:91], a[48:51], a[152:155], 0
  v_mfma_f32_16x16x32_bf16 v[88:91], a[52:55], a[156:159], v[88:91]
  v_mfma_f32_16x16x32_bf16 v[92:95], a[56:59], a[152:155], 0
  v_mfma_f32_16x16x32_bf16 v[92:95], a[60:63], a[156:159], v[92:95]
  s_waitcnt vmcnt(6) lgkmcnt(0)
  s_barrier
  v_mfma_f32_16x16x32_bf16 v[0:3], a[0:3], a[64:67], v[0:3]
  ds_read_b128 a[112:115], v132 offset:49152
  ds_read_b128 a[116:119], v133 offset:49152
  v_mfma_f32_16x16x32_bf16 v[0:3], a[4:7], a[68:71], v[0:3]
  buffer_load_dwordx4 v146, s[48:51], s72 offen offset:2048 lds
  v_mfma_f32_16x16x32_bf16 v[4:7], a[8:11], a[64:67], v[4:7]
  ds_read_b64_tr_b16 a[32:33], v136 offset:0
  v_mfma_f32_16x16x32_bf16 v[4:7], a[12:15], a[68:71], v[4:7]
  ds_read_b64_tr_b16 a[34:35], v136 offset:1024
  v_mfma_f32_16x16x32_bf16 v[8:11], a[16:19], a[64:67], v[8:11]
  buffer_load_dwordx4 v147, s[48:51], s72 offen offset:3072 lds
  v_mfma_f32_16x16x32_bf16 v[8:11], a[20:23], a[68:71], v[8:11]
  ds_read_b64_tr_b16 a[36:37], v136 offset:8192
  v_mfma_f32_16x16x32_bf16 v[12:15], a[24:27], a[64:67], v[12:15]
  ds_read_b64_tr_b16 a[38:39], v136 offset:9216
  v_mfma_f32_16x16x32_bf16 v[12:15], a[28:31], a[68:71], v[12:15]
  s_add_u32 m0, s74, 143360
  v_mfma_f32_16x16x32_bf16 v[16:19], a[0:3], a[72:75], v[16:19]
  buffer_load_dwordx4 v148, s[48:51], s72 offen offset:0 lds
  v_mfma_f32_16x16x32_bf16 v[16:19], a[4:7], a[76:79], v[16:19]
  ds_read_b64_tr_b16 a[40:41], v137 offset:0
  v_mfma_f32_16x16x32_bf16 v[20:23], a[8:11], a[72:75], v[20:23]
  ds_read_b64_tr_b16 a[42:43], v137 offset:1024
  v_mfma_f32_16x16x32_bf16 v[20:23], a[12:15], a[76:79], v[20:23]
  ds_read_b64_tr_b16 a[44:45], v137 offset:8192
  v_mfma_f32_16x16x32_bf16 v[24:27], a[16:19], a[72:75], v[24:27]
  buffer_load_dwordx4 v149, s[48:51], s72 offen offset:1024 lds
  v_mfma_f32_16x16x32_bf16 v[24:27], a[20:23], a[76:79], v[24:27]
  s_add_u32 s93, s93, 1
  s_cmp_eq_u32 s93, 10
  s_cselect_b32 s71, -9, 1
  s_lshl_b32 s71, s71, 7
  s_add_u32 s72, s72, s71
  v_mfma_f32_16x16x32_bf16 v[28:31], a[24:27], a[72:75], v[28:31]
  ds_read_b64_tr_b16 a[46:47], v137 offset:9216
  v_mfma_f32_16x16x32_bf16 v[28:31], a[28:31], a[76:79], v[28:31]
  s_add_u32 m0, s75, 106496
  v_mfma_f32_16x16x32_bf16 v[32:35], a[0:3], a[80:83], v[32:35]
  buffer_load_dwordx4 v150, s[52:55], s73 offen offset:0 lds
  v_mfma_f32_16x16x32_bf16 v[32:35], a[4:7], a[84:87], v[32:35]
  ds_read_b64_tr_b16 a[48:49], v138 offset:0
  v_mfma_f32_16x16x32_bf16 v[36:39], a[8:11], a[80:83], v[36:39]
  ds_read_b64_tr_b16 a[50:51], v138 offset:1024
  v_mfma_f32_16x16x32_bf16 v[36:39], a[12:15], a[84:87], v[36:39]
  ds_read_b64_tr_b16 a[52:53], v138 offset:8192
  v_mfma_f32_16x16x32_bf16 v[40:43], a[16:19], a[80:83], v[40:43]
  buffer_load_dwordx4 v151, s[52:55], s73 offen offset:1024 lds
  v_mfma_f32_16x16x32_bf16 v[40:43], a[20:23], a[84:87], v[40:43]
  ds_read_b64_tr_b16 a[54:55], v138 offset:9216
  v_mfma_f32_16x16x32_bf16 v[44:47], a[24:27], a[80:83], v[44:47]
  ds_read_b64_tr_b16 a[56:57], v139 offset:0
  v_mfma_f32_16x16x32_bf16 v[44:47], a[28:31], a[84:87], v[44:47]
  ds_read_b64_tr_b16 a[58:59], v139 offset:1024
  v_mfma_f32_16x16x32_bf16 v[48:51], a[0:3], a[88:91], v[48:51]
  buffer_load_dwordx4 v152, s[52:55], s73 offen offset:2048 lds
  v_mfma_f32_16x16x32_bf16 v[48:51], a[4:7], a[92:95], v[48:51]
  ds_read_b64_tr_b16 a[60:61], v139 offset:8192
  v_mfma_f32_16x16x32_bf16 v[52:55], a[8:11], a[88:91], v[52:55]
  ds_read_b64_tr_b16 a[62:63], v139 offset:9216
  v_mfma_f32_16x16x32_bf16 v[52:55], a[12:15], a[92:95], v[52:55]
  buffer_load_dwordx4 v153, s[52:55], s73 offen offset:3072 lds
  v_mfma_f32_16x16x32_bf16 v[56:59], a[16:19], a[88:91], v[56:59]
  s_add_u32 s88, s88, 1
  s_cmp_eq_u32 s88, 10
  s_cselect_b32 s71, -9, 1
  s_lshl_b32 s71, s71, 19
  s_add_u32 s73, s73, s71
  v_mfma_f32_16x16x32_bf16 v[56:59], a[20:23], a[92:95], v[56:59]
  ds_read_b128 a[120:123], v132 offset:51200
  v_mfma_f32_16x16x32_bf16 v[60:63], a[24:27], a[88:91], v[60:63]
  s_add_u32 m0, s74, 32768
  v_mfma_f32_16x16x32_bf16 v[60:63], a[28:31], a[92:95], v[60:63]
  buffer_load_dwordx4 v144, s[48:51], s72 offen offset:0 lds
  v_mfma_f32_16x16x32_bf16 v[64:67], a[0:3], a[96:99], v[64:67]
  ds_read_b128 a[124:127], v133 offset:51200
  v_mfma_f32_16x16x32_bf16 v[64:67], a[4:7], a[100:103], v[64:67]
  ds_read_b128 a[128:131], v132 offset:53248
  v_mfma_f32_16x16x32_bf16 v[68:71], a[8:11], a[96:99], v[68:71]
  ds_read_b128 a[132:135], v133 offset:53248
  v_mfma_f32_16x16x32_bf16 v[68:71], a[12:15], a[100:103], v[68:71]
  buffer_load_dwordx4 v145, s[48:51], s72 offen offset:1024 lds
  v_mfma_f32_16x16x32_bf16 v[72:75], a[16:19], a[96:99], v[72:75]
  ds_read_b128 a[136:139], v132 offset:55296
  v_mfma_f32_16x16x32_bf16 v[72:75], a[20:23], a[100:103], v[72:75]
  ds_read_b128 a[140:143], v133 offset:55296
  v_mfma_f32_16x16x32_bf16 v[76:79], a[24:27], a[96:99], v[76:79]
  ds_read_b128 a[144:147], v132 offset:57344
  v_mfma_f32_16x16x32_bf16 v[76:79], a[28:31], a[100:103], v[76:79]
  buffer_load_dwordx4 v146, s[48:51], s72 offen offset:2048 lds
  v_mfma_f32_16x16x32_bf16 v[80:83], a[0:3], a[104:107], v[80:83]
  ds_read_b128 a[148:151], v133 offset:57344
  v_mfma_f32_16x16x32_bf16 v[80:83], a[4:7], a[108:111], v[80:83]
  ds_read_b128 a[152:155], v132 offset:59392
  v_mfma_f32_16x16x32_bf16 v[84:87], a[8:11], a[104:107], v[84:87]
  ds_read_b128 a[156:159], v133 offset:59392
  v_mfma_f32_16x16x32_bf16 v[84:87], a[12:15], a[108:111], v[84:87]
  buffer_load_dwordx4 v147, s[48:51], s72 offen offset:3072 lds
  v_mfma_f32_16x16x32_bf16 v[88:91], a[16:19], a[104:107], v[88:91]
  v_mfma_f32_16x16x32_bf16 v[88:91], a[20:23], a[108:111], v[88:91]
  v_mfma_f32_16x16x32_bf16 v[92:95], a[24:27], a[104:107], v[92:95]
  v_mfma_f32_16x16x32_bf16 v[92:95], a[28:31], a[108:111], v[92:95]
  s_waitcnt vmcnt(8) lgkmcnt(0)
  s_barrier
  v_mfma_f32_16x16x32_bf16 v[0:3], a[32:35], a[112:115], v[0:3]
  s_add_u32 m0, s74, 36864
  v_mfma_f32_16x16x32_bf16 v[0:3], a[36:39], a[116:119], v[0:3]
  buffer_load_dwordx4 v148, s[48:51], s72 offen offset:0 lds
  v_mfma_f32_16x16x32_bf16 v[4:7], a[40:43], a[112:115], v[4:7]
  ds_read_b128 a[64:67], v134 offset:0
  ds_read_b128 a[68:71], v135 offset:0
  v_mfma_f32_16x16x32_bf16 v[4:7], a[44:47], a[116:119], v[4:7]
  ds_read_b64_tr_b16 a[0:1], v136 offset:16384
  ds_read_b64_tr_b16 a[2:3], v136 offset:17408
  v_mfma_f32_16x16x32_bf16 v[8:11], a[48:51], a[112:115], v[8:11]
  buffer_load_dwordx4 v149, s[48:51], s72 offen offset:1024 lds
  v_mfma_f32_16x16x32_bf16 v[8:11], a[52:55], a[116:119], v[8:11]
  s_add_u32 s93, s93, 1
  s_cmp_eq_u32 s93, 10
  s_cselect_b32 s71, -9, 1
  s_lshl_b32 s71, s71, 7
  s_add_u32 s72, s72, s71
  v_mfma_f32_16x16x32_bf16 v[12:15], a[56:59], a[112:115], v[12:15]
  ds_read_b64_tr_b16 a[4:5], v136 offset:24576
  ds_read_b64_tr_b16 a[6:7], v136 offset:25600
  v_mfma_f32_16x16x32_bf16 v[12:15], a[60:63], a[116:119], v[12:15]
  s_add_u32 m0, s75, 122880
  v_mfma_f32_16x16x32_bf16 v[16:19], a[32:35], a[120:123], v[16:19]
  buffer_load_dwordx4 v150, s[52:55], s73 offen offset:0 lds
  v_mfma_f32_16x16x32_bf16 v[16:19], a[36:39], a[124:127], v[16:19]
  ds_read_b64_tr_b16 a[8:9], v137 offset:16384
  ds_read_b64_tr_b16 a[10:11], v137 offset:17408
  v_mfma_f32_16x16x32_bf16 v[20:23], a[40:43], a[120:123], v[20:23]
  ds_read_b64_tr_b16 a[12:13], v137 offset:24576
  ds_read_b64_tr_b16 a[14:15], v137 offset:25600
  v_mfma_f32_16x16x32_bf16 v[20:23], a[44:47], a[124:127], v[20:23]
  buffer_load_dwordx4 v151, s[52:55], s73 offen offset:1024 lds
  v_mfma_f32_16x16x32_bf16 v[24:27], a[48:51], a[120:123], v[24:27]
  ds_read_b64_tr_b16 a[16:17], v138 offset:16384
  v_mfma_f32_16x16x32_bf16 v[24:27], a[52:55], a[124:127], v[24:27]
  ds_read_b64_tr_b16 a[18:19], v138 offset:17408
  v_mfma_f32_16x16x32_bf16 v[28:31], a[56:59], a[120:123], v[28:31]
  ds_read_b64_tr_b16 a[20:21], v138 offset:24576
  v_mfma_f32_16x16x32_bf16 v[28:31], a[60:63], a[124:127], v[28:31]
  buffer_load_dwordx4 v152, s[52:55], s73 offen offset:2048 lds
  v_mfma_f32_16x16x32_bf16 v[32:35], a[32:35], a[128:131], v[32:35]
  ds_read_b64_tr_b16 a[22:23], v138 offset:25600
  v_mfma_f32_16x16x32_bf16 v[32:35], a[36:39], a[132:135], v[32:35]
  ds_read_b64_tr_b16 a[24:25], v139 offset:16384
  v_mfma_f32_16x16x32_bf16 v[36:39], a[40:43], a[128:131], v[36:39]
  buffer_load_dwordx4 v153, s[52:55], s73 offen offset:3072 lds
  v_mfma_f32_16x16x32_bf16 v[36:39], a[44:47], a[132:135], v[36:39]
  s_add_u32 s88, s88, 1
  s_cmp_eq_u32 s88, 10
  s_cselect_b32 s71, -9, 1
  s_lshl_b32 s71, s71, 19
  s_add_u32 s73, s73, s71
  v_mfma_f32_16x16x32_bf16 v[40:43], a[48:51], a[128:131], v[40:43]
  ds_read_b64_tr_b16 a[26:27], v139 offset:17408
  v_mfma_f32_16x16x32_bf16 v[40:43], a[52:55], a[132:135], v[40:43]
  s_add_u32 m0, s74, 57344
  v_mfma_f32_16x16x32_bf16 v[44:47], a[56:59], a[128:131], v[44:47]
  buffer_load_dwordx4 v144, s[48:51], s72 offen offset:0 lds
  v_mfma_f32_16x16x32_bf16 v[44:47], a[60:63], a[132:135], v[44:47]
  ds_read_b64_tr_b16 a[28:29], v139 offset:24576
  v_mfma_f32_16x16x32_bf16 v[48:51], a[32:35], a[136:139], v[48:51]
  ds_read_b64_tr_b16 a[30:31], v139 offset:25600
  v_mfma_f32_16x16x32_bf16 v[48:51], a[36:39], a[140:143], v[48:51]
  buffer_load_dwordx4 v145, s[48:51], s72 offen offset:1024 lds
  v_mfma_f32_16x16x32_bf16 v[52:55], a[40:43], a[136:139], v[52:55]
  ds_read_b128 a[72:75], v134 offset:2048
  v_mfma_f32_16x16x32_bf16 v[52:55], a[44:47], a[140:143], v[52:55]
  ds_read_b128 a[76:79], v135 offset:2048
  v_mfma_f32_16x16x32_bf16 v[56:59], a[48:51], a[136:139], v[56:59]
  ds_read_b128 a[80:83], v134 offset:4096
  v_mfma_f32_16x16x32_bf16 v[56:59], a[52:55], a[140:143], v[56:59]
  buffer_load_dwordx4 v146, s[48:51], s72 offen offset:2048 lds
  v_mfma_f32_16x16x32_bf16 v[60:63], a[56:59], a[136:139], v[60:63]
  ds_read_b128 a[84:87], v135 offset:4096
  v_mfma_f32_16x16x32_bf16 v[60:63], a[60:63], a[140:143], v[60:63]
  ds_read_b128 a[88:91], v134 offset:6144
  v_mfma_f32_16x16x32_bf16 v[64:67], a[32:35], a[144:147], v[64:67]
  buffer_load_dwordx4 v147, s[48:51], s72 offen offset:3072 lds
  v_mfma_f32_16x16x32_bf16 v[64:67], a[36:39], a[148:151], v[64:67]
  ds_read_b128 a[92:95], v135 offset:6144
  v_mfma_f32_16x16x32_bf16 v[68:71], a[40:43], a[144:147], v[68:71]
  ds_read_b128 a[96:99], v134 offset:8192
  v_mfma_f32_16x16x32_bf16 v[68:71], a[44:47], a[148:151], v[68:71]
  s_add_u32 m0, s74, 61440
  v_mfma_f32_16x16x32_bf16 v[72:75], a[48:51], a[144:147], v[72:75]
  buffer_load_dwordx4 v148, s[48:51], s72 offen offset:0 lds
  v_mfma_f32_16x16x32_bf16 v[72:75], a[52:55], a[148:151], v[72:75]
  ds_read_b128 a[100:103], v135 offset:8192
  v_mfma_f32_16x16x32_bf16 v[76:79], a[56:59], a[144:147], v[76:79]
  ds_read_b128 a[104:107], v134 offset:10240
  v_mfma_f32_16x16x32_bf16 v[76:79], a[60:63], a[148:151], v[76:79]
  buffer_load_dwordx4 v149, s[48:51], s72 offen offset:1024 lds
  v_mfma_f32_16x16x32_bf16 v[80:83], a[32:35], a[152:155], v[80:83]
  s_add_u32 s93, s93, 1
  s_cmp_eq_u32 s93, 10
  s_cselect_b32 s71, -9, 1
  s_lshl_b32 s71, s71, 7
  s_add_u32 s72, s72, s71
  v_mfma_f32_16x16x32_bf16 v[80:83], a[36:39], a[156:159], v[80:83]
  ds_read_b128 a[108:111], v135 offset:10240
  v_mfma_f32_16x16x32_bf16 v[84:87], a[40:43], a[152:155], v[84:87]
  s_add_u32 m0, s75, 0
  v_mfma_f32_16x16x32_bf16 v[84:87], a[44:47], a[156:159], v[84:87]
  buffer_load_dwordx4 v150, s[52:55], s73 offen offset:0 lds
  v_mfma_f32_16x16x32_bf16 v[88:91], a[48:51], a[152:155], v[88:91]
  v_mfma_f32_16x16x32_bf16 v[88:91], a[52:55], a[156:159], v[88:91]
  v_mfma_f32_16x16x32_bf16 v[92:95], a[56:59], a[152:155], v[92:95]
  v_mfma_f32_16x16x32_bf16 v[92:95], a[60:63], a[156:159], v[92:95]
  s_waitcnt vmcnt(11) lgkmcnt(0)
  s_barrier
  v_mfma_f32_16x16x32_bf16 v[0:3], a[0:3], a[64:67], v[0:3]
  ds_read_b128 a[112:115], v132 offset:0
  ds_read_b128 a[116:119], v133 offset:0
  s_memrealtime s[36:37]
  s_memtime s[38:39]
  v_mfma_f32_16x16x32_bf16 v[0:3], a[4:7], a[68:71], v[0:3]
  buffer_load_dwordx4 v151, s[52:55], s73 offen offset:1024 lds
  v_mfma_f32_16x16x32_bf16 v[4:7], a[8:11], a[64:67], v[4:7]
  ds_read_b64_tr_b16 a[32:33], v140 offset:0
  ds_read_b64_tr_b16 a[34:35], v140 offset:1024
  v_mfma_f32_16x16x32_bf16 v[4:7], a[12:15], a[68:71], v[4:7]
  ds_read_b64_tr_b16 a[36:37], v140 offset:8192
  v_mfma_f32_16x16x32_bf16 v[8:11], a[16:19], a[64:67], v[8:11]
  buffer_load_dwordx4 v152, s[52:55], s73 offen offset:2048 lds
  v_mfma_f32_16x16x32_bf16 v[8:11], a[20:23], a[68:71], v[8:11]
  ds_read_b64_tr_b16 a[38:39], v140 offset:9216
  v_mfma_f32_16x16x32_bf16 v[12:15], a[24:27], a[64:67], v[12:15]
  ds_read_b64_tr_b16 a[40:41], v141 offset:0
  v_mfma_f32_16x16x32_bf16 v[12:15], a[28:31], a[68:71], v[12:15]
  ds_read_b64_tr_b16 a[42:43], v141 offset:1024
  v_mfma_f32_16x16x32_bf16 v[16:19], a[0:3], a[72:75], v[16:19]
  buffer_load_dwordx4 v153, s[52:55], s73 offen offset:3072 lds
  v_mfma_f32_16x16x32_bf16 v[16:19], a[4:7], a[76:79], v[16:19]
  s_add_u32 s88, s88, 1
  s_cmp_eq_u32 s88, 10
  s_cselect_b32 s71, -9, 1
  s_lshl_b32 s71, s71, 19
  s_add_u32 s73, s73, s71
  v_mfma_f32_16x16x32_bf16 v[20:23], a[8:11], a[72:75], v[20:23]
  s_add_u32 m0, s74, 81920
  v_mfma_f32_16x16x32_bf16 v[20:23], a[12:15], a[76:79], v[20:23]
  buffer_load_dwordx4 v144, s[48:51], s72 offen offset:0 lds
  v_mfma_f32_16x16x32_bf16 v[24:27], a[16:19], a[72:75], v[24:27]
  ds_read_b64_tr_b16 a[44:45], v141 offset:8192
  v_mfma_f32_16x16x32_bf16 v[24:27], a[20:23], a[76:79], v[24:27]
  ds_read_b64_tr_b16 a[46:47], v141 offset:9216
  v_mfma_f32_16x16x32_bf16 v[28:31], a[24:27], a[72:75], v[28:31]
  ds_read_b64_tr_b16 a[48:49], v142 offset:0
  v_mfma_f32_16x16x32_bf16 v[28:31], a[28:31], a[76:79], v[28:31]
  buffer_load_dwordx4 v145, s[48:51], s72 offen offset:1024 lds
  v_mfma_f32_16x16x32_bf16 v[32:35], a[0:3], a[80:83], v[32:35]
  ds_read_b64_tr_b16 a[50:51], v142 offset:1024
  v_mfma_f32_16x16x32_bf16 v[32:35], a[4:7], a[84:87], v[32:35]
  ds_read_b64_tr_b16 a[52:53], v142 offset:8192
  v_mfma_f32_16x16x32_bf16 v[36:39], a[8:11], a[80:83], v[36:39]
  buffer_load_dwordx4 v146, s[48:51], s72 offen offset:2048 lds
  v_mfma_f32_16x16x32_bf16 v[36:39], a[12:15], a[84:87], v[36:39]
  ds_read_b64_tr_b16 a[54:55], v142 offset:9216
  v_mfma_f32_16x16x32_bf16 v[40:43], a[16:19], a[80:83], v[40:43]
  ds_read_b64_tr_b16 a[56:57], v143 offset:0
  v_mfma_f32_16x16x32_bf16 v[40:43], a[20:23], a[84:87], v[40:43]
  ds_read_b64_tr_b16 a[58:59], v143 offset:1024
  v_mfma_f32_16x16x32_bf16 v[44:47], a[24:27], a[80:83], v[44:47]
  buffer_load_dwordx4 v147, s[48:51], s72 offen offset:3072 lds
  v_mfma_f32_16x16x32_bf16 v[44:47], a[28:31], a[84:87], v[44:47]
  ds_read_b64_tr_b16 a[60:61], v143 offset:8192
  v_mfma_f32_16x16x32_bf16 v[48:51], a[0:3], a[88:91], v[48:51]
  s_add_u32 m0, s74, 86016
  v_mfma_f32_16x16x32_bf16 v[48:51], a[4:7], a[92:95], v[48:51]
  buffer_load_dwordx4 v148, s[48:51], s72 offen offset:0 lds
  v_mfma_f32_16x16x32_bf16 v[52:55], a[8:11], a[88:91], v[52:55]
  ds_read_b64_tr_b16 a[62:63], v143 offset:9216
  v_mfma_f32_16x16x32_bf16 v[52:55], a[12:15], a[92:95], v[52:55]
  ds_read_b128 a[120:123], v132 offset:2048
  v_mfma_f32_16x16x32_bf16 v[56:59], a[16:19], a[88:91], v[56:59]
  ds_read_b128 a[124:127], v133 offset:2048
  v_mfma_f32_16x16x32_bf16 v[56:59], a[20:23], a[92:95], v[56:59]
  buffer_load_dwordx4 v149, s[48:51], s72 offen offset:1024 lds
  v_mfma_f32_16x16x32_bf16 v[60:63], a[24:27], a[88:91], v[60:63]
  s_add_u32 s93, s93, 1
  s_cmp_eq_u32 s93, 10
  s_cselect_b32 s71, -9, 1
  s_lshl_b32 s71, s71, 7
  s_add_u32 s72, s72, s71
  v_mfma_f32_16x16x32_bf16 v[60:63], a[28:31], a[92:95], v[60:63]
  s_add_u32 m0, s75, 16384
  v_mfma_f32_16x16x32_bf16 v[64:67], a[0:3], a[96:99], v[64:67]
  buffer_load_dwordx4 v150, s[52:55], s73 offen offset:0 lds
  v_mfma_f32_16x16x32_bf16 v[64:67], a[4:7], a[100:103], v[64:67]
  ds_read_b128 a[128:131], v132 offset:4096
  v_mfma_f32_16x16x32_bf16 v[68:71], a[8:11], a[96:99], v[68:71]
  ds_read_b128 a[132:135], v133 offset:4096
  v_mfma_f32_16x16x32_bf16 v[68:71], a[12:15], a[100:103], v[68:71]
  ds_read_b128 a[136:139], v132 offset:6144
  v_mfma_f32_16x16x32_bf16 v[72:75], a[16:19], a[96:99], v[72:75]
  buffer_load_dwordx4 v151, s[52:55], s73 offen offset:1024 lds
  v_mfma_f32_16x16x32_bf16 v[72:75], a[20:23], a[100:103], v[72:75]
  ds_read_b128 a[140:143], v133 offset:6144
  v_mfma_f32_16x16x32_bf16 v[76:79], a[24:27], a[96:99], v[76:79]
  ds_read_b128 a[144:147], v132 offset:8192
  v_mfma_f32_16x16x32_bf16 v[76:79], a[28:31], a[100:103], v[76:79]
  buffer_load_dwordx4 v152, s[52:55], s73 offen offset:2048 lds
  v_mfma_f32_16x16x32_bf16 v[80:83], a[0:3], a[104:107], v[80:83]
  ds_read_b128 a[148:151], v133 offset:8192
  v_mfma_f32_16x16x32_bf16 v[80:83], a[4:7], a[108:111], v[80:83]
  ds_read_b128 a[152:155], v132 offset:10240
  v_mfma_f32_16x16x32_bf16 v[84:87], a[8:11], a[104:107], v[84:87]
  ds_read_b128 a[156:159], v133 offset:10240
  v_mfma_f32_16x16x32_bf16 v[84:87], a[12:15], a[108:111], v[84:87]
  buffer_load_dwordx4 v153, s[52:55], s73 offen offset:3072 lds
  v_mfma_f32_16x16x32_bf16 v[88:91], a[16:19], a[104:107], v[88:91]
  s_add_u32 s88, s88, 1
  s_cmp_eq_u32 s88, 10
  s_cselect_b32 s71, -9, 1
  s_lshl_b32 s71, s71, 19
  s_add_u32 s73, s73, s71
  v_mfma_f32_16x16x32_bf16 v[88:91], a[20:23], a[108:111], v[88:91]
  v_mfma_f32_16x16x32_bf16 v[92:95], a[24:27], a[104:107], v[92:95]
  v_mfma_f32_16x16x32_bf16 v[92:95], a[28:31], a[108:111], v[92:95]
  s_waitcnt vmcnt(14) lgkmcnt(0)
  s_barrier
  v_mfma_f32_16x16x32_bf16 v[0:3], a[32:35], a[112:115], v[0:3]
  s_add_u32 m0, s74, 139264
  v_mfma_f32_16x16x32_bf16 v[0:3], a[36:39], a[116:119], v[0:3]
  buffer_load_dwordx4 v144, s[48:51], s72 offen offset:0 lds
  v_mfma_f32_16x16x32_bf16 v[4:7], a[40:43], a[112:115], v[4:7]
  ds_read_b128 a[64:67], v132 offset:24576
  ds_read_b128 a[68:71], v133 offset:24576
  v_mfma_f32_16x16x32_bf16 v[4:7], a[44:47], a[116:119], v[4:7]
  ds_read_b64_tr_b16 a[0:1], v140 offset:16384
  ds_read_b64_tr_b16 a[2:3], v140 offset:17408
  v_mfma_f32_16x16x32_bf16 v[8:11], a[48:51], a[112:115], v[8:11]
  buffer_load_dwordx4 v145, s[48:51], s72 offen offset:1024 lds
  v_mfma_f32_16x16x32_bf16 v[8:11], a[52:55], a[116:119], v[8:11]
  ds_read_b64_tr_b16 a[4:5], v140 offset:24576
  ds_read_b64_tr_b16 a[6:7], v140 offset:25600
  v_mfma_f32_16x16x32_bf16 v[12:15], a[56:59], a[112:115], v[12:15]
  ds_read_b64_tr_b16 a[8:9], v141 offset:16384
  v_mfma_f32_16x16x32_bf16 v[12:15], a[60:63], a[116:119], v[12:15]
  ds_read_b64_tr_b16 a[10:11], v141 offset:17408
  v_mfma_f32_16x16x32_bf16 v[16:19], a[32:35], a[120:123], v[16:19]
  buffer_load_dwordx4 v146, s[48:51], s72 offen offset:2048 lds
  v_mfma_f32_16x16x32_bf16 v[16:19], a[36:39], a[124:127], v[16:19]
  ds_read_b64_tr_b16 a[12:13], v141 offset:24576
  v_mfma_f32_16x16x32_bf16 v[20:23], a[40:43], a[120:123], v[20:23]
  ds_read_b64_tr_b16 a[14:15], v141 offset:25600
  v_mfma_f32_16x16x32_bf16 v[20:23], a[44:47], a[124:127], v[20:23]
  buffer_load_dwordx4 v147, s[48:51], s72 offen offset:3072 lds
  v_mfma_f32_16x16x32_bf16 v[24:27], a[48:51], a[120:123], v[24:27]
  ds_read_b64_tr_b16 a[16:17], v142 offset:16384
  v_mfma_f32_16x16x32_bf16 v[24:27], a[52:55], a[124:127], v[24:27]
  ds_read_b64_tr_b16 a[18:19], v142 offset:17408
  v_mfma_f32_16x16x32_bf16 v[28:31], a[56:59], a[120:123], v[28:31]
  s_add_u32 m0, s74, 143360
  v_mfma_f32_16x16x32_bf16 v[28:31], a[60:63], a[124:127], v[28:31]
  buffer_load_dwordx4 v148, s[48:51], s72 offen offset:0 lds
  v_mfma_f32_16x16x32_bf16 v[32:35], a[32:35], a[128:131], v[32:35]
  ds_read_b64_tr_b16 a[20:21], v142 offset:24576
  v_mfma_f32_16x16x32_bf16 v[32:35], a[36:39], a[132:135], v[32:35]
  ds_read_b64_tr_b16 a[22:23], v142 offset:25600
  v_mfma_f32_16x16x32_bf16 v[36:39], a[40:43], a[128:131], v[36:39]
  buffer_load_dwordx4 v149, s[48:51], s72 offen offset:1024 lds
  v_mfma_f32_16x16x32_bf16 v[36:39], a[44:47], a[132:135], v[36:39]
  s_add_u32 s93, s93, 1
  s_cmp_eq_u32 s93, 10
  s_cselect_b32 s71, -9, 1
  s_lshl_b32 s71, s71, 7
  s_add_u32 s72, s72, s71
  v_mfma_f32_16x16x32_bf16 v[40:43], a[48:51], a[128:131], v[40:43]
  ds_read_b64_tr_b16 a[24:25], v143 offset:16384
  v_mfma_f32_16x16x32_bf16 v[40:43], a[52:55], a[132:135], v[40:43]
  s_add_u32 m0, s75, 106496
  v_mfma_f32_16x16x32_bf16 v[44:47], a[56:59], a[128:131], v[44:47]
  buffer_load_dwordx4 v150, s[52:55], s73 offen offset:0 lds
  v_mfma_f32_16x16x32_bf16 v[44:47], a[60:63], a[132:135], v[44:47]
  ds_read_b64_tr_b16 a[26:27], v143 offset:17408
  v_mfma_f32_16x16x32_bf16 v[48:51], a[32:35], a[136:139], v[48:51]
  ds_read_b64_tr_b16 a[28:29], v143 offset:24576
  v_mfma_f32_16x16x32_bf16 v[48:51], a[36:39], a[140:143], v[48:51]
  buffer_load_dwordx4 v151, s[52:55], s73 offen offset:1024 lds
  v_mfma_f32_16x16x32_bf16 v[52:55], a[40:43], a[136:139], v[52:55]
  ds_read_b64_tr_b16 a[30:31], v143 offset:25600
  v_mfma_f32_16x16x32_bf16 v[52:55], a[44:47], a[140:143], v[52:55]
  ds_read_b128 a[72:75], v132 offset:26624
  v_mfma_f32_16x16x32_bf16 v[56:59], a[48:51], a[136:139], v[56:59]
  ds_read_b128 a[76:79], v133 offset:26624
  v_mfma_f32_16x16x32_bf16 v[56:59], a[52:55], a[140:143], v[56:59]
  buffer_load_dwordx4 v152, s[52:55], s73 offen offset:2048 lds
  v_mfma_f32_16x16x32_bf16 v[60:63], a[56:59], a[136:139], v[60:63]
  ds_read_b128 a[80:83], v132 offset:28672
  v_mfma_f32_16x16x32_bf16 v[60:63], a[60:63], a[140:143], v[60:63]
  ds_read_b128 a[84:87], v133 offset:28672
  v_mfma_f32_16x16x32_bf16 v[64:67], a[32:35], a[144:147], v[64:67]
  buffer_load_dwordx4 v153, s[52:55], s73 offen offset:3072 lds
  v_mfma_f32_16x16x32_bf16 v[64:67], a[36:39], a[148:151], v[64:67]
  s_add_u32 s88, s88, 1
  s_cmp_eq_u32 s88, 10
  s_cselect_b32 s71, -9, 1
  s_lshl_b32 s71, s71, 19
  s_add_u32 s73, s73, s71
  v_mfma_f32_16x16x32_bf16 v[68:71], a[40:43], a[144:147], v[68:71]
  ds_read_b128 a[88:91], v132 offset:30720
  v_mfma_f32_16x16x32_bf16 v[68:71], a[44:47], a[148:151], v[68:71]
  s_add_u32 m0, s74, 32768
  v_mfma_f32_16x16x32_bf16 v[72:75], a[48:51], a[144:147], v[72:75]
  buffer_load_dwordx4 v144, s[48:51], s72 offen offset:0 lds
  v_mfma_f32_16x16x32_bf16 v[72:75], a[52:55], a[148:151], v[72:75]
  ds_read_b128 a[92:95], v133 offset:30720
  v_mfma_f32_16x16x32_bf16 v[76:79], a[56:59], a[144:147], v[76:79]
  ds_read_b128 a[96:99], v132 offset:32768
  v_mfma_f32_16x16x32_bf16 v[76:79], a[60:63], a[148:151], v[76:79]
  buffer_load_dwordx4 v145, s[48:51], s72 offen offset:1024 lds
  v_mfma_f32_16x16x32_bf16 v[80:83], a[32:35], a[152:155], v[80:83]
  ds_read_b128 a[100:103], v133 offset:32768
  v_mfma_f32_16x16x32_bf16 v[80:83], a[36:39], a[156:159], v[80:83]
  ds_read_b128 a[104:107], v132 offset:34816
  v_mfma_f32_16x16x32_bf16 v[84:87], a[40:43], a[152:155], v[84:87]
  ds_read_b128 a[108:111], v133 offset:34816
  v_mfma_f32_16x16x32_bf16 v[84:87], a[44:47], a[156:159], v[84:87]
  buffer_load_dwordx4 v146, s[48:51], s72 offen offset:2048 lds
  v_mfma_f32_16x16x32_bf16 v[88:91], a[48:51], a[152:155], v[88:91]
  v_mfma_f32_16x16x32_bf16 v[88:91], a[52:55], a[156:159], v[88:91]
  v_mfma_f32_16x16x32_bf16 v[92:95], a[56:59], a[152:155], v[92:95]
  v_mfma_f32_16x16x32_bf16 v[92:95], a[60:63], a[156:159], v[92:95]
  s_waitcnt vmcnt(17) lgkmcnt(0)
  s_barrier
  v_mfma_f32_16x16x32_bf16 v[0:3], a[0:3], a[64:67], v[0:3]
  ds_read_b128 a[112:115], v132 offset:49152
  ds_read_b128 a[116:119], v133 offset:49152
  v_mfma_f32_16x16x32_bf16 v[0:3], a[4:7], a[68:71], v[0:3]
  buffer_load_dwordx4 v147, s[48:51], s72 offen offset:3072 lds
  v_mfma_f32_16x16x32_bf16 v[4:7], a[8:11], a[64:67], v[4:7]
  ds_read_b64_tr_b16 a[32:33], v136 offset:0
  ds_read_b64_tr_b16 a[34:35], v136 offset:1024
  v_mfma_f32_16x16x32_bf16 v[4:7], a[12:15], a[68:71], v[4:7]
  s_add_u32 m0, s74, 36864
  v_mfma_f32_16x16x32_bf16 v[8:11], a[16:19], a[64:67], v[8:11]
  buffer_load_dwordx4 v148, s[48:51], s72 offen offset:0 lds
  v_mfma_f32_16x16x32_bf16 v[8:11], a[20:23], a[68:71], v[8:11]
  ds_read_b64_tr_b16 a[36:37], v136 offset:8192
  v_mfma_f32_16x16x32_bf16 v[12:15], a[24:27], a[64:67], v[12:15]
  ds_read_b64_tr_b16 a[38:39], v136 offset:9216
  v_mfma_f32_16x16x32_bf16 v[12:15], a[28:31], a[68:71], v[12:15]
  ds_read_b64_tr_b16 a[40:41], v137 offset:0
  v_mfma_f32_16x16x32_bf16 v[16:19], a[0:3], a[72:75], v[16:19]
  buffer_load_dwordx4 v149, s[48:51], s72 offen offset:1024 lds
  v_mfma_f32_16x16x32_bf16 v[16:19], a[4:7], a[76:79], v[16:19]
  s_add_u32 s93, s93, 1
  s_cmp_eq_u32 s93, 10
  s_cselect_b32 s71, -9, 1
  s_lshl_b32 s71, s71, 7
  s_add_u32 s72, s72, s71
  v_mfma_f32_16x16x32_bf16 v[20:23], a[8:11], a[72:75], v[20:23]
  s_add_u32 m0, s75, 122880
  v_mfma_f32_16x16x32_bf16 v[20:23], a[12:15], a[76:79], v[20:23]
  buffer_load_dwordx4 v150, s[52:55], s73 offen offset:0 lds
  v_mfma_f32_16x16x32_bf16 v[24:27], a[16:19], a[72:75], v[24:27]
  ds_read_b64_tr_b16 a[42:43], v137 offset:1024
  v_mfma_f32_16x16x32_bf16 v[24:27], a[20:23], a[76:79], v[24:27]
  ds_read_b64_tr_b16 a[44:45], v137 offset:8192
  v_mfma_f32_16x16x32_bf16 v[28:31], a[24:27], a[72:75], v[28:31]
  ds_read_b64_tr_b16 a[46:47], v137 offset:9216
  v_mfma_f32_16x16x32_bf16 v[28:31], a[28:31], a[76:79], v[28:31]
  buffer_load_dwordx4 v151, s[52:55], s73 offen offset:1024 lds
  v_mfma_f32_16x16x32_bf16 v[32:35], a[0:3], a[80:83], v[32:35]
  ds_read_b64_tr_b16 a[48:49], v138 offset:0
  v_mfma_f32_16x16x32_bf16 v[32:35], a[4:7], a[84:87], v[32:35]
  ds_read_b64_tr_b16 a[50:51], v138 offset:1024
  v_mfma_f32_16x16x32_bf16 v[36:39], a[8:11], a[80:83], v[36:39]
  buffer_load_dwordx4 v152, s[52:55], s73 offen offset:2048 lds
  v_mfma_f32_16x16x32_bf16 v[36:39], a[12:15], a[84:87], v[36:39]
  ds_read_b64_tr_b16 a[52:53], v138 offset:8192
  v_mfma_f32_16x16x32_bf16 v[40:43], a[16:19], a[80:83], v[40:43]
  ds_read_b64_tr_b16 a[54:55], v138 offset:9216
  v_mfma_f32_16x16x32_bf16 v[40:43], a[20:23], a[84:87], v[40:43]
  ds_read_b64_tr_b16 a[56:57], v139 offset:0
  v_mfma_f32_16x16x32_bf16 v[44:47], a[24:27], a[80:83], v[44:47]
  buffer_load_dwordx4 v153, s[52:55], s73 offen offset:3072 lds
  v_mfma_f32_16x16x32_bf16 v[44:47], a[28:31], a[84:87], v[44:47]
  ds_read_b64_tr_b16 a[58:59], v139 offset:1024
  v_mfma_f32_16x16x32_bf16 v[48:51], a[0:3], a[88:91], v[48:51]
  s_add_u32 m0, s74, 57344
  v_mfma_f32_16x16x32_bf16 v[48:51], a[4:7], a[92:95], v[48:51]
  buffer_load_dwordx4 v144, s[48:51], s72 offen offset:0 lds
  v_mfma_f32_16x16x32_bf16 v[52:55], a[8:11], a[88:91], v[52:55]
  ds_read_b64_tr_b16 a[60:61], v139 offset:8192
  v_mfma_f32_16x16x32_bf16 v[52:55], a[12:15], a[92:95], v[52:55]
  ds_read_b64_tr_b16 a[62:63], v139 offset:9216
  v_mfma_f32_16x16x32_bf16 v[56:59], a[16:19], a[88:91], v[56:59]
  ds_read_b128 a[120:123], v132 offset:51200
  v_mfma_f32_16x16x32_bf16 v[56:59], a[20:23], a[92:95], v[56:59]
  buffer_load_dwordx4 v145, s[48:51], s72 offen offset:1024 lds
  v_mfma_f32_16x16x32_bf16 v[60:63], a[24:27], a[88:91], v[60:63]
  ds_read_b128 a[124:127], v133 offset:51200
  v_mfma_f32_16x16x32_bf16 v[60:63], a[28:31], a[92:95], v[60:63]
  ds_read_b128 a[128:131], v132 offset:53248
  v_mfma_f32_16x16x32_bf16 v[64:67], a[0:3], a[96:99], v[64:67]
  buffer_load_dwordx4 v146, s[48:51], s72 offen offset:2048 lds
  v_mfma_f32_16x16x32_bf16 v[64:67], a[4:7], a[100:103], v[64:67]
  ds_read_b128 a[132:135], v133 offset:53248
  v_mfma_f32_16x16x32_bf16 v[68:71], a[8:11], a[96:99], v[68:71]
  ds_read_b128 a[136:139], v132 offset:55296
  v_mfma_f32_16x16x32_bf16 v[68:71], a[12:15], a[100:103], v[68:71]
  ds_read_b128 a[140:143], v133 offset:55296
  v_mfma_f32_16x16x32_bf16 v[72:75], a[16:19], a[96:99], v[72:75]
  buffer_load_dwordx4 v147, s[48:51], s72 offen offset:3072 lds
  v_mfma_f32_16x16x32_bf16 v[72:75], a[20:23], a[100:103], v[72:75]
  ds_read_b128 a[144:147], v132 offset:57344
  v_mfma_f32_16x16x32_bf16 v[76:79], a[24:27], a[96:99], v[76:79]
  s_add_u32 m0, s74, 61440
  v_mfma_f32_16x16x32_bf16 v[76:79], a[28:31], a[100:103], v[76:79]
  buffer_load_dwordx4 v148, s[48:51], s72 offen offset:0 lds
  v_mfma_f32_16x16x32_bf16 v[80:83], a[0:3], a[104:107], v[80:83]
  ds_read_b128 a[148:151], v133 offset:57344
  v_mfma_f32_16x16x32_bf16 v[80:83], a[4:7], a[108:111], v[80:83]
  ds_read_b128 a[152:155], v132 offset:59392
  v_mfma_f32_16x16x32_bf16 v[84:87], a[8:11], a[104:107], v[84:87]
  ds_read_b128 a[156:159], v133 offset:59392
  v_mfma_f32_16x16x32_bf16 v[84:87], a[12:15], a[108:111], v[84:87]
  buffer_load_dwordx4 v149, s[48:51], s72 offen offset:1024 lds
  v_mfma_f32_16x16x32_bf16 v[88:91], a[16:19], a[104:107], v[88:91]
  v_mfma_f32_16x16x32_bf16 v[88:91], a[20:23], a[108:111], v[88:91]
  v_mfma_f32_16x16x32_bf16 v[92:95], a[24:27], a[104:107], v[92:95]
  v_mfma_f32_16x16x32_bf16 v[92:95], a[28:31], a[108:111], v[92:95]
  s_waitcnt vmcnt(20) lgkmcnt(0)
  s_barrier
  v_mfma_f32_16x16x32_bf16 v[0:3], a[32:35], a[112:115], v[0:3]
  s_add_u32 s68, s60, 0
  s_add_u32 m0, s61, 0
  v_mfma_f32_16x16x32_bf16 v[0:3], a[36:39], a[116:119], v[0:3]
  buffer_load_dwordx4 v240, s[40:43], s68 offen offset:0 nt lds
  v_mfma_f32_16x16x32_bf16 v[4:7], a[40:43], a[112:115], v[4:7]
  ds_read_b128 a[64:67], v134 offset:0
  v_mfma_f32_16x16x32_bf16 v[4:7], a[44:47], a[116:119], v[4:7]
  ds_read_b128 a[68:71], v135 offset:0
  v_mfma_f32_16x16x32_bf16 v[8:11], a[48:51], a[112:115], v[8:11]
  ds_read_b64_tr_b16 a[0:1], v136 offset:16384
  v_mfma_f32_16x16x32_bf16 v[8:11], a[52:55], a[116:119], v[8:11]
  ds_read_b64_tr_b16 a[2:3], v136 offset:17408
  v_mfma_f32_16x16x32_bf16 v[12:15], a[56:59], a[112:115], v[12:15]
  buffer_load_dwordx4 v241, s[40:43], s68 offen offset:1024 nt lds
  v_mfma_f32_16x16x32_bf16 v[12:15], a[60:63], a[116:119], v[12:15]
  ds_read_b64_tr_b16 a[4:5], v136 offset:24576
  v_mfma_f32_16x16x32_bf16 v[16:19], a[32:35], a[120:123], v[16:19]
  ds_read_b64_tr_b16 a[6:7], v136 offset:25600
  v_mfma_f32_16x16x32_bf16 v[16:19], a[36:39], a[124:127], v[16:19]
  ds_read_b64_tr_b16 a[8:9], v137 offset:16384
  v_mfma_f32_16x16x32_bf16 v[20:23], a[40:43], a[120:123], v[20:23]
  ds_read_b64_tr_b16 a[10:11], v137 offset:17408
  v_mfma_f32_16x16x32_bf16 v[20:23], a[44:47], a[124:127], v[20:23]
  ds_read_b64_tr_b16 a[12:13], v137 offset:24576
  v_mfma_f32_16x16x32_bf16 v[24:27], a[48:51], a[120:123], v[24:27]
  buffer_load_dwordx4 v242, s[40:43], s68 offen offset:2048 nt lds
  v_mfma_f32_16x16x32_bf16 v[24:27], a[52:55], a[124:127], v[24:27]
  ds_read_b64_tr_b16 a[14:15], v137 offset:25600
  v_mfma_f32_16x16x32_bf16 v[28:31], a[56:59], a[120:123], v[28:31]
  ds_read_b64_tr_b16 a[16:17], v138 offset:16384
  v_mfma_f32_16x16x32_bf16 v[28:31], a[60:63], a[124:127], v[28:31]
  ds_read_b64_tr_b16 a[18:19], v138 offset:17408
  v_mfma_f32_16x16x32_bf16 v[32:35], a[32:35], a[128:131], v[32:35]
  ds_read_b64_tr_b16 a[20:21], v138 offset:24576
  v_mfma_f32_16x16x32_bf16 v[32:35], a[36:39], a[132:135], v[32:35]
  ds_read_b64_tr_b16 a[22:23], v138 offset:25600
  v_mfma_f32_16x16x32_bf16 v[36:39], a[40:43], a[128:131], v[36:39]
  buffer_load_dwordx4 v243, s[40:43], s68 offen offset:3072 nt lds
  v_mfma_f32_16x16x32_bf16 v[36:39], a[44:47], a[132:135], v[36:39]
  ds_read_b64_tr_b16 a[24:25], v139 offset:16384
  v_mfma_f32_16x16x32_bf16 v[40:43], a[48:51], a[128:131], v[40:43]
  ds_read_b64_tr_b16 a[26:27], v139 offset:17408
  v_mfma_f32_16x16x32_bf16 v[40:43], a[52:55], a[132:135], v[40:43]
  ds_read_b64_tr_b16 a[28:29], v139 offset:24576
  v_mfma_f32_16x16x32_bf16 v[44:47], a[56:59], a[128:131], v[44:47]
  ds_read_b64_tr_b16 a[30:31], v139 offset:25600
  v_mfma_f32_16x16x32_bf16 v[44:47], a[60:63], a[132:135], v[44:47]
  s_add_u32 s68, s60, 0
  s_add_u32 m0, s62, 0
  v_mfma_f32_16x16x32_bf16 v[48:51], a[32:35], a[136:139], v[48:51]
  buffer_load_dwordx4 v244, s[44:47], s68 offen offset:0 nt lds
  v_mfma_f32_16x16x32_bf16 v[48:51], a[36:39], a[140:143], v[48:51]
  ds_read_b128 a[72:75], v134 offset:2048
  v_mfma_f32_16x16x32_bf16 v[52:55], a[40:43], a[136:139], v[52:55]
  ds_read_b128 a[76:79], v135 offset:2048
  v_mfma_f32_16x16x32_bf16 v[52:55], a[44:47], a[140:143], v[52:55]
  ds_read_b128 a[80:83], v134 offset:4096
  v_mfma_f32_16x16x32_bf16 v[56:59], a[48:51], a[136:139], v[56:59]
  ds_read_b128 a[84:87], v135 offset:4096
  v_mfma_f32_16x16x32_bf16 v[56:59], a[52:55], a[140:143], v[56:59]
  buffer_load_dwordx4 v245, s[44:47], s68 offen offset:1024 nt lds
  v_mfma_f32_16x16x32_bf16 v[60:63], a[56:59], a[136:139], v[60:63]
  ds_read_b128 a[88:91], v134 offset:6144
  v_mfma_f32_16x16x32_bf16 v[60:63], a[60:63], a[140:143], v[60:63]
  ds_read_b128 a[92:95], v135 offset:6144
  v_mfma_f32_16x16x32_bf16 v[64:67], a[32:35], a[144:147], v[64:67]
  ds_read_b128 a[96:99], v134 offset:8192
  v_mfma_f32_16x16x32_bf16 v[64:67], a[36:39], a[148:151], v[64:67]
  ds_read_b128 a[100:103], v135 offset:8192
  v_mfma_f32_16x16x32_bf16 v[68:71], a[40:43], a[144:147], v[68:71]
  ds_read_b128 a[104:107], v134 offset:10240
  v_mfma_f32_16x16x32_bf16 v[68:71], a[44:47], a[148:151], v[68:71]
  buffer_load_dwordx4 v244, s[44:47], s68 offen offset:2048 nt lds
  v_mfma_f32_16x16x32_bf16 v[72:75], a[48:51], a[144:147], v[72:75]
  ds_read_b128 a[108:111], v135 offset:10240
  v_mfma_f32_16x16x32_bf16 v[72:75], a[52:55], a[148:151], v[72:75]
  v_mfma_f32_16x16x32_bf16 v[76:79], a[56:59], a[144:147], v[76:79]
  v_mfma_f32_16x16x32_bf16 v[76:79], a[60:63], a[148:151], v[76:79]
  v_mfma_f32_16x16x32_bf16 v[80:83], a[32:35], a[152:155], v[80:83]
  v_mfma_f32_16x16x32_bf16 v[80:83], a[36:39], a[156:159], v[80:83]
  buffer_load_dwordx4 v245, s[44:47], s68 offen offset:3072 nt lds
  v_mfma_f32_16x16x32_bf16 v[84:87], a[40:43], a[152:155], v[84:87]
  v_mfma_f32_16x16x32_bf16 v[84:87], a[44:47], a[156:159], v[84:87]
  v_mfma_f32_16x16x32_bf16 v[88:91], a[48:51], a[152:155], v[88:91]
  v_mfma_f32_16x16x32_bf16 v[88:91], a[52:55], a[156:159], v[88:91]
  v_mfma_f32_16x16x32_bf16 v[92:95], a[56:59], a[152:155], v[92:95]
  v_mfma_f32_16x16x32_bf16 v[92:95], a[60:63], a[156:159], v[92:95]
  s_waitcnt vmcnt(8) lgkmcnt(0)
  s_barrier
  v_mfma_f32_16x16x32_bf16 v[0:3], a[0:3], a[64:67], v[0:3]
  s_add_u32 s68, s60, 16384
  s_add_u32 m0, s61, 49152
  v_mfma_f32_16x16x32_bf16 v[0:3], a[4:7], a[68:71], v[0:3]
  buffer_load_dwordx4 v240, s[40:43], s68 offen offset:0 nt lds
  v_mfma_f32_16x16x32_bf16 v[4:7], a[8:11], a[64:67], v[4:7]
  ds_read_b128 a[112:115], v132 offset:0
  ds_read_b128 a[116:119], v133 offset:0
  v_mfma_f32_16x16x32_bf16 v[4:7], a[12:15], a[68:71], v[4:7]
  ds_read_b64_tr_b16 a[32:33], v140 offset:0
  ds_read_b64_tr_b16 a[34:35], v140 offset:1024
  v_mfma_f32_16x16x32_bf16 v[8:11], a[16:19], a[64:67], v[8:11]
  ds_read_b64_tr_b16 a[36:37], v140 offset:8192
  ds_read_b64_tr_b16 a[38:39], v140 offset:9216
  v_mfma_f32_16x16x32_bf16 v[8:11], a[20:23], a[68:71], v[8:11]
  ds_read_b64_tr_b16 a[40:41], v141 offset:0
  ds_read_b64_tr_b16 a[42:43], v141 offset:1024
  v_mfma_f32_16x16x32_bf16 v[12:15], a[24:27], a[64:67], v[12:15]
  ds_read_b64_tr_b16 a[44:45], v141 offset:8192
  ds_read_b64_tr_b16 a[46:47], v141 offset:9216
  v_mfma_f32_16x16x32_bf16 v[12:15], a[28:31], a[68:71], v[12:15]
  ds_read_b64_tr_b16 a[48:49], v142 offset:0
  ds_read_b64_tr_b16 a[50:51], v142 offset:1024
  v_mfma_f32_16x16x32_bf16 v[16:19], a[0:3], a[72:75], v[16:19]
  ds_read_b64_tr_b16 a[52:53], v142 offset:8192
  ds_read_b64_tr_b16 a[54:55], v142 offset:9216
  v_mfma_f32_16x16x32_bf16 v[16:19], a[4:7], a[76:79], v[16:19]
  ds_read_b64_tr_b16 a[56:57], v143 offset:0
  ds_read_b64_tr_b16 a[58:59], v143 offset:1024
  v_mfma_f32_16x16x32_bf16 v[20:23], a[8:11], a[72:75], v[20:23]
  ds_read_b64_tr_b16 a[60:61], v143 offset:8192
  ds_read_b64_tr_b16 a[62:63], v143 offset:9216
  v_mfma_f32_16x16x32_bf16 v[20:23], a[12:15], a[76:79], v[20:23]
  ds_read_b128 a[120:123], v132 offset:2048
  ds_read_b128 a[124:127], v133 offset:2048
  v_mfma_f32_16x16x32_bf16 v[24:27], a[16:19], a[72:75], v[24:27]
  buffer_load_dwordx4 v241, s[40:43], s68 offen offset:1024 nt lds
  v_mfma_f32_16x16x32_bf16 v[24:27], a[20:23], a[76:79], v[24:27]
  ds_read_b128 a[128:131], v132 offset:4096
  ds_read_b128 a[132:135], v133 offset:4096
  v_mfma_f32_16x16x32_bf16 v[28:31], a[24:27], a[72:75], v[28:31]
  ds_read_b128 a[136:139], v132 offset:6144
  ds_read_b128 a[140:143], v133 offset:6144
  v_mfma_f32_16x16x32_bf16 v[28:31], a[28:31], a[76:79], v[28:31]
  ds_read_b128 a[144:147], v132 offset:8192
  ds_read_b128 a[148:151], v133 offset:8192
  v_mfma_f32_16x16x32_bf16 v[32:35], a[0:3], a[80:83], v[32:35]
  ds_read_b128 a[152:155], v132 offset:10240
  ds_read_b128 a[156:159], v133 offset:10240
  v_mfma_f32_16x16x32_bf16 v[32:35], a[4:7], a[84:87], v[32:35]
  ds_read_b128 a[192:195], v132 offset:24576
  ds_read_b128 a[196:199], v133 offset:24576
  v_mfma_f32_16x16x32_bf16 v[36:39], a[8:11], a[80:83], v[36:39]
  ds_read_b64_tr_b16 a[160:161], v140 offset:16384
  ds_read_b64_tr_b16 a[162:163], v140 offset:17408
  v_mfma_f32_16x16x32_bf16 v[36:39], a[12:15], a[84:87], v[36:39]
  ds_read_b64_tr_b16 a[164:165], v140 offset:24576
  ds_read_b64_tr_b16 a[166:167], v140 offset:25600
  v_mfma_f32_16x16x32_bf16 v[40:43], a[16:19], a[80:83], v[40:43]
  ds_read_b64_tr_b16 a[168:169], v141 offset:16384
  ds_read_b64_tr_b16 a[170:171], v141 offset:17408
  v_mfma_f32_16x16x32_bf16 v[40:43], a[20:23], a[84:87], v[40:43]
  ds_read_b64_tr_b16 a[172:173], v141 offset:24576
  v_mfma_f32_16x16x32_bf16 v[44:47], a[24:27], a[80:83], v[44:47]
  ds_read_b64_tr_b16 a[174:175], v141 offset:25600
  v_mfma_f32_16x16x32_bf16 v[44:47], a[28:31], a[84:87], v[44:47]
  ds_read_b64_tr_b16 a[176:177], v142 offset:16384
  v_mfma_f32_16x16x32_bf16 v[48:51], a[0:3], a[88:91], v[48:51]
  buffer_load_dwordx4 v242, s[40:43], s68 offen offset:2048 nt lds
  v_mfma_f32_16x16x32_bf16 v[48:51], a[4:7], a[92:95], v[48:51]
  ds_read_b64_tr_b16 a[178:179], v142 offset:17408
  v_mfma_f32_16x16x32_bf16 v[52:55], a[8:11], a[88:91], v[52:55]
  ds_read_b64_tr_b16 a[180:181], v142 offset:24576
  v_mfma_f32_16x16x32_bf16 v[52:55], a[12:15], a[92:95], v[52:55]
  ds_read_b64_tr_b16 a[182:183], v142 offset:25600
  v_mfma_f32_16x16x32_bf16 v[56:59], a[16:19], a[88:91], v[56:59]
  ds_read_b64_tr_b16 a[184:185], v143 offset:16384
  v_mfma_f32_16x16x32_bf16 v[56:59], a[20:23], a[92:95], v[56:59]
  ds_read_b64_tr_b16 a[186:187], v143 offset:17408
  v_mfma_f32_16x16x32_bf16 v[60:63], a[24:27], a[88:91], v[60:63]
  ds_read_b64_tr_b16 a[188:189], v143 offset:24576
  v_mfma_f32_16x16x32_bf16 v[60:63], a[28:31], a[92:95], v[60:63]
  ds_read_b64_tr_b16 a[190:191], v143 offset:25600
  v_mfma_f32_16x16x32_bf16 v[64:67], a[0:3], a[96:99], v[64:67]
  ds_read_b128 a[200:203], v132 offset:26624
  v_mfma_f32_16x16x32_bf16 v[64:67], a[4:7], a[100:103], v[64:67]
  ds_read_b128 a[204:207], v133 offset:26624
  v_mfma_f32_16x16x32_bf16 v[68:71], a[8:11], a[96:99], v[68:71]
  ds_read_b128 a[208:211], v132 offset:28672
  v_mfma_f32_16x16x32_bf16 v[68:71], a[12:15], a[100:103], v[68:71]
  buffer_load_dwordx4 v243, s[40:43], s68 offen offset:3072 nt lds
  v_mfma_f32_16x16x32_bf16 v[72:75], a[16:19], a[96:99], v[72:75]
  ds_read_b128 a[212:215], v133 offset:28672
  v_mfma_f32_16x16x32_bf16 v[72:75], a[20:23], a[100:103], v[72:75]
  ds_read_b128 a[216:219], v132 offset:30720
  v_mfma_f32_16x16x32_bf16 v[76:79], a[24:27], a[96:99], v[76:79]
  ds_read_b128 a[220:223], v133 offset:30720
  v_mfma_f32_16x16x32_bf16 v[76:79], a[28:31], a[100:103], v[76:79]
  ds_read_b128 a[224:227], v132 offset:32768
  v_mfma_f32_16x16x32_bf16 v[80:83], a[0:3], a[104:107], v[80:83]
  ds_read_b128 a[228:231], v133 offset:32768
  v_mfma_f32_16x16x32_bf16 v[80:83], a[4:7], a[108:111], v[80:83]
  ds_read_b128 a[232:235], v132 offset:34816
  v_mfma_f32_16x16x32_bf16 v[84:87], a[8:11], a[104:107], v[84:87]
  ds_read_b128 a[236:239], v133 offset:34816
  v_mfma_f32_16x16x32_bf16 v[84:87], a[12:15], a[108:111], v[84:87]
  v_mfma_f32_16x16x32_bf16 v[88:91], a[16:19], a[104:107], v[88:91]
  v_mfma_f32_16x16x32_bf16 v[88:91], a[20:23], a[108:111], v[88:91]
  v_mfma_f32_16x16x32_bf16 v[92:95], a[24:27], a[104:107], v[92:95]
  v_mfma_f32_16x16x32_bf16 v[92:95], a[28:31], a[108:111], v[92:95]
  s_waitcnt lgkmcnt(0)
  s_barrier
  v_mfma_f32_16x16x32_bf16 v[0:3], a[32:35], a[112:115], v[0:3]
  s_add_u32 s68, s60, 32768
  s_add_u32 m0, s61, 16384
  v_mfma_f32_16x16x32_bf16 v[0:3], a[36:39], a[116:119], v[0:3]
  buffer_load_dwordx4 v240, s[40:43], s68 offen offset:0 nt lds
  v_mfma_f32_16x16x32_bf16 v[0:3], a[160:163], a[192:195], v[0:3]
  v_mfma_f32_16x16x32_bf16 v[0:3], a[164:167], a[196:199], v[0:3]
  v_mfma_f32_16x16x32_bf16 v[4:7], a[40:43], a[112:115], v[4:7]
  v_mfma_f32_16x16x32_bf16 v[4:7], a[44:47], a[116:119], v[4:7]
  v_mfma_f32_16x16x32_bf16 v[4:7], a[168:171], a[192:195], v[4:7]
  v_mfma_f32_16x16x32_bf16 v[4:7], a[172:175], a[196:199], v[4:7]
  s_nop 3
  v_mul_f32 v0, s76, v0
  v_mul_f32 v1, s76, v1
  v_mfma_f32_16x16x32_bf16 v[8:11], a[48:51], a[112:115], v[8:11]
  v_mul_f32 v2, s76, v2
  v_mul_f32 v3, s76, v3
  v_mfma_f32_16x16x32_bf16 v[8:11], a[52:55], a[116:119], v[8:11]
  v_cvt_pk_bf16_f32 v0, v0, v1
  v_cvt_pk_bf16_f32 v1, v2, v3
  v_mfma_f32_16x16x32_bf16 v[8:11], a[176:179], a[192:195], v[8:11]
  ds_write_b64 v154, v[0:1] offset:0
  v_mfma_f32_16x16x32_bf16 v[8:11], a[180:183], a[196:199], v[8:11]
  v_mul_f32 v4, s76, v4
  v_mul_f32 v5, s76, v5
  v_mfma_f32_16x16x32_bf16 v[12:15], a[56:59], a[112:115], v[12:15]
  buffer_load_dwordx4 v241, s[40:43], s68 offen offset:1024 nt lds
  v_mul_f32 v6, s76, v6
  v_mul_f32 v7, s76, v7
  v_mfma_f32_16x16x32_bf16 v[12:15], a[60:63], a[116:119], v[12:15]
  v_cvt_pk_bf16_f32 v4, v4, v5
  v_cvt_pk_bf16_f32 v5, v6, v7
  v_mfma_f32_16x16x32_bf16 v[12:15], a[184:187], a[192:195], v[12:15]
  ds_write_b64 v155, v[4:5] offset:0
  v_mfma_f32_16x16x32_bf16 v[12:15], a[188:191], a[196:199], v[12:15]
  v_mul_f32 v8, s76, v8
  v_mul_f32 v9, s76, v9
  v_mfma_f32_16x16x32_bf16 v[16:19], a[32:35], a[120:123], v[16:19]
  v_mul_f32 v10, s76, v10
  v_mul_f32 v11, s76, v11
  v_mfma_f32_16x16x32_bf16 v[16:19], a[36:39], a[124:127], v[16:19]
  v_cvt_pk_bf16_f32 v8, v8, v9
  v_cvt_pk_bf16_f32 v9, v10, v11
  v_mfma_f32_16x16x32_bf16 v[16:19], a[160:163], a[200:203], v[16:19]
  ds_write_b64 v156, v[8:9] offset:0
  v_mfma_f32_16x16x32_bf16 v[16:19], a[164:167], a[204:207], v[16:19]
  v_mul_f32 v12, s76, v12
  v_mul_f32 v13, s76, v13
  v_mfma_f32_16x16x32_bf16 v[20:23], a[40:43], a[120:123], v[20:23]
  v_mul_f32 v14, s76, v14
  v_mul_f32 v15, s76, v15
  v_mfma_f32_16x16x32_bf16 v[20:23], a[44:47], a[124:127], v[20:23]
  v_cvt_pk_bf16_f32 v12, v12, v13
  v_cvt_pk_bf16_f32 v13, v14, v15
  v_mfma_f32_16x16x32_bf16 v[20:23], a[168:171], a[200:203], v[20:23]
  ds_write_b64 v157, v[12:13] offset:0
  v_mfma_f32_16x16x32_bf16 v[20:23], a[172:175], a[204:207], v[20:23]
  v_mul_f32 v16, s76, v16
  v_mul_f32 v17, s76, v17
  v_mfma_f32_16x16x32_bf16 v[24:27], a[48:51], a[120:123], v[24:27]
  buffer_load_dwordx4 v242, s[40:43], s68 offen offset:2048 nt lds
  v_mul_f32 v18, s76, v18
  v_mul_f32 v19, s76, v19
  v_mfma_f32_16x16x32_bf16 v[24:27], a[52:55], a[124:127], v[24:27]
  v_cvt_pk_bf16_f32 v16, v16, v17
  v_cvt_pk_bf16_f32 v17, v18, v19
  v_mfma_f32_16x16x32_bf16 v[24:27], a[176:179], a[200:203], v[24:27]
  ds_write_b64 v154, v[16:17] offset:4096
  v_mfma_f32_16x16x32_bf16 v[24:27], a[180:183], a[204:207], v[24:27]
  v_mul_f32 v20, s76, v20
  v_mul_f32 v21, s76, v21
  v_mfma_f32_16x16x32_bf16 v[28:31], a[56:59], a[120:123], v[28:31]
  v_mul_f32 v22, s76, v22
  v_mul_f32 v23, s76, v23
  v_mfma_f32_16x16x32_bf16 v[28:31], a[60:63], a[124:127], v[28:31]
  v_cvt_pk_bf16_f32 v20, v20, v21
  v_cvt_pk_bf16_f32 v21, v22, v23
  v_mfma_f32_16x16x32_bf16 v[28:31], a[184:187], a[200:203], v[28:31]
  ds_write_b64 v155, v[20:21] offset:4096
  v_mfma_f32_16x16x32_bf16 v[28:31], a[188:191], a[204:207], v[28:31]
  v_mul_f32 v24, s76, v24
  v_mul_f32 v25, s76, v25
  v_mfma_f32_16x16x32_bf16 v[32:35], a[32:35], a[128:131], v[32:35]
  v_mul_f32 v26, s76, v26
  v_mul_f32 v27, s76, v27
  v_mfma_f32_16x16x32_bf16 v[32:35], a[36:39], a[132:135], v[32:35]
  v_cvt_pk_bf16_f32 v24, v24, v25
  v_cvt_pk_bf16_f32 v25, v26, v27
  v_mfma_f32_16x16x32_bf16 v[32:35], a[160:163], a[208:211], v[32:35]
  ds_write_b64 v156, v[24:25] offset:4096
  v_mfma_f32_16x16x32_bf16 v[32:35], a[164:167], a[212:215], v[32:35]
  v_mul_f32 v28, s76, v28
  v_mul_f32 v29, s76, v29
  v_mfma_f32_16x16x32_bf16 v[36:39], a[40:43], a[128:131], v[36:39]
  buffer_load_dwordx4 v243, s[40:43], s68 offen offset:3072 nt lds
  v_mul_f32 v30, s76, v30
  v_mul_f32 v31, s76, v31
  v_mfma_f32_16x16x32_bf16 v[36:39], a[44:47], a[132:135], v[36:39]
  v_cvt_pk_bf16_f32 v28, v28, v29
  v_cvt_pk_bf16_f32 v29, v30, v31
  v_mfma_f32_16x16x32_bf16 v[36:39], a[168:171], a[208:211], v[36:39]
  ds_write_b64 v157, v[28:29] offset:4096
  v_mfma_f32_16x16x32_bf16 v[36:39], a[172:175], a[212:215], v[36:39]
  v_mul_f32 v32, s76, v32
  v_mul_f32 v33, s76, v33
  v_mfma_f32_16x16x32_bf16 v[40:43], a[48:51], a[128:131], v[40:43]
  v_mul_f32 v34, s76, v34
  v_mul_f32 v35, s76, v35
  v_mfma_f32_16x16x32_bf16 v[40:43], a[52:55], a[132:135], v[40:43]
  v_cvt_pk_bf16_f32 v32, v32, v33
  v_cvt_pk_bf16_f32 v33, v34, v35
  v_mfma_f32_16x16x32_bf16 v[40:43], a[176:179], a[208:211], v[40:43]
  ds_write_b64 v154, v[32:33] offset:8192
  v_mfma_f32_16x16x32_bf16 v[40:43], a[180:183], a[212:215], v[40:43]
  v_mul_f32 v36, s76, v36
  v_mul_f32 v37, s76, v37
  v_mfma_f32_16x16x32_bf16 v[44:47], a[56:59], a[128:131], v[44:47]
  v_mul_f32 v38, s76, v38
  v_mul_f32 v39, s76, v39
  v_mfma_f32_16x16x32_bf16 v[44:47], a[60:63], a[132:135], v[44:47]
  v_cvt_pk_bf16_f32 v36, v36, v37
  v_cvt_pk_bf16_f32 v37, v38, v39
  v_mfma_f32_16x16x32_bf16 v[44:47], a[184:187], a[208:211], v[44:47]
  ds_write_b64 v155, v[36:37] offset:8192
  v_mfma_f32_16x16x32_bf16 v[44:47], a[188:191], a[212:215], v[44:47]
  s_add_u32 s68, s60, 16384
  s_add_u32 m0, s62, 16384
  v_mul_f32 v40, s76, v40
  v_mul_f32 v41, s76, v41
  v_mfma_f32_16x16x32_bf16 v[48:51], a[32:35], a[136:139], v[48:51]
  buffer_load_dwordx4 v244, s[44:47], s68 offen offset:0 nt lds
  v_mul_f32 v42, s76, v42
  v_mul_f32 v43, s76, v43
  v_mfma_f32_16x16x32_bf16 v[48:51], a[36:39], a[140:143], v[48:51]
  v_cvt_pk_bf16_f32 v40, v40, v41
  v_cvt_pk_bf16_f32 v41, v42, v43
  v_mfma_f32_16x16x32_bf16 v[48:51], a[160:163], a[216:219], v[48:51]
  ds_write_b64 v156, v[40:41] offset:8192
  v_mfma_f32_16x16x32_bf16 v[48:51], a[164:167], a[220:223], v[48:51]
  v_mul_f32 v44, s76, v44
  v_mul_f32 v45, s76, v45
  v_mfma_f32_16x16x32_bf16 v[52:55], a[40:43], a[136:139], v[52:55]
  v_mul_f32 v46, s76, v46
  v_mul_f32 v47, s76, v47
  v_mfma_f32_16x16x32_bf16 v[52:55], a[44:47], a[140:143], v[52:55]
  v_cvt_pk_bf16_f32 v44, v44, v45
  v_cvt_pk_bf16_f32 v45, v46, v47
  v_mfma_f32_16x16x32_bf16 v[52:55], a[168:171], a[216:219], v[52:55]
  ds_write_b64 v157, v[44:45] offset:8192
  v_mfma_f32_16x16x32_bf16 v[52:55], a[172:175], a[220:223], v[52:55]
  v_mul_f32 v48, s76, v48
  v_mul_f32 v49, s76, v49
  v_mfma_f32_16x16x32_bf16 v[56:59], a[48:51], a[136:139], v[56:59]
  v_mul_f32 v50, s76, v50
  v_mul_f32 v51, s76, v51
  v_mfma_f32_16x16x32_bf16 v[56:59], a[52:55], a[140:143], v[56:59]
  v_cvt_pk_bf16_f32 v48, v48, v49
  v_cvt_pk_bf16_f32 v49, v50, v51
  v_mfma_f32_16x16x32_bf16 v[56:59], a[176:179], a[216:219], v[56:59]
  ds_write_b64 v154, v[48:49] offset:12288
  v_mfma_f32_16x16x32_bf16 v[56:59], a[180:183], a[220:223], v[56:59]
  buffer_load_dwordx4 v245, s[44:47], s68 offen offset:1024 nt lds
  v_mul_f32 v52, s76, v52
  v_mul_f32 v53, s76, v53
  v_mfma_f32_16x16x32_bf16 v[60:63], a[56:59], a[136:139], v[60:63]
  v_mul_f32 v54, s76, v54
  v_mul_f32 v55, s76, v55
  v_mfma_f32_16x16x32_bf16 v[60:63], a[60:63], a[140:143], v[60:63]
  v_cvt_pk_bf16_f32 v52, v52, v53
  v_cvt_pk_bf16_f32 v53, v54, v55
  v_mfma_f32_16x16x32_bf16 v[60:63], a[184:187], a[216:219], v[60:63]
  ds_write_b64 v155, v[52:53] offset:12288
  v_mfma_f32_16x16x32_bf16 v[60:63], a[188:191], a[220:223], v[60:63]
  v_mul_f32 v56, s76, v56
  v_mul_f32 v57, s76, v57
  v_mfma_f32_16x16x32_bf16 v[64:67], a[32:35], a[144:147], v[64:67]
  v_mul_f32 v58, s76, v58
  v_mul_f32 v59, s76, v59
  v_mfma_f32_16x16x32_bf16 v[64:67], a[36:39], a[148:151], v[64:67]
  v_cvt_pk_bf16_f32 v56, v56, v57
  v_cvt_pk_bf16_f32 v57, v58, v59
  v_mfma_f32_16x16x32_bf16 v[64:67], a[160:163], a[224:227], v[64:67]
  ds_write_b64 v156, v[56:57] offset:12288
  v_mfma_f32_16x16x32_bf16 v[64:67], a[164:167], a[228:231], v[64:67]
  v_mul_f32 v60, s76, v60
  v_mul_f32 v61, s76, v61
  v_mfma_f32_16x16x32_bf16 v[68:71], a[40:43], a[144:147], v[68:71]
  v_mul_f32 v62, s76, v62
  v_mul_f32 v63, s76, v63
  v_mfma_f32_16x16x32_bf16 v[68:71], a[44:47], a[148:151], v[68:71]
  v_cvt_pk_bf16_f32 v60, v60, v61
  v_cvt_pk_bf16_f32 v61, v62, v63
  v_mfma_f32_16x16x32_bf16 v[68:71], a[168:171], a[224:227], v[68:71]
  ds_write_b64 v157, v[60:61] offset:12288
  v_mfma_f32_16x16x32_bf16 v[68:71], a[172:175], a[228:231], v[68:71]
  buffer_load_dwordx4 v244, s[44:47], s68 offen offset:2048 nt lds
  v_mul_f32 v64, s76, v64
  v_mul_f32 v65, s76, v65
  v_mfma_f32_16x16x32_bf16 v[72:75], a[48:51], a[144:147], v[72:75]
  v_mul_f32 v66, s76, v66
  v_mul_f32 v67, s76, v67
  v_mfma_f32_16x16x32_bf16 v[72:75], a[52:55], a[148:151], v[72:75]
  v_cvt_pk_bf16_f32 v64, v64, v65
  v_cvt_pk_bf16_f32 v65, v66, v67
  v_mfma_f32_16x16x32_bf16 v[72:75], a[176:179], a[224:227], v[72:75]
  ds_write_b64 v154, v[64:65] offset:16384
  v_mfma_f32_16x16x32_bf16 v[72:75], a[180:183], a[228:231], v[72:75]
  v_mul_f32 v68, s76, v68
  v_mul_f32 v69, s76, v69
  v_mfma_f32_16x16x32_bf16 v[76:79], a[56:59], a[144:147], v[76:79]
  v_mul_f32 v70, s76, v70
  v_mul_f32 v71, s76, v71
  v_mfma_f32_16x16x32_bf16 v[76:79], a[60:63], a[148:151], v[76:79]
  v_cvt_pk_bf16_f32 v68, v68, v69
  v_cvt_pk_bf16_f32 v69, v70, v71
  v_mfma_f32_16x16x32_bf16 v[76:79], a[184:187], a[224:227], v[76:79]
  ds_write_b64 v155, v[68:69] offset:16384
  v_mfma_f32_16x16x32_bf16 v[76:79], a[188:191], a[228:231], v[76:79]
  v_mul_f32 v72, s76, v72
  v_mul_f32 v73, s76, v73
  v_mfma_f32_16x16x32_bf16 v[80:83], a[32:35], a[152:155], v[80:83]
  v_mul_f32 v74, s76, v74
  v_mul_f32 v75, s76, v75
  v_mfma_f32_16x16x32_bf16 v[80:83], a[36:39], a[156:159], v[80:83]
  v_cvt_pk_bf16_f32 v72, v72, v73
  v_cvt_pk_bf16_f32 v73, v74, v75
  v_mfma_f32_16x16x32_bf16 v[80:83], a[160:163], a[232:235], v[80:83]
  ds_write_b64 v156, v[72:73] offset:16384
  v_mfma_f32_16x16x32_bf16 v[80:83], a[164:167], a[236:239], v[80:83]
  buffer_load_dwordx4 v245, s[44:47], s68 offen offset:3072 nt lds
  v_mul_f32 v76, s76, v76
  v_mul_f32 v77, s76, v77
  v_mfma_f32_16x16x32_bf16 v[84:87], a[40:43], a[152:155], v[84:87]
  v_mul_f32 v78, s76, v78
  v_mul_f32 v79, s76, v79
  v_mfma_f32_16x16x32_bf16 v[84:87], a[44:47], a[156:159], v[84:87]
  v_cvt_pk_bf16_f32 v76, v76, v77
  v_cvt_pk_bf16_f32 v77, v78, v79
  v_mfma_f32_16x16x32_bf16 v[84:87], a[168:171], a[232:235], v[84:87]
  ds_write_b64 v157, v[76:77] offset:16384
  v_mfma_f32_16x16x32_bf16 v[84:87], a[172:175], a[236:239], v[84:87]
  v_mul_f32 v80, s76, v80
  v_mul_f32 v81, s76, v81
  v_mfma_f32_16x16x32_bf16 v[88:91], a[48:51], a[152:155], v[88:91]
  v_mul_f32 v82, s76, v82
  v_mul_f32 v83, s76, v83
  v_mfma_f32_16x16x32_bf16 v[88:91], a[52:55], a[156:159], v[88:91]
  v_cvt_pk_bf16_f32 v80, v80, v81
  v_cvt_pk_bf16_f32 v81, v82, v83
  v_mfma_f32_16x16x32_bf16 v[88:91], a[176:179], a[232:235], v[88:91]
  ds_write_b64 v154, v[80:81] offset:20480
  v_mfma_f32_16x16x32_bf16 v[88:91], a[180:183], a[236:239], v[88:91]
  v_mul_f32 v84, s76, v84
  v_mul_f32 v85, s76, v85
  v_mfma_f32_16x16x32_bf16 v[92:95], a[56:59], a[152:155], v[92:95]
  v_mul_f32 v86, s76, v86
  v_mul_f32 v87, s76, v87
  v_mfma_f32_16x16x32_bf16 v[92:95], a[60:63], a[156:159], v[92:95]
  v_cvt_pk_bf16_f32 v84, v84, v85
  v_cvt_pk_bf16_f32 v85, v86, v87
  v_mfma_f32_16x16x32_bf16 v[92:95], a[184:187], a[232:235], v[92:95]
  ds_write_b64 v155, v[84:85] offset:20480
  v_mfma_f32_16x16x32_bf16 v[92:95], a[188:191], a[236:239], v[92:95]
  v_mul_f32 v88, s76, v88
  v_mul_f32 v89, s76, v89
  v_mul_f32 v90, s76, v90
  v_mul_f32 v91, s76, v91
  v_cvt_pk_bf16_f32 v88, v88, v89
  v_cvt_pk_bf16_f32 v89, v90, v91
  ds_write_b64 v156, v[88:89] offset:20480
  s_nop 0
  v_mul_f32 v92, s76, v92
  v_mul_f32 v93, s76, v93
  v_mul_f32 v94, s76, v94
  v_mul_f32 v95, s76, v95
  v_cvt_pk_bf16_f32 v92, v92, v93
  v_cvt_pk_bf16_f32 v93, v94, v95
  ds_write_b64 v157, v[92:93] offset:20480
  s_memrealtime s[82:83]
  s_memtime s[98:99]
  s_waitcnt vmcnt(16) lgkmcnt(0)
  s_barrier
  ds_read_b128 a[0:3], v228 offset:0
  ds_read_b128 a[4:7], v229 offset:0
  ds_read_b128 a[8:11], v230 offset:0
  ds_read_b128 a[12:15], v231 offset:0
  ds_read_b128 a[128:131], v120 offset:0
  ds_read_b128 a[132:135], v121 offset:0
  ds_read_b128 a[136:139], v122 offset:0
  ds_read_b128 a[140:143], v123 offset:0
  ds_read_b128 a[144:147], v120 offset:4096
  ds_read_b128 a[148:151], v121 offset:4096
  ds_read_b128 a[152:155], v122 offset:4096
  ds_read_b128 a[156:159], v123 offset:4096
  ds_read_b128 a[160:163], v120 offset:8192
  ds_read_b128 a[164:167], v121 offset:8192
  ds_read_b128 a[168:171], v122 offset:8192
  ds_read_b128 a[172:175], v123 offset:8192
  s_sub_u32 s82, s82, s36
  s_sub_u32 s98, s98, s38
  s_mul_i32 s82, s82, 1612
  s_mul_i32 s98, s98, 100
  s_sub_i32 s98, s98, s82
  v_mov_b32 v205, s98
  buffer_store_dwordx2 v[204:205], v215, s[56:59], 0 offen offset:0 sc1
  s_cmp_eq_u32 s67, 0
  s_cbranch_scc1 NOFULL%=
  s_waitcnt lgkmcnt(0)
  s_lshl_b32 s70, s64, 12
  s_mov_b32 s71, 0
  v_subrev_u32 v120, 90112, v228
  v_subrev_u32 v121, 90112, v229
  v_subrev_u32 v122, 90112, v230
  v_subrev_u32 v123, 90112, v231
FULL%=:
  s_waitcnt vmcnt(0)
  s_barrier
  s_add_u32 m0, s62, 49152
  s_nop 0
  buffer_load_dwordx4 v240, s[40:43], s70 offen offset:0 nt lds
  buffer_load_dwordx4 v241, s[40:43], s70 offen offset:1024 nt lds
  buffer_load_dwordx4 v242, s[40:43], s70 offen offset:2048 nt lds
  buffer_load_dwordx4 v243, s[40:43], s70 offen offset:3072 nt lds
  s_waitcnt vmcnt(0)
  s_barrier
  ds_read_b128 a[48:51], v120 offset:49152
  ds_read_b128 a[52:55], v121 offset:49152
  ds_read_b128 a[56:59], v122 offset:49152
  ds_read_b128 a[60:63], v123 offset:49152
  s_waitcnt lgkmcnt(0)
  v_mfma_f32_16x16x32_bf16 v[156:159], a[48:51], a[128:131], 0
  v_mfma_f32_16x16x32_bf16 v[156:159], a[52:55], a[132:135], v[156:159]
  v_mfma_f32_16x16x32_bf16 v[156:159], a[56:59], a[136:139], v[156:159]
  v_mfma_f32_16x16x32_bf16 v[156:159], a[60:63], a[140:143], v[156:159]
  v_mfma_f32_16x16x32_bf16 v[160:163], a[48:51], a[144:147], 0
  v_mfma_f32_16x16x32_bf16 v[160:163], a[52:55], a[148:151], v[160:163]
  v_mfma_f32_16x16x32_bf16 v[160:163], a[56:59], a[152:155], v[160:163]
  v_mfma_f32_16x16x32_bf16 v[160:163], a[60:63], a[156:159], v[160:163]
  v_mfma_f32_16x16x32_bf16 v[164:167], a[48:51], a[160:163], 0
  v_mfma_f32_16x16x32_bf16 v[164:167], a[52:55], a[164:167], v[164:167]
  v_mfma_f32_16x16x32_bf16 v[164:167], a[56:59], a[168:171], v[164:167]
  v_mfma_f32_16x16x32_bf16 v[164:167], a[60:63], a[172:175], v[164:167]
  s_nop 7
  v_max3_f32 v208, v208, v156, v157
  v_max3_f32 v208, v208, v158, v159
  v_max3_f32 v209, v209, v160, v161
  v_max3_f32 v209, v209, v162, v163
  v_max3_f32 v210, v210, v164, v165
  v_max3_f32 v210, v210, v166, v167
  s_nop 3
  ds_read_b128 a[48:51], v120 offset:53248
  ds_read_b128 a[52:55], v121 offset:53248
  ds_read_b128 a[56:59], v122 offset:53248
  ds_read_b128 a[60:63], v123 offset:53248
  s_waitcnt lgkmcnt(0)
  v_mfma_f32_16x16x32_bf16 v[156:159], a[48:51], a[128:131], 0
  v_mfma_f32_16x16x32_bf16 v[156:159], a[52:55], a[132:135], v[156:159]
  v_mfma_f32_16x16x32_bf16 v[156:159], a[56:59], a[136:139], v[156:159]
  v_mfma_f32_16x16x32_bf16 v[156:159], a[60:63], a[140:143], v[156:159]
  v_mfma_f32_16x16x32_bf16 v[160:163], a[48:51], a[144:147], 0
  v_mfma_f32_16x16x32_bf16 v[160:163], a[52:55], a[148:151], v[160:163]
  v_mfma_f32_16x16x32_bf16 v[160:163], a[56:59], a[152:155], v[160:163]
  v_mfma_f32_16x16x32_bf16 v[160:163], a[60:63], a[156:159], v[160:163]
  v_mfma_f32_16x16x32_bf16 v[164:167], a[48:51], a[160:163], 0
  v_mfma_f32_16x16x32_bf16 v[164:167], a[52:55], a[164:167], v[164:167]
  v_mfma_f32_16x16x32_bf16 v[164:167], a[56:59], a[168:171], v[164:167]
  v_mfma_f32_16x16x32_bf16 v[164:167], a[60:63], a[172:175], v[164:167]
  s_nop 7
  v_max3_f32 v208, v208, v156, v157
  v_max3_f32 v208, v208, v158, v159
  v_max3_f32 v209, v209, v160, v161
  v_max3_f32 v209, v209, v162, v163
  v_max3_f32 v210, v210, v164, v165
  v_max3_f32 v210, v210, v166, v167
  s_nop 3
  ds_read_b128 a[48:51], v120 offset:57344
  ds_read_b128 a[52:55], v121 offset:57344
  ds_read_b128 a[56:59], v122 offset:57344
  ds_read_b128 a[60:63], v123 offset:57344
  s_waitcnt lgkmcnt(0)
  v_mfma_f32_16x16x32_bf16 v[156:159], a[48:51], a[128:131], 0
  v_mfma_f32_16x16x32_bf16 v[156:159], a[52:55], a[132:135], v[156:159]
  v_mfma_f32_16x16x32_bf16 v[156:159], a[56:59], a[136:139], v[156:159]
  v_mfma_f32_16x16x32_bf16 v[156:159], a[60:63], a[140:143], v[156:159]
  v_mfma_f32_16x16x32_bf16 v[160:163], a[48:51], a[144:147], 0
  v_mfma_f32_16x16x32_bf16 v[160:163], a[52:55], a[148:151], v[160:163]
  v_mfma_f32_16x16x32_bf16 v[160:163], a[56:59], a[152:155], v[160:163]
  v_mfma_f32_16x16x32_bf16 v[160:163], a[60:63], a[156:159], v[160:163]
  v_mfma_f32_16x16x32_bf16 v[164:167], a[48:51], a[160:163], 0
  v_mfma_f32_16x16x32_bf16 v[164:167], a[52:55], a[164:167], v[164:167]
  v_mfma_f32_16x16x32_bf16 v[164:167], a[56:59], a[168:171], v[164:167]
  v_mfma_f32_16x16x32_bf16 v[164:167], a[60:63], a[172:175], v[164:167]
  s_nop 7
  v_max3_f32 v208, v208, v156, v157
  v_max3_f32 v208, v208, v158, v159
  v_max3_f32 v209, v209, v160, v161
  v_max3_f32 v209, v209, v162, v163
  v_max3_f32 v210, v210, v164, v165
  v_max3_f32 v210, v210, v166, v167
  s_nop 3
  ds_read_b128 a[48:51], v120 offset:61440
  ds_read_b128 a[52:55], v121 offset:61440
  ds_read_b128 a[56:59], v122 offset:61440
  ds_read_b128 a[60:63], v123 offset:61440
  s_waitcnt lgkmcnt(0)
  v_mfma_f32_16x16x32_bf16 v[156:159], a[48:51], a[128:131], 0
  v_mfma_f32_16x16x32_bf16 v[156:159], a[52:55], a[132:135], v[156:159]
  v_mfma_f32_16x16x32_bf16 v[156:159], a[56:59], a[136:139], v[156:159]
  v_mfma_f32_16x16x32_bf16 v[156:159], a[60:63], a[140:143], v[156:159]
  v_mfma_f32_16x16x32_bf16 v[160:163], a[48:51], a[144:147], 0
  v_mfma_f32_16x16x32_bf16 v[160:163], a[52:55], a[148:151], v[160:163]
  v_mfma_f32_16x16x32_bf16 v[160:163], a[56:59], a[152:155], v[160:163]
  v_mfma_f32_16x16x32_bf16 v[160:163], a[60:63], a[156:159], v[160:163]
  v_mfma_f32_16x16x32_bf16 v[164:167], a[48:51], a[160:163], 0
  v_mfma_f32_16x16x32_bf16 v[164:167], a[52:55], a[164:167], v[164:167]
  v_mfma_f32_16x16x32_bf16 v[164:167], a[56:59], a[168:171], v[164:167]
  v_mfma_f32_16x16x32_bf16 v[164:167], a[60:63], a[172:175], v[164:167]
  s_nop 7
  v_max3_f32 v208, v208, v156, v157
  v_max3_f32 v208, v208, v158, v159
  v_max3_f32 v209, v209, v160, v161
  v_max3_f32 v209, v209, v162, v163
  v_max3_f32 v210, v210, v164, v165
  v_max3_f32 v210, v210, v166, v167
  s_nop 3
  s_add_u32 s70, s70, 16384
  s_add_u32 s71, s71, 1
  s_cmp_lt_u32 s71, 32
  s_cbranch_scc1 FULL%=
  s_nop 7
NOFULL%=:
  s_waitcnt lgkmcnt(11)
  v_mfma_f32_16x16x32_bf16 v[156:159], a[0:3], a[128:131], 0
  ds_read_b128 a[16:19], v228 offset:4096
  ds_read_b128 a[20:23], v229 offset:4096
  s_waitcnt lgkmcnt(12)
  v_mfma_f32_16x16x32_bf16 v[156:159], a[4:7], a[132:135], v[156:159]
  ds_read_b128 a[24:27], v230 offset:4096
  ds_read_b128 a[28:31], v231 offset:4096
  s_waitcnt lgkmcnt(13)
  v_mfma_f32_16x16x32_bf16 v[156:159], a[8:11], a[136:139], v[156:159]
  ds_read_b128 a[32:35], v228 offset:8192
  ds_read_b128 a[36:39], v229 offset:8192
  s_waitcnt lgkmcnt(14)
  v_mfma_f32_16x16x32_bf16 v[156:159], a[12:15], a[140:143], v[156:159]
  ds_read_b128 a[40:43], v230 offset:8192
  ds_read_b128 a[44:47], v231 offset:8192
  s_waitcnt lgkmcnt(14)
  v_mfma_f32_16x16x32_bf16 v[160:163], a[0:3], a[144:147], 0
  ds_read_b128 a[48:51], v228 offset:12288
  ds_read_b128 a[52:55], v229 offset:12288
  v_mfma_f32_16x16x32_bf16 v[160:163], a[4:7], a[148:151], v[160:163]
  ds_read_b128 a[56:59], v230 offset:12288
  ds_read_b128 a[60:63], v231 offset:12288
  s_waitcnt lgkmcnt(14)
  v_mfma_f32_16x16x32_bf16 v[160:163], a[8:11], a[152:155], v[160:163]
  v_mfma_f32_16x16x32_bf16 v[160:163], a[12:15], a[156:159], v[160:163]
  v_max3_f32 v208, v208, v156, v157
  v_max3_f32 v208, v208, v158, v159
  v_mov_b32 v211, v208
  v_mfma_f32_16x16x32_bf16 v[164:167], a[0:3], a[160:163], 0
  v_mfma_f32_16x16x32_bf16 v[164:167], a[4:7], a[164:167], v[164:167]
  v_permlane32_swap_b32 v208, v211
  v_max_f32 v208, v208, v211
  v_mov_b32 v211, v208
  s_waitcnt lgkmcnt(13)
  v_mfma_f32_16x16x32_bf16 v[164:167], a[8:11], a[168:171], v[164:167]
  s_waitcnt lgkmcnt(12)
  v_mfma_f32_16x16x32_bf16 v[164:167], a[12:15], a[172:175], v[164:167]
  v_max3_f32 v209, v209, v160, v161
  v_max3_f32 v209, v209, v162, v163
  v_mov_b32 v212, v209
  s_waitcnt lgkmcnt(11)
  v_mfma_f32_16x16x32_bf16 v[168:171], a[16:19], a[128:131], 0
  v_permlane16_swap_b32 v208, v211
  v_min_f32_e64 v96, -v208, -v211
  v_min_f32_e64 v97, -v208, -v211
  v_min_f32_e64 v98, -v208, -v211
  v_min_f32_e64 v99, -v208, -v211
  s_waitcnt lgkmcnt(10)
  v_mfma_f32_16x16x32_bf16 v[168:171], a[20:23], a[132:135], v[168:171]
  v_permlane32_swap_b32 v209, v212
  v_max_f32 v209, v209, v212
  v_mov_b32 v212, v209
  s_waitcnt vmcnt(9) lgkmcnt(0)
  s_barrier
  v_mfma_f32_16x16x32_bf16 v[168:171], a[24:27], a[136:139], v[168:171]
  ds_read_b128 a[0:3], v228 offset:49152
  ds_read_b128 a[4:7], v229 offset:49152
  s_add_u32 s68, s60, 49152
  v_add_f32 v156, v96, v156
  v_add_f32 v157, v96, v157
  v_add_f32 v158, v96, v158
  v_add_f32 v159, v96, v159
  v_mfma_f32_16x16x32_bf16 v[168:171], a[28:31], a[140:143], v[168:171]
  ds_read_b128 a[8:11], v230 offset:49152
  v_max3_f32 v210, v210, v164, v165
  v_max3_f32 v210, v210, v166, v167
  v_mov_b32 v213, v210
  v_exp_f32 v156, v156
  v_exp_f32 v157, v157
  v_exp_f32 v158, v158
  v_mfma_f32_16x16x32_bf16 v[172:175], a[16:19], a[144:147], 0
  ds_read_b128 a[12:15], v231 offset:49152
  s_add_u32 m0, s61, 0
  v_permlane16_swap_b32 v209, v212
  v_min_f32_e64 v100, -v209, -v212
  v_min_f32_e64 v101, -v209, -v212
  v_min_f32_e64 v102, -v209, -v212
  v_min_f32_e64 v103, -v209, -v212
  v_cvt_pk_bf16_f32 v132, v156, v157
  v_exp_f32 v159, v159
  v_mfma_f32_16x16x32_bf16 v[172:175], a[20:23], a[148:151], v[172:175]
  ds_read_b64_tr_b16 a[64:65], v232 offset:0
  ds_read_b64_tr_b16 a[66:67], v232 offset:4096
  v_permlane32_swap_b32 v210, v213
  v_max_f32 v210, v210, v213
  v_mov_b32 v213, v210
  v_cvt_pk_bf16_f32 v133, v158, v159
  v_mfma_f32_16x16x32_bf16 v[172:175], a[24:27], a[152:155], v[172:175]
  ds_read_b64_tr_b16 a[68:69], v232 offset:8192
  v_add_f32 v160, v100, v160
  v_add_f32 v161, v100, v161
  v_add_f32 v162, v100, v162
  v_add_f32 v163, v100, v163
  v_mfma_f32_16x16x32_bf16 v[172:175], a[28:31], a[156:159], v[172:175]
  ds_read_b64_tr_b16 a[70:71], v232 offset:12288
  buffer_load_dwordx4 v240, s[40:43], s68 offen offset:0 nt lds
  v_exp_f32 v160, v160
  v_exp_f32 v161, v161
  v_exp_f32 v162, v162
  v_mfma_f32_16x16x32_bf16 v[176:179], a[16:19], a[160:163], 0
  ds_read_b64_tr_b16 a[72:73], v233 offset:0
  ds_read_b64_tr_b16 a[74:75], v233 offset:4096
  v_permlane16_swap_b32 v210, v213
  v_min_f32_e64 v104, -v210, -v213
  v_min_f32_e64 v105, -v210, -v213
  v_min_f32_e64 v106, -v210, -v213
  v_min_f32_e64 v107, -v210, -v213
  v_cvt_pk_bf16_f32 v140, v160, v161
  v_exp_f32 v163, v163
  v_mfma_f32_16x16x32_bf16 v[176:179], a[20:23], a[164:167], v[176:179]
  ds_read_b64_tr_b16 a[76:77], v233 offset:8192
  v_cvt_pk_bf16_f32 v141, v162, v163
  v_mfma_f32_16x16x32_bf16 v[176:179], a[24:27], a[168:171], v[176:179]
  ds_read_b64_tr_b16 a[78:79], v233 offset:12288
  buffer_load_dwordx4 v241, s[40:43], s68 offen offset:1024 nt lds
  v_add_f32 v164, v104, v164
  v_add_f32 v165, v104, v165
  v_add_f32 v166, v104, v166
  v_add_f32 v167, v104, v167
  v_mfma_f32_16x16x32_bf16 v[176:179], a[28:31], a[172:175], v[176:179]
  ds_read_b64_tr_b16 a[80:81], v234 offset:0
  ds_read_b64_tr_b16 a[82:83], v234 offset:4096
  v_exp_f32 v164, v164
  v_exp_f32 v165, v165
  v_exp_f32 v166, v166
  v_mfma_f32_16x16x32_bf16 v[108:111], a[32:35], a[128:131], v[96:99]
  ds_read_b64_tr_b16 a[84:85], v234 offset:8192
  v_cvt_pk_bf16_f32 v148, v164, v165
  v_exp_f32 v167, v167
  v_mfma_f32_16x16x32_bf16 v[108:111], a[36:39], a[132:135], v[108:111]
  ds_read_b64_tr_b16 a[86:87], v234 offset:12288
  buffer_load_dwordx4 v242, s[40:43], s68 offen offset:2048 nt lds
  v_cvt_pk_bf16_f32 v149, v166, v167
  v_add_f32 v168, v96, v168
  v_add_f32 v169, v96, v169
  v_add_f32 v170, v96, v170
  v_add_f32 v171, v96, v171
  v_mfma_f32_16x16x32_bf16 v[108:111], a[40:43], a[136:139], v[108:111]
  ds_read_b128 a[16:19], v228 offset:53248
  ds_read_b128 a[20:23], v229 offset:53248
  v_exp_f32 v168, v168
  v_exp_f32 v169, v169
  v_exp_f32 v170, v170
  v_mfma_f32_16x16x32_bf16 v[108:111], a[44:47], a[140:143], v[108:111]
  ds_read_b128 a[24:27], v230 offset:53248
  buffer_load_dwordx4 v243, s[40:43], s68 offen offset:3072 nt lds
  v_cvt_pk_bf16_f32 v134, v168, v169
  v_exp_f32 v171, v171
  v_mfma_f32_16x16x32_bf16 v[112:115], a[32:35], a[144:147], v[100:103]
  ds_read_b128 a[28:31], v231 offset:53248
  v_cvt_pk_bf16_f32 v135, v170, v171
  v_add_f32 v172, v100, v172
  v_add_f32 v173, v100, v173
  v_add_f32 v174, v100, v174
  v_add_f32 v175, v100, v175
  v_mfma_f32_16x16x32_bf16 v[112:115], a[36:39], a[148:151], v[112:115]
  ds_read_b64_tr_b16 a[88:89], v235 offset:0
  ds_read_b64_tr_b16 a[90:91], v235 offset:4096
  v_exp_f32 v172, v172
  v_exp_f32 v173, v173
  v_exp_f32 v174, v174
  v_mfma_f32_16x16x32_bf16 v[112:115], a[40:43], a[152:155], v[112:115]
  ds_read_b64_tr_b16 a[92:93], v235 offset:8192
  s_add_u32 s68, s60, 32768
  v_cvt_pk_bf16_f32 v142, v172, v173
  v_exp_f32 v175, v175
  v_mfma_f32_16x16x32_bf16 v[112:115], a[44:47], a[156:159], v[112:115]
  ds_read_b64_tr_b16 a[94:95], v235 offset:12288
  v_cvt_pk_bf16_f32 v143, v174, v175
  v_add_f32 v176, v104, v176
  v_add_f32 v177, v104, v177
  v_add_f32 v178, v104, v178
  v_add_f32 v179, v104, v179
  v_mfma_f32_16x16x32_bf16 v[116:119], a[32:35], a[160:163], v[104:107]
  ds_read_b64_tr_b16 a[96:97], v236 offset:0
  ds_read_b64_tr_b16 a[98:99], v236 offset:4096
  v_exp_f32 v176, v176
  v_exp_f32 v177, v177
  v_exp_f32 v178, v178
  v_exp_f32 v108, v108
  v_exp_f32 v109, v109
  v_mfma_f32_16x16x32_bf16 v[116:119], a[36:39], a[164:167], v[116:119]
  ds_read_b64_tr_b16 a[100:101], v236 offset:8192
  s_add_u32 m0, s62, 32768
  v_cvt_pk_bf16_f32 v150, v176, v177
  v_exp_f32 v179, v179
  v_exp_f32 v110, v110
  v_cvt_pk_bf16_f32 v136, v108, v109
  v_mfma_f32_16x16x32_bf16 v[116:119], a[40:43], a[168:171], v[116:119]
  ds_read_b64_tr_b16 a[102:103], v236 offset:12288
  v_cvt_pk_bf16_f32 v151, v178, v179
  v_exp_f32 v111, v111
  v_mfma_f32_16x16x32_bf16 v[116:119], a[44:47], a[172:175], v[116:119]
  ds_read_b64_tr_b16 a[104:105], v237 offset:0
  ds_read_b64_tr_b16 a[106:107], v237 offset:4096
  v_cvt_pk_bf16_f32 v137, v110, v111
  v_exp_f32 v112, v112
  v_exp_f32 v113, v113
  v_mfma_f32_16x16x32_bf16 v[108:111], a[48:51], a[128:131], v[96:99]
  ds_read_b64_tr_b16 a[108:109], v237 offset:8192
  buffer_load_dwordx4 v244, s[44:47], s68 offen offset:0 nt lds
  v_exp_f32 v114, v114
  v_cvt_pk_bf16_f32 v144, v112, v113
  v_mfma_f32_16x16x32_bf16 v[108:111], a[52:55], a[132:135], v[108:111]
  ds_read_b64_tr_b16 a[110:111], v237 offset:12288
  v_exp_f32 v115, v115
  v_mfma_f32_16x16x32_bf16 v[108:111], a[56:59], a[136:139], v[108:111]
  ds_read_b128 a[32:35], v228 offset:57344
  buffer_load_dwordx4 v245, s[44:47], s68 offen offset:1024 nt lds
  v_cvt_pk_bf16_f32 v145, v114, v115
  v_exp_f32 v116, v116
  v_exp_f32 v117, v117
  v_mfma_f32_16x16x32_bf16 v[108:111], a[60:63], a[140:143], v[108:111]
  v_exp_f32 v118, v118
  v_cvt_pk_bf16_f32 v152, v116, v117
  v_mfma_f32_16x16x32_bf16 v[112:115], a[48:51], a[144:147], v[100:103]
  ds_read_b128 a[36:39], v229 offset:57344
  v_exp_f32 v119, v119
  v_mfma_f32_16x16x32_bf16 v[112:115], a[52:55], a[148:151], v[112:115]
  buffer_load_dwordx4 v244, s[44:47], s68 offen offset:2048 nt lds
  v_cvt_pk_bf16_f32 v153, v118, v119
  v_mfma_f32_16x16x32_bf16 v[112:115], a[56:59], a[152:155], v[112:115]
  ds_read_b128 a[40:43], v230 offset:57344
  v_exp_f32 v108, v108
  v_exp_f32 v109, v109
  v_mfma_f32_16x16x32_bf16 v[112:115], a[60:63], a[156:159], v[112:115]
  v_exp_f32 v110, v110
  v_cvt_pk_bf16_f32 v138, v108, v109
  v_mfma_f32_16x16x32_bf16 v[116:119], a[48:51], a[160:163], v[104:107]
  ds_read_b128 a[44:47], v231 offset:57344
  buffer_load_dwordx4 v245, s[44:47], s68 offen offset:3072 nt lds
  v_exp_f32 v111, v111
  v_mfma_f32_16x16x32_bf16 v[116:119], a[52:55], a[164:167], v[116:119]
  v_cvt_pk_bf16_f32 v139, v110, v111
  v_mfma_f32_16x16x32_bf16 v[116:119], a[56:59], a[168:171], v[116:119]
  v_exp_f32 v112, v112
  v_exp_f32 v113, v113
  v_mfma_f32_16x16x32_bf16 v[116:119], a[60:63], a[172:175], v[116:119]
  v_exp_f32 v114, v114
  v_cvt_pk_bf16_f32 v146, v112, v113
  v_exp_f32 v115, v115
  s_nop 0
  v_cvt_pk_bf16_f32 v147, v114, v115
  s_waitcnt lgkmcnt(14)
  v_mfma_f32_16x16x32_bf16 v[108:111], a[0:3], a[128:131], v[96:99]
  s_nop 0
  v_exp_f32 v116, v116
  v_mfma_f32_16x16x32_bf16 v[108:111], a[4:7], a[132:135], v[108:111]
  s_memrealtime s[82:83]
  ds_read_b128 a[48:51], v228 offset:61440
  v_mfma_f32_16x16x32_bf16 v[108:111], a[8:11], a[136:139], v[108:111]
  v_exp_f32 v117, v117
  v_mfma_f32_16x16x32_bf16 v[108:111], a[12:15], a[140:143], v[108:111]
  ds_read_b128 a[52:55], v229 offset:61440
  v_cvt_pk_bf16_f32 v154, v116, v117
  v_mfma_f32_16x16x32_bf16 v[0:3], a[64:67], v[132:135], 0
  v_exp_f32 v118, v118
  v_mfma_f32_16x16x32_bf16 v[0:3], a[68:71], v[136:139], v[0:3]
  ds_read_b128 a[56:59], v230 offset:61440
  ds_read_b128 a[60:63], v231 offset:61440
  v_mfma_f32_16x16x32_bf16 v[32:35], a[64:67], v[140:143], 0
  v_exp_f32 v119, v119
  v_mfma_f32_16x16x32_bf16 v[32:35], a[68:71], v[144:147], v[32:35]
  ds_read_b64_tr_b16 a[112:113], v238 offset:0
  v_cvt_pk_bf16_f32 v155, v118, v119
  v_mfma_f32_16x16x32_bf16 v[112:115], a[0:3], a[144:147], v[100:103]
  v_exp_f32 v108, v108
  v_mfma_f32_16x16x32_bf16 v[112:115], a[4:7], a[148:151], v[112:115]
  ds_read_b64_tr_b16 a[114:115], v238 offset:4096
  ds_read_b64_tr_b16 a[116:117], v238 offset:8192
  ds_read_b64_tr_b16 a[118:119], v238 offset:12288
  ds_read_b64_tr_b16 a[120:121], v239 offset:0
  ds_read_b64_tr_b16 a[122:123], v239 offset:4096
  ds_read_b64_tr_b16 a[124:125], v239 offset:8192
  ds_read_b64_tr_b16 a[126:127], v239 offset:12288
  v_mfma_f32_16x16x32_bf16 v[112:115], a[8:11], a[152:155], v[112:115]
  v_exp_f32 v109, v109
  v_mfma_f32_16x16x32_bf16 v[112:115], a[12:15], a[156:159], v[112:115]
  v_cvt_pk_bf16_f32 v156, v108, v109
  v_mfma_f32_16x16x32_bf16 v[64:67], a[64:67], v[148:151], 0
  v_exp_f32 v110, v110
  v_mfma_f32_16x16x32_bf16 v[64:67], a[68:71], v[152:155], v[64:67]
  s_waitcnt vmcnt(9) lgkmcnt(0)
  s_barrier
  v_mfma_f32_16x16x32_bf16 v[4:7], a[72:75], v[132:135], 0
  v_exp_f32 v111, v111
  v_mfma_f32_16x16x32_bf16 v[4:7], a[76:79], v[136:139], v[4:7]
  s_sub_u32 s84, s82, s85
  v_cvt_pk_bf16_f32 v157, v110, v111
  v_mfma_f32_16x16x32_bf16 v[116:119], a[0:3], a[160:163], v[104:107]
  v_exp_f32 v112, v112
  v_mfma_f32_16x16x32_bf16 v[116:119], a[4:7], a[164:167], v[116:119]
  s_cmp_lt_u32 s84, s94
  s_cbranch_scc1 PC0_%=
PC0_%=e:
  v_mfma_f32_16x16x32_bf16 v[116:119], a[8:11], a[168:171], v[116:119]
  v_exp_f32 v113, v113
  v_mfma_f32_16x16x32_bf16 v[116:119], a[12:15], a[172:175], v[116:119]
  ds_read_b64_tr_b16 a[64:65], v232 offset:16384
  v_cvt_pk_bf16_f32 v164, v112, v113
  v_mfma_f32_16x16x32_bf16 v[36:39], a[72:75], v[140:143], 0
  v_exp_f32 v114, v114
  v_mfma_f32_16x16x32_bf16 v[36:39], a[76:79], v[144:147], v[36:39]
  buffer_load_dword v251, v249, s[56:59], 0 offen offset:0 sc1
  v_mfma_f32_16x16x32_bf16 v[68:71], a[72:75], v[148:151], 0
  v_exp_f32 v115, v115
  v_mfma_f32_16x16x32_bf16 v[68:71], a[76:79], v[152:155], v[68:71]
  s_add_u32 s68, s60, 65536
  v_cvt_pk_bf16_f32 v165, v114, v115
  v_mfma_f32_16x16x32_bf16 v[8:11], a[80:83], v[132:135], 0
  s_add_u32 m0, s61, 49152
  ds_read_b64_tr_b16 a[66:67], v232 offset:20480
  v_mfma_f32_16x16x32_bf16 v[8:11], a[84:87], v[136:139], v[8:11]
  ds_read_b64_tr_b16 a[68:69], v232 offset:24576
  ds_read_b64_tr_b16 a[70:71], v232 offset:28672
  v_mfma_f32_16x16x32_bf16 v[108:111], a[16:19], a[128:131], v[96:99]
  v_exp_f32 v116, v116
  v_mfma_f32_16x16x32_bf16 v[108:111], a[20:23], a[132:135], v[108:111]
  buffer_load_dwordx4 v240, s[40:43], s68 offen offset:0 nt lds
  v_mfma_f32_16x16x32_bf16 v[108:111], a[24:27], a[136:139], v[108:111]
  v_exp_f32 v117, v117
  v_mfma_f32_16x16x32_bf16 v[108:111], a[28:31], a[140:143], v[108:111]
  ds_read_b128 a[0:3], v228 offset:16384
  v_cvt_pk_bf16_f32 v172, v116, v117
  v_mfma_f32_16x16x32_bf16 v[40:43], a[80:83], v[140:143], 0
  v_exp_f32 v118, v118
  v_mfma_f32_16x16x32_bf16 v[40:43], a[84:87], v[144:147], v[40:43]
  ds_read_b128 a[4:7], v229 offset:16384
  ds_read_b128 a[8:11], v230 offset:16384
  v_mfma_f32_16x16x32_bf16 v[72:75], a[80:83], v[148:151], 0
  v_exp_f32 v119, v119
  v_mfma_f32_16x16x32_bf16 v[72:75], a[84:87], v[152:155], v[72:75]
  ds_read_b128 a[12:15], v231 offset:16384
  v_cvt_pk_bf16_f32 v173, v118, v119
  v_mfma_f32_16x16x32_bf16 v[112:115], a[16:19], a[144:147], v[100:103]
  v_exp_f32 v108, v108
  v_mfma_f32_16x16x32_bf16 v[112:115], a[20:23], a[148:151], v[112:115]
  buffer_load_dwordx4 v241, s[40:43], s68 offen offset:1024 nt lds
  v_mfma_f32_16x16x32_bf16 v[112:115], a[24:27], a[152:155], v[112:115]
  v_exp_f32 v109, v109
  v_mfma_f32_16x16x32_bf16 v[112:115], a[28:31], a[156:159], v[112:115]
  ds_read_b64_tr_b16 a[72:73], v233 offset:16384
  v_cvt_pk_bf16_f32 v158, v108, v109
  v_mfma_f32_16x16x32_bf16 v[12:15], a[88:91], v[132:135], 0
  v_exp_f32 v110, v110
  v_mfma_f32_16x16x32_bf16 v[12:15], a[92:95], v[136:139], v[12:15]
  ds_read_b64_tr_b16 a[74:75], v233 offset:20480
  ds_read_b64_tr_b16 a[76:77], v233 offset:24576
  v_mfma_f32_16x16x32_bf16 v[44:47], a[88:91], v[140:143], 0
  v_exp_f32 v111, v111
  v_mfma_f32_16x16x32_bf16 v[44:47], a[92:95], v[144:147], v[44:47]
  ds_read_b64_tr_b16 a[78:79], v233 offset:28672
  v_cvt_pk_bf16_f32 v159, v110, v111
  v_mfma_f32_16x16x32_bf16 v[116:119], a[16:19], a[160:163], v[104:107]
  v_exp_f32 v112, v112
  v_mfma_f32_16x16x32_bf16 v[116:119], a[20:23], a[164:167], v[116:119]
  s_lshl_b32 s97, s84, 4
  ds_read_b64_tr_b16 a[80:81], v234 offset:16384
  ds_read_b64_tr_b16 a[82:83], v234 offset:20480
  v_mfma_f32_16x16x32_bf16 v[116:119], a[24:27], a[168:171], v[116:119]
  ds_read_b64_tr_b16 a[84:85], v234 offset:24576
  v_exp_f32 v113, v113
  v_mfma_f32_16x16x32_bf16 v[116:119], a[28:31], a[172:175], v[116:119]
  s_add_i32 s97, s97, 112
  v_cvt_pk_bf16_f32 v166, v112, v113
  v_mfma_f32_16x16x32_bf16 v[76:79], a[88:91], v[148:151], 0
  v_exp_f32 v114, v114
  v_mfma_f32_16x16x32_bf16 v[76:79], a[92:95], v[152:155], v[76:79]
  s_add_u32 s97, s97, s95
  buffer_load_dwordx4 v242, s[40:43], s68 offen offset:2048 nt lds
  v_mfma_f32_16x16x32_bf16 v[16:19], a[96:99], v[132:135], 0
  v_exp_f32 v115, v115
  v_mfma_f32_16x16x32_bf16 v[16:19], a[100:103], v[136:139], v[16:19]
  s_lshr_b32 s83, s97, 4
  v_cvt_pk_bf16_f32 v167, v114, v115
  v_mfma_f32_16x16x32_bf16 v[108:111], a[32:35], a[128:131], v[96:99]
  v_exp_f32 v116, v116
  v_mfma_f32_16x16x32_bf16 v[108:111], a[36:39], a[132:135], v[108:111]
  s_add_u32 s94, s84, s96
  ds_read_b64_tr_b16 a[86:87], v234 offset:28672
  ds_read_b128 a[16:19], v228 offset:20480
  v_mfma_f32_16x16x32_bf16 v[108:111], a[40:43], a[136:139], v[108:111]
  ds_read_b128 a[20:23], v229 offset:20480
  v_exp_f32 v117, v117
  v_mfma_f32_16x16x32_bf16 v[108:111], a[44:47], a[140:143], v[108:111]
  s_add_i32 s94, s94, 7
  v_cvt_pk_bf16_f32 v174, v116, v117
  v_mfma_f32_16x16x32_bf16 v[48:51], a[96:99], v[140:143], 0
  v_exp_f32 v118, v118
  v_mfma_f32_16x16x32_bf16 v[48:51], a[100:103], v[144:147], v[48:51]
  s_max_u32 s94, s94, s83
  buffer_load_dwordx4 v243, s[40:43], s68 offen offset:3072 nt lds
  v_mfma_f32_16x16x32_bf16 v[80:83], a[96:99], v[148:151], 0
  v_exp_f32 v119, v119
  v_mfma_f32_16x16x32_bf16 v[80:83], a[100:103], v[152:155], v[80:83]
  s_mov_b32 s65, 110
  v_cvt_pk_bf16_f32 v175, v118, v119
  v_mfma_f32_16x16x32_bf16 v[20:23], a[104:107], v[132:135], 0
  s_mov_b32 s66, s84
  s_add_u32 s68, s60, 49152
  v_mfma_f32_16x16x32_bf16 v[20:23], a[108:111], v[136:139], v[20:23]
  ds_read_b128 a[24:27], v230 offset:20480
  ds_read_b128 a[28:31], v231 offset:20480
  v_mfma_f32_16x16x32_bf16 v[112:115], a[32:35], a[144:147], v[100:103]
  v_exp_f32 v108, v108
  v_mfma_f32_16x16x32_bf16 v[112:115], a[36:39], a[148:151], v[112:115]
  s_add_u32 m0, s62, 0
  ds_read_b64_tr_b16 a[88:89], v235 offset:16384
  v_mfma_f32_16x16x32_bf16 v[112:115], a[40:43], a[152:155], v[112:115]
  v_exp_f32 v109, v109
  v_mfma_f32_16x16x32_bf16 v[112:115], a[44:47], a[156:159], v[112:115]
  ds_read_b64_tr_b16 a[90:91], v235 offset:20480
  v_cvt_pk_bf16_f32 v160, v108, v109
  v_mfma_f32_16x16x32_bf16 v[52:55], a[104:107], v[140:143], 0
  v_exp_f32 v110, v110
  v_mfma_f32_16x16x32_bf16 v[52:55], a[108:111], v[144:147], v[52:55]
  buffer_load_dwordx4 v244, s[44:47], s68 offen offset:0 nt lds
  v_mfma_f32_16x16x32_bf16 v[84:87], a[104:107], v[148:151], 0
  v_exp_f32 v111, v111
  v_mfma_f32_16x16x32_bf16 v[84:87], a[108:111], v[152:155], v[84:87]
  ds_read_b64_tr_b16 a[92:93], v235 offset:24576
  v_cvt_pk_bf16_f32 v161, v110, v111
  v_mfma_f32_16x16x32_bf16 v[116:119], a[32:35], a[160:163], v[104:107]
  v_exp_f32 v112, v112
  v_mfma_f32_16x16x32_bf16 v[116:119], a[36:39], a[164:167], v[116:119]
  ds_read_b64_tr_b16 a[94:95], v235 offset:28672
  ds_read_b64_tr_b16 a[96:97], v236 offset:16384
  v_mfma_f32_16x16x32_bf16 v[116:119], a[40:43], a[168:171], v[116:119]
  v_exp_f32 v113, v113
  v_mfma_f32_16x16x32_bf16 v[116:119], a[44:47], a[172:175], v[116:119]
  ds_read_b64_tr_b16 a[98:99], v236 offset:20480
  v_cvt_pk_bf16_f32 v168, v112, v113
  v_mfma_f32_16x16x32_bf16 v[24:27], a[112:115], v[132:135], 0
  v_exp_f32 v114, v114
  v_mfma_f32_16x16x32_bf16 v[24:27], a[116:119], v[136:139], v[24:27]
  buffer_load_dwordx4 v245, s[44:47], s68 offen offset:1024 nt lds
  v_mfma_f32_16x16x32_bf16 v[56:59], a[112:115], v[140:143], 0
  v_exp_f32 v115, v115
  v_mfma_f32_16x16x32_bf16 v[56:59], a[116:119], v[144:147], v[56:59]
  ds_read_b64_tr_b16 a[100:101], v236 offset:24576
  v_cvt_pk_bf16_f32 v169, v114, v115
  v_mfma_f32_16x16x32_bf16 v[108:111], a[48:51], a[128:131], v[96:99]
  v_exp_f32 v116, v116
  v_mfma_f32_16x16x32_bf16 v[108:111], a[52:55], a[132:135], v[108:111]
  ds_read_b64_tr_b16 a[102:103], v236 offset:28672
  ds_read_b64_tr_b16 a[104:105], v237 offset:16384
  v_mfma_f32_16x16x32_bf16 v[108:111], a[56:59], a[136:139], v[108:111]
  v_exp_f32 v117, v117
  v_mfma_f32_16x16x32_bf16 v[108:111], a[60:63], a[140:143], v[108:111]
  ds_read_b64_tr_b16 a[106:107], v237 offset:20480
  v_cvt_pk_bf16_f32 v176, v116, v117
  v_mfma_f32_16x16x32_bf16 v[88:91], a[112:115], v[148:151], 0
  v_exp_f32 v118, v118
  v_mfma_f32_16x16x32_bf16 v[88:91], a[116:119], v[152:155], v[88:91]
  buffer_load_dwordx4 v244, s[44:47], s68 offen offset:2048 nt lds
  v_mfma_f32_16x16x32_bf16 v[28:31], a[120:123], v[132:135], 0
  v_exp_f32 v119, v119
  v_mfma_f32_16x16x32_bf16 v[28:31], a[124:127], v[136:139], v[28:31]
  ds_read_b64_tr_b16 a[108:109], v237 offset:24576
  v_cvt_pk_bf16_f32 v177, v118, v119
  v_mfma_f32_16x16x32_bf16 v[60:63], a[120:123], v[140:143], 0
  ds_read_b64_tr_b16 a[110:111], v237 offset:28672
  ds_read_b128 a[32:35], v228 offset:24576
  v_mfma_f32_16x16x32_bf16 v[60:63], a[124:127], v[144:147], v[60:63]
  ds_read_b128 a[36:39], v229 offset:24576
  ds_read_b128 a[40:43], v230 offset:24576
  v_mfma_f32_16x16x32_bf16 v[112:115], a[48:51], a[144:147], v[100:103]
  v_exp_f32 v108, v108
  v_mfma_f32_16x16x32_bf16 v[112:115], a[52:55], a[148:151], v[112:115]
  ds_read_b128 a[44:47], v231 offset:24576
  ds_read_b64_tr_b16 a[112:113], v238 offset:16384
  v_mfma_f32_16x16x32_bf16 v[112:115], a[56:59], a[152:155], v[112:115]
  v_exp_f32 v109, v109
  v_mfma_f32_16x16x32_bf16 v[112:115], a[60:63], a[156:159], v[112:115]
  ds_read_b64_tr_b16 a[114:115], v238 offset:20480
  v_cvt_pk_bf16_f32 v162, v108, v109
  v_mfma_f32_16x16x32_bf16 v[92:95], a[120:123], v[148:151], 0
  v_exp_f32 v110, v110
  v_mfma_f32_16x16x32_bf16 v[92:95], a[124:127], v[152:155], v[92:95]
  buffer_load_dwordx4 v245, s[44:47], s68 offen offset:3072 nt lds
  v_mfma_f32_16x16x32_bf16 v[120:123], a[240:243], v[132:135], 0
  v_exp_f32 v111, v111
  v_mfma_f32_16x16x32_bf16 v[120:123], a[240:243], v[136:139], v[120:123]
  ds_read_b64_tr_b16 a[116:117], v238 offset:24576
  v_cvt_pk_bf16_f32 v163, v110, v111
  v_mfma_f32_16x16x32_bf16 v[116:119], a[48:51], a[160:163], v[104:107]
  v_exp_f32 v112, v112
  v_mfma_f32_16x16x32_bf16 v[116:119], a[52:55], a[164:167], v[116:119]
  ds_read_b64_tr_b16 a[118:119], v238 offset:28672
  ds_read_b64_tr_b16 a[120:121], v239 offset:16384
  v_mfma_f32_16x16x32_bf16 v[116:119], a[56:59], a[168:171], v[116:119]
  v_exp_f32 v113, v113
  v_mfma_f32_16x16x32_bf16 v[116:119], a[60:63], a[172:175], v[116:119]
  ds_read_b64_tr_b16 a[122:123], v239 offset:20480
  v_cvt_pk_bf16_f32 v170, v112, v113
  v_mfma_f32_16x16x32_bf16 v[124:127], a[240:243], v[140:143], 0
  v_exp_f32 v114, v114
  v_mfma_f32_16x16x32_bf16 v[124:127], a[240:243], v[144:147], v[124:127]
  ds_read_b64_tr_b16 a[124:125], v239 offset:24576
  ds_read_b64_tr_b16 a[126:127], v239 offset:28672
  v_mfma_f32_16x16x32_bf16 v[128:131], a[240:243], v[148:151], 0
  v_exp_f32 v115, v115
  v_mfma_f32_16x16x32_bf16 v[128:131], a[240:243], v[152:155], v[128:131]
  v_cvt_pk_bf16_f32 v171, v114, v115
  s_waitcnt lgkmcnt(8)
  v_mfma_f32_16x16x32_bf16 v[108:111], a[0:3], a[128:131], v[96:99]
  v_exp_f32 v116, v116
  v_mfma_f32_16x16x32_bf16 v[108:111], a[4:7], a[132:135], v[108:111]
  s_memrealtime s[82:83]
  ds_read_b128 a[48:51], v228 offset:28672
  v_mfma_f32_16x16x32_bf16 v[108:111], a[8:11], a[136:139], v[108:111]
  v_exp_f32 v117, v117
  v_mfma_f32_16x16x32_bf16 v[108:111], a[12:15], a[140:143], v[108:111]
  ds_read_b128 a[52:55], v229 offset:28672
  v_cvt_pk_bf16_f32 v178, v116, v117
  v_mfma_f32_16x16x32_bf16 v[0:3], a[64:67], v[156:159], v[0:3]
  v_exp_f32 v118, v118
  v_mfma_f32_16x16x32_bf16 v[0:3], a[68:71], v[160:163], v[0:3]
  ds_read_b128 a[56:59], v230 offset:28672
  ds_read_b128 a[60:63], v231 offset:28672
  v_mfma_f32_16x16x32_bf16 v[32:35], a[64:67], v[164:167], v[32:35]
  v_exp_f32 v119, v119
  v_mfma_f32_16x16x32_bf16 v[32:35], a[68:71], v[168:171], v[32:35]
  v_cvt_pk_bf16_f32 v179, v118, v119
  v_mfma_f32_16x16x32_bf16 v[112:115], a[0:3], a[144:147], v[100:103]
  v_exp_f32 v108, v108
  v_mfma_f32_16x16x32_bf16 v[112:115], a[4:7], a[148:151], v[112:115]
  v_mfma_f32_16x16x32_bf16 v[112:115], a[8:11], a[152:155], v[112:115]
  v_exp_f32 v109, v109
  v_mfma_f32_16x16x32_bf16 v[112:115], a[12:15], a[156:159], v[112:115]
  v_cvt_pk_bf16_f32 v132, v108, v109
  v_mfma_f32_16x16x32_bf16 v[64:67], a[64:67], v[172:175], v[64:67]
  v_exp_f32 v110, v110
  v_mfma_f32_16x16x32_bf16 v[64:67], a[68:71], v[176:179], v[64:67]
  s_waitcnt vmcnt(9) lgkmcnt(0)
  s_barrier
  v_mfma_f32_16x16x32_bf16 v[4:7], a[72:75], v[156:159], v[4:7]
  v_exp_f32 v111, v111
  v_mfma_f32_16x16x32_bf16 v[4:7], a[76:79], v[160:163], v[4:7]
  s_sub_u32 s84, s82, s85
  v_cvt_pk_bf16_f32 v133, v110, v111
  v_mfma_f32_16x16x32_bf16 v[116:119], a[0:3], a[160:163], v[104:107]
  v_exp_f32 v112, v112
  v_mfma_f32_16x16x32_bf16 v[116:119], a[4:7], a[164:167], v[116:119]
  s_cmp_lt_u32 s84, s94
  s_cbranch_scc1 PC1_%=
PC1_%=e:
  v_mfma_f32_16x16x32_bf16 v[116:119], a[8:11], a[168:171], v[116:119]
  v_exp_f32 v113, v113
  v_mfma_f32_16x16x32_bf16 v[116:119], a[12:15], a[172:175], v[116:119]
  s_add_u32 s68, s60, 81920
  v_cvt_pk_bf16_f32 v140, v112, v113
  v_mfma_f32_16x16x32_bf16 v[36:39], a[72:75], v[164:167], v[36:39]
  v_exp_f32 v114, v114
  v_mfma_f32_16x16x32_bf16 v[36:39], a[76:79], v[168:171], v[36:39]
  s_add_u32 m0, s61, 16384
  ds_read_b64_tr_b16 a[64:65], v232 offset:32768
  v_mfma_f32_16x16x32_bf16 v[68:71], a[72:75], v[172:175], v[68:71]
  v_exp_f32 v115, v115
  v_mfma_f32_16x16x32_bf16 v[68:71], a[76:79], v[176:179], v[68:71]
  ds_read_b64_tr_b16 a[66:67], v232 offset:36864
  v_cvt_pk_bf16_f32 v141, v114, v115
  v_mfma_f32_16x16x32_bf16 v[8:11], a[80:83], v[156:159], v[8:11]
  buffer_load_dwordx4 v240, s[40:43], s68 offen offset:0 nt lds
  v_mfma_f32_16x16x32_bf16 v[8:11], a[84:87], v[160:163], v[8:11]
  ds_read_b64_tr_b16 a[68:69], v232 offset:40960
  ds_read_b64_tr_b16 a[70:71], v232 offset:45056
  v_mfma_f32_16x16x32_bf16 v[108:111], a[16:19], a[128:131], v[96:99]
  v_exp_f32 v116, v116
  v_mfma_f32_16x16x32_bf16 v[108:111], a[20:23], a[132:135], v[108:111]
  buffer_load_dwordx4 v241, s[40:43], s68 offen offset:1024 nt lds
  v_mfma_f32_16x16x32_bf16 v[108:111], a[24:27], a[136:139], v[108:111]
  v_exp_f32 v117, v117
  v_mfma_f32_16x16x32_bf16 v[108:111], a[28:31], a[140:143], v[108:111]
  ds_read_b128 a[0:3], v228 offset:0
  v_cvt_pk_bf16_f32 v148, v116, v117
  v_mfma_f32_16x16x32_bf16 v[40:43], a[80:83], v[164:167], v[40:43]
  v_exp_f32 v118, v118
  v_mfma_f32_16x16x32_bf16 v[40:43], a[84:87], v[168:171], v[40:43]
  ds_read_b128 a[4:7], v229 offset:0
  ds_read_b128 a[8:11], v230 offset:0
  v_mfma_f32_16x16x32_bf16 v[72:75], a[80:83], v[172:175], v[72:75]
  v_exp_f32 v119, v119
  v_mfma_f32_16x16x32_bf16 v[72:75], a[84:87], v[176:179], v[72:75]
  ds_read_b128 a[12:15], v231 offset:0
  v_cvt_pk_bf16_f32 v149, v118, v119
  v_mfma_f32_16x16x32_bf16 v[112:115], a[16:19], a[144:147], v[100:103]
  v_exp_f32 v108, v108
  v_mfma_f32_16x16x32_bf16 v[112:115], a[20:23], a[148:151], v[112:115]
  ds_read_b64_tr_b16 a[72:73], v233 offset:32768
  ds_read_b64_tr_b16 a[74:75], v233 offset:36864
  v_mfma_f32_16x16x32_bf16 v[112:115], a[24:27], a[152:155], v[112:115]
  v_exp_f32 v109, v109
  v_mfma_f32_16x16x32_bf16 v[112:115], a[28:31], a[156:159], v[112:115]
  ds_read_b64_tr_b16 a[76:77], v233 offset:40960
  v_cvt_pk_bf16_f32 v134, v108, v109
  v_mfma_f32_16x16x32_bf16 v[12:15], a[88:91], v[156:159], v[12:15]
  v_exp_f32 v110, v110
  v_mfma_f32_16x16x32_bf16 v[12:15], a[92:95], v[160:163], v[12:15]
  buffer_load_dwordx4 v242, s[40:43], s68 offen offset:2048 nt lds
  v_mfma_f32_16x16x32_bf16 v[44:47], a[88:91], v[164:167], v[44:47]
  v_exp_f32 v111, v111
  v_mfma_f32_16x16x32_bf16 v[44:47], a[92:95], v[168:171], v[44:47]
  ds_read_b64_tr_b16 a[78:79], v233 offset:45056
  v_cvt_pk_bf16_f32 v135, v110, v111
  v_mfma_f32_16x16x32_bf16 v[116:119], a[16:19], a[160:163], v[104:107]
  v_exp_f32 v112, v112
  v_mfma_f32_16x16x32_bf16 v[116:119], a[20:23], a[164:167], v[116:119]
  s_add_i32 s97, s97, 48
  ds_read_b64_tr_b16 a[80:81], v234 offset:32768
  v_mfma_f32_16x16x32_bf16 v[116:119], a[24:27], a[168:171], v[116:119]
  v_exp_f32 v113, v113
  v_mfma_f32_16x16x32_bf16 v[116:119], a[28:31], a[172:175], v[116:119]
  s_add_u32 s97, s97, s95
  v_cvt_pk_bf16_f32 v142, v112, v113
  v_mfma_f32_16x16x32_bf16 v[76:79], a[88:91], v[172:175], v[76:79]
  v_exp_f32 v114, v114
  v_mfma_f32_16x16x32_bf16 v[76:79], a[92:95], v[176:179], v[76:79]
  s_lshr_b32 s83, s97, 4
  ds_read_b64_tr_b16 a[82:83], v234 offset:36864
  ds_read_b64_tr_b16 a[84:85], v234 offset:40960
  v_mfma_f32_16x16x32_bf16 v[16:19], a[96:99], v[156:159], v[16:19]
  ds_read_b64_tr_b16 a[86:87], v234 offset:45056
  v_exp_f32 v115, v115
  v_mfma_f32_16x16x32_bf16 v[16:19], a[100:103], v[160:163], v[16:19]
  s_add_u32 s94, s84, s96
  ds_read_b128 a[16:19], v228 offset:4096
  v_cvt_pk_bf16_f32 v143, v114, v115
  v_mfma_f32_16x16x32_bf16 v[108:111], a[32:35], a[128:131], v[96:99]
  ds_read_b128 a[20:23], v229 offset:4096
  v_exp_f32 v116, v116
  v_mfma_f32_16x16x32_bf16 v[108:111], a[36:39], a[132:135], v[108:111]
  s_add_i32 s94, s94, 3
  buffer_load_dwordx4 v243, s[40:43], s68 offen offset:3072 nt lds
  v_mfma_f32_16x16x32_bf16 v[108:111], a[40:43], a[136:139], v[108:111]
  ds_read_b128 a[24:27], v230 offset:4096
  v_exp_f32 v117, v117
  v_mfma_f32_16x16x32_bf16 v[108:111], a[44:47], a[140:143], v[108:111]
  s_max_u32 s94, s94, s83
  ds_read_b128 a[28:31], v231 offset:4096
  v_cvt_pk_bf16_f32 v150, v116, v117
  v_mfma_f32_16x16x32_bf16 v[48:51], a[96:99], v[164:167], v[48:51]
  ds_read_b64_tr_b16 a[88:89], v235 offset:32768
  v_exp_f32 v118, v118
  v_mfma_f32_16x16x32_bf16 v[48:51], a[100:103], v[168:171], v[48:51]
  s_sub_u32 s83, s84, s66
  s_add_u32 s68, s60, 65536
  v_mfma_f32_16x16x32_bf16 v[80:83], a[96:99], v[172:175], v[80:83]
  v_exp_f32 v119, v119
  v_mfma_f32_16x16x32_bf16 v[80:83], a[100:103], v[176:179], v[80:83]
  s_min_u32 s83, s83, 200
  v_cvt_pk_bf16_f32 v151, v118, v119
  v_mfma_f32_16x16x32_bf16 v[20:23], a[104:107], v[156:159], v[20:23]
  s_add_u32 s65, s65, s83
  s_add_u32 m0, s62, 16384
  v_mfma_f32_16x16x32_bf16 v[20:23], a[108:111], v[160:163], v[20:23]
  s_lshr_b32 s65, s65, 1
  buffer_load_dwordx4 v244, s[44:47], s68 offen offset:0 nt lds
  v_mfma_f32_16x16x32_bf16 v[112:115], a[32:35], a[144:147], v[100:103]
  v_exp_f32 v108, v108
  v_mfma_f32_16x16x32_bf16 v[112:115], a[36:39], a[148:151], v[112:115]
  s_add_u32 s83, s65, s84
  ds_read_b64_tr_b16 a[90:91], v235 offset:36864
  v_mfma_f32_16x16x32_bf16 v[112:115], a[40:43], a[152:155], v[112:115]
  v_exp_f32 v109, v109
  v_mfma_f32_16x16x32_bf16 v[112:115], a[44:47], a[156:159], v[112:115]
  s_sub_u32 s83, s83, 28
  v_cvt_pk_bf16_f32 v136, v108, v109
  v_mfma_f32_16x16x32_bf16 v[52:55], a[104:107], v[164:167], v[52:55]
  v_exp_f32 v110, v110
  v_mfma_f32_16x16x32_bf16 v[52:55], a[108:111], v[168:171], v[52:55]
  s_max_u32 s94, s94, s83
  ds_read_b64_tr_b16 a[92:93], v235 offset:40960
  v_mfma_f32_16x16x32_bf16 v[84:87], a[104:107], v[172:175], v[84:87]
  v_exp_f32 v111, v111
  v_mfma_f32_16x16x32_bf16 v[84:87], a[108:111], v[176:179], v[84:87]
  s_mov_b32 s66, s84
  v_cvt_pk_bf16_f32 v137, v110, v111
  v_mfma_f32_16x16x32_bf16 v[116:119], a[32:35], a[160:163], v[104:107]
  v_exp_f32 v112, v112
  v_mfma_f32_16x16x32_bf16 v[116:119], a[36:39], a[164:167], v[116:119]
  buffer_load_dwordx4 v245, s[44:47], s68 offen offset:1024 nt lds
  v_mfma_f32_16x16x32_bf16 v[116:119], a[40:43], a[168:171], v[116:119]
  v_exp_f32 v113, v113
  v_mfma_f32_16x16x32_bf16 v[116:119], a[44:47], a[172:175], v[116:119]
  ds_read_b64_tr_b16 a[94:95], v235 offset:45056
  v_cvt_pk_bf16_f32 v144, v112, v113
  v_mfma_f32_16x16x32_bf16 v[24:27], a[112:115], v[156:159], v[24:27]
  v_exp_f32 v114, v114
  v_mfma_f32_16x16x32_bf16 v[24:27], a[116:119], v[160:163], v[24:27]
  ds_read_b64_tr_b16 a[96:97], v236 offset:32768
  ds_read_b64_tr_b16 a[98:99], v236 offset:36864
  v_mfma_f32_16x16x32_bf16 v[56:59], a[112:115], v[164:167], v[56:59]
  v_exp_f32 v115, v115
  v_mfma_f32_16x16x32_bf16 v[56:59], a[116:119], v[168:171], v[56:59]
  ds_read_b64_tr_b16 a[100:101], v236 offset:40960
  v_cvt_pk_bf16_f32 v145, v114, v115
  v_mfma_f32_16x16x32_bf16 v[108:111], a[48:51], a[128:131], v[96:99]
  v_exp_f32 v116, v116
  v_mfma_f32_16x16x32_bf16 v[108:111], a[52:55], a[132:135], v[108:111]
  ds_read_b64_tr_b16 a[102:103], v236 offset:45056
  ds_read_b64_tr_b16 a[104:105], v237 offset:32768
  v_mfma_f32_16x16x32_bf16 v[108:111], a[56:59], a[136:139], v[108:111]
  v_exp_f32 v117, v117
  v_mfma_f32_16x16x32_bf16 v[108:111], a[60:63], a[140:143], v[108:111]
  ds_read_b64_tr_b16 a[106:107], v237 offset:36864
  v_cvt_pk_bf16_f32 v152, v116, v117
  v_mfma_f32_16x16x32_bf16 v[88:91], a[112:115], v[172:175], v[88:91]
  v_exp_f32 v118, v118
  v_mfma_f32_16x16x32_bf16 v[88:91], a[116:119], v[176:179], v[88:91]
  buffer_load_dwordx4 v244, s[44:47], s68 offen offset:2048 nt lds
  v_mfma_f32_16x16x32_bf16 v[28:31], a[120:123], v[156:159], v[28:31]
  v_exp_f32 v119, v119
  v_mfma_f32_16x16x32_bf16 v[28:31], a[124:127], v[160:163], v[28:31]
  ds_read_b64_tr_b16 a[108:109], v237 offset:40960
  v_cvt_pk_bf16_f32 v153, v118, v119
  v_mfma_f32_16x16x32_bf16 v[60:63], a[120:123], v[164:167], v[60:63]
  ds_read_b64_tr_b16 a[110:111], v237 offset:45056
  ds_read_b128 a[32:35], v228 offset:8192
  v_mfma_f32_16x16x32_bf16 v[60:63], a[124:127], v[168:171], v[60:63]
  ds_read_b128 a[36:39], v229 offset:8192
  ds_read_b128 a[40:43], v230 offset:8192
  v_mfma_f32_16x16x32_bf16 v[112:115], a[48:51], a[144:147], v[100:103]
  v_exp_f32 v108, v108
  v_mfma_f32_16x16x32_bf16 v[112:115], a[52:55], a[148:151], v[112:115]
  ds_read_b128 a[44:47], v231 offset:8192
  ds_read_b64_tr_b16 a[112:113], v238 offset:32768
  v_mfma_f32_16x16x32_bf16 v[112:115], a[56:59], a[152:155], v[112:115]
  v_exp_f32 v109, v109
  v_mfma_f32_16x16x32_bf16 v[112:115], a[60:63], a[156:159], v[112:115]
  ds_read_b64_tr_b16 a[114:115], v238 offset:36864
  v_cvt_pk_bf16_f32 v138, v108, v109
  v_mfma_f32_16x16x32_bf16 v[92:95], a[120:123], v[172:175], v[92:95]
  v_exp_f32 v110, v110
  v_mfma_f32_16x16x32_bf16 v[92:95], a[124:127], v[176:179], v[92:95]
  buffer_load_dwordx4 v245, s[44:47], s68 offen offset:3072 nt lds
  v_mfma_f32_16x16x32_bf16 v[120:123], a[240:243], v[156:159], v[120:123]
  v_exp_f32 v111, v111
  v_mfma_f32_16x16x32_bf16 v[120:123], a[240:243], v[160:163], v[120:123]
  ds_read_b64_tr_b16 a[116:117], v238 offset:40960
  v_cvt_pk_bf16_f32 v139, v110, v111
  v_mfma_f32_16x16x32_bf16 v[116:119], a[48:51], a[160:163], v[104:107]
  v_exp_f32 v112, v112
  v_mfma_f32_16x16x32_bf16 v[116:119], a[52:55], a[164:167], v[116:119]
  ds_read_b64_tr_b16 a[118:119], v238 offset:45056
  ds_read_b64_tr_b16 a[120:121], v239 offset:32768
  v_mfma_f32_16x16x32_bf16 v[116:119], a[56:59], a[168:171], v[116:119]
  v_exp_f32 v113, v113
  v_mfma_f32_16x16x32_bf16 v[116:119], a[60:63], a[172:175], v[116:119]
  ds_read_b64_tr_b16 a[122:123], v239 offset:36864
  v_cvt_pk_bf16_f32 v146, v112, v113
  v_mfma_f32_16x16x32_bf16 v[124:127], a[240:243], v[164:167], v[124:127]
  v_exp_f32 v114, v114
  v_mfma_f32_16x16x32_bf16 v[124:127], a[240:243], v[168:171], v[124:127]
  ds_read_b64_tr_b16 a[124:125], v239 offset:40960
  ds_read_b64_tr_b16 a[126:127], v239 offset:45056
  v_mfma_f32_16x16x32_bf16 v[128:131], a[240:243], v[172:175], v[128:131]
  v_exp_f32 v115, v115
  v_mfma_f32_16x16x32_bf16 v[128:131], a[240:243], v[176:179], v[128:131]
  v_cvt_pk_bf16_f32 v147, v114, v115
  s_waitcnt lgkmcnt(8)
  v_mfma_f32_16x16x32_bf16 v[108:111], a[0:3], a[128:131], v[96:99]
  v_exp_f32 v116, v116
  v_mfma_f32_16x16x32_bf16 v[108:111], a[4:7], a[132:135], v[108:111]
  s_memrealtime s[82:83]
  ds_read_b128 a[48:51], v228 offset:12288
  v_mfma_f32_16x16x32_bf16 v[108:111], a[8:11], a[136:139], v[108:111]
  v_exp_f32 v117, v117
  v_mfma_f32_16x16x32_bf16 v[108:111], a[12:15], a[140:143], v[108:111]
  ds_read_b128 a[52:55], v229 offset:12288
  v_cvt_pk_bf16_f32 v154, v116, v117
  v_mfma_f32_16x16x32_bf16 v[0:3], a[64:67], v[132:135], v[0:3]
  v_exp_f32 v118, v118
  v_mfma_f32_16x16x32_bf16 v[0:3], a[68:71], v[136:139], v[0:3]
  ds_read_b128 a[56:59], v230 offset:12288
  ds_read_b128 a[60:63], v231 offset:12288
  v_mfma_f32_16x16x32_bf16 v[32:35], a[64:67], v[140:143], v[32:35]
  v_exp_f32 v119, v119
  v_mfma_f32_16x16x32_bf16 v[32:35], a[68:71], v[144:147], v[32:35]
  v_cvt_pk_bf16_f32 v155, v118, v119
  v_mfma_f32_16x16x32_bf16 v[112:115], a[0:3], a[144:147], v[100:103]
  v_exp_f32 v108, v108
  v_mfma_f32_16x16x32_bf16 v[112:115], a[4:7], a[148:151], v[112:115]
  v_mfma_f32_16x16x32_bf16 v[112:115], a[8:11], a[152:155], v[112:115]
  v_exp_f32 v109, v109
  v_mfma_f32_16x16x32_bf16 v[112:115], a[12:15], a[156:159], v[112:115]
  v_cvt_pk_bf16_f32 v156, v108, v109
  v_mfma_f32_16x16x32_bf16 v[64:67], a[64:67], v[148:151], v[64:67]
  v_exp_f32 v110, v110
  v_mfma_f32_16x16x32_bf16 v[64:67], a[68:71], v[152:155], v[64:67]
  s_waitcnt vmcnt(8) lgkmcnt(0)
  s_barrier
  v_mfma_f32_16x16x32_bf16 v[4:7], a[72:75], v[132:135], v[4:7]
  v_exp_f32 v111, v111
  s_cmp_lg_u32 s67, 0
  s_cbranch_scc1 NOEXP%=
  v_add_u32 v247, 1, v251
NOEXP%=:
  v_mfma_f32_16x16x32_bf16 v[4:7], a[76:79], v[136:139], v[4:7]
  s_sub_u32 s84, s82, s85
  v_cvt_pk_bf16_f32 v157, v110, v111
  v_mfma_f32_16x16x32_bf16 v[116:119], a[0:3], a[160:163], v[104:107]
  v_exp_f32 v112, v112
  v_readlane_b32 s77, v247, 16
  v_mfma_f32_16x16x32_bf16 v[116:119], a[4:7], a[164:167], v[116:119]
  s_cmp_lt_u32 s84, s94
  s_cbranch_scc1 PC2_%=
PC2_%=e:
  v_mfma_f32_16x16x32_bf16 v[116:119], a[8:11], a[168:171], v[116:119]
  v_exp_f32 v113, v113
  v_mfma_f32_16x16x32_bf16 v[116:119], a[12:15], a[172:175], v[116:119]
  ds_read_b64_tr_b16 a[64:65], v232 offset:0
  v_cvt_pk_bf16_f32 v164, v112, v113
  v_mfma_f32_16x16x32_bf16 v[36:39], a[72:75], v[140:143], v[36:39]
  buffer_store_dword v247, v216, s[56:59], 0 offen offset:0 sc1
  v_exp_f32 v114, v114
  v_mov_b32 v252, s77
  v_mfma_f32_16x16x32_bf16 v[36:39], a[76:79], v[144:147], v[36:39]
  buffer_load_dwordx2 v[204:205], v214, s[56:59], 0 offen offset:0 sc1
  v_mfma_f32_16x16x32_bf16 v[68:71], a[72:75], v[148:151], v[68:71]
  v_exp_f32 v115, v115
  v_mfma_f32_16x16x32_bf16 v[68:71], a[76:79], v[152:155], v[68:71]
  s_add_u32 s68, s60, 98304
  v_cvt_pk_bf16_f32 v165, v114, v115
  v_mfma_f32_16x16x32_bf16 v[8:11], a[80:83], v[132:135], v[8:11]
  s_add_u32 m0, s61, 0
  ds_read_b64_tr_b16 a[66:67], v232 offset:4096
  v_mfma_f32_16x16x32_bf16 v[8:11], a[84:87], v[136:139], v[8:11]
  ds_read_b64_tr_b16 a[68:69], v232 offset:8192
  ds_read_b64_tr_b16 a[70:71], v232 offset:12288
  v_mfma_f32_16x16x32_bf16 v[108:111], a[16:19], a[128:131], v[96:99]
  v_exp_f32 v116, v116
  v_mfma_f32_16x16x32_bf16 v[108:111], a[20:23], a[132:135], v[108:111]
  buffer_load_dwordx4 v240, s[40:43], s68 offen offset:0 nt lds
  v_mfma_f32_16x16x32_bf16 v[108:111], a[24:27], a[136:139], v[108:111]
  v_exp_f32 v117, v117
  v_mfma_f32_16x16x32_bf16 v[108:111], a[28:31], a[140:143], v[108:111]
  ds_read_b128 a[0:3], v228 offset:49152
  v_cvt_pk_bf16_f32 v172, v116, v117
  v_mfma_f32_16x16x32_bf16 v[40:43], a[80:83], v[140:143], v[40:43]
  v_exp_f32 v118, v118
  v_mfma_f32_16x16x32_bf16 v[40:43], a[84:87], v[144:147], v[40:43]
  ds_read_b128 a[4:7], v229 offset:49152
  ds_read_b128 a[8:11], v230 offset:49152
  v_mfma_f32_16x16x32_bf16 v[72:75], a[80:83], v[148:151], v[72:75]
  v_exp_f32 v119, v119
  v_mfma_f32_16x16x32_bf16 v[72:75], a[84:87], v[152:155], v[72:75]
  ds_read_b128 a[12:15], v231 offset:49152
  v_cvt_pk_bf16_f32 v173, v118, v119
  v_mfma_f32_16x16x32_bf16 v[112:115], a[16:19], a[144:147], v[100:103]
  v_exp_f32 v108, v108
  v_mfma_f32_16x16x32_bf16 v[112:115], a[20:23], a[148:151], v[112:115]
  buffer_load_dwordx4 v241, s[40:43], s68 offen offset:1024 nt lds
  v_mfma_f32_16x16x32_bf16 v[112:115], a[24:27], a[152:155], v[112:115]
  v_exp_f32 v109, v109
  v_mfma_f32_16x16x32_bf16 v[112:115], a[28:31], a[156:159], v[112:115]
  ds_read_b64_tr_b16 a[72:73], v233 offset:0
  v_cvt_pk_bf16_f32 v158, v108, v109
  v_mfma_f32_16x16x32_bf16 v[12:15], a[88:91], v[132:135], v[12:15]
  v_exp_f32 v110, v110
  v_mfma_f32_16x16x32_bf16 v[12:15], a[92:95], v[136:139], v[12:15]
  ds_read_b64_tr_b16 a[74:75], v233 offset:4096
  ds_read_b64_tr_b16 a[76:77], v233 offset:8192
  v_mfma_f32_16x16x32_bf16 v[44:47], a[88:91], v[140:143], v[44:47]
  v_exp_f32 v111, v111
  v_mfma_f32_16x16x32_bf16 v[44:47], a[92:95], v[144:147], v[44:47]
  ds_read_b64_tr_b16 a[78:79], v233 offset:12288
  v_cvt_pk_bf16_f32 v159, v110, v111
  v_mfma_f32_16x16x32_bf16 v[116:119], a[16:19], a[160:163], v[104:107]
  v_exp_f32 v112, v112
  v_mfma_f32_16x16x32_bf16 v[116:119], a[20:23], a[164:167], v[116:119]
  s_add_u32 s97, s97, s95
  ds_read_b64_tr_b16 a[80:81], v234 offset:0
  ds_read_b64_tr_b16 a[82:83], v234 offset:4096
  v_mfma_f32_16x16x32_bf16 v[116:119], a[24:27], a[168:171], v[116:119]
  ds_read_b64_tr_b16 a[84:85], v234 offset:8192
  v_exp_f32 v113, v113
  v_mfma_f32_16x16x32_bf16 v[116:119], a[28:31], a[172:175], v[116:119]
  s_lshr_b32 s83, s97, 4
  v_cvt_pk_bf16_f32 v166, v112, v113
  v_mfma_f32_16x16x32_bf16 v[76:79], a[88:91], v[148:151], v[76:79]
  v_exp_f32 v114, v114
  v_mfma_f32_16x16x32_bf16 v[76:79], a[92:95], v[152:155], v[76:79]
  s_add_u32 s94, s84, s96
  buffer_load_dwordx4 v242, s[40:43], s68 offen offset:2048 nt lds
  v_mfma_f32_16x16x32_bf16 v[16:19], a[96:99], v[132:135], v[16:19]
  v_exp_f32 v115, v115
  v_mfma_f32_16x16x32_bf16 v[16:19], a[100:103], v[136:139], v[16:19]
  s_max_u32 s94, s94, s83
  v_cvt_pk_bf16_f32 v167, v114, v115
  v_mfma_f32_16x16x32_bf16 v[108:111], a[32:35], a[128:131], v[96:99]
  v_exp_f32 v116, v116
  v_mfma_f32_16x16x32_bf16 v[108:111], a[36:39], a[132:135], v[108:111]
  s_sub_u32 s83, s84, s66
  ds_read_b64_tr_b16 a[86:87], v234 offset:12288
  ds_read_b128 a[16:19], v228 offset:53248
  v_mfma_f32_16x16x32_bf16 v[108:111], a[40:43], a[136:139], v[108:111]
  ds_read_b128 a[20:23], v229 offset:53248
  v_exp_f32 v117, v117
  v_mfma_f32_16x16x32_bf16 v[108:111], a[44:47], a[140:143], v[108:111]
  s_min_u32 s83, s83, 200
  v_cvt_pk_bf16_f32 v174, v116, v117
  v_mfma_f32_16x16x32_bf16 v[48:51], a[96:99], v[140:143], v[48:51]
  v_exp_f32 v118, v118
  v_mfma_f32_16x16x32_bf16 v[48:51], a[100:103], v[144:147], v[48:51]
  s_add_u32 s65, s65, s83
  buffer_load_dwordx4 v243, s[40:43], s68 offen offset:3072 nt lds
  v_mfma_f32_16x16x32_bf16 v[80:83], a[96:99], v[148:151], v[80:83]
  v_exp_f32 v119, v119
  v_mfma_f32_16x16x32_bf16 v[80:83], a[100:103], v[152:155], v[80:83]
  s_lshr_b32 s65, s65, 1
  v_cvt_pk_bf16_f32 v175, v118, v119
  v_mfma_f32_16x16x32_bf16 v[20:23], a[104:107], v[132:135], v[20:23]
  s_add_u32 s83, s65, s84
  s_add_u32 s68, s60, 81920
  v_mfma_f32_16x16x32_bf16 v[20:23], a[108:111], v[136:139], v[20:23]
  s_sub_u32 s83, s83, 28
  ds_read_b128 a[24:27], v230 offset:53248
  ds_read_b128 a[28:31], v231 offset:53248
  v_mfma_f32_16x16x32_bf16 v[112:115], a[32:35], a[144:147], v[100:103]
  ds_read_b64_tr_b16 a[88:89], v235 offset:0
  v_exp_f32 v108, v108
  v_mfma_f32_16x16x32_bf16 v[112:115], a[36:39], a[148:151], v[112:115]
  s_max_u32 s94, s94, s83
  s_add_u32 m0, s62, 32768
  v_mfma_f32_16x16x32_bf16 v[112:115], a[40:43], a[152:155], v[112:115]
  v_exp_f32 v109, v109
  v_mfma_f32_16x16x32_bf16 v[112:115], a[44:47], a[156:159], v[112:115]
  s_mov_b32 s66, s84
  v_cvt_pk_bf16_f32 v160, v108, v109
  v_mfma_f32_16x16x32_bf16 v[52:55], a[104:107], v[140:143], v[52:55]
  v_exp_f32 v110, v110
  v_mfma_f32_16x16x32_bf16 v[52:55], a[108:111], v[144:147], v[52:55]
  buffer_load_dwordx4 v244, s[44:47], s68 offen offset:0 nt lds
  v_mfma_f32_16x16x32_bf16 v[84:87], a[104:107], v[148:151], v[84:87]
  v_exp_f32 v111, v111
  v_mfma_f32_16x16x32_bf16 v[84:87], a[108:111], v[152:155], v[84:87]
  ds_read_b64_tr_b16 a[90:91], v235 offset:4096
  v_cvt_pk_bf16_f32 v161, v110, v111
  v_mfma_f32_16x16x32_bf16 v[116:119], a[32:35], a[160:163], v[104:107]
  v_exp_f32 v112, v112
  v_mfma_f32_16x16x32_bf16 v[116:119], a[36:39], a[164:167], v[116:119]
  ds_read_b64_tr_b16 a[92:93], v235 offset:8192
  ds_read_b64_tr_b16 a[94:95], v235 offset:12288
  v_mfma_f32_16x16x32_bf16 v[116:119], a[40:43], a[168:171], v[116:119]
  v_exp_f32 v113, v113
  v_mfma_f32_16x16x32_bf16 v[116:119], a[44:47], a[172:175], v[116:119]
  ds_read_b64_tr_b16 a[96:97], v236 offset:0
  v_cvt_pk_bf16_f32 v168, v112, v113
  v_mfma_f32_16x16x32_bf16 v[24:27], a[112:115], v[132:135], v[24:27]
  v_exp_f32 v114, v114
  v_mfma_f32_16x16x32_bf16 v[24:27], a[116:119], v[136:139], v[24:27]
  buffer_load_dwordx4 v245, s[44:47], s68 offen offset:1024 nt lds
  v_mfma_f32_16x16x32_bf16 v[56:59], a[112:115], v[140:143], v[56:59]
  v_exp_f32 v115, v115
  v_mfma_f32_16x16x32_bf16 v[56:59], a[116:119], v[144:147], v[56:59]
  ds_read_b64_tr_b16 a[98:99], v236 offset:4096
  v_cvt_pk_bf16_f32 v169, v114, v115
  v_mfma_f32_16x16x32_bf16 v[108:111], a[48:51], a[128:131], v[96:99]
  v_exp_f32 v116, v116
  v_mfma_f32_16x16x32_bf16 v[108:111], a[52:55], a[132:135], v[108:111]
  ds_read_b64_tr_b16 a[100:101], v236 offset:8192
  ds_read_b64_tr_b16 a[102:103], v236 offset:12288
  v_mfma_f32_16x16x32_bf16 v[108:111], a[56:59], a[136:139], v[108:111]
  v_exp_f32 v117, v117
  v_mfma_f32_16x16x32_bf16 v[108:111], a[60:63], a[140:143], v[108:111]
  ds_read_b64_tr_b16 a[104:105], v237 offset:0
  v_cvt_pk_bf16_f32 v176, v116, v117
  v_mfma_f32_16x16x32_bf16 v[88:91], a[112:115], v[148:151], v[88:91]
  v_exp_f32 v118, v118
  v_mfma_f32_16x16x32_bf16 v[88:91], a[116:119], v[152:155], v[88:91]
  buffer_load_dwordx4 v244, s[44:47], s68 offen offset:2048 nt lds
  v_mfma_f32_16x16x32_bf16 v[28:31], a[120:123], v[132:135], v[28:31]
  v_exp_f32 v119, v119
  v_mfma_f32_16x16x32_bf16 v[28:31], a[124:127], v[136:139], v[28:31]
  ds_read_b64_tr_b16 a[106:107], v237 offset:4096
  v_cvt_pk_bf16_f32 v177, v118, v119
  v_mfma_f32_16x16x32_bf16 v[60:63], a[120:123], v[140:143], v[60:63]
  ds_read_b64_tr_b16 a[108:109], v237 offset:8192
  ds_read_b64_tr_b16 a[110:111], v237 offset:12288
  v_mfma_f32_16x16x32_bf16 v[60:63], a[124:127], v[144:147], v[60:63]
  ds_read_b128 a[32:35], v228 offset:57344
  ds_read_b128 a[36:39], v229 offset:57344
  v_mfma_f32_16x16x32_bf16 v[112:115], a[48:51], a[144:147], v[100:103]
  v_exp_f32 v108, v108
  v_mfma_f32_16x16x32_bf16 v[112:115], a[52:55], a[148:151], v[112:115]
  ds_read_b128 a[40:43], v230 offset:57344
  ds_read_b128 a[44:47], v231 offset:57344
  v_mfma_f32_16x16x32_bf16 v[112:115], a[56:59], a[152:155], v[112:115]
  v_exp_f32 v109, v109
  v_mfma_f32_16x16x32_bf16 v[112:115], a[60:63], a[156:159], v[112:115]
  ds_read_b64_tr_b16 a[112:113], v238 offset:0
  v_cvt_pk_bf16_f32 v162, v108, v109
  v_mfma_f32_16x16x32_bf16 v[92:95], a[120:123], v[148:151], v[92:95]
  v_exp_f32 v110, v110
  v_mfma_f32_16x16x32_bf16 v[92:95], a[124:127], v[152:155], v[92:95]
  buffer_load_dwordx4 v245, s[44:47], s68 offen offset:3072 nt lds
  v_mfma_f32_16x16x32_bf16 v[120:123], a[240:243], v[132:135], v[120:123]
  v_exp_f32 v111, v111
  v_mfma_f32_16x16x32_bf16 v[120:123], a[240:243], v[136:139], v[120:123]
  ds_read_b64_tr_b16 a[114:115], v238 offset:4096
  v_cvt_pk_bf16_f32 v163, v110, v111
  v_mfma_f32_16x16x32_bf16 v[116:119], a[48:51], a[160:163], v[104:107]
  v_exp_f32 v112, v112
  v_mfma_f32_16x16x32_bf16 v[116:119], a[52:55], a[164:167], v[116:119]
  ds_read_b64_tr_b16 a[116:117], v238 offset:8192
  ds_read_b64_tr_b16 a[118:119], v238 offset:12288
  v_mfma_f32_16x16x32_bf16 v[116:119], a[56:59], a[168:171], v[116:119]
  v_exp_f32 v113, v113
  v_mfma_f32_16x16x32_bf16 v[116:119], a[60:63], a[172:175], v[116:119]
  ds_read_b64_tr_b16 a[120:121], v239 offset:0
  v_cvt_pk_bf16_f32 v170, v112, v113
  v_mfma_f32_16x16x32_bf16 v[124:127], a[240:243], v[140:143], v[124:127]
  v_exp_f32 v114, v114
  v_mfma_f32_16x16x32_bf16 v[124:127], a[240:243], v[144:147], v[124:127]
  ds_read_b64_tr_b16 a[122:123], v239 offset:4096
  ds_read_b64_tr_b16 a[124:125], v239 offset:8192
  ds_read_b64_tr_b16 a[126:127], v239 offset:12288
  v_mfma_f32_16x16x32_bf16 v[128:131], a[240:243], v[148:151], v[128:131]
  v_exp_f32 v115, v115
  v_mfma_f32_16x16x32_bf16 v[128:131], a[240:243], v[152:155], v[128:131]
  v_cvt_pk_bf16_f32 v171, v114, v115
  s_waitcnt lgkmcnt(8)
  v_mfma_f32_16x16x32_bf16 v[108:111], a[0:3], a[128:131], v[96:99]
  v_exp_f32 v116, v116
  v_mfma_f32_16x16x32_bf16 v[108:111], a[4:7], a[132:135], v[108:111]
  s_memrealtime s[82:83]
  ds_read_b128 a[48:51], v228 offset:61440
  v_mfma_f32_16x16x32_bf16 v[108:111], a[8:11], a[136:139], v[108:111]
  v_exp_f32 v117, v117
  v_mfma_f32_16x16x32_bf16 v[108:111], a[12:15], a[140:143], v[108:111]
  ds_read_b128 a[52:55], v229 offset:61440
  v_cvt_pk_bf16_f32 v178, v116, v117
  v_mfma_f32_16x16x32_bf16 v[0:3], a[64:67], v[156:159], v[0:3]
  v_exp_f32 v118, v118
  v_mfma_f32_16x16x32_bf16 v[0:3], a[68:71], v[160:163], v[0:3]
  ds_read_b128 a[56:59], v230 offset:61440
  ds_read_b128 a[60:63], v231 offset:61440
  v_mfma_f32_16x16x32_bf16 v[32:35], a[64:67], v[164:167], v[32:35]
  v_exp_f32 v119, v119
  v_mfma_f32_16x16x32_bf16 v[32:35], a[68:71], v[168:171], v[32:35]
  v_cvt_pk_bf16_f32 v179, v118, v119
  v_mfma_f32_16x16x32_bf16 v[112:115], a[0:3], a[144:147], v[100:103]
  v_exp_f32 v108, v108
  v_mfma_f32_16x16x32_bf16 v[112:115], a[4:7], a[148:151], v[112:115]
  v_mfma_f32_16x16x32_bf16 v[112:115], a[8:11], a[152:155], v[112:115]
  v_exp_f32 v109, v109
  v_mfma_f32_16x16x32_bf16 v[112:115], a[12:15], a[156:159], v[112:115]
  v_cvt_pk_bf16_f32 v132, v108, v109
  v_mfma_f32_16x16x32_bf16 v[64:67], a[64:67], v[172:175], v[64:67]
  v_exp_f32 v110, v110
  v_mfma_f32_16x16x32_bf16 v[64:67], a[68:71], v[176:179], v[64:67]
  s_waitcnt vmcnt(10) lgkmcnt(0)
  s_barrier
  v_mfma_f32_16x16x32_bf16 v[4:7], a[72:75], v[156:159], v[4:7]
  v_exp_f32 v111, v111
  v_mfma_f32_16x16x32_bf16 v[4:7], a[76:79], v[160:163], v[4:7]
  s_sub_u32 s84, s82, s85
  v_cvt_pk_bf16_f32 v133, v110, v111
  v_mfma_f32_16x16x32_bf16 v[116:119], a[0:3], a[160:163], v[104:107]
  v_exp_f32 v112, v112
  v_mfma_f32_16x16x32_bf16 v[116:119], a[4:7], a[164:167], v[116:119]
  s_cmp_lt_u32 s84, s94
  s_cbranch_scc1 PC3_%=
PC3_%=e:
  v_mfma_f32_16x16x32_bf16 v[116:119], a[8:11], a[168:171], v[116:119]
  v_exp_f32 v113, v113
  v_mfma_f32_16x16x32_bf16 v[116:119], a[12:15], a[172:175], v[116:119]
  s_add_u32 s68, s60, 114688
  v_cvt_pk_bf16_f32 v140, v112, v113
  v_mfma_f32_16x16x32_bf16 v[36:39], a[72:75], v[164:167], v[36:39]
  v_exp_f32 v114, v114
  v_mfma_f32_16x16x32_bf16 v[36:39], a[76:79], v[168:171], v[36:39]
  s_add_u32 m0, s61, 49152
  ds_read_b64_tr_b16 a[64:65], v232 offset:16384
  v_mfma_f32_16x16x32_bf16 v[68:71], a[72:75], v[172:175], v[68:71]
  v_exp_f32 v115, v115
  v_mfma_f32_16x16x32_bf16 v[68:71], a[76:79], v[176:179], v[68:71]
  ds_read_b64_tr_b16 a[66:67], v232 offset:20480
  v_cvt_pk_bf16_f32 v141, v114, v115
  v_mfma_f32_16x16x32_bf16 v[8:11], a[80:83], v[156:159], v[8:11]
  buffer_load_dwordx4 v240, s[40:43], s68 offen offset:0 nt lds
  v_mfma_f32_16x16x32_bf16 v[8:11], a[84:87], v[160:163], v[8:11]
  ds_read_b64_tr_b16 a[68:69], v232 offset:24576
  ds_read_b64_tr_b16 a[70:71], v232 offset:28672
  v_mfma_f32_16x16x32_bf16 v[108:111], a[16:19], a[128:131], v[96:99]
  v_exp_f32 v116, v116
  v_mfma_f32_16x16x32_bf16 v[108:111], a[20:23], a[132:135], v[108:111]
  buffer_load_dwordx4 v241, s[40:43], s68 offen offset:1024 nt lds
  v_mfma_f32_16x16x32_bf16 v[108:111], a[24:27], a[136:139], v[108:111]
  v_exp_f32 v117, v117
  v_mfma_f32_16x16x32_bf16 v[108:111], a[28:31], a[140:143], v[108:111]
  ds_read_b128 a[0:3], v228 offset:16384
  v_cvt_pk_bf16_f32 v148, v116, v117
  v_mfma_f32_16x16x32_bf16 v[40:43], a[80:83], v[164:167], v[40:43]
  v_exp_f32 v118, v118
  v_mfma_f32_16x16x32_bf16 v[40:43], a[84:87], v[168:171], v[40:43]
  ds_read_b128 a[4:7], v229 offset:16384
  ds_read_b128 a[8:11], v230 offset:16384
  v_mfma_f32_16x16x32_bf16 v[72:75], a[80:83], v[172:175], v[72:75]
  v_exp_f32 v119, v119
  v_mfma_f32_16x16x32_bf16 v[72:75], a[84:87], v[176:179], v[72:75]
  ds_read_b128 a[12:15], v231 offset:16384
  v_cvt_pk_bf16_f32 v149, v118, v119
  v_mfma_f32_16x16x32_bf16 v[112:115], a[16:19], a[144:147], v[100:103]
  v_exp_f32 v108, v108
  v_mfma_f32_16x16x32_bf16 v[112:115], a[20:23], a[148:151], v[112:115]
  ds_read_b64_tr_b16 a[72:73], v233 offset:16384
  ds_read_b64_tr_b16 a[74:75], v233 offset:20480
  v_mfma_f32_16x16x32_bf16 v[112:115], a[24:27], a[152:155], v[112:115]
  v_exp_f32 v109, v109
  v_mfma_f32_16x16x32_bf16 v[112:115], a[28:31], a[156:159], v[112:115]
  ds_read_b64_tr_b16 a[76:77], v233 offset:24576
  v_cvt_pk_bf16_f32 v134, v108, v109
  v_mfma_f32_16x16x32_bf16 v[12:15], a[88:91], v[156:159], v[12:15]
  v_exp_f32 v110, v110
  v_mfma_f32_16x16x32_bf16 v[12:15], a[92:95], v[160:163], v[12:15]
  buffer_load_dwordx4 v242, s[40:43], s68 offen offset:2048 nt lds
  v_mfma_f32_16x16x32_bf16 v[44:47], a[88:91], v[164:167], v[44:47]
  v_exp_f32 v111, v111
  v_mfma_f32_16x16x32_bf16 v[44:47], a[92:95], v[168:171], v[44:47]
  ds_read_b64_tr_b16 a[78:79], v233 offset:28672
  v_cvt_pk_bf16_f32 v135, v110, v111
  v_mfma_f32_16x16x32_bf16 v[116:119], a[16:19], a[160:163], v[104:107]
  v_exp_f32 v112, v112
  v_mfma_f32_16x16x32_bf16 v[116:119], a[20:23], a[164:167], v[116:119]
  s_add_i32 s97, s97, -48
  ds_read_b64_tr_b16 a[80:81], v234 offset:16384
  s_mov_b64 s[52:53], s[20:21]
  v_mfma_f32_16x16x32_bf16 v[116:119], a[24:27], a[168:171], v[116:119]
  v_exp_f32 v113, v113
  v_mfma_f32_16x16x32_bf16 v[116:119], a[28:31], a[172:175], v[116:119]
  s_add_u32 s97, s97, s95
  v_cvt_pk_bf16_f32 v142, v112, v113
  s_mov_b32 s54, 0
  v_mfma_f32_16x16x32_bf16 v[76:79], a[88:91], v[172:175], v[76:79]
  v_exp_f32 v114, v114
  v_mfma_f32_16x16x32_bf16 v[76:79], a[92:95], v[176:179], v[76:79]
  s_lshr_b32 s83, s97, 4
  ds_read_b64_tr_b16 a[82:83], v234 offset:20480
  ds_read_b64_tr_b16 a[84:85], v234 offset:24576
  s_mul_i32 s55, s64, 1310720
  v_mfma_f32_16x16x32_bf16 v[16:19], a[96:99], v[156:159], v[16:19]
  ds_read_b64_tr_b16 a[86:87], v234 offset:28672
  v_exp_f32 v115, v115
  v_mfma_f32_16x16x32_bf16 v[16:19], a[100:103], v[160:163], v[16:19]
  s_add_u32 s94, s84, s96
  ds_read_b128 a[16:19], v228 offset:20480
  v_cvt_pk_bf16_f32 v143, v114, v115
  s_add_u32 s54, s54, s55
  v_mfma_f32_16x16x32_bf16 v[108:111], a[32:35], a[128:131], v[96:99]
  ds_read_b128 a[20:23], v229 offset:20480
  v_exp_f32 v116, v116
  v_mfma_f32_16x16x32_bf16 v[108:111], a[36:39], a[132:135], v[108:111]
  s_add_i32 s94, s94, -3
  buffer_load_dwordx4 v243, s[40:43], s68 offen offset:3072 nt lds
  v_mfma_f32_16x16x32_bf16 v[108:111], a[40:43], a[136:139], v[108:111]
  ds_read_b128 a[24:27], v230 offset:20480
  v_exp_f32 v117, v117
  v_mfma_f32_16x16x32_bf16 v[108:111], a[44:47], a[140:143], v[108:111]
  s_max_u32 s94, s94, s83
  ds_read_b128 a[28:31], v231 offset:20480
  v_cvt_pk_bf16_f32 v150, v116, v117
  v_mfma_f32_16x16x32_bf16 v[48:51], a[96:99], v[164:167], v[48:51]
  ds_read_b64_tr_b16 a[88:89], v235 offset:16384
  v_exp_f32 v118, v118
  v_mfma_f32_16x16x32_bf16 v[48:51], a[100:103], v[168:171], v[48:51]
  s_sub_u32 s83, s84, s66
  s_add_u32 s68, s60, 98304
  s_mul_i32 s55, s79, 128
  v_mfma_f32_16x16x32_bf16 v[80:83], a[96:99], v[172:175], v[80:83]
  v_exp_f32 v119, v119
  v_mfma_f32_16x16x32_bf16 v[80:83], a[100:103], v[176:179], v[80:83]
  s_min_u32 s83, s83, 200
  v_cvt_pk_bf16_f32 v151, v118, v119
  s_add_u32 s54, s54, s55
  v_mfma_f32_16x16x32_bf16 v[20:23], a[104:107], v[156:159], v[20:23]
  s_add_u32 s65, s65, s83
  s_add_u32 m0, s62, 0
  v_mfma_f32_16x16x32_bf16 v[20:23], a[108:111], v[160:163], v[20:23]
  s_lshr_b32 s65, s65, 1
  buffer_load_dwordx4 v244, s[44:47], s68 offen offset:0 nt lds
  v_mfma_f32_16x16x32_bf16 v[112:115], a[32:35], a[144:147], v[100:103]
  v_exp_f32 v108, v108
  s_add_u32 s52, s52, s54
  s_addc_u32 s53, s53, 0
  v_mfma_f32_16x16x32_bf16 v[112:115], a[36:39], a[148:151], v[112:115]
  s_add_u32 s83, s65, s84
  ds_read_b64_tr_b16 a[90:91], v235 offset:20480
  v_mfma_f32_16x16x32_bf16 v[112:115], a[40:43], a[152:155], v[112:115]
  v_exp_f32 v109, v109
  s_and_b32 s53, s53, 0xffff
  v_mfma_f32_16x16x32_bf16 v[112:115], a[44:47], a[156:159], v[112:115]
  s_sub_u32 s83, s83, 28
  v_cvt_pk_bf16_f32 v136, v108, v109
  v_mfma_f32_16x16x32_bf16 v[52:55], a[104:107], v[164:167], v[52:55]
  v_exp_f32 v110, v110
  s_mov_b32 s54, 1309568
  v_mfma_f32_16x16x32_bf16 v[52:55], a[108:111], v[168:171], v[52:55]
  s_max_u32 s94, s94, s83
  ds_read_b64_tr_b16 a[92:93], v235 offset:24576
  v_mfma_f32_16x16x32_bf16 v[84:87], a[104:107], v[172:175], v[84:87]
  v_exp_f32 v111, v111
  s_mov_b32 s55, 0x20000
  v_mfma_f32_16x16x32_bf16 v[84:87], a[108:111], v[176:179], v[84:87]
  s_mov_b32 s66, s84
  v_cvt_pk_bf16_f32 v137, v110, v111
  v_mfma_f32_16x16x32_bf16 v[116:119], a[32:35], a[160:163], v[104:107]
  v_exp_f32 v112, v112
  s_and_b32 s93, s27, 6
  v_mfma_f32_16x16x32_bf16 v[116:119], a[36:39], a[164:167], v[116:119]
  buffer_load_dwordx4 v245, s[44:47], s68 offen offset:1024 nt lds
  v_mfma_f32_16x16x32_bf16 v[116:119], a[40:43], a[168:171], v[116:119]
  v_exp_f32 v113, v113
  s_lshl_b32 s93, s93, 2
  v_mfma_f32_16x16x32_bf16 v[116:119], a[44:47], a[172:175], v[116:119]
  ds_read_b64_tr_b16 a[94:95], v235 offset:28672
  v_cvt_pk_bf16_f32 v144, v112, v113
  v_mfma_f32_16x16x32_bf16 v[24:27], a[112:115], v[156:159], v[24:27]
  v_exp_f32 v114, v114
  s_lshr_b32 s88, s27, 1
  s_mul_i32 s88, s88, 6
  s_sub_u32 s88, s31, s88
  s_lshr_b32 s88, s88, 0
  s_mul_i32 s88, s88, 59
  s_lshr_b32 s88, s88, 4
  v_mfma_f32_16x16x32_bf16 v[24:27], a[116:119], v[160:163], v[24:27]
  ds_read_b64_tr_b16 a[96:97], v236 offset:16384
  ds_read_b64_tr_b16 a[98:99], v236 offset:20480
  v_mfma_f32_16x16x32_bf16 v[56:59], a[112:115], v[164:167], v[56:59]
  v_exp_f32 v115, v115
  s_cmp_lg_u32 s63, 0
  s_cselect_b32 s54, 0, s54
  v_mfma_f32_16x16x32_bf16 v[56:59], a[116:119], v[168:171], v[56:59]
  ds_read_b64_tr_b16 a[100:101], v236 offset:24576
  v_cvt_pk_bf16_f32 v145, v114, v115
  v_mfma_f32_16x16x32_bf16 v[108:111], a[48:51], a[128:131], v[96:99]
  v_exp_f32 v116, v116
  v_mfma_f32_16x16x32_bf16 v[108:111], a[52:55], a[132:135], v[108:111]
  ds_read_b64_tr_b16 a[102:103], v236 offset:28672
  ds_read_b64_tr_b16 a[104:105], v237 offset:16384
  v_mfma_f32_16x16x32_bf16 v[108:111], a[56:59], a[136:139], v[108:111]
  v_exp_f32 v117, v117
  v_mfma_f32_16x16x32_bf16 v[108:111], a[60:63], a[140:143], v[108:111]
  ds_read_b64_tr_b16 a[106:107], v237 offset:20480
  v_cvt_pk_bf16_f32 v152, v116, v117
  v_mfma_f32_16x16x32_bf16 v[88:91], a[112:115], v[172:175], v[88:91]
  v_exp_f32 v118, v118
  v_mfma_f32_16x16x32_bf16 v[88:91], a[116:119], v[176:179], v[88:91]
  buffer_load_dwordx4 v244, s[44:47], s68 offen offset:2048 nt lds
  v_mfma_f32_16x16x32_bf16 v[28:31], a[120:123], v[156:159], v[28:31]
  v_exp_f32 v119, v119
  v_mfma_f32_16x16x32_bf16 v[28:31], a[124:127], v[160:163], v[28:31]
  ds_read_b64_tr_b16 a[108:109], v237 offset:24576
  v_cvt_pk_bf16_f32 v153, v118, v119
  v_mfma_f32_16x16x32_bf16 v[60:63], a[120:123], v[164:167], v[60:63]
  ds_read_b64_tr_b16 a[110:111], v237 offset:28672
  ds_read_b128 a[32:35], v228 offset:24576
  v_mfma_f32_16x16x32_bf16 v[60:63], a[124:127], v[168:171], v[60:63]
  ds_read_b128 a[36:39], v229 offset:24576
  ds_read_b128 a[40:43], v230 offset:24576
  v_mfma_f32_16x16x32_bf16 v[112:115], a[48:51], a[144:147], v[100:103]
  v_exp_f32 v108, v108
  v_mfma_f32_16x16x32_bf16 v[112:115], a[52:55], a[148:151], v[112:115]
  ds_read_b128 a[44:47], v231 offset:24576
  ds_read_b64_tr_b16 a[112:113], v238 offset:16384
  v_mfma_f32_16x16x32_bf16 v[112:115], a[56:59], a[152:155], v[112:115]
  v_exp_f32 v109, v109
  v_mfma_f32_16x16x32_bf16 v[112:115], a[60:63], a[156:159], v[112:115]
  ds_read_b64_tr_b16 a[114:115], v238 offset:20480
  v_cvt_pk_bf16_f32 v138, v108, v109
  v_mfma_f32_16x16x32_bf16 v[92:95], a[120:123], v[172:175], v[92:95]
  v_exp_f32 v110, v110
  v_mfma_f32_16x16x32_bf16 v[92:95], a[124:127], v[176:179], v[92:95]
  buffer_load_dwordx4 v245, s[44:47], s68 offen offset:3072 nt lds
  v_mfma_f32_16x16x32_bf16 v[120:123], a[240:243], v[156:159], v[120:123]
  v_exp_f32 v111, v111
  v_mfma_f32_16x16x32_bf16 v[120:123], a[240:243], v[160:163], v[120:123]
  ds_read_b64_tr_b16 a[116:117], v238 offset:24576
  v_cvt_pk_bf16_f32 v139, v110, v111
  v_mfma_f32_16x16x32_bf16 v[116:119], a[48:51], a[160:163], v[104:107]
  v_exp_f32 v112, v112
  v_mfma_f32_16x16x32_bf16 v[116:119], a[52:55], a[164:167], v[116:119]
  ds_read_b64_tr_b16 a[118:119], v238 offset:28672
  ds_read_b64_tr_b16 a[120:121], v239 offset:16384
  v_mfma_f32_16x16x32_bf16 v[116:119], a[56:59], a[168:171], v[116:119]
  v_exp_f32 v113, v113
  v_mfma_f32_16x16x32_bf16 v[116:119], a[60:63], a[172:175], v[116:119]
  ds_read_b64_tr_b16 a[122:123], v239 offset:20480
  v_cvt_pk_bf16_f32 v146, v112, v113
  v_mfma_f32_16x16x32_bf16 v[124:127], a[240:243], v[164:167], v[124:127]
  v_exp_f32 v114, v114
  v_mfma_f32_16x16x32_bf16 v[124:127], a[240:243], v[168:171], v[124:127]
  ds_read_b64_tr_b16 a[124:125], v239 offset:24576
  ds_read_b64_tr_b16 a[126:127], v239 offset:28672
  v_mfma_f32_16x16x32_bf16 v[128:131], a[240:243], v[172:175], v[128:131]
  v_exp_f32 v115, v115
  v_mfma_f32_16x16x32_bf16 v[128:131], a[240:243], v[176:179], v[128:131]
  v_cvt_pk_bf16_f32 v147, v114, v115
  s_waitcnt lgkmcnt(8)
  v_mfma_f32_16x16x32_bf16 v[108:111], a[0:3], a[128:131], v[96:99]
  v_exp_f32 v116, v116
  v_mfma_f32_16x16x32_bf16 v[108:111], a[4:7], a[132:135], v[108:111]
  s_memrealtime s[82:83]
  ds_read_b128 a[48:51], v228 offset:28672
  v_mfma_f32_16x16x32_bf16 v[108:111], a[8:11], a[136:139], v[108:111]
  v_exp_f32 v117, v117
  v_mfma_f32_16x16x32_bf16 v[108:111], a[12:15], a[140:143], v[108:111]
  ds_read_b128 a[52:55], v229 offset:28672
  v_cvt_pk_bf16_f32 v154, v116, v117
  v_mfma_f32_16x16x32_bf16 v[0:3], a[64:67], v[132:135], v[0:3]
  v_exp_f32 v118, v118
  v_mfma_f32_16x16x32_bf16 v[0:3], a[68:71], v[136:139], v[0:3]
  ds_read_b128 a[56:59], v230 offset:28672
  ds_read_b128 a[60:63], v231 offset:28672
  v_mfma_f32_16x16x32_bf16 v[32:35], a[64:67], v[140:143], v[32:35]
  v_exp_f32 v119, v119
  v_mfma_f32_16x16x32_bf16 v[32:35], a[68:71], v[144:147], v[32:35]
  v_cvt_pk_bf16_f32 v155, v118, v119
  v_mfma_f32_16x16x32_bf16 v[112:115], a[0:3], a[144:147], v[100:103]
  v_exp_f32 v108, v108
  v_mfma_f32_16x16x32_bf16 v[112:115], a[4:7], a[148:151], v[112:115]
  v_mfma_f32_16x16x32_bf16 v[112:115], a[8:11], a[152:155], v[112:115]
  v_exp_f32 v109, v109
  v_mfma_f32_16x16x32_bf16 v[112:115], a[12:15], a[156:159], v[112:115]
  v_cvt_pk_bf16_f32 v156, v108, v109
  v_mfma_f32_16x16x32_bf16 v[64:67], a[64:67], v[148:151], v[64:67]
  v_exp_f32 v110, v110
  v_mfma_f32_16x16x32_bf16 v[64:67], a[68:71], v[152:155], v[64:67]
  s_waitcnt vmcnt(8) lgkmcnt(0)
  s_barrier
  v_mfma_f32_16x16x32_bf16 v[4:7], a[72:75], v[132:135], v[4:7]
  v_exp_f32 v111, v111
  v_mfma_f32_16x16x32_bf16 v[4:7], a[76:79], v[136:139], v[4:7]
  s_sub_u32 s84, s82, s85
  v_cvt_pk_bf16_f32 v157, v110, v111
  v_mfma_f32_16x16x32_bf16 v[116:119], a[0:3], a[160:163], v[104:107]
  v_exp_f32 v112, v112
  v_mfma_f32_16x16x32_bf16 v[116:119], a[4:7], a[164:167], v[116:119]
  s_cmp_lt_u32 s84, s94
  s_cbranch_scc1 PC4_%=
PC4_%=e:
  v_mfma_f32_16x16x32_bf16 v[116:119], a[8:11], a[168:171], v[116:119]
  v_exp_f32 v113, v113
  v_mfma_f32_16x16x32_bf16 v[116:119], a[12:15], a[172:175], v[116:119]
  s_add_u32 s68, s60, 131072
  v_cvt_pk_bf16_f32 v164, v112, v113
  v_mfma_f32_16x16x32_bf16 v[36:39], a[72:75], v[140:143], v[36:39]
  v_exp_f32 v114, v114
  v_mfma_f32_16x16x32_bf16 v[36:39], a[76:79], v[144:147], v[36:39]
  s_add_u32 m0, s61, 16384
  ds_read_b64_tr_b16 a[64:65], v232 offset:32768
  v_subrev_u32 v204, s85, v204
  v_mfma_f32_16x16x32_bf16 v[68:71], a[72:75], v[148:151], v[68:71]
  v_exp_f32 v115, v115
  v_mfma_f32_16x16x32_bf16 v[68:71], a[76:79], v[152:155], v[68:71]
  ds_read_b64_tr_b16 a[66:67], v232 offset:36864
  v_cvt_pk_bf16_f32 v165, v114, v115
  v_mfma_f32_16x16x32_bf16 v[8:11], a[80:83], v[132:135], v[8:11]
  buffer_load_dwordx4 v240, s[40:43], s68 offen offset:0 nt lds
  v_add_u32 v204, 300, v204
  v_mfma_f32_16x16x32_bf16 v[8:11], a[84:87], v[136:139], v[8:11]
  buffer_load_dwordx4 v241, s[40:43], s68 offen offset:1024 nt lds
  v_mfma_f32_16x16x32_bf16 v[108:111], a[16:19], a[128:131], v[96:99]
  v_exp_f32 v116, v116
  v_mfma_f32_16x16x32_bf16 v[108:111], a[20:23], a[132:135], v[108:111]
  ds_read_b64_tr_b16 a[68:69], v232 offset:40960
  ds_read_b64_tr_b16 a[70:71], v232 offset:45056
  v_mov_b32 v215, 0x1000000
  v_mfma_f32_16x16x32_bf16 v[108:111], a[24:27], a[136:139], v[108:111]
  v_exp_f32 v117, v117
  v_mfma_f32_16x16x32_bf16 v[108:111], a[28:31], a[140:143], v[108:111]
  ds_read_b128 a[0:3], v228 offset:0
  v_cvt_pk_bf16_f32 v172, v116, v117
  v_mfma_f32_16x16x32_bf16 v[40:43], a[80:83], v[140:143], v[40:43]
  v_exp_f32 v118, v118
  v_cmp_gt_u32 vcc, 600, v204
  s_nop 1
  v_cndmask_b32 v205, v215, v205, vcc
  s_nop 0
  v_mfma_f32_16x16x32_bf16 v[40:43], a[84:87], v[144:147], v[40:43]
  ds_read_b128 a[4:7], v229 offset:0
  ds_read_b128 a[8:11], v230 offset:0
  v_mfma_f32_16x16x32_bf16 v[72:75], a[80:83], v[148:151], v[72:75]
  v_exp_f32 v119, v119
  v_mfma_f32_16x16x32_bf16 v[72:75], a[84:87], v[152:155], v[72:75]
  ds_read_b128 a[12:15], v231 offset:0
  v_cvt_pk_bf16_f32 v173, v118, v119
  s_mov_b32 s70, 0
  v_mfma_f32_16x16x32_bf16 v[112:115], a[16:19], a[144:147], v[100:103]
  v_exp_f32 v108, v108
  v_mfma_f32_16x16x32_bf16 v[112:115], a[20:23], a[148:151], v[112:115]
  buffer_load_dwordx4 v242, s[40:43], s68 offen offset:2048 nt lds
  v_mfma_f32_16x16x32_bf16 v[112:115], a[24:27], a[152:155], v[112:115]
  v_exp_f32 v109, v109
  v_readlane_b32 s69, v205, 0
  s_add_i32 s70, s70, s69
  v_mfma_f32_16x16x32_bf16 v[112:115], a[28:31], a[156:159], v[112:115]
  ds_read_b64_tr_b16 a[72:73], v233 offset:32768
  v_cvt_pk_bf16_f32 v158, v108, v109
  v_mfma_f32_16x16x32_bf16 v[12:15], a[88:91], v[132:135], v[12:15]
  v_exp_f32 v110, v110
  v_mfma_f32_16x16x32_bf16 v[12:15], a[92:95], v[136:139], v[12:15]
  ds_read_b64_tr_b16 a[74:75], v233 offset:36864
  ds_read_b64_tr_b16 a[76:77], v233 offset:40960
  ds_read_b64_tr_b16 a[78:79], v233 offset:45056
  v_readlane_b32 s69, v205, 1
  s_add_i32 s70, s70, s69
  v_mfma_f32_16x16x32_bf16 v[44:47], a[88:91], v[140:143], v[44:47]
  ds_read_b64_tr_b16 a[80:81], v234 offset:32768
  v_exp_f32 v111, v111
  v_mfma_f32_16x16x32_bf16 v[44:47], a[92:95], v[144:147], v[44:47]
  buffer_load_dwordx4 v243, s[40:43], s68 offen offset:3072 nt lds
  v_cvt_pk_bf16_f32 v159, v110, v111
  v_mfma_f32_16x16x32_bf16 v[116:119], a[16:19], a[160:163], v[104:107]
  ds_read_b64_tr_b16 a[82:83], v234 offset:36864
  v_exp_f32 v112, v112
  v_mfma_f32_16x16x32_bf16 v[116:119], a[20:23], a[164:167], v[116:119]
  s_add_i32 s97, s97, -64
  s_add_u32 s72, s93, 0
  s_and_b32 s72, s72, 31
  s_mul_i32 s72, s72, 40960
  ds_read_b64_tr_b16 a[84:85], v234 offset:40960
  v_mfma_f32_16x16x32_bf16 v[116:119], a[24:27], a[168:171], v[116:119]
  ds_read_b64_tr_b16 a[86:87], v234 offset:45056
  v_exp_f32 v113, v113
  v_mfma_f32_16x16x32_bf16 v[116:119], a[28:31], a[172:175], v[116:119]
  s_add_u32 s97, s97, s95
  v_cvt_pk_bf16_f32 v166, v112, v113
  v_readlane_b32 s69, v205, 2
  s_add_i32 s70, s70, s69
  v_mfma_f32_16x16x32_bf16 v[76:79], a[88:91], v[148:151], v[76:79]
  v_exp_f32 v114, v114
  v_mfma_f32_16x16x32_bf16 v[76:79], a[92:95], v[152:155], v[76:79]
  s_lshr_b32 s83, s97, 4
  s_add_u32 m0, s62, 49152
  ds_read_b128 a[16:19], v228 offset:4096
  v_mfma_f32_16x16x32_bf16 v[16:19], a[96:99], v[132:135], v[16:19]
  ds_read_b128 a[20:23], v229 offset:4096
  v_exp_f32 v115, v115
  v_readlane_b32 s69, v205, 3
  s_add_i32 s70, s70, s69
  v_mfma_f32_16x16x32_bf16 v[16:19], a[100:103], v[136:139], v[16:19]
  s_add_u32 s94, s84, s96
  ds_read_b128 a[24:27], v230 offset:4096
  v_cvt_pk_bf16_f32 v167, v114, v115
  v_mfma_f32_16x16x32_bf16 v[108:111], a[32:35], a[128:131], v[96:99]
  ds_read_b128 a[28:31], v231 offset:4096
  v_exp_f32 v116, v116
  v_mfma_f32_16x16x32_bf16 v[108:111], a[36:39], a[132:135], v[108:111]
  s_add_i32 s94, s94, -4
  buffer_load_dwordx4 v180, s[52:55], s72 offen offset:0 lds
  v_mfma_f32_16x16x32_bf16 v[108:111], a[40:43], a[136:139], v[108:111]
  ds_read_b64_tr_b16 a[88:89], v235 offset:32768
  v_exp_f32 v117, v117
  v_mfma_f32_16x16x32_bf16 v[108:111], a[44:47], a[140:143], v[108:111]
  s_max_u32 s94, s94, s83
  ds_read_b64_tr_b16 a[90:91], v235 offset:36864
  v_cvt_pk_bf16_f32 v174, v116, v117
  v_mfma_f32_16x16x32_bf16 v[48:51], a[96:99], v[140:143], v[48:51]
  ds_read_b64_tr_b16 a[92:93], v235 offset:40960
  v_exp_f32 v118, v118
  v_mfma_f32_16x16x32_bf16 v[48:51], a[100:103], v[144:147], v[48:51]
  s_sub_u32 s83, s84, s66
  s_add_u32 s68, s60, 114688
  v_readlane_b32 s69, v205, 4
  s_add_i32 s70, s70, s69
  v_mfma_f32_16x16x32_bf16 v[80:83], a[96:99], v[148:151], v[80:83]
  v_exp_f32 v119, v119
  v_mfma_f32_16x16x32_bf16 v[80:83], a[100:103], v[152:155], v[80:83]
  s_min_u32 s83, s83, 200
  v_cvt_pk_bf16_f32 v175, v118, v119
  v_mfma_f32_16x16x32_bf16 v[20:23], a[104:107], v[132:135], v[20:23]
  s_add_u32 s65, s65, s83
  s_add_u32 m0, s62, 16384
  v_readlane_b32 s69, v205, 5
  s_add_i32 s70, s70, s69
  v_mfma_f32_16x16x32_bf16 v[20:23], a[108:111], v[136:139], v[20:23]
  s_lshr_b32 s65, s65, 1
  buffer_load_dwordx4 v244, s[44:47], s68 offen offset:0 nt lds
  v_mfma_f32_16x16x32_bf16 v[112:115], a[32:35], a[144:147], v[100:103]
  v_exp_f32 v108, v108
  v_mfma_f32_16x16x32_bf16 v[112:115], a[36:39], a[148:151], v[112:115]
  s_add_u32 s83, s65, s84
  ds_read_b64_tr_b16 a[94:95], v235 offset:45056
  v_readlane_b32 s69, v205, 6
  s_add_i32 s70, s70, s69
  v_mfma_f32_16x16x32_bf16 v[112:115], a[40:43], a[152:155], v[112:115]
  v_exp_f32 v109, v109
  v_mfma_f32_16x16x32_bf16 v[112:115], a[44:47], a[156:159], v[112:115]
  s_sub_u32 s83, s83, 28
  v_cvt_pk_bf16_f32 v160, v108, v109
  v_mfma_f32_16x16x32_bf16 v[52:55], a[104:107], v[140:143], v[52:55]
  v_exp_f32 v110, v110
  v_readlane_b32 s69, v205, 7
  s_add_i32 s70, s70, s69
  v_mfma_f32_16x16x32_bf16 v[52:55], a[108:111], v[144:147], v[52:55]
  s_max_u32 s94, s94, s83
  ds_read_b64_tr_b16 a[96:97], v236 offset:32768
  v_mfma_f32_16x16x32_bf16 v[84:87], a[104:107], v[148:151], v[84:87]
  v_exp_f32 v111, v111
  v_mfma_f32_16x16x32_bf16 v[84:87], a[108:111], v[152:155], v[84:87]
  s_mov_b32 s66, s84
  v_cvt_pk_bf16_f32 v161, v110, v111
  s_cmp_lt_i32 s70, 0
  s_cselect_b32 s36, 144, 0
  v_mfma_f32_16x16x32_bf16 v[116:119], a[32:35], a[160:163], v[104:107]
  v_exp_f32 v112, v112
  v_mfma_f32_16x16x32_bf16 v[116:119], a[36:39], a[164:167], v[116:119]
  buffer_load_dwordx4 v245, s[44:47], s68 offen offset:1024 nt lds
  v_mfma_f32_16x16x32_bf16 v[116:119], a[40:43], a[168:171], v[116:119]
  v_exp_f32 v113, v113
  v_mfma_f32_16x16x32_bf16 v[116:119], a[44:47], a[172:175], v[116:119]
  ds_read_b64_tr_b16 a[98:99], v236 offset:36864
  v_cvt_pk_bf16_f32 v168, v112, v113
  v_mfma_f32_16x16x32_bf16 v[24:27], a[112:115], v[132:135], v[24:27]
  v_exp_f32 v114, v114
  v_mfma_f32_16x16x32_bf16 v[24:27], a[116:119], v[136:139], v[24:27]
  ds_read_b64_tr_b16 a[100:101], v236 offset:40960
  ds_read_b64_tr_b16 a[102:103], v236 offset:45056
  v_mfma_f32_16x16x32_bf16 v[56:59], a[112:115], v[140:143], v[56:59]
  v_exp_f32 v115, v115
  v_mfma_f32_16x16x32_bf16 v[56:59], a[116:119], v[144:147], v[56:59]
  ds_read_b64_tr_b16 a[104:105], v237 offset:32768
  v_cvt_pk_bf16_f32 v169, v114, v115
  v_mfma_f32_16x16x32_bf16 v[108:111], a[48:51], a[128:131], v[96:99]
  v_exp_f32 v116, v116
  v_mfma_f32_16x16x32_bf16 v[108:111], a[52:55], a[132:135], v[108:111]
  buffer_load_dwordx4 v244, s[44:47], s68 offen offset:2048 nt lds
  v_mfma_f32_16x16x32_bf16 v[108:111], a[56:59], a[136:139], v[108:111]
  v_exp_f32 v117, v117
  v_mfma_f32_16x16x32_bf16 v[108:111], a[60:63], a[140:143], v[108:111]
  ds_read_b64_tr_b16 a[106:107], v237 offset:36864
  v_cvt_pk_bf16_f32 v176, v116, v117
  v_mfma_f32_16x16x32_bf16 v[88:91], a[112:115], v[148:151], v[88:91]
  v_exp_f32 v118, v118
  v_mfma_f32_16x16x32_bf16 v[88:91], a[116:119], v[152:155], v[88:91]
  ds_read_b64_tr_b16 a[108:109], v237 offset:40960
  ds_read_b64_tr_b16 a[110:111], v237 offset:45056
  v_mfma_f32_16x16x32_bf16 v[28:31], a[120:123], v[132:135], v[28:31]
  v_exp_f32 v119, v119
  v_mfma_f32_16x16x32_bf16 v[28:31], a[124:127], v[136:139], v[28:31]
  ds_read_b128 a[32:35], v228 offset:8192
  v_cvt_pk_bf16_f32 v177, v118, v119
  v_mfma_f32_16x16x32_bf16 v[60:63], a[120:123], v[140:143], v[60:63]
  buffer_load_dwordx4 v245, s[44:47], s68 offen offset:3072 nt lds
  v_mfma_f32_16x16x32_bf16 v[60:63], a[124:127], v[144:147], v[60:63]
  ds_read_b128 a[36:39], v229 offset:8192
  ds_read_b128 a[40:43], v230 offset:8192
  v_mfma_f32_16x16x32_bf16 v[112:115], a[48:51], a[144:147], v[100:103]
  v_exp_f32 v108, v108
  v_mfma_f32_16x16x32_bf16 v[112:115], a[52:55], a[148:151], v[112:115]
  ds_read_b128 a[44:47], v231 offset:8192
  ds_read_b64_tr_b16 a[112:113], v238 offset:32768
  v_mfma_f32_16x16x32_bf16 v[112:115], a[56:59], a[152:155], v[112:115]
  v_exp_f32 v109, v109
  v_mfma_f32_16x16x32_bf16 v[112:115], a[60:63], a[156:159], v[112:115]
  s_add_u32 m0, s62, 49152
  v_cvt_pk_bf16_f32 v162, v108, v109
  v_mfma_f32_16x16x32_bf16 v[92:95], a[120:123], v[148:151], v[92:95]
  v_exp_f32 v110, v110
  v_mfma_f32_16x16x32_bf16 v[92:95], a[124:127], v[152:155], v[92:95]
  buffer_load_dwordx4 v181, s[52:55], s72 offen offset:1024 lds
  v_mfma_f32_16x16x32_bf16 v[120:123], a[240:243], v[132:135], v[120:123]
  v_exp_f32 v111, v111
  v_mfma_f32_16x16x32_bf16 v[120:123], a[240:243], v[136:139], v[120:123]
  ds_read_b64_tr_b16 a[114:115], v238 offset:36864
  v_cvt_pk_bf16_f32 v163, v110, v111
  v_mfma_f32_16x16x32_bf16 v[116:119], a[48:51], a[160:163], v[104:107]
  v_exp_f32 v112, v112
  v_mfma_f32_16x16x32_bf16 v[116:119], a[52:55], a[164:167], v[116:119]
  ds_read_b64_tr_b16 a[116:117], v238 offset:40960
  ds_read_b64_tr_b16 a[118:119], v238 offset:45056
  v_mfma_f32_16x16x32_bf16 v[116:119], a[56:59], a[168:171], v[116:119]
  v_exp_f32 v113, v113
  v_mfma_f32_16x16x32_bf16 v[116:119], a[60:63], a[172:175], v[116:119]
  ds_read_b64_tr_b16 a[120:121], v239 offset:32768
  v_cvt_pk_bf16_f32 v170, v112, v113
  v_mfma_f32_16x16x32_bf16 v[124:127], a[240:243], v[140:143], v[124:127]
  v_exp_f32 v114, v114
  v_mfma_f32_16x16x32_bf16 v[124:127], a[240:243], v[144:147], v[124:127]
  ds_read_b64_tr_b16 a[122:123], v239 offset:36864
  ds_read_b64_tr_b16 a[124:125], v239 offset:40960
  ds_read_b64_tr_b16 a[126:127], v239 offset:45056
  v_mfma_f32_16x16x32_bf16 v[128:131], a[240:243], v[148:151], v[128:131]
  v_exp_f32 v115, v115
  v_mfma_f32_16x16x32_bf16 v[128:131], a[240:243], v[152:155], v[128:131]
  v_cvt_pk_bf16_f32 v171, v114, v115
  s_waitcnt lgkmcnt(8)
  v_mfma_f32_16x16x32_bf16 v[108:111], a[0:3], a[128:131], v[96:99]
  v_exp_f32 v116, v116
  v_mfma_f32_16x16x32_bf16 v[108:111], a[4:7], a[132:135], v[108:111]
  s_memrealtime s[82:83]
  ds_read_b128 a[48:51], v228 offset:12288
  v_mfma_f32_16x16x32_bf16 v[108:111], a[8:11], a[136:139], v[108:111]
  v_exp_f32 v117, v117
  v_mfma_f32_16x16x32_bf16 v[108:111], a[12:15], a[140:143], v[108:111]
  ds_read_b128 a[52:55], v229 offset:12288
  v_cvt_pk_bf16_f32 v178, v116, v117
  v_mfma_f32_16x16x32_bf16 v[0:3], a[64:67], v[156:159], v[0:3]
  v_exp_f32 v118, v118
  v_mfma_f32_16x16x32_bf16 v[0:3], a[68:71], v[160:163], v[0:3]
  ds_read_b128 a[56:59], v230 offset:12288
  ds_read_b128 a[60:63], v231 offset:12288
  v_mfma_f32_16x16x32_bf16 v[32:35], a[64:67], v[164:167], v[32:35]
  v_exp_f32 v119, v119
  v_mfma_f32_16x16x32_bf16 v[32:35], a[68:71], v[168:171], v[32:35]
  v_cvt_pk_bf16_f32 v179, v118, v119
  v_mfma_f32_16x16x32_bf16 v[112:115], a[0:3], a[144:147], v[100:103]
  v_exp_f32 v108, v108
  v_mfma_f32_16x16x32_bf16 v[112:115], a[4:7], a[148:151], v[112:115]
  v_mfma_f32_16x16x32_bf16 v[112:115], a[8:11], a[152:155], v[112:115]
  v_exp_f32 v109, v109
  v_mfma_f32_16x16x32_bf16 v[112:115], a[12:15], a[156:159], v[112:115]
  v_cvt_pk_bf16_f32 v132, v108, v109
  v_mfma_f32_16x16x32_bf16 v[64:67], a[64:67], v[172:175], v[64:67]
  v_exp_f32 v110, v110
  v_mfma_f32_16x16x32_bf16 v[64:67], a[68:71], v[176:179], v[64:67]
  s_waitcnt vmcnt(10) lgkmcnt(0)
  s_barrier
  v_mfma_f32_16x16x32_bf16 v[4:7], a[72:75], v[156:159], v[4:7]
  v_exp_f32 v111, v111
  v_mfma_f32_16x16x32_bf16 v[4:7], a[76:79], v[160:163], v[4:7]
  s_sub_u32 s84, s82, s85
  v_cvt_pk_bf16_f32 v133, v110, v111
  v_mfma_f32_16x16x32_bf16 v[116:119], a[0:3], a[160:163], v[104:107]
  v_exp_f32 v112, v112
  v_mfma_f32_16x16x32_bf16 v[116:119], a[4:7], a[164:167], v[116:119]
  s_cmp_lt_u32 s84, s94
  s_cbranch_scc1 PC5_%=
PC5_%=e:
  v_mfma_f32_16x16x32_bf16 v[116:119], a[8:11], a[168:171], v[116:119]
  v_exp_f32 v113, v113
  v_mfma_f32_16x16x32_bf16 v[116:119], a[12:15], a[172:175], v[116:119]
  ds_read_b64_tr_b16 a[64:65], v232 offset:0
  v_cvt_pk_bf16_f32 v140, v112, v113
  v_mfma_f32_16x16x32_bf16 v[36:39], a[72:75], v[164:167], v[36:39]
  v_exp_f32 v114, v114
  v_mfma_f32_16x16x32_bf16 v[36:39], a[76:79], v[168:171], v[36:39]
  buffer_load_dword v218, v217, s[56:59], 0 offen offset:0 sc1
  v_mfma_f32_16x16x32_bf16 v[68:71], a[72:75], v[172:175], v[68:71]
  v_exp_f32 v115, v115
  v_mfma_f32_16x16x32_bf16 v[68:71], a[76:79], v[176:179], v[68:71]
  s_add_u32 s68, s60, 147456
  v_cvt_pk_bf16_f32 v141, v114, v115
  v_mfma_f32_16x16x32_bf16 v[8:11], a[80:83], v[156:159], v[8:11]
  s_add_u32 m0, s61, 0
  ds_read_b64_tr_b16 a[66:67], v232 offset:4096
  v_mfma_f32_16x16x32_bf16 v[8:11], a[84:87], v[160:163], v[8:11]
  buffer_load_dwordx4 v240, s[40:43], s68 offen offset:0 nt lds
  v_mfma_f32_16x16x32_bf16 v[108:111], a[16:19], a[128:131], v[96:99]
  v_exp_f32 v116, v116
  v_mfma_f32_16x16x32_bf16 v[108:111], a[20:23], a[132:135], v[108:111]
  ds_read_b64_tr_b16 a[68:69], v232 offset:8192
  ds_read_b64_tr_b16 a[70:71], v232 offset:12288
  v_mfma_f32_16x16x32_bf16 v[108:111], a[24:27], a[136:139], v[108:111]
  v_exp_f32 v117, v117
  v_mfma_f32_16x16x32_bf16 v[108:111], a[28:31], a[140:143], v[108:111]
  ds_read_b128 a[0:3], v228 offset:49152
  v_cvt_pk_bf16_f32 v148, v116, v117
  v_mfma_f32_16x16x32_bf16 v[40:43], a[80:83], v[164:167], v[40:43]
  v_exp_f32 v118, v118
  v_mfma_f32_16x16x32_bf16 v[40:43], a[84:87], v[168:171], v[40:43]
  ds_read_b128 a[4:7], v229 offset:49152
  ds_read_b128 a[8:11], v230 offset:49152
  v_mfma_f32_16x16x32_bf16 v[72:75], a[80:83], v[172:175], v[72:75]
  v_exp_f32 v119, v119
  v_mfma_f32_16x16x32_bf16 v[72:75], a[84:87], v[176:179], v[72:75]
  ds_read_b128 a[12:15], v231 offset:49152
  v_cvt_pk_bf16_f32 v149, v118, v119
  v_mfma_f32_16x16x32_bf16 v[112:115], a[16:19], a[144:147], v[100:103]
  v_exp_f32 v108, v108
  v_mfma_f32_16x16x32_bf16 v[112:115], a[20:23], a[148:151], v[112:115]
  buffer_load_dwordx4 v241, s[40:43], s68 offen offset:1024 nt lds
  v_mfma_f32_16x16x32_bf16 v[112:115], a[24:27], a[152:155], v[112:115]
  v_exp_f32 v109, v109
  v_mfma_f32_16x16x32_bf16 v[112:115], a[28:31], a[156:159], v[112:115]
  ds_read_b64_tr_b16 a[72:73], v233 offset:0
  v_cvt_pk_bf16_f32 v134, v108, v109
  v_mfma_f32_16x16x32_bf16 v[12:15], a[88:91], v[156:159], v[12:15]
  v_exp_f32 v110, v110
  v_mfma_f32_16x16x32_bf16 v[12:15], a[92:95], v[160:163], v[12:15]
  buffer_load_dwordx4 v242, s[40:43], s68 offen offset:2048 nt lds
  v_mfma_f32_16x16x32_bf16 v[44:47], a[88:91], v[164:167], v[44:47]
  v_exp_f32 v111, v111
  v_mfma_f32_16x16x32_bf16 v[44:47], a[92:95], v[168:171], v[44:47]
  ds_read_b64_tr_b16 a[74:75], v233 offset:4096
  v_cvt_pk_bf16_f32 v135, v110, v111
  v_mfma_f32_16x16x32_bf16 v[116:119], a[16:19], a[160:163], v[104:107]
  v_exp_f32 v112, v112
  v_mfma_f32_16x16x32_bf16 v[116:119], a[20:23], a[164:167], v[116:119]
  s_add_i32 s97, s97, -48
  ds_read_b64_tr_b16 a[76:77], v233 offset:8192
  ds_read_b64_tr_b16 a[78:79], v233 offset:12288
  v_mfma_f32_16x16x32_bf16 v[116:119], a[24:27], a[168:171], v[116:119]
  ds_read_b64_tr_b16 a[80:81], v234 offset:0
  v_exp_f32 v113, v113
  v_mfma_f32_16x16x32_bf16 v[116:119], a[28:31], a[172:175], v[116:119]
  s_add_u32 s97, s97, s95
  ds_read_b64_tr_b16 a[82:83], v234 offset:4096
  v_cvt_pk_bf16_f32 v142, v112, v113
  v_mfma_f32_16x16x32_bf16 v[76:79], a[88:91], v[172:175], v[76:79]
  ds_read_b64_tr_b16 a[84:85], v234 offset:8192
  v_exp_f32 v114, v114
  v_mfma_f32_16x16x32_bf16 v[76:79], a[92:95], v[176:179], v[76:79]
  s_lshr_b32 s83, s97, 4
  buffer_load_dwordx4 v243, s[40:43], s68 offen offset:3072 nt lds
  v_mfma_f32_16x16x32_bf16 v[16:19], a[96:99], v[156:159], v[16:19]
  ds_read_b64_tr_b16 a[86:87], v234 offset:12288
  v_exp_f32 v115, v115
  v_mfma_f32_16x16x32_bf16 v[16:19], a[100:103], v[160:163], v[16:19]
  s_add_u32 s94, s84, s96
  ds_read_b128 a[16:19], v228 offset:53248
  v_cvt_pk_bf16_f32 v143, v114, v115
  v_mfma_f32_16x16x32_bf16 v[108:111], a[32:35], a[128:131], v[96:99]
  ds_read_b128 a[20:23], v229 offset:53248
  v_exp_f32 v116, v116
  v_mfma_f32_16x16x32_bf16 v[108:111], a[36:39], a[132:135], v[108:111]
  s_add_i32 s94, s94, -3
  s_add_u32 m0, s62, 49152
  ds_read_b128 a[24:27], v230 offset:53248
  v_mfma_f32_16x16x32_bf16 v[108:111], a[40:43], a[136:139], v[108:111]
  ds_read_b128 a[28:31], v231 offset:53248
  v_exp_f32 v117, v117
  v_mfma_f32_16x16x32_bf16 v[108:111], a[44:47], a[140:143], v[108:111]
  s_max_u32 s94, s94, s83
  ds_read_b64_tr_b16 a[88:89], v235 offset:0
  v_cvt_pk_bf16_f32 v150, v116, v117
  v_mfma_f32_16x16x32_bf16 v[48:51], a[96:99], v[164:167], v[48:51]
  ds_read_b64_tr_b16 a[90:91], v235 offset:4096
  v_exp_f32 v118, v118
  v_mfma_f32_16x16x32_bf16 v[48:51], a[100:103], v[168:171], v[48:51]
  s_sub_u32 s83, s84, s66
  buffer_load_dwordx4 v182, s[52:55], s72 offen offset:2048 lds
  v_mfma_f32_16x16x32_bf16 v[80:83], a[96:99], v[172:175], v[80:83]
  v_exp_f32 v119, v119
  v_mfma_f32_16x16x32_bf16 v[80:83], a[100:103], v[176:179], v[80:83]
  s_min_u32 s83, s83, 200
  v_cvt_pk_bf16_f32 v151, v118, v119
  v_mfma_f32_16x16x32_bf16 v[20:23], a[104:107], v[156:159], v[20:23]
  s_add_u32 s65, s65, s83
  s_add_u32 s68, s60, 131072
  v_mfma_f32_16x16x32_bf16 v[20:23], a[108:111], v[160:163], v[20:23]
  s_lshr_b32 s65, s65, 1
  s_add_u32 m0, s62, 32768
  v_mfma_f32_16x16x32_bf16 v[112:115], a[32:35], a[144:147], v[100:103]
  v_exp_f32 v108, v108
  v_mfma_f32_16x16x32_bf16 v[112:115], a[36:39], a[148:151], v[112:115]
  s_add_u32 s83, s65, s84
  ds_read_b64_tr_b16 a[92:93], v235 offset:8192
  ds_read_b64_tr_b16 a[94:95], v235 offset:12288
  v_mfma_f32_16x16x32_bf16 v[112:115], a[40:43], a[152:155], v[112:115]
  ds_read_b64_tr_b16 a[96:97], v236 offset:0
  v_exp_f32 v109, v109
  v_mfma_f32_16x16x32_bf16 v[112:115], a[44:47], a[156:159], v[112:115]
  s_sub_u32 s83, s83, 28
  ds_read_b64_tr_b16 a[98:99], v236 offset:4096
  v_cvt_pk_bf16_f32 v136, v108, v109
  v_mfma_f32_16x16x32_bf16 v[52:55], a[104:107], v[164:167], v[52:55]
  ds_read_b64_tr_b16 a[100:101], v236 offset:8192
  v_exp_f32 v110, v110
  v_mfma_f32_16x16x32_bf16 v[52:55], a[108:111], v[168:171], v[52:55]
  s_max_u32 s94, s94, s83
  buffer_load_dwordx4 v244, s[44:47], s68 offen offset:0 nt lds
  v_mfma_f32_16x16x32_bf16 v[84:87], a[104:107], v[172:175], v[84:87]
  v_exp_f32 v111, v111
  v_mfma_f32_16x16x32_bf16 v[84:87], a[108:111], v[176:179], v[84:87]
  s_mov_b32 s66, s84
  v_cvt_pk_bf16_f32 v137, v110, v111
  v_mfma_f32_16x16x32_bf16 v[116:119], a[32:35], a[160:163], v[104:107]
  v_exp_f32 v112, v112
  v_mfma_f32_16x16x32_bf16 v[116:119], a[36:39], a[164:167], v[116:119]
  buffer_load_dwordx4 v245, s[44:47], s68 offen offset:1024 nt lds
  v_mfma_f32_16x16x32_bf16 v[116:119], a[40:43], a[168:171], v[116:119]
  v_exp_f32 v113, v113
  v_mfma_f32_16x16x32_bf16 v[116:119], a[44:47], a[172:175], v[116:119]
  ds_read_b64_tr_b16 a[102:103], v236 offset:12288
  v_cvt_pk_bf16_f32 v144, v112, v113
  v_mfma_f32_16x16x32_bf16 v[24:27], a[112:115], v[156:159], v[24:27]
  v_exp_f32 v114, v114
  v_mfma_f32_16x16x32_bf16 v[24:27], a[116:119], v[160:163], v[24:27]
  ds_read_b64_tr_b16 a[104:105], v237 offset:0
  ds_read_b64_tr_b16 a[106:107], v237 offset:4096
  v_mfma_f32_16x16x32_bf16 v[56:59], a[112:115], v[164:167], v[56:59]
  v_exp_f32 v115, v115
  v_mfma_f32_16x16x32_bf16 v[56:59], a[116:119], v[168:171], v[56:59]
  ds_read_b64_tr_b16 a[108:109], v237 offset:8192
  v_cvt_pk_bf16_f32 v145, v114, v115
  v_mfma_f32_16x16x32_bf16 v[108:111], a[48:51], a[128:131], v[96:99]
  v_exp_f32 v116, v116
  v_mfma_f32_16x16x32_bf16 v[108:111], a[52:55], a[132:135], v[108:111]
  buffer_load_dwordx4 v244, s[44:47], s68 offen offset:2048 nt lds
  v_mfma_f32_16x16x32_bf16 v[108:111], a[56:59], a[136:139], v[108:111]
  v_exp_f32 v117, v117
  v_mfma_f32_16x16x32_bf16 v[108:111], a[60:63], a[140:143], v[108:111]
  ds_read_b64_tr_b16 a[110:111], v237 offset:12288
  v_cvt_pk_bf16_f32 v152, v116, v117
  v_mfma_f32_16x16x32_bf16 v[88:91], a[112:115], v[172:175], v[88:91]
  v_exp_f32 v118, v118
  v_mfma_f32_16x16x32_bf16 v[88:91], a[116:119], v[176:179], v[88:91]
  ds_read_b128 a[32:35], v228 offset:57344
  ds_read_b128 a[36:39], v229 offset:57344
  v_mfma_f32_16x16x32_bf16 v[28:31], a[120:123], v[156:159], v[28:31]
  v_exp_f32 v119, v119
  v_mfma_f32_16x16x32_bf16 v[28:31], a[124:127], v[160:163], v[28:31]
  ds_read_b128 a[40:43], v230 offset:57344
  v_cvt_pk_bf16_f32 v153, v118, v119
  v_mfma_f32_16x16x32_bf16 v[60:63], a[120:123], v[164:167], v[60:63]
  buffer_load_dwordx4 v245, s[44:47], s68 offen offset:3072 nt lds
  v_mfma_f32_16x16x32_bf16 v[60:63], a[124:127], v[168:171], v[60:63]
  ds_read_b128 a[44:47], v231 offset:57344
  ds_read_b64_tr_b16 a[112:113], v238 offset:0
  v_mfma_f32_16x16x32_bf16 v[112:115], a[48:51], a[144:147], v[100:103]
  v_exp_f32 v108, v108
  v_mfma_f32_16x16x32_bf16 v[112:115], a[52:55], a[148:151], v[112:115]
  ds_read_b64_tr_b16 a[114:115], v238 offset:4096
  ds_read_b64_tr_b16 a[116:117], v238 offset:8192
  v_mfma_f32_16x16x32_bf16 v[112:115], a[56:59], a[152:155], v[112:115]
  v_exp_f32 v109, v109
  v_mfma_f32_16x16x32_bf16 v[112:115], a[60:63], a[156:159], v[112:115]
  s_add_u32 m0, s62, 49152
  v_cvt_pk_bf16_f32 v138, v108, v109
  v_mfma_f32_16x16x32_bf16 v[92:95], a[120:123], v[172:175], v[92:95]
  v_exp_f32 v110, v110
  v_mfma_f32_16x16x32_bf16 v[92:95], a[124:127], v[176:179], v[92:95]
  ds_read_b64_tr_b16 a[118:119], v238 offset:12288
  v_mfma_f32_16x16x32_bf16 v[120:123], a[240:243], v[156:159], v[120:123]
  v_exp_f32 v111, v111
  v_mfma_f32_16x16x32_bf16 v[120:123], a[240:243], v[160:163], v[120:123]
  ds_read_b64_tr_b16 a[120:121], v239 offset:0
  v_cvt_pk_bf16_f32 v139, v110, v111
  v_mfma_f32_16x16x32_bf16 v[116:119], a[48:51], a[160:163], v[104:107]
  v_exp_f32 v112, v112
  v_mfma_f32_16x16x32_bf16 v[116:119], a[52:55], a[164:167], v[116:119]
  buffer_load_dwordx4 v183, s[52:55], s72 offen offset:3072 lds
  v_mfma_f32_16x16x32_bf16 v[116:119], a[56:59], a[168:171], v[116:119]
  v_exp_f32 v113, v113
  v_mfma_f32_16x16x32_bf16 v[116:119], a[60:63], a[172:175], v[116:119]
  ds_read_b64_tr_b16 a[122:123], v239 offset:4096
  v_cvt_pk_bf16_f32 v146, v112, v113
  v_mfma_f32_16x16x32_bf16 v[124:127], a[240:243], v[164:167], v[124:127]
  v_exp_f32 v114, v114
  v_mfma_f32_16x16x32_bf16 v[124:127], a[240:243], v[168:171], v[124:127]
  ds_read_b64_tr_b16 a[124:125], v239 offset:8192
  ds_read_b64_tr_b16 a[126:127], v239 offset:12288
  v_mfma_f32_16x16x32_bf16 v[128:131], a[240:243], v[172:175], v[128:131]
  v_exp_f32 v115, v115
  v_mfma_f32_16x16x32_bf16 v[128:131], a[240:243], v[176:179], v[128:131]
  v_cvt_pk_bf16_f32 v147, v114, v115
  s_waitcnt lgkmcnt(8)
  v_mfma_f32_16x16x32_bf16 v[108:111], a[0:3], a[128:131], v[96:99]
  v_exp_f32 v116, v116
  v_mfma_f32_16x16x32_bf16 v[108:111], a[4:7], a[132:135], v[108:111]
  s_memrealtime s[82:83]
  ds_read_b128 a[48:51], v228 offset:61440
  v_mfma_f32_16x16x32_bf16 v[108:111], a[8:11], a[136:139], v[108:111]
  v_exp_f32 v117, v117
  v_mfma_f32_16x16x32_bf16 v[108:111], a[12:15], a[140:143], v[108:111]
  ds_read_b128 a[52:55], v229 offset:61440
  v_cvt_pk_bf16_f32 v154, v116, v117
  v_mfma_f32_16x16x32_bf16 v[0:3], a[64:67], v[132:135], v[0:3]
  v_exp_f32 v118, v118
  v_mfma_f32_16x16x32_bf16 v[0:3], a[68:71], v[136:139], v[0:3]
  ds_read_b128 a[56:59], v230 offset:61440
  ds_read_b128 a[60:63], v231 offset:61440
  v_mfma_f32_16x16x32_bf16 v[32:35], a[64:67], v[140:143], v[32:35]
  v_exp_f32 v119, v119
  v_mfma_f32_16x16x32_bf16 v[32:35], a[68:71], v[144:147], v[32:35]
  v_cvt_pk_bf16_f32 v155, v118, v119
  v_mfma_f32_16x16x32_bf16 v[112:115], a[0:3], a[144:147], v[100:103]
  v_exp_f32 v108, v108
  v_mfma_f32_16x16x32_bf16 v[112:115], a[4:7], a[148:151], v[112:115]
  v_mfma_f32_16x16x32_bf16 v[112:115], a[8:11], a[152:155], v[112:115]
  v_exp_f32 v109, v109
  v_mfma_f32_16x16x32_bf16 v[112:115], a[12:15], a[156:159], v[112:115]
  v_cvt_pk_bf16_f32 v156, v108, v109
  v_mfma_f32_16x16x32_bf16 v[64:67], a[64:67], v[148:151], v[64:67]
  v_exp_f32 v110, v110
  v_mfma_f32_16x16x32_bf16 v[64:67], a[68:71], v[152:155], v[64:67]
  s_waitcnt vmcnt(12) lgkmcnt(0)
  s_barrier
  v_mfma_f32_16x16x32_bf16 v[4:7], a[72:75], v[132:135], v[4:7]
  v_exp_f32 v111, v111
  v_mfma_f32_16x16x32_bf16 v[4:7], a[76:79], v[136:139], v[4:7]
  s_sub_u32 s84, s82, s85
  v_cvt_pk_bf16_f32 v157, v110, v111
  v_mfma_f32_16x16x32_bf16 v[116:119], a[0:3], a[160:163], v[104:107]
  v_exp_f32 v112, v112
  v_mfma_f32_16x16x32_bf16 v[116:119], a[4:7], a[164:167], v[116:119]
  s_cmp_lt_u32 s84, s94
  s_cbranch_scc1 PC6_%=
PC6_%=e:
  v_mfma_f32_16x16x32_bf16 v[116:119], a[8:11], a[168:171], v[116:119]
  v_exp_f32 v113, v113
  v_mfma_f32_16x16x32_bf16 v[116:119], a[12:15], a[172:175], v[116:119]
  s_add_u32 s68, s60, 163840
  v_cvt_pk_bf16_f32 v164, v112, v113
  v_mfma_f32_16x16x32_bf16 v[36:39], a[72:75], v[140:143], v[36:39]
  v_exp_f32 v114, v114
  v_mfma_f32_16x16x32_bf16 v[36:39], a[76:79], v[144:147], v[36:39]
  s_add_u32 m0, s61, 49152
  ds_read_b64_tr_b16 a[64:65], v232 offset:16384
  v_mfma_f32_16x16x32_bf16 v[68:71], a[72:75], v[148:151], v[68:71]
  v_exp_f32 v115, v115
  v_mfma_f32_16x16x32_bf16 v[68:71], a[76:79], v[152:155], v[68:71]
  ds_read_b64_tr_b16 a[66:67], v232 offset:20480
  v_cvt_pk_bf16_f32 v165, v114, v115
  v_mfma_f32_16x16x32_bf16 v[8:11], a[80:83], v[132:135], v[8:11]
  buffer_load_dwordx4 v240, s[40:43], s68 offen offset:0 nt lds
  v_mfma_f32_16x16x32_bf16 v[8:11], a[84:87], v[136:139], v[8:11]
  buffer_load_dwordx4 v241, s[40:43], s68 offen offset:1024 nt lds
  v_mfma_f32_16x16x32_bf16 v[108:111], a[16:19], a[128:131], v[96:99]
  v_exp_f32 v116, v116
  v_mfma_f32_16x16x32_bf16 v[108:111], a[20:23], a[132:135], v[108:111]
  ds_read_b64_tr_b16 a[68:69], v232 offset:24576
  ds_read_b64_tr_b16 a[70:71], v232 offset:28672
  v_mfma_f32_16x16x32_bf16 v[108:111], a[24:27], a[136:139], v[108:111]
  v_exp_f32 v117, v117
  v_mfma_f32_16x16x32_bf16 v[108:111], a[28:31], a[140:143], v[108:111]
  ds_read_b128 a[0:3], v228 offset:16384
  v_cvt_pk_bf16_f32 v172, v116, v117
  v_mfma_f32_16x16x32_bf16 v[40:43], a[80:83], v[140:143], v[40:43]
  v_exp_f32 v118, v118
  v_mfma_f32_16x16x32_bf16 v[40:43], a[84:87], v[144:147], v[40:43]
  ds_read_b128 a[4:7], v229 offset:16384
  ds_read_b128 a[8:11], v230 offset:16384
  v_mfma_f32_16x16x32_bf16 v[72:75], a[80:83], v[148:151], v[72:75]
  v_exp_f32 v119, v119
  v_mfma_f32_16x16x32_bf16 v[72:75], a[84:87], v[152:155], v[72:75]
  ds_read_b128 a[12:15], v231 offset:16384
  v_cvt_pk_bf16_f32 v173, v118, v119
  v_mfma_f32_16x16x32_bf16 v[112:115], a[16:19], a[144:147], v[100:103]
  v_exp_f32 v108, v108
  v_mfma_f32_16x16x32_bf16 v[112:115], a[20:23], a[148:151], v[112:115]
  buffer_load_dwordx4 v242, s[40:43], s68 offen offset:2048 nt lds
  v_mfma_f32_16x16x32_bf16 v[112:115], a[24:27], a[152:155], v[112:115]
  v_exp_f32 v109, v109
  v_mfma_f32_16x16x32_bf16 v[112:115], a[28:31], a[156:159], v[112:115]
  ds_read_b64_tr_b16 a[72:73], v233 offset:16384
  v_cvt_pk_bf16_f32 v158, v108, v109
  v_mfma_f32_16x16x32_bf16 v[12:15], a[88:91], v[132:135], v[12:15]
  v_exp_f32 v110, v110
  v_mfma_f32_16x16x32_bf16 v[12:15], a[92:95], v[136:139], v[12:15]
  ds_read_b64_tr_b16 a[74:75], v233 offset:20480
  ds_read_b64_tr_b16 a[76:77], v233 offset:24576
  ds_read_b64_tr_b16 a[78:79], v233 offset:28672
  v_mfma_f32_16x16x32_bf16 v[44:47], a[88:91], v[140:143], v[44:47]
  ds_read_b64_tr_b16 a[80:81], v234 offset:16384
  v_exp_f32 v111, v111
  v_mfma_f32_16x16x32_bf16 v[44:47], a[92:95], v[144:147], v[44:47]
  buffer_load_dwordx4 v243, s[40:43], s68 offen offset:3072 nt lds
  v_cvt_pk_bf16_f32 v159, v110, v111
  v_mfma_f32_16x16x32_bf16 v[116:119], a[16:19], a[160:163], v[104:107]
  ds_read_b64_tr_b16 a[82:83], v234 offset:20480
  v_exp_f32 v112, v112
  v_mfma_f32_16x16x32_bf16 v[116:119], a[20:23], a[164:167], v[116:119]
  s_sub_i32 s95, s34, s84
  s_add_u32 s72, s93, 1
  s_and_b32 s72, s72, 31
  s_mul_i32 s72, s72, 40960
  ds_read_b64_tr_b16 a[84:85], v234 offset:24576
  v_mfma_f32_16x16x32_bf16 v[116:119], a[24:27], a[168:171], v[116:119]
  ds_read_b64_tr_b16 a[86:87], v234 offset:28672
  v_exp_f32 v113, v113
  v_mfma_f32_16x16x32_bf16 v[116:119], a[28:31], a[172:175], v[116:119]
  s_mul_i32 s95, s95, 186
  v_cvt_pk_bf16_f32 v166, v112, v113
  v_mfma_f32_16x16x32_bf16 v[76:79], a[88:91], v[148:151], v[76:79]
  v_exp_f32 v114, v114
  v_mfma_f32_16x16x32_bf16 v[76:79], a[92:95], v[152:155], v[76:79]
  s_ashr_i32 s95, s95, 8
  s_add_u32 m0, s62, 65536
  ds_read_b128 a[16:19], v228 offset:20480
  v_mfma_f32_16x16x32_bf16 v[16:19], a[96:99], v[132:135], v[16:19]
  ds_read_b128 a[20:23], v229 offset:20480
  v_exp_f32 v115, v115
  v_mfma_f32_16x16x32_bf16 v[16:19], a[100:103], v[136:139], v[16:19]
  s_lshl_b32 s96, s81, 4
  ds_read_b128 a[24:27], v230 offset:20480
  v_cvt_pk_bf16_f32 v167, v114, v115
  v_mfma_f32_16x16x32_bf16 v[108:111], a[32:35], a[128:131], v[96:99]
  ds_read_b128 a[28:31], v231 offset:20480
  v_exp_f32 v116, v116
  v_mfma_f32_16x16x32_bf16 v[108:111], a[36:39], a[132:135], v[108:111]
  s_max_i32 s95, s95, s96
  buffer_load_dwordx4 v180, s[52:55], s72 offen offset:0 lds
  v_mfma_f32_16x16x32_bf16 v[108:111], a[40:43], a[136:139], v[108:111]
  ds_read_b64_tr_b16 a[88:89], v235 offset:16384
  v_exp_f32 v117, v117
  v_mfma_f32_16x16x32_bf16 v[108:111], a[44:47], a[140:143], v[108:111]
  s_sub_u32 s95, s95, s36
  ds_read_b64_tr_b16 a[90:91], v235 offset:20480
  v_cvt_pk_bf16_f32 v174, v116, v117
  v_mfma_f32_16x16x32_bf16 v[48:51], a[96:99], v[140:143], v[48:51]
  ds_read_b64_tr_b16 a[92:93], v235 offset:24576
  v_exp_f32 v118, v118
  v_mfma_f32_16x16x32_bf16 v[48:51], a[100:103], v[144:147], v[48:51]
  s_lshr_b32 s96, s95, 4
  s_add_u32 s68, s60, 147456
  v_mfma_f32_16x16x32_bf16 v[80:83], a[96:99], v[148:151], v[80:83]
  v_exp_f32 v119, v119
  v_mfma_f32_16x16x32_bf16 v[80:83], a[100:103], v[152:155], v[80:83]
  s_sub_u32 s96, s96, 4
  v_cvt_pk_bf16_f32 v175, v118, v119
  v_mfma_f32_16x16x32_bf16 v[20:23], a[104:107], v[132:135], v[20:23]
  s_lshl_b32 s97, s84, 4
  s_add_u32 m0, s62, 0
  v_mfma_f32_16x16x32_bf16 v[20:23], a[108:111], v[136:139], v[20:23]
  s_add_u32 s97, s97, s95
  buffer_load_dwordx4 v244, s[44:47], s68 offen offset:0 lds
  v_mfma_f32_16x16x32_bf16 v[112:115], a[32:35], a[144:147], v[100:103]
  v_exp_f32 v108, v108
  v_mfma_f32_16x16x32_bf16 v[112:115], a[36:39], a[148:151], v[112:115]
  s_lshr_b32 s83, s97, 4
  ds_read_b64_tr_b16 a[94:95], v235 offset:28672
  v_mfma_f32_16x16x32_bf16 v[112:115], a[40:43], a[152:155], v[112:115]
  v_exp_f32 v109, v109
  v_mfma_f32_16x16x32_bf16 v[112:115], a[44:47], a[156:159], v[112:115]
  s_add_u32 s94, s84, s96
  v_cvt_pk_bf16_f32 v160, v108, v109
  v_mfma_f32_16x16x32_bf16 v[52:55], a[104:107], v[140:143], v[52:55]
  v_exp_f32 v110, v110
  v_mfma_f32_16x16x32_bf16 v[52:55], a[108:111], v[144:147], v[52:55]
  s_max_u32 s94, s94, s83
  ds_read_b64_tr_b16 a[96:97], v236 offset:16384
  ds_read_b64_tr_b16 a[98:99], v236 offset:20480
  v_mfma_f32_16x16x32_bf16 v[84:87], a[104:107], v[148:151], v[84:87]
  ds_read_b64_tr_b16 a[100:101], v236 offset:24576
  v_exp_f32 v111, v111
  v_mfma_f32_16x16x32_bf16 v[84:87], a[108:111], v[152:155], v[84:87]
  s_sub_u32 s83, s84, s66
  v_cvt_pk_bf16_f32 v161, v110, v111
  v_mfma_f32_16x16x32_bf16 v[116:119], a[32:35], a[160:163], v[104:107]
  v_exp_f32 v112, v112
  v_mfma_f32_16x16x32_bf16 v[116:119], a[36:39], a[164:167], v[116:119]
  s_min_u32 s83, s83, 200
  buffer_load_dwordx4 v245, s[44:47], s68 offen offset:1024 lds
  v_mfma_f32_16x16x32_bf16 v[116:119], a[40:43], a[168:171], v[116:119]
  v_exp_f32 v113, v113
  v_mfma_f32_16x16x32_bf16 v[116:119], a[44:47], a[172:175], v[116:119]
  s_add_u32 s65, s65, s83
  v_cvt_pk_bf16_f32 v168, v112, v113
  v_mfma_f32_16x16x32_bf16 v[24:27], a[112:115], v[132:135], v[24:27]
  v_exp_f32 v114, v114
  v_mfma_f32_16x16x32_bf16 v[24:27], a[116:119], v[136:139], v[24:27]
  s_lshr_b32 s65, s65, 1
  ds_read_b64_tr_b16 a[102:103], v236 offset:28672
  ds_read_b64_tr_b16 a[104:105], v237 offset:16384
  v_mfma_f32_16x16x32_bf16 v[56:59], a[112:115], v[140:143], v[56:59]
  ds_read_b64_tr_b16 a[106:107], v237 offset:20480
  v_exp_f32 v115, v115
  v_mfma_f32_16x16x32_bf16 v[56:59], a[116:119], v[144:147], v[56:59]
  s_add_u32 s83, s65, s84
  v_cvt_pk_bf16_f32 v169, v114, v115
  v_mfma_f32_16x16x32_bf16 v[108:111], a[48:51], a[128:131], v[96:99]
  v_exp_f32 v116, v116
  v_mfma_f32_16x16x32_bf16 v[108:111], a[52:55], a[132:135], v[108:111]
  s_sub_u32 s83, s83, 28
  buffer_load_dwordx4 v244, s[44:47], s68 offen offset:2048 lds
  v_mfma_f32_16x16x32_bf16 v[108:111], a[56:59], a[136:139], v[108:111]
  v_exp_f32 v117, v117
  v_mfma_f32_16x16x32_bf16 v[108:111], a[60:63], a[140:143], v[108:111]
  s_max_u32 s94, s94, s83
  v_cvt_pk_bf16_f32 v176, v116, v117
  v_mfma_f32_16x16x32_bf16 v[88:91], a[112:115], v[148:151], v[88:91]
  v_exp_f32 v118, v118
  v_mfma_f32_16x16x32_bf16 v[88:91], a[116:119], v[152:155], v[88:91]
  s_mov_b32 s66, s84
  ds_read_b64_tr_b16 a[108:109], v237 offset:24576
  v_mfma_f32_16x16x32_bf16 v[28:31], a[120:123], v[132:135], v[28:31]
  v_exp_f32 v119, v119
  v_mfma_f32_16x16x32_bf16 v[28:31], a[124:127], v[136:139], v[28:31]
  ds_read_b64_tr_b16 a[110:111], v237 offset:28672
  v_cvt_pk_bf16_f32 v177, v118, v119
  v_mfma_f32_16x16x32_bf16 v[60:63], a[120:123], v[140:143], v[60:63]
  buffer_load_dwordx4 v245, s[44:47], s68 offen offset:3072 lds
  v_mfma_f32_16x16x32_bf16 v[60:63], a[124:127], v[144:147], v[60:63]
  ds_read_b128 a[32:35], v228 offset:24576
  ds_read_b128 a[36:39], v229 offset:24576
  v_mfma_f32_16x16x32_bf16 v[112:115], a[48:51], a[144:147], v[100:103]
  v_exp_f32 v108, v108
  v_mfma_f32_16x16x32_bf16 v[112:115], a[52:55], a[148:151], v[112:115]
  ds_read_b128 a[40:43], v230 offset:24576
  ds_read_b128 a[44:47], v231 offset:24576
  v_mfma_f32_16x16x32_bf16 v[112:115], a[56:59], a[152:155], v[112:115]
  v_exp_f32 v109, v109
  v_mfma_f32_16x16x32_bf16 v[112:115], a[60:63], a[156:159], v[112:115]
  s_add_u32 m0, s62, 65536
  v_cvt_pk_bf16_f32 v162, v108, v109
  v_mfma_f32_16x16x32_bf16 v[92:95], a[120:123], v[148:151], v[92:95]
  v_exp_f32 v110, v110
  v_mfma_f32_16x16x32_bf16 v[92:95], a[124:127], v[152:155], v[92:95]
  buffer_load_dwordx4 v181, s[52:55], s72 offen offset:1024 lds
  v_mfma_f32_16x16x32_bf16 v[120:123], a[240:243], v[132:135], v[120:123]
  v_exp_f32 v111, v111
  v_mfma_f32_16x16x32_bf16 v[120:123], a[240:243], v[136:139], v[120:123]
  ds_read_b64_tr_b16 a[112:113], v238 offset:16384
  v_cvt_pk_bf16_f32 v163, v110, v111
  v_mfma_f32_16x16x32_bf16 v[116:119], a[48:51], a[160:163], v[104:107]
  v_exp_f32 v112, v112
  v_mfma_f32_16x16x32_bf16 v[116:119], a[52:55], a[164:167], v[116:119]
  ds_read_b64_tr_b16 a[114:115], v238 offset:20480
  ds_read_b64_tr_b16 a[116:117], v238 offset:24576
  v_mfma_f32_16x16x32_bf16 v[116:119], a[56:59], a[168:171], v[116:119]
  v_exp_f32 v113, v113
  v_mfma_f32_16x16x32_bf16 v[116:119], a[60:63], a[172:175], v[116:119]
  ds_read_b64_tr_b16 a[118:119], v238 offset:28672
  v_cvt_pk_bf16_f32 v170, v112, v113
  v_mfma_f32_16x16x32_bf16 v[124:127], a[240:243], v[140:143], v[124:127]
  v_exp_f32 v114, v114
  v_mfma_f32_16x16x32_bf16 v[124:127], a[240:243], v[144:147], v[124:127]
  ds_read_b64_tr_b16 a[120:121], v239 offset:16384
  ds_read_b64_tr_b16 a[122:123], v239 offset:20480
  ds_read_b64_tr_b16 a[124:125], v239 offset:24576
  v_mfma_f32_16x16x32_bf16 v[128:131], a[240:243], v[148:151], v[128:131]
  ds_read_b64_tr_b16 a[126:127], v239 offset:28672
  v_exp_f32 v115, v115
  v_mfma_f32_16x16x32_bf16 v[128:131], a[240:243], v[152:155], v[128:131]
  v_cvt_pk_bf16_f32 v171, v114, v115
  s_waitcnt lgkmcnt(8)
  v_mfma_f32_16x16x32_bf16 v[108:111], a[0:3], a[128:131], v[96:99]
  v_exp_f32 v116, v116
  v_mfma_f32_16x16x32_bf16 v[108:111], a[4:7], a[132:135], v[108:111]
  s_memrealtime s[82:83]
  ds_read_b128 a[48:51], v228 offset:28672
  v_mfma_f32_16x16x32_bf16 v[108:111], a[8:11], a[136:139], v[108:111]
  v_exp_f32 v117, v117
  v_mfma_f32_16x16x32_bf16 v[108:111], a[12:15], a[140:143], v[108:111]
  ds_read_b128 a[52:55], v229 offset:28672
  v_cvt_pk_bf16_f32 v178, v116, v117
  v_mfma_f32_16x16x32_bf16 v[0:3], a[64:67], v[156:159], v[0:3]
  v_exp_f32 v118, v118
  v_mfma_f32_16x16x32_bf16 v[0:3], a[68:71], v[160:163], v[0:3]
  ds_read_b128 a[56:59], v230 offset:28672
  ds_read_b128 a[60:63], v231 offset:28672
  v_mfma_f32_16x16x32_bf16 v[32:35], a[64:67], v[164:167], v[32:35]
  v_exp_f32 v119, v119
  v_mfma_f32_16x16x32_bf16 v[32:35], a[68:71], v[168:171], v[32:35]
  v_cvt_pk_bf16_f32 v179, v118, v119
  v_mfma_f32_16x16x32_bf16 v[112:115], a[0:3], a[144:147], v[100:103]
  v_exp_f32 v108, v108
  v_mfma_f32_16x16x32_bf16 v[112:115], a[4:7], a[148:151], v[112:115]
  v_mfma_f32_16x16x32_bf16 v[112:115], a[8:11], a[152:155], v[112:115]
  v_exp_f32 v109, v109
  v_mfma_f32_16x16x32_bf16 v[112:115], a[12:15], a[156:159], v[112:115]
  v_cvt_pk_bf16_f32 v132, v108, v109
  v_mfma_f32_16x16x32_bf16 v[64:67], a[64:67], v[172:175], v[64:67]
  v_exp_f32 v110, v110
  v_mfma_f32_16x16x32_bf16 v[64:67], a[68:71], v[176:179], v[64:67]
  s_waitcnt vmcnt(10) lgkmcnt(0)
  s_barrier
  v_mfma_f32_16x16x32_bf16 v[4:7], a[72:75], v[156:159], v[4:7]
  v_exp_f32 v111, v111
  v_mfma_f32_16x16x32_bf16 v[4:7], a[76:79], v[160:163], v[4:7]
  s_sub_u32 s84, s82, s85
  v_cvt_pk_bf16_f32 v133, v110, v111
  v_mfma_f32_16x16x32_bf16 v[116:119], a[0:3], a[160:163], v[104:107]
  v_exp_f32 v112, v112
  v_mfma_f32_16x16x32_bf16 v[116:119], a[4:7], a[164:167], v[116:119]
  s_cmp_lt_u32 s84, s94
  s_cbranch_scc1 PC7_%=
PC7_%=e:
  v_mfma_f32_16x16x32_bf16 v[116:119], a[8:11], a[168:171], v[116:119]
  v_exp_f32 v113, v113
  v_mfma_f32_16x16x32_bf16 v[116:119], a[12:15], a[172:175], v[116:119]
  s_add_u32 s68, s60, 180224
  ds_read_b64_tr_b16 a[176:177], v184 offset:0
  v_cvt_pk_bf16_f32 v140, v112, v113
  v_mfma_f32_16x16x32_bf16 v[36:39], a[72:75], v[164:167], v[36:39]
  v_exp_f32 v114, v114
  v_mfma_f32_16x16x32_bf16 v[36:39], a[76:79], v[168:171], v[36:39]
  s_add_u32 m0, s61, 16384
  ds_read_b64_tr_b16 a[64:65], v232 offset:32768
  v_mfma_f32_16x16x32_bf16 v[68:71], a[72:75], v[172:175], v[68:71]
  v_exp_f32 v115, v115
  v_mfma_f32_16x16x32_bf16 v[68:71], a[76:79], v[176:179], v[68:71]
  ds_read_b64_tr_b16 a[66:67], v232 offset:36864
  v_cvt_pk_bf16_f32 v141, v114, v115
  v_mfma_f32_16x16x32_bf16 v[8:11], a[80:83], v[156:159], v[8:11]
  buffer_load_dwordx4 v240, s[40:43], s68 offen offset:0 nt lds
  v_mfma_f32_16x16x32_bf16 v[8:11], a[84:87], v[160:163], v[8:11]
  buffer_load_dwordx4 v241, s[40:43], s68 offen offset:1024 nt lds
  ds_read_b64_tr_b16 a[178:179], v184 offset:2048
  v_mfma_f32_16x16x32_bf16 v[108:111], a[16:19], a[128:131], v[96:99]
  v_exp_f32 v116, v116
  v_mfma_f32_16x16x32_bf16 v[108:111], a[20:23], a[132:135], v[108:111]
  ds_read_b64_tr_b16 a[68:69], v232 offset:40960
  ds_read_b64_tr_b16 a[70:71], v232 offset:45056
  v_mfma_f32_16x16x32_bf16 v[108:111], a[24:27], a[136:139], v[108:111]
  v_exp_f32 v117, v117
  v_mfma_f32_16x16x32_bf16 v[108:111], a[28:31], a[140:143], v[108:111]
  ds_read_b128 a[0:3], v228 offset:0
  v_cvt_pk_bf16_f32 v148, v116, v117
  v_mfma_f32_16x16x32_bf16 v[40:43], a[80:83], v[164:167], v[40:43]
  v_exp_f32 v118, v118
  v_mfma_f32_16x16x32_bf16 v[40:43], a[84:87], v[168:171], v[40:43]
  ds_read_b128 a[4:7], v229 offset:0
  ds_read_b128 a[8:11], v230 offset:0
  ds_read_b64_tr_b16 a[180:181], v185 offset:0
  v_mfma_f32_16x16x32_bf16 v[72:75], a[80:83], v[172:175], v[72:75]
  v_exp_f32 v119, v119
  v_mfma_f32_16x16x32_bf16 v[72:75], a[84:87], v[176:179], v[72:75]
  ds_read_b128 a[12:15], v231 offset:0
  v_cvt_pk_bf16_f32 v149, v118, v119
  v_mfma_f32_16x16x32_bf16 v[112:115], a[16:19], a[144:147], v[100:103]
  v_exp_f32 v108, v108
  v_mfma_f32_16x16x32_bf16 v[112:115], a[20:23], a[148:151], v[112:115]
  buffer_load_dwordx4 v242, s[40:43], s68 offen offset:2048 nt lds
  v_mfma_f32_16x16x32_bf16 v[112:115], a[24:27], a[152:155], v[112:115]
  v_exp_f32 v109, v109
  v_mfma_f32_16x16x32_bf16 v[112:115], a[28:31], a[156:159], v[112:115]
  ds_read_b64_tr_b16 a[72:73], v233 offset:32768
  ds_read_b64_tr_b16 a[182:183], v185 offset:2048
  v_cvt_pk_bf16_f32 v134, v108, v109
  v_mfma_f32_16x16x32_bf16 v[12:15], a[88:91], v[156:159], v[12:15]
  v_exp_f32 v110, v110
  v_mfma_f32_16x16x32_bf16 v[12:15], a[92:95], v[160:163], v[12:15]
  ds_read_b64_tr_b16 a[74:75], v233 offset:36864
  ds_read_b64_tr_b16 a[76:77], v233 offset:40960
  v_mfma_f32_16x16x32_bf16 v[44:47], a[88:91], v[164:167], v[44:47]
  v_exp_f32 v111, v111
  v_mfma_f32_16x16x32_bf16 v[44:47], a[92:95], v[168:171], v[44:47]
  buffer_load_dwordx4 v243, s[40:43], s68 offen offset:3072 nt lds
  v_cvt_pk_bf16_f32 v135, v110, v111
  v_mfma_f32_16x16x32_bf16 v[116:119], a[16:19], a[160:163], v[104:107]
  v_exp_f32 v112, v112
  v_mfma_f32_16x16x32_bf16 v[116:119], a[20:23], a[164:167], v[116:119]
  s_add_u32 s97, s97, s95
  ds_read_b64_tr_b16 a[78:79], v233 offset:45056
  ds_read_b64_tr_b16 a[80:81], v234 offset:32768
  ds_read_b64_tr_b16 a[184:185], v186 offset:0
  v_mfma_f32_16x16x32_bf16 v[116:119], a[24:27], a[168:171], v[116:119]
  ds_read_b64_tr_b16 a[82:83], v234 offset:36864
  v_exp_f32 v113, v113
  v_mfma_f32_16x16x32_bf16 v[116:119], a[28:31], a[172:175], v[116:119]
  s_lshr_b32 s83, s97, 4
  ds_read_b64_tr_b16 a[84:85], v234 offset:40960
  v_cvt_pk_bf16_f32 v142, v112, v113
  v_mfma_f32_16x16x32_bf16 v[76:79], a[88:91], v[172:175], v[76:79]
  ds_read_b64_tr_b16 a[86:87], v234 offset:45056
  v_exp_f32 v114, v114
  v_mfma_f32_16x16x32_bf16 v[76:79], a[92:95], v[176:179], v[76:79]
  s_add_u32 s94, s84, s96
  s_add_u32 m0, s62, 65536
  ds_read_b128 a[16:19], v228 offset:4096
  v_mfma_f32_16x16x32_bf16 v[16:19], a[96:99], v[156:159], v[16:19]
  ds_read_b128 a[20:23], v229 offset:4096
  v_exp_f32 v115, v115
  v_mfma_f32_16x16x32_bf16 v[16:19], a[100:103], v[160:163], v[16:19]
  s_max_u32 s94, s94, s83
  ds_read_b128 a[24:27], v230 offset:4096
  v_cvt_pk_bf16_f32 v143, v114, v115
  v_mfma_f32_16x16x32_bf16 v[108:111], a[32:35], a[128:131], v[96:99]
  ds_read_b128 a[28:31], v231 offset:4096
  v_exp_f32 v116, v116
  v_mfma_f32_16x16x32_bf16 v[108:111], a[36:39], a[132:135], v[108:111]
  s_sub_u32 s83, s84, s66
  buffer_load_dwordx4 v182, s[52:55], s72 offen offset:2048 lds
  v_mfma_f32_16x16x32_bf16 v[108:111], a[40:43], a[136:139], v[108:111]
  ds_read_b64_tr_b16 a[88:89], v235 offset:32768
  v_exp_f32 v117, v117
  v_mfma_f32_16x16x32_bf16 v[108:111], a[44:47], a[140:143], v[108:111]
  s_min_u32 s83, s83, 200
  ds_read_b64_tr_b16 a[90:91], v235 offset:36864
  v_cvt_pk_bf16_f32 v150, v116, v117
  v_mfma_f32_16x16x32_bf16 v[48:51], a[96:99], v[164:167], v[48:51]
  ds_read_b64_tr_b16 a[92:93], v235 offset:40960
  v_exp_f32 v118, v118
  v_mfma_f32_16x16x32_bf16 v[48:51], a[100:103], v[168:171], v[48:51]
  s_add_u32 s65, s65, s83
  s_add_u32 s68, s60, 163840
  ds_read_b64_tr_b16 a[186:187], v186 offset:2048
  v_mfma_f32_16x16x32_bf16 v[80:83], a[96:99], v[172:175], v[80:83]
  v_exp_f32 v119, v119
  v_mfma_f32_16x16x32_bf16 v[80:83], a[100:103], v[176:179], v[80:83]
  s_lshr_b32 s65, s65, 1
  v_cvt_pk_bf16_f32 v151, v118, v119
  v_mfma_f32_16x16x32_bf16 v[20:23], a[104:107], v[156:159], v[20:23]
  s_add_u32 s83, s65, s84
  s_add_u32 m0, s62, 16384
  v_mfma_f32_16x16x32_bf16 v[20:23], a[108:111], v[160:163], v[20:23]
  s_sub_u32 s83, s83, 28
  buffer_load_dwordx4 v244, s[44:47], s68 offen offset:0 lds
  v_mfma_f32_16x16x32_bf16 v[112:115], a[32:35], a[144:147], v[100:103]
  v_exp_f32 v108, v108
  v_mfma_f32_16x16x32_bf16 v[112:115], a[36:39], a[148:151], v[112:115]
  s_max_u32 s94, s94, s83
  ds_read_b64_tr_b16 a[94:95], v235 offset:45056
  ds_read_b64_tr_b16 a[188:189], v187 offset:0
  v_mfma_f32_16x16x32_bf16 v[112:115], a[40:43], a[152:155], v[112:115]
  v_exp_f32 v109, v109
  v_mfma_f32_16x16x32_bf16 v[112:115], a[44:47], a[156:159], v[112:115]
  s_mov_b32 s66, s84
  v_cvt_pk_bf16_f32 v136, v108, v109
  v_mfma_f32_16x16x32_bf16 v[52:55], a[104:107], v[164:167], v[52:55]
  v_exp_f32 v110, v110
  v_mfma_f32_16x16x32_bf16 v[52:55], a[108:111], v[168:171], v[52:55]
  ds_read_b64_tr_b16 a[96:97], v236 offset:32768
  ds_read_b64_tr_b16 a[98:99], v236 offset:36864
  v_mfma_f32_16x16x32_bf16 v[84:87], a[104:107], v[172:175], v[84:87]
  v_exp_f32 v111, v111
  v_mfma_f32_16x16x32_bf16 v[84:87], a[108:111], v[176:179], v[84:87]
  ds_read_b64_tr_b16 a[100:101], v236 offset:40960
  ds_read_b64_tr_b16 a[190:191], v187 offset:2048
  v_cvt_pk_bf16_f32 v137, v110, v111
  v_mfma_f32_16x16x32_bf16 v[116:119], a[32:35], a[160:163], v[104:107]
  v_exp_f32 v112, v112
  v_mfma_f32_16x16x32_bf16 v[116:119], a[36:39], a[164:167], v[116:119]
  buffer_load_dwordx4 v245, s[44:47], s68 offen offset:1024 lds
  v_mfma_f32_16x16x32_bf16 v[116:119], a[40:43], a[168:171], v[116:119]
  v_exp_f32 v113, v113
  v_mfma_f32_16x16x32_bf16 v[116:119], a[44:47], a[172:175], v[116:119]
  ds_read_b64_tr_b16 a[102:103], v236 offset:45056
  v_cvt_pk_bf16_f32 v144, v112, v113
  v_mfma_f32_16x16x32_bf16 v[24:27], a[112:115], v[156:159], v[24:27]
  v_exp_f32 v114, v114
  v_mfma_f32_16x16x32_bf16 v[24:27], a[116:119], v[160:163], v[24:27]
  ds_read_b64_tr_b16 a[104:105], v237 offset:32768
  ds_read_b64_tr_b16 a[106:107], v237 offset:36864
  v_mfma_f32_16x16x32_bf16 v[56:59], a[112:115], v[164:167], v[56:59]
  v_exp_f32 v115, v115
  v_mfma_f32_16x16x32_bf16 v[56:59], a[116:119], v[168:171], v[56:59]
  ds_read_b64_tr_b16 a[108:109], v237 offset:40960
  v_cvt_pk_bf16_f32 v145, v114, v115
  v_mfma_f32_16x16x32_bf16 v[108:111], a[48:51], a[128:131], v[96:99]
  v_exp_f32 v116, v116
  v_mfma_f32_16x16x32_bf16 v[108:111], a[52:55], a[132:135], v[108:111]
  buffer_load_dwordx4 v244, s[44:47], s68 offen offset:2048 lds
  v_mfma_f32_16x16x32_bf16 v[108:111], a[56:59], a[136:139], v[108:111]
  v_exp_f32 v117, v117
  v_mfma_f32_16x16x32_bf16 v[108:111], a[60:63], a[140:143], v[108:111]
  ds_read_b64_tr_b16 a[110:111], v237 offset:45056
  v_cvt_pk_bf16_f32 v152, v116, v117
  v_mfma_f32_16x16x32_bf16 v[88:91], a[112:115], v[172:175], v[88:91]
  v_exp_f32 v118, v118
  v_mfma_f32_16x16x32_bf16 v[88:91], a[116:119], v[176:179], v[88:91]
  ds_read_b128 a[32:35], v228 offset:8192
  ds_read_b128 a[36:39], v229 offset:8192
  v_mfma_f32_16x16x32_bf16 v[28:31], a[120:123], v[156:159], v[28:31]
  v_exp_f32 v119, v119
  v_mfma_f32_16x16x32_bf16 v[28:31], a[124:127], v[160:163], v[28:31]
  ds_read_b128 a[40:43], v230 offset:8192
  v_cvt_pk_bf16_f32 v153, v118, v119
  v_mfma_f32_16x16x32_bf16 v[60:63], a[120:123], v[164:167], v[60:63]
  buffer_load_dwordx4 v245, s[44:47], s68 offen offset:3072 lds
  v_mfma_f32_16x16x32_bf16 v[60:63], a[124:127], v[168:171], v[60:63]
  ds_read_b128 a[44:47], v231 offset:8192
  ds_read_b64_tr_b16 a[112:113], v238 offset:32768
  v_mfma_f32_16x16x32_bf16 v[112:115], a[48:51], a[144:147], v[100:103]
  v_exp_f32 v108, v108
  v_mfma_f32_16x16x32_bf16 v[112:115], a[52:55], a[148:151], v[112:115]
  ds_read_b64_tr_b16 a[114:115], v238 offset:36864
  ds_read_b64_tr_b16 a[116:117], v238 offset:40960
  v_mfma_f32_16x16x32_bf16 v[112:115], a[56:59], a[152:155], v[112:115]
  v_exp_f32 v109, v109
  v_mfma_f32_16x16x32_bf16 v[112:115], a[60:63], a[156:159], v[112:115]
  s_add_u32 m0, s62, 65536
  v_cvt_pk_bf16_f32 v138, v108, v109
  v_mfma_f32_16x16x32_bf16 v[92:95], a[120:123], v[172:175], v[92:95]
  v_exp_f32 v110, v110
  v_mfma_f32_16x16x32_bf16 v[92:95], a[124:127], v[176:179], v[92:95]
  buffer_load_dwordx4 v183, s[52:55], s72 offen offset:3072 lds
  v_mfma_f32_16x16x32_bf16 v[120:123], a[240:243], v[156:159], v[120:123]
  v_exp_f32 v111, v111
  v_mfma_f32_16x16x32_bf16 v[120:123], a[240:243], v[160:163], v[120:123]
  ds_read_b64_tr_b16 a[118:119], v238 offset:45056
  v_cvt_pk_bf16_f32 v139, v110, v111
  v_mfma_f32_16x16x32_bf16 v[116:119], a[48:51], a[160:163], v[104:107]
  v_exp_f32 v112, v112
  v_mfma_f32_16x16x32_bf16 v[116:119], a[52:55], a[164:167], v[116:119]
  ds_read_b64_tr_b16 a[120:121], v239 offset:32768
  ds_read_b64_tr_b16 a[122:123], v239 offset:36864
  v_mfma_f32_16x16x32_bf16 v[116:119], a[56:59], a[168:171], v[116:119]
  v_exp_f32 v113, v113
  v_mfma_f32_16x16x32_bf16 v[116:119], a[60:63], a[172:175], v[116:119]
  ds_read_b64_tr_b16 a[124:125], v239 offset:40960
  v_cvt_pk_bf16_f32 v146, v112, v113
  v_mfma_f32_16x16x32_bf16 v[124:127], a[240:243], v[164:167], v[124:127]
  v_exp_f32 v114, v114
  v_mfma_f32_16x16x32_bf16 v[124:127], a[240:243], v[168:171], v[124:127]
  ds_read_b64_tr_b16 a[126:127], v239 offset:45056
  v_mfma_f32_16x16x32_bf16 v[128:131], a[240:243], v[172:175], v[128:131]
  v_exp_f32 v115, v115
  v_mfma_f32_16x16x32_bf16 v[128:131], a[240:243], v[176:179], v[128:131]
  v_cvt_pk_bf16_f32 v147, v114, v115
  s_waitcnt lgkmcnt(8)
  v_mfma_f32_16x16x32_bf16 v[108:111], a[0:3], a[128:131], v[96:99]
  v_exp_f32 v116, v116
  v_mfma_f32_16x16x32_bf16 v[108:111], a[4:7], a[132:135], v[108:111]
  s_memrealtime s[82:83]
  ds_read_b128 a[48:51], v228 offset:12288
  v_mfma_f32_16x16x32_bf16 v[108:111], a[8:11], a[136:139], v[108:111]
  v_exp_f32 v117, v117
  v_mfma_f32_16x16x32_bf16 v[108:111], a[12:15], a[140:143], v[108:111]
  ds_read_b128 a[52:55], v229 offset:12288
  v_cvt_pk_bf16_f32 v154, v116, v117
  v_mfma_f32_16x16x32_bf16 v[0:3], a[64:67], v[132:135], v[0:3]
  v_exp_f32 v118, v118
  v_mfma_f32_16x16x32_bf16 v[0:3], a[68:71], v[136:139], v[0:3]
  ds_read_b128 a[56:59], v230 offset:12288
  ds_read_b128 a[60:63], v231 offset:12288
  v_mfma_f32_16x16x32_bf16 v[32:35], a[64:67], v[140:143], v[32:35]
  v_exp_f32 v119, v119
  v_mfma_f32_16x16x32_bf16 v[32:35], a[68:71], v[144:147], v[32:35]
  v_cvt_pk_bf16_f32 v155, v118, v119
  v_mfma_f32_16x16x32_bf16 v[112:115], a[0:3], a[144:147], v[100:103]
  v_exp_f32 v108, v108
  v_mfma_f32_16x16x32_bf16 v[112:115], a[4:7], a[148:151], v[112:115]
  v_mfma_f32_16x16x32_bf16 v[112:115], a[8:11], a[152:155], v[112:115]
  v_exp_f32 v109, v109
  v_mfma_f32_16x16x32_bf16 v[112:115], a[12:15], a[156:159], v[112:115]
  v_cvt_pk_bf16_f32 v156, v108, v109
  v_mfma_f32_16x16x32_bf16 v[64:67], a[64:67], v[148:151], v[64:67]
  v_exp_f32 v110, v110
  v_mfma_f32_16x16x32_bf16 v[64:67], a[68:71], v[152:155], v[64:67]
  s_waitcnt vmcnt(11) lgkmcnt(0)
  s_barrier
  v_mfma_f32_16x16x32_bf16 v[4:7], a[72:75], v[132:135], v[4:7]
  v_exp_f32 v111, v111
  v_mfma_f32_16x16x32_bf16 v[4:7], a[76:79], v[136:139], v[4:7]
  s_sub_u32 s84, s82, s85
  v_cvt_pk_bf16_f32 v157, v110, v111
  v_cmp_ne_u32 vcc, v218, v252
  s_nop 1
  s_and_b64 vcc, vcc, s[90:91]
  s_cbranch_scc0 PCIe%=
  s_mov_b32 s78, 0
PCI%=:
  s_sleep 2
  buffer_load_dword v218, v217, s[56:59], 0 offen offset:0 sc1
  s_waitcnt vmcnt(0)
  v_cmp_ne_u32 vcc, v218, v252
  s_nop 1
  s_and_b64 vcc, vcc, s[90:91]
  s_cbranch_scc0 PCIe%=
  s_add_u32 s78, s78, 1
  s_cmp_lt_u32 s78, 20000
  s_cbranch_scc1 PCI%=
PCIe%=:
  v_mfma_f32_16x16x32_bf16 v[116:119], a[0:3], a[160:163], v[104:107]
  v_exp_f32 v112, v112
  v_mfma_f32_16x16x32_bf16 v[116:119], a[4:7], a[164:167], v[116:119]
  s_cmp_lt_u32 s84, s94
  s_cbranch_scc1 PC8_%=
PC8_%=e:
  v_mfma_f32_16x16x32_bf16 v[116:119], a[8:11], a[168:171], v[116:119]
  v_exp_f32 v113, v113
  v_mfma_f32_16x16x32_bf16 v[116:119], a[12:15], a[172:175], v[116:119]
  s_add_u32 s68, s60, 196608
  v_cvt_pk_bf16_f32 v164, v112, v113
  v_mfma_f32_16x16x32_bf16 v[36:39], a[72:75], v[140:143], v[36:39]
  v_exp_f32 v114, v114
  v_mfma_f32_16x16x32_bf16 v[36:39], a[76:79], v[144:147], v[36:39]
  s_add_u32 m0, s61, 0
  ds_read_b64_tr_b16 a[64:65], v232 offset:0
  v_mfma_f32_16x16x32_bf16 v[68:71], a[72:75], v[148:151], v[68:71]
  v_exp_f32 v115, v115
  v_mfma_f32_16x16x32_bf16 v[68:71], a[76:79], v[152:155], v[68:71]
  ds_read_b64_tr_b16 a[66:67], v232 offset:4096
  v_cvt_pk_bf16_f32 v165, v114, v115
  v_mfma_f32_16x16x32_bf16 v[8:11], a[80:83], v[132:135], v[8:11]
  buffer_load_dwordx4 v240, s[40:43], s68 offen offset:0 nt lds
  v_mfma_f32_16x16x32_bf16 v[8:11], a[84:87], v[136:139], v[8:11]
  buffer_load_dwordx4 v241, s[40:43], s68 offen offset:1024 nt lds
  v_mfma_f32_16x16x32_bf16 v[108:111], a[16:19], a[128:131], v[96:99]
  v_exp_f32 v116, v116
  v_mfma_f32_16x16x32_bf16 v[108:111], a[20:23], a[132:135], v[108:111]
  ds_read_b64_tr_b16 a[68:69], v232 offset:8192
  ds_read_b64_tr_b16 a[70:71], v232 offset:12288
  v_mfma_f32_16x16x32_bf16 v[108:111], a[24:27], a[136:139], v[108:111]
  v_exp_f32 v117, v117
  v_mfma_f32_16x16x32_bf16 v[108:111], a[28:31], a[140:143], v[108:111]
  ds_read_b128 a[0:3], v228 offset:49152
  v_cvt_pk_bf16_f32 v172, v116, v117
  v_mfma_f32_16x16x32_bf16 v[40:43], a[80:83], v[140:143], v[40:43]
  v_exp_f32 v118, v118
  v_mfma_f32_16x16x32_bf16 v[40:43], a[84:87], v[144:147], v[40:43]
  ds_read_b128 a[4:7], v229 offset:49152
  ds_read_b128 a[8:11], v230 offset:49152
  v_mfma_f32_16x16x32_bf16 v[72:75], a[80:83], v[148:151], v[72:75]
  v_exp_f32 v119, v119
  v_mfma_f32_16x16x32_bf16 v[72:75], a[84:87], v[152:155], v[72:75]
  ds_read_b128 a[12:15], v231 offset:49152
  v_cvt_pk_bf16_f32 v173, v118, v119
  v_mfma_f32_16x16x32_bf16 v[112:115], a[16:19], a[144:147], v[100:103]
  v_exp_f32 v108, v108
  v_mfma_f32_16x16x32_bf16 v[112:115], a[20:23], a[148:151], v[112:115]
  buffer_load_dwordx4 v242, s[40:43], s68 offen offset:2048 nt lds
  v_mfma_f32_16x16x32_bf16 v[112:115], a[24:27], a[152:155], v[112:115]
  v_exp_f32 v109, v109
  v_mfma_f32_16x16x32_bf16 v[112:115], a[28:31], a[156:159], v[112:115]
  ds_read_b64_tr_b16 a[72:73], v233 offset:0
  v_cvt_pk_bf16_f32 v158, v108, v109
  v_mfma_f32_16x16x32_bf16 v[12:15], a[88:91], v[132:135], v[12:15]
  v_exp_f32 v110, v110
  v_mfma_f32_16x16x32_bf16 v[12:15], a[92:95], v[136:139], v[12:15]
  ds_read_b64_tr_b16 a[74:75], v233 offset:4096
  ds_read_b64_tr_b16 a[76:77], v233 offset:8192
  ds_read_b64_tr_b16 a[78:79], v233 offset:12288
  v_mfma_f32_16x16x32_bf16 v[44:47], a[88:91], v[140:143], v[44:47]
  ds_read_b64_tr_b16 a[80:81], v234 offset:0
  v_exp_f32 v111, v111
  v_mfma_f32_16x16x32_bf16 v[44:47], a[92:95], v[144:147], v[44:47]
  buffer_load_dwordx4 v243, s[40:43], s68 offen offset:3072 nt lds
  v_cvt_pk_bf16_f32 v159, v110, v111
  v_mfma_f32_16x16x32_bf16 v[116:119], a[16:19], a[160:163], v[104:107]
  ds_read_b64_tr_b16 a[82:83], v234 offset:4096
  v_exp_f32 v112, v112
  v_mfma_f32_16x16x32_bf16 v[116:119], a[20:23], a[164:167], v[116:119]
  s_add_u32 s97, s97, s95
  s_add_u32 s72, s93, 2
  s_and_b32 s72, s72, 31
  s_mul_i32 s72, s72, 40960
  ds_read_b64_tr_b16 a[84:85], v234 offset:8192
  v_mfma_f32_16x16x32_bf16 v[116:119], a[24:27], a[168:171], v[116:119]
  ds_read_b64_tr_b16 a[86:87], v234 offset:12288
  v_exp_f32 v113, v113
  v_mfma_f32_16x16x32_bf16 v[116:119], a[28:31], a[172:175], v[116:119]
  s_lshr_b32 s83, s97, 4
  v_cvt_pk_bf16_f32 v166, v112, v113
  v_mfma_f32_16x16x32_bf16 v[76:79], a[88:91], v[148:151], v[76:79]
  v_exp_f32 v114, v114
  v_mfma_f32_16x16x32_bf16 v[76:79], a[92:95], v[152:155], v[76:79]
  s_add_u32 s94, s84, s96
  s_add_u32 m0, s62, 49152
  ds_read_b128 a[16:19], v228 offset:53248
  v_mfma_f32_16x16x32_bf16 v[16:19], a[96:99], v[132:135], v[16:19]
  ds_read_b128 a[20:23], v229 offset:53248
  v_exp_f32 v115, v115
  v_mfma_f32_16x16x32_bf16 v[16:19], a[100:103], v[136:139], v[16:19]
  s_max_u32 s94, s94, s83
  ds_read_b128 a[24:27], v230 offset:53248
  v_cvt_pk_bf16_f32 v167, v114, v115
  v_mfma_f32_16x16x32_bf16 v[108:111], a[32:35], a[128:131], v[96:99]
  ds_read_b128 a[28:31], v231 offset:53248
  v_exp_f32 v116, v116
  v_mfma_f32_16x16x32_bf16 v[108:111], a[36:39], a[132:135], v[108:111]
  s_sub_u32 s83, s84, s66
  buffer_load_dwordx4 v180, s[52:55], s72 offen offset:0 lds
  v_mfma_f32_16x16x32_bf16 v[108:111], a[40:43], a[136:139], v[108:111]
  ds_read_b64_tr_b16 a[88:89], v235 offset:0
  v_exp_f32 v117, v117
  v_mfma_f32_16x16x32_bf16 v[108:111], a[44:47], a[140:143], v[108:111]
  s_min_u32 s83, s83, 200
  ds_read_b64_tr_b16 a[90:91], v235 offset:4096
  v_cvt_pk_bf16_f32 v174, v116, v117
  v_mfma_f32_16x16x32_bf16 v[48:51], a[96:99], v[140:143], v[48:51]
  ds_read_b64_tr_b16 a[92:93], v235 offset:8192
  v_exp_f32 v118, v118
  v_mfma_f32_16x16x32_bf16 v[48:51], a[100:103], v[144:147], v[48:51]
  s_add_u32 s65, s65, s83
  s_add_u32 s68, s60, 180224
  v_mfma_f32_16x16x32_bf16 v[80:83], a[96:99], v[148:151], v[80:83]
  v_exp_f32 v119, v119
  v_mfma_f32_16x16x32_bf16 v[80:83], a[100:103], v[152:155], v[80:83]
  s_lshr_b32 s65, s65, 1
  v_cvt_pk_bf16_f32 v175, v118, v119
  v_mfma_f32_16x16x32_bf16 v[20:23], a[104:107], v[132:135], v[20:23]
  s_add_u32 s83, s65, s84
  s_add_u32 m0, s62, 32768
  v_mfma_f32_16x16x32_bf16 v[20:23], a[108:111], v[136:139], v[20:23]
  s_sub_u32 s83, s83, 28
  buffer_load_dwordx4 v244, s[44:47], s68 offen offset:0 lds
  v_mfma_f32_16x16x32_bf16 v[112:115], a[32:35], a[144:147], v[100:103]
  v_exp_f32 v108, v108
  v_mfma_f32_16x16x32_bf16 v[112:115], a[36:39], a[148:151], v[112:115]
  s_max_u32 s94, s94, s83
  ds_read_b64_tr_b16 a[94:95], v235 offset:12288
  v_mfma_f32_16x16x32_bf16 v[112:115], a[40:43], a[152:155], v[112:115]
  v_exp_f32 v109, v109
  v_mfma_f32_16x16x32_bf16 v[112:115], a[44:47], a[156:159], v[112:115]
  s_mov_b32 s66, s84
  v_cvt_pk_bf16_f32 v160, v108, v109
  v_mfma_f32_16x16x32_bf16 v[52:55], a[104:107], v[140:143], v[52:55]
  v_exp_f32 v110, v110
  v_mfma_f32_16x16x32_bf16 v[52:55], a[108:111], v[144:147], v[52:55]
  ds_read_b64_tr_b16 a[96:97], v236 offset:0
  ds_read_b64_tr_b16 a[98:99], v236 offset:4096
  v_mfma_f32_16x16x32_bf16 v[84:87], a[104:107], v[148:151], v[84:87]
  v_exp_f32 v111, v111
  v_mfma_f32_16x16x32_bf16 v[84:87], a[108:111], v[152:155], v[84:87]
  ds_read_b64_tr_b16 a[100:101], v236 offset:8192
  v_cvt_pk_bf16_f32 v161, v110, v111
  v_mfma_f32_16x16x32_bf16 v[116:119], a[32:35], a[160:163], v[104:107]
  v_exp_f32 v112, v112
  v_mfma_f32_16x16x32_bf16 v[116:119], a[36:39], a[164:167], v[116:119]
  buffer_load_dwordx4 v245, s[44:47], s68 offen offset:1024 lds
  v_mfma_f32_16x16x32_bf16 v[116:119], a[40:43], a[168:171], v[116:119]
  v_exp_f32 v113, v113
  v_mfma_f32_16x16x32_bf16 v[116:119], a[44:47], a[172:175], v[116:119]
  ds_read_b64_tr_b16 a[102:103], v236 offset:12288
  v_cvt_pk_bf16_f32 v168, v112, v113
  v_mfma_f32_16x16x32_bf16 v[24:27], a[112:115], v[132:135], v[24:27]
  v_exp_f32 v114, v114
  v_mfma_f32_16x16x32_bf16 v[24:27], a[116:119], v[136:139], v[24:27]
  ds_read_b64_tr_b16 a[104:105], v237 offset:0
  ds_read_b64_tr_b16 a[106:107], v237 offset:4096
  v_mfma_f32_16x16x32_bf16 v[56:59], a[112:115], v[140:143], v[56:59]
  v_exp_f32 v115, v115
  v_mfma_f32_16x16x32_bf16 v[56:59], a[116:119], v[144:147], v[56:59]
  ds_read_b64_tr_b16 a[108:109], v237 offset:8192
  v_cvt_pk_bf16_f32 v169, v114, v115
  v_mfma_f32_16x16x32_bf16 v[108:111], a[48:51], a[128:131], v[96:99]
  v_exp_f32 v116, v116
  v_mfma_f32_16x16x32_bf16 v[108:111], a[52:55], a[132:135], v[108:111]
  buffer_load_dwordx4 v244, s[44:47], s68 offen offset:2048 lds
  v_mfma_f32_16x16x32_bf16 v[108:111], a[56:59], a[136:139], v[108:111]
  v_exp_f32 v117, v117
  v_mfma_f32_16x16x32_bf16 v[108:111], a[60:63], a[140:143], v[108:111]
  ds_read_b64_tr_b16 a[110:111], v237 offset:12288
  v_cvt_pk_bf16_f32 v176, v116, v117
  v_mfma_f32_16x16x32_bf16 v[88:91], a[112:115], v[148:151], v[88:91]
  v_exp_f32 v118, v118
  v_mfma_f32_16x16x32_bf16 v[88:91], a[116:119], v[152:155], v[88:91]
  ds_read_b128 a[32:35], v228 offset:57344
  ds_read_b128 a[36:39], v229 offset:57344
  v_mfma_f32_16x16x32_bf16 v[28:31], a[120:123], v[132:135], v[28:31]
  v_exp_f32 v119, v119
  v_mfma_f32_16x16x32_bf16 v[28:31], a[124:127], v[136:139], v[28:31]
  ds_read_b128 a[40:43], v230 offset:57344
  v_cvt_pk_bf16_f32 v177, v118, v119
  v_mfma_f32_16x16x32_bf16 v[60:63], a[120:123], v[140:143], v[60:63]
  buffer_load_dwordx4 v245, s[44:47], s68 offen offset:3072 lds
  v_mfma_f32_16x16x32_bf16 v[60:63], a[124:127], v[144:147], v[60:63]
  ds_read_b128 a[44:47], v231 offset:57344
  ds_read_b64_tr_b16 a[112:113], v238 offset:0
  v_mfma_f32_16x16x32_bf16 v[112:115], a[48:51], a[144:147], v[100:103]
  v_exp_f32 v108, v108
  v_mfma_f32_16x16x32_bf16 v[112:115], a[52:55], a[148:151], v[112:115]
  ds_read_b64_tr_b16 a[114:115], v238 offset:4096
  ds_read_b64_tr_b16 a[116:117], v238 offset:8192
  v_mfma_f32_16x16x32_bf16 v[112:115], a[56:59], a[152:155], v[112:115]
  v_exp_f32 v109, v109
  v_mfma_f32_16x16x32_bf16 v[112:115], a[60:63], a[156:159], v[112:115]
  s_add_u32 m0, s62, 49152
  v_cvt_pk_bf16_f32 v162, v108, v109
  v_mfma_f32_16x16x32_bf16 v[92:95], a[120:123], v[148:151], v[92:95]
  v_exp_f32 v110, v110
  v_mfma_f32_16x16x32_bf16 v[92:95], a[124:127], v[152:155], v[92:95]
  buffer_load_dwordx4 v181, s[52:55], s72 offen offset:1024 lds
  v_mfma_f32_16x16x32_bf16 v[120:123], a[240:243], v[132:135], v[120:123]
  v_exp_f32 v111, v111
  v_mfma_f32_16x16x32_bf16 v[120:123], a[240:243], v[136:139], v[120:123]
  ds_read_b64_tr_b16 a[118:119], v238 offset:12288
  v_cvt_pk_bf16_f32 v163, v110, v111
  v_mfma_f32_16x16x32_bf16 v[116:119], a[48:51], a[160:163], v[104:107]
  v_exp_f32 v112, v112
  v_mfma_f32_16x16x32_bf16 v[116:119], a[52:55], a[164:167], v[116:119]
  ds_read_b64_tr_b16 a[120:121], v239 offset:0
  ds_read_b64_tr_b16 a[122:123], v239 offset:4096
  v_mfma_f32_16x16x32_bf16 v[116:119], a[56:59], a[168:171], v[116:119]
  v_exp_f32 v113, v113
  v_mfma_f32_16x16x32_bf16 v[116:119], a[60:63], a[172:175], v[116:119]
  ds_read_b64_tr_b16 a[124:125], v239 offset:8192
  v_cvt_pk_bf16_f32 v170, v112, v113
  v_mfma_f32_16x16x32_bf16 v[124:127], a[240:243], v[140:143], v[124:127]
  v_exp_f32 v114, v114
  v_mfma_f32_16x16x32_bf16 v[124:127], a[240:243], v[144:147], v[124:127]
  ds_read_b64_tr_b16 a[126:127], v239 offset:12288
  v_mfma_f32_16x16x32_bf16 v[128:131], a[240:243], v[148:151], v[128:131]
  v_exp_f32 v115, v115
  v_mfma_f32_16x16x32_bf16 v[128:131], a[240:243], v[152:155], v[128:131]
  v_cvt_pk_bf16_f32 v171, v114, v115
  s_waitcnt lgkmcnt(8)
  v_mfma_f32_16x16x32_bf16 v[108:111], a[0:3], a[128:131], v[96:99]
  v_exp_f32 v116, v116
  v_mfma_f32_16x16x32_bf16 v[108:111], a[4:7], a[132:135], v[108:111]
  s_memrealtime s[82:83]
  ds_read_b128 a[48:51], v228 offset:61440
  v_mfma_f32_16x16x32_bf16 v[108:111], a[8:11], a[136:139], v[108:111]
  v_exp_f32 v117, v117
  v_mfma_f32_16x16x32_bf16 v[108:111], a[12:15], a[140:143], v[108:111]
  ds_read_b128 a[52:55], v229 offset:61440
  v_cvt_pk_bf16_f32 v178, v116, v117
  v_mfma_f32_16x16x32_bf16 v[0:3], a[64:67], v[156:159], v[0:3]
  v_exp_f32 v118, v118
  v_mfma_f32_16x16x32_bf16 v[0:3], a[68:71], v[160:163], v[0:3]
  ds_read_b128 a[56:59], v230 offset:61440
  ds_read_b128 a[60:63], v231 offset:61440
  v_mfma_f32_16x16x32_bf16 v[32:35], a[64:67], v[164:167], v[32:35]
  v_exp_f32 v119, v119
  v_mfma_f32_16x16x32_bf16 v[32:35], a[68:71], v[168:171], v[32:35]
  v_cvt_pk_bf16_f32 v179, v118, v119
  v_mfma_f32_16x16x32_bf16 v[112:115], a[0:3], a[144:147], v[100:103]
  v_exp_f32 v108, v108
  v_mfma_f32_16x16x32_bf16 v[112:115], a[4:7], a[148:151], v[112:115]
  v_mfma_f32_16x16x32_bf16 v[112:115], a[8:11], a[152:155], v[112:115]
  v_exp_f32 v109, v109
  v_mfma_f32_16x16x32_bf16 v[112:115], a[12:15], a[156:159], v[112:115]
  v_cvt_pk_bf16_f32 v132, v108, v109
  v_mfma_f32_16x16x32_bf16 v[64:67], a[64:67], v[172:175], v[64:67]
  v_exp_f32 v110, v110
  v_mfma_f32_16x16x32_bf16 v[64:67], a[68:71], v[176:179], v[64:67]
  s_waitcnt vmcnt(10) lgkmcnt(0)
  s_barrier
  v_mfma_f32_16x16x32_bf16 v[4:7], a[72:75], v[156:159], v[4:7]
  v_exp_f32 v111, v111
  v_mfma_f32_16x16x32_bf16 v[4:7], a[76:79], v[160:163], v[4:7]
  s_sub_u32 s84, s82, s85
  v_cvt_pk_bf16_f32 v133, v110, v111
  v_mfma_f32_16x16x32_bf16 v[116:119], a[0:3], a[160:163], v[104:107]
  v_exp_f32 v112, v112
  v_mfma_f32_16x16x32_bf16 v[116:119], a[4:7], a[164:167], v[116:119]
  s_cmp_lt_u32 s84, s94
  s_cbranch_scc1 PC9_%=
PC9_%=e:
  v_mfma_f32_16x16x32_bf16 v[116:119], a[8:11], a[168:171], v[116:119]
  v_exp_f32 v113, v113
  v_mfma_f32_16x16x32_bf16 v[116:119], a[12:15], a[172:175], v[116:119]
  s_add_u32 s68, s60, 212992
  ds_read_b64_tr_b16 a[192:193], v184 offset:16384
  v_cvt_pk_bf16_f32 v140, v112, v113
  v_mfma_f32_16x16x32_bf16 v[36:39], a[72:75], v[164:167], v[36:39]
  v_exp_f32 v114, v114
  v_mfma_f32_16x16x32_bf16 v[36:39], a[76:79], v[168:171], v[36:39]
  s_add_u32 m0, s61, 49152
  ds_read_b64_tr_b16 a[64:65], v232 offset:16384
  v_mfma_f32_16x16x32_bf16 v[68:71], a[72:75], v[172:175], v[68:71]
  v_exp_f32 v115, v115
  v_mfma_f32_16x16x32_bf16 v[68:71], a[76:79], v[176:179], v[68:71]
  ds_read_b64_tr_b16 a[66:67], v232 offset:20480
  v_cvt_pk_bf16_f32 v141, v114, v115
  v_mfma_f32_16x16x32_bf16 v[8:11], a[80:83], v[156:159], v[8:11]
  buffer_load_dwordx4 v240, s[40:43], s68 offen offset:0 nt lds
  v_mfma_f32_16x16x32_bf16 v[8:11], a[84:87], v[160:163], v[8:11]
  buffer_load_dwordx4 v241, s[40:43], s68 offen offset:1024 nt lds
  ds_read_b64_tr_b16 a[194:195], v184 offset:18432
  v_mfma_f32_16x16x32_bf16 v[108:111], a[16:19], a[128:131], v[96:99]
  v_exp_f32 v116, v116
  v_mfma_f32_16x16x32_bf16 v[108:111], a[20:23], a[132:135], v[108:111]
  ds_read_b64_tr_b16 a[68:69], v232 offset:24576
  ds_read_b64_tr_b16 a[70:71], v232 offset:28672
  v_mfma_f32_16x16x32_bf16 v[108:111], a[24:27], a[136:139], v[108:111]
  v_exp_f32 v117, v117
  v_mfma_f32_16x16x32_bf16 v[108:111], a[28:31], a[140:143], v[108:111]
  ds_read_b128 a[0:3], v228 offset:16384
  v_cvt_pk_bf16_f32 v148, v116, v117
  v_mfma_f32_16x16x32_bf16 v[40:43], a[80:83], v[164:167], v[40:43]
  v_exp_f32 v118, v118
  v_mfma_f32_16x16x32_bf16 v[40:43], a[84:87], v[168:171], v[40:43]
  ds_read_b128 a[4:7], v229 offset:16384
  ds_read_b128 a[8:11], v230 offset:16384
  ds_read_b64_tr_b16 a[196:197], v185 offset:16384
  v_mfma_f32_16x16x32_bf16 v[72:75], a[80:83], v[172:175], v[72:75]
  v_exp_f32 v119, v119
  v_mfma_f32_16x16x32_bf16 v[72:75], a[84:87], v[176:179], v[72:75]
  ds_read_b128 a[12:15], v231 offset:16384
  v_cvt_pk_bf16_f32 v149, v118, v119
  v_mfma_f32_16x16x32_bf16 v[112:115], a[16:19], a[144:147], v[100:103]
  v_exp_f32 v108, v108
  v_mfma_f32_16x16x32_bf16 v[112:115], a[20:23], a[148:151], v[112:115]
  buffer_load_dwordx4 v242, s[40:43], s68 offen offset:2048 nt lds
  v_mfma_f32_16x16x32_bf16 v[112:115], a[24:27], a[152:155], v[112:115]
  v_exp_f32 v109, v109
  v_mfma_f32_16x16x32_bf16 v[112:115], a[28:31], a[156:159], v[112:115]
  ds_read_b64_tr_b16 a[72:73], v233 offset:16384
  ds_read_b64_tr_b16 a[198:199], v185 offset:18432
  v_cvt_pk_bf16_f32 v134, v108, v109
  v_mfma_f32_16x16x32_bf16 v[12:15], a[88:91], v[156:159], v[12:15]
  v_exp_f32 v110, v110
  v_mfma_f32_16x16x32_bf16 v[12:15], a[92:95], v[160:163], v[12:15]
  buffer_load_dwordx4 v243, s[40:43], s68 offen offset:3072 nt lds
  v_mfma_f32_16x16x32_bf16 v[44:47], a[88:91], v[164:167], v[44:47]
  v_exp_f32 v111, v111
  v_mfma_f32_16x16x32_bf16 v[44:47], a[92:95], v[168:171], v[44:47]
  ds_read_b64_tr_b16 a[74:75], v233 offset:20480
  v_cvt_pk_bf16_f32 v135, v110, v111
  v_mfma_f32_16x16x32_bf16 v[116:119], a[16:19], a[160:163], v[104:107]
  v_exp_f32 v112, v112
  v_mfma_f32_16x16x32_bf16 v[116:119], a[20:23], a[164:167], v[116:119]
  s_add_u32 s97, s97, s95
  ds_read_b64_tr_b16 a[76:77], v233 offset:24576
  ds_read_b64_tr_b16 a[78:79], v233 offset:28672
  ds_read_b64_tr_b16 a[200:201], v186 offset:16384
  v_mfma_f32_16x16x32_bf16 v[116:119], a[24:27], a[168:171], v[116:119]
  ds_read_b64_tr_b16 a[80:81], v234 offset:16384
  v_exp_f32 v113, v113
  v_mfma_f32_16x16x32_bf16 v[116:119], a[28:31], a[172:175], v[116:119]
  s_lshr_b32 s83, s97, 4
  ds_read_b64_tr_b16 a[82:83], v234 offset:20480
  v_cvt_pk_bf16_f32 v142, v112, v113
  v_mfma_f32_16x16x32_bf16 v[76:79], a[88:91], v[172:175], v[76:79]
  ds_read_b64_tr_b16 a[84:85], v234 offset:24576
  v_exp_f32 v114, v114
  v_mfma_f32_16x16x32_bf16 v[76:79], a[92:95], v[176:179], v[76:79]
  s_add_u32 s94, s84, s96
  s_add_u32 m0, s62, 49152
  ds_read_b64_tr_b16 a[86:87], v234 offset:28672
  v_mfma_f32_16x16x32_bf16 v[16:19], a[96:99], v[156:159], v[16:19]
  ds_read_b128 a[16:19], v228 offset:20480
  v_exp_f32 v115, v115
  v_mfma_f32_16x16x32_bf16 v[16:19], a[100:103], v[160:163], v[16:19]
  s_max_u32 s94, s94, s83
  ds_read_b128 a[20:23], v229 offset:20480
  v_cvt_pk_bf16_f32 v143, v114, v115
  v_mfma_f32_16x16x32_bf16 v[108:111], a[32:35], a[128:131], v[96:99]
  ds_read_b128 a[24:27], v230 offset:20480
  v_exp_f32 v116, v116
  v_mfma_f32_16x16x32_bf16 v[108:111], a[36:39], a[132:135], v[108:111]
  s_sub_u32 s83, s84, s66
  buffer_load_dwordx4 v182, s[52:55], s72 offen offset:2048 lds
  v_mfma_f32_16x16x32_bf16 v[108:111], a[40:43], a[136:139], v[108:111]
  ds_read_b128 a[28:31], v231 offset:20480
  v_exp_f32 v117, v117
  v_mfma_f32_16x16x32_bf16 v[108:111], a[44:47], a[140:143], v[108:111]
  s_min_u32 s83, s83, 200
  ds_read_b64_tr_b16 a[88:89], v235 offset:16384
  v_cvt_pk_bf16_f32 v150, v116, v117
  v_mfma_f32_16x16x32_bf16 v[48:51], a[96:99], v[164:167], v[48:51]
  ds_read_b64_tr_b16 a[90:91], v235 offset:20480
  v_exp_f32 v118, v118
  v_mfma_f32_16x16x32_bf16 v[48:51], a[100:103], v[168:171], v[48:51]
  s_add_u32 s65, s65, s83
  s_add_u32 s68, s60, 196608
  ds_read_b64_tr_b16 a[202:203], v186 offset:18432
  v_mfma_f32_16x16x32_bf16 v[80:83], a[96:99], v[172:175], v[80:83]
  v_exp_f32 v119, v119
  v_mfma_f32_16x16x32_bf16 v[80:83], a[100:103], v[176:179], v[80:83]
  s_lshr_b32 s65, s65, 1
  v_cvt_pk_bf16_f32 v151, v118, v119
  v_mfma_f32_16x16x32_bf16 v[20:23], a[104:107], v[156:159], v[20:23]
  s_add_u32 s83, s65, s84
  s_add_u32 m0, s62, 0
  v_mfma_f32_16x16x32_bf16 v[20:23], a[108:111], v[160:163], v[20:23]
  s_sub_u32 s83, s83, 28
  buffer_load_dwordx4 v244, s[44:47], s68 offen offset:0 nt lds
  v_mfma_f32_16x16x32_bf16 v[112:115], a[32:35], a[144:147], v[100:103]
  v_exp_f32 v108, v108
  v_mfma_f32_16x16x32_bf16 v[112:115], a[36:39], a[148:151], v[112:115]
  s_max_u32 s94, s94, s83
  ds_read_b64_tr_b16 a[92:93], v235 offset:24576
  ds_read_b64_tr_b16 a[204:205], v187 offset:16384
  v_mfma_f32_16x16x32_bf16 v[112:115], a[40:43], a[152:155], v[112:115]
  v_exp_f32 v109, v109
  v_mfma_f32_16x16x32_bf16 v[112:115], a[44:47], a[156:159], v[112:115]
  s_mov_b32 s66, s84
  v_cvt_pk_bf16_f32 v136, v108, v109
  v_mfma_f32_16x16x32_bf16 v[52:55], a[104:107], v[164:167], v[52:55]
  v_exp_f32 v110, v110
  v_mfma_f32_16x16x32_bf16 v[52:55], a[108:111], v[168:171], v[52:55]
  buffer_load_dwordx4 v245, s[44:47], s68 offen offset:1024 nt lds
  v_mfma_f32_16x16x32_bf16 v[84:87], a[104:107], v[172:175], v[84:87]
  v_exp_f32 v111, v111
  v_mfma_f32_16x16x32_bf16 v[84:87], a[108:111], v[176:179], v[84:87]
  ds_read_b64_tr_b16 a[94:95], v235 offset:28672
  ds_read_b64_tr_b16 a[206:207], v187 offset:18432
  v_cvt_pk_bf16_f32 v137, v110, v111
  v_mfma_f32_16x16x32_bf16 v[116:119], a[32:35], a[160:163], v[104:107]
  v_exp_f32 v112, v112
  v_mfma_f32_16x16x32_bf16 v[116:119], a[36:39], a[164:167], v[116:119]
  buffer_load_dwordx4 v244, s[44:47], s68 offen offset:2048 nt lds
  v_mfma_f32_16x16x32_bf16 v[116:119], a[40:43], a[168:171], v[116:119]
  v_exp_f32 v113, v113
  v_mfma_f32_16x16x32_bf16 v[116:119], a[44:47], a[172:175], v[116:119]
  ds_read_b64_tr_b16 a[96:97], v236 offset:16384
  v_cvt_pk_bf16_f32 v144, v112, v113
  v_mfma_f32_16x16x32_bf16 v[24:27], a[112:115], v[156:159], v[24:27]
  v_exp_f32 v114, v114
  v_mfma_f32_16x16x32_bf16 v[24:27], a[116:119], v[160:163], v[24:27]
  ds_read_b64_tr_b16 a[98:99], v236 offset:20480
  ds_read_b64_tr_b16 a[100:101], v236 offset:24576
  v_mfma_f32_16x16x32_bf16 v[56:59], a[112:115], v[164:167], v[56:59]
  v_exp_f32 v115, v115
  v_mfma_f32_16x16x32_bf16 v[56:59], a[116:119], v[168:171], v[56:59]
  ds_read_b64_tr_b16 a[102:103], v236 offset:28672
  v_cvt_pk_bf16_f32 v145, v114, v115
  v_mfma_f32_16x16x32_bf16 v[108:111], a[48:51], a[128:131], v[96:99]
  v_exp_f32 v116, v116
  v_mfma_f32_16x16x32_bf16 v[108:111], a[52:55], a[132:135], v[108:111]
  buffer_load_dwordx4 v245, s[44:47], s68 offen offset:3072 nt lds
  v_mfma_f32_16x16x32_bf16 v[108:111], a[56:59], a[136:139], v[108:111]
  v_exp_f32 v117, v117
  v_mfma_f32_16x16x32_bf16 v[108:111], a[60:63], a[140:143], v[108:111]
  v_cvt_pk_bf16_f32 v152, v116, v117
  s_getpc_b64 s[48:49]
RW9_%=:
  s_and_b32 s49, s49, 0xffff
  s_mov_b32 s50, CEND%=-RW9_%=
  s_mov_b32 s51, 0x20000
  s_lshl_b32 s70, s28, 2
  s_add_u32 s70, s70, s64
  s_lshl_b32 s70, s70, 10
  v_mfma_f32_16x16x32_bf16 v[88:91], a[112:115], v[172:175], v[88:91]
  v_exp_f32 v118, v118
  v_mfma_f32_16x16x32_bf16 v[88:91], a[116:119], v[176:179], v[88:91]
  ds_read_b64_tr_b16 a[104:105], v237 offset:16384
  ds_read_b64_tr_b16 a[106:107], v237 offset:20480
  v_mfma_f32_16x16x32_bf16 v[28:31], a[120:123], v[156:159], v[28:31]
  v_exp_f32 v119, v119
  v_mfma_f32_16x16x32_bf16 v[28:31], a[124:127], v[160:163], v[28:31]
  ds_read_b64_tr_b16 a[108:109], v237 offset:24576
  v_cvt_pk_bf16_f32 v153, v118, v119
  v_mfma_f32_16x16x32_bf16 v[60:63], a[120:123], v[164:167], v[60:63]
  buffer_load_dwordx4 v[224:227], v248, s[48:51], s70 offen offset:0
  v_mfma_f32_16x16x32_bf16 v[60:63], a[124:127], v[168:171], v[60:63]
  ds_read_b64_tr_b16 a[110:111], v237 offset:28672
  ds_read_b128 a[32:35], v228 offset:24576
  v_mfma_f32_16x16x32_bf16 v[112:115], a[48:51], a[144:147], v[100:103]
  v_exp_f32 v108, v108
  v_mfma_f32_16x16x32_bf16 v[112:115], a[52:55], a[148:151], v[112:115]
  ds_read_b128 a[36:39], v229 offset:24576
  ds_read_b128 a[40:43], v230 offset:24576
  v_mfma_f32_16x16x32_bf16 v[112:115], a[56:59], a[152:155], v[112:115]
  v_exp_f32 v109, v109
  v_mfma_f32_16x16x32_bf16 v[112:115], a[60:63], a[156:159], v[112:115]
  s_add_u32 m0, s62, 49152
  v_cvt_pk_bf16_f32 v138, v108, v109
  v_mfma_f32_16x16x32_bf16 v[92:95], a[120:123], v[172:175], v[92:95]
  v_exp_f32 v110, v110
  v_mfma_f32_16x16x32_bf16 v[92:95], a[124:127], v[176:179], v[92:95]
  ds_read_b128 a[44:47], v231 offset:24576
  ds_read_b64_tr_b16 a[112:113], v238 offset:16384
  v_mfma_f32_16x16x32_bf16 v[120:123], a[240:243], v[156:159], v[120:123]
  v_exp_f32 v111, v111
  v_mfma_f32_16x16x32_bf16 v[120:123], a[240:243], v[160:163], v[120:123]
  ds_read_b64_tr_b16 a[114:115], v238 offset:20480
  v_cvt_pk_bf16_f32 v139, v110, v111
  v_mfma_f32_16x16x32_bf16 v[116:119], a[48:51], a[160:163], v[104:107]
  v_exp_f32 v112, v112
  v_mfma_f32_16x16x32_bf16 v[116:119], a[52:55], a[164:167], v[116:119]
  buffer_load_dwordx4 v183, s[52:55], s72 offen offset:3072 lds
  v_mfma_f32_16x16x32_bf16 v[116:119], a[56:59], a[168:171], v[116:119]
  v_exp_f32 v113, v113
  v_mfma_f32_16x16x32_bf16 v[116:119], a[60:63], a[172:175], v[116:119]
  ds_read_b64_tr_b16 a[116:117], v238 offset:24576
  v_cvt_pk_bf16_f32 v146, v112, v113
  v_mfma_f32_16x16x32_bf16 v[124:127], a[240:243], v[164:167], v[124:127]
  v_exp_f32 v114, v114
  v_mfma_f32_16x16x32_bf16 v[124:127], a[240:243], v[168:171], v[124:127]
  ds_read_b64_tr_b16 a[118:119], v238 offset:28672
  ds_read_b64_tr_b16 a[120:121], v239 offset:16384
  ds_read_b64_tr_b16 a[122:123], v239 offset:20480
  v_mfma_f32_16x16x32_bf16 v[128:131], a[240:243], v[172:175], v[128:131]
  ds_read_b64_tr_b16 a[124:125], v239 offset:24576
  ds_read_b64_tr_b16 a[126:127], v239 offset:28672
  v_exp_f32 v115, v115
  v_mfma_f32_16x16x32_bf16 v[128:131], a[240:243], v[176:179], v[128:131]
  v_cvt_pk_bf16_f32 v147, v114, v115
  s_waitcnt lgkmcnt(8)
  v_mfma_f32_16x16x32_bf16 v[108:111], a[0:3], a[128:131], v[96:99]
  v_exp_f32 v116, v116
  v_mfma_f32_16x16x32_bf16 v[108:111], a[4:7], a[132:135], v[108:111]
  s_memrealtime s[82:83]
  ds_read_b128 a[48:51], v228 offset:28672
  v_mfma_f32_16x16x32_bf16 v[108:111], a[8:11], a[136:139], v[108:111]
  v_exp_f32 v117, v117
  v_mfma_f32_16x16x32_bf16 v[108:111], a[12:15], a[140:143], v[108:111]
  ds_read_b128 a[52:55], v229 offset:28672
  v_cvt_pk_bf16_f32 v154, v116, v117
  v_mfma_f32_16x16x32_bf16 v[0:3], a[64:67], v[132:135], v[0:3]
  v_exp_f32 v118, v118
  v_mfma_f32_16x16x32_bf16 v[0:3], a[68:71], v[136:139], v[0:3]
  ds_read_b128 a[56:59], v230 offset:28672
  ds_read_b128 a[60:63], v231 offset:28672
  v_mfma_f32_16x16x32_bf16 v[32:35], a[64:67], v[140:143], v[32:35]
  v_exp_f32 v119, v119
  v_mfma_f32_16x16x32_bf16 v[32:35], a[68:71], v[144:147], v[32:35]
  v_cvt_pk_bf16_f32 v155, v118, v119
  v_mfma_f32_16x16x32_bf16 v[112:115], a[0:3], a[144:147], v[100:103]
  v_exp_f32 v108, v108
  v_mfma_f32_16x16x32_bf16 v[112:115], a[4:7], a[148:151], v[112:115]
  v_mfma_f32_16x16x32_bf16 v[112:115], a[8:11], a[152:155], v[112:115]
  v_exp_f32 v109, v109
  v_mfma_f32_16x16x32_bf16 v[112:115], a[12:15], a[156:159], v[112:115]
  v_cvt_pk_bf16_f32 v156, v108, v109
  v_mfma_f32_16x16x32_bf16 v[64:67], a[64:67], v[148:151], v[64:67]
  v_exp_f32 v110, v110
  v_mfma_f32_16x16x32_bf16 v[64:67], a[68:71], v[152:155], v[64:67]
  s_waitcnt vmcnt(12) lgkmcnt(0)
  s_barrier
  v_mfma_f32_16x16x32_bf16 v[4:7], a[72:75], v[132:135], v[4:7]
  v_exp_f32 v111, v111
  v_mfma_f32_16x16x32_bf16 v[4:7], a[76:79], v[136:139], v[4:7]
  s_sub_u32 s84, s82, s85
  v_cvt_pk_bf16_f32 v157, v110, v111
  v_mfma_f32_16x16x32_bf16 v[116:119], a[0:3], a[160:163], v[104:107]
  v_exp_f32 v112, v112
  v_mfma_f32_16x16x32_bf16 v[116:119], a[4:7], a[164:167], v[116:119]
  s_cmp_lt_u32 s84, s94
  s_cbranch_scc1 PC10_%=
PC10_%=e:
  v_mfma_f32_16x16x32_bf16 v[116:119], a[8:11], a[168:171], v[116:119]
  v_exp_f32 v113, v113
  v_mfma_f32_16x16x32_bf16 v[116:119], a[12:15], a[172:175], v[116:119]
  s_add_u32 s68, s60, 229376
  v_cvt_pk_bf16_f32 v164, v112, v113
  v_mfma_f32_16x16x32_bf16 v[36:39], a[72:75], v[140:143], v[36:39]
  v_exp_f32 v114, v114
  v_mfma_f32_16x16x32_bf16 v[36:39], a[76:79], v[144:147], v[36:39]
  s_add_u32 m0, s61, 16384
  ds_read_b64_tr_b16 a[64:65], v232 offset:32768
  v_mfma_f32_16x16x32_bf16 v[68:71], a[72:75], v[148:151], v[68:71]
  v_exp_f32 v115, v115
  v_mfma_f32_16x16x32_bf16 v[68:71], a[76:79], v[152:155], v[68:71]
  ds_read_b64_tr_b16 a[66:67], v232 offset:36864
  v_cvt_pk_bf16_f32 v165, v114, v115
  v_mfma_f32_16x16x32_bf16 v[8:11], a[80:83], v[132:135], v[8:11]
  buffer_load_dwordx4 v240, s[40:43], s68 offen offset:0 nt lds
  v_mfma_f32_16x16x32_bf16 v[8:11], a[84:87], v[136:139], v[8:11]
  buffer_load_dwordx4 v241, s[40:43], s68 offen offset:1024 nt lds
  v_mfma_f32_16x16x32_bf16 v[108:111], a[16:19], a[128:131], v[96:99]
  v_exp_f32 v116, v116
  v_mfma_f32_16x16x32_bf16 v[108:111], a[20:23], a[132:135], v[108:111]
  ds_read_b64_tr_b16 a[68:69], v232 offset:40960
  ds_read_b64_tr_b16 a[70:71], v232 offset:45056
  v_mfma_f32_16x16x32_bf16 v[108:111], a[24:27], a[136:139], v[108:111]
  v_exp_f32 v117, v117
  v_mfma_f32_16x16x32_bf16 v[108:111], a[28:31], a[140:143], v[108:111]
  ds_read_b128 a[0:3], v228 offset:0
  v_cvt_pk_bf16_f32 v172, v116, v117
  v_mfma_f32_16x16x32_bf16 v[40:43], a[80:83], v[140:143], v[40:43]
  v_exp_f32 v118, v118
  v_mfma_f32_16x16x32_bf16 v[40:43], a[84:87], v[144:147], v[40:43]
  ds_read_b128 a[4:7], v229 offset:0
  ds_read_b128 a[8:11], v230 offset:0
  v_mfma_f32_16x16x32_bf16 v[72:75], a[80:83], v[148:151], v[72:75]
  v_exp_f32 v119, v119
  v_mfma_f32_16x16x32_bf16 v[72:75], a[84:87], v[152:155], v[72:75]
  ds_read_b128 a[12:15], v231 offset:0
  v_cvt_pk_bf16_f32 v173, v118, v119
  v_mfma_f32_16x16x32_bf16 v[112:115], a[16:19], a[144:147], v[100:103]
  v_exp_f32 v108, v108
  v_mfma_f32_16x16x32_bf16 v[112:115], a[20:23], a[148:151], v[112:115]
  buffer_load_dwordx4 v242, s[40:43], s68 offen offset:2048 nt lds
  v_mfma_f32_16x16x32_bf16 v[112:115], a[24:27], a[152:155], v[112:115]
  v_exp_f32 v109, v109
  v_mfma_f32_16x16x32_bf16 v[112:115], a[28:31], a[156:159], v[112:115]
  ds_read_b64_tr_b16 a[72:73], v233 offset:32768
  v_cvt_pk_bf16_f32 v158, v108, v109
  v_mfma_f32_16x16x32_bf16 v[12:15], a[88:91], v[132:135], v[12:15]
  v_exp_f32 v110, v110
  v_mfma_f32_16x16x32_bf16 v[12:15], a[92:95], v[136:139], v[12:15]
  ds_read_b64_tr_b16 a[74:75], v233 offset:36864
  ds_read_b64_tr_b16 a[76:77], v233 offset:40960
  ds_read_b64_tr_b16 a[78:79], v233 offset:45056
  v_mfma_f32_16x16x32_bf16 v[44:47], a[88:91], v[140:143], v[44:47]
  ds_read_b64_tr_b16 a[80:81], v234 offset:32768
  v_exp_f32 v111, v111
  v_mfma_f32_16x16x32_bf16 v[44:47], a[92:95], v[144:147], v[44:47]
  buffer_load_dwordx4 v243, s[40:43], s68 offen offset:3072 nt lds
  v_cvt_pk_bf16_f32 v159, v110, v111
  v_mfma_f32_16x16x32_bf16 v[116:119], a[16:19], a[160:163], v[104:107]
  ds_read_b64_tr_b16 a[82:83], v234 offset:36864
  v_exp_f32 v112, v112
  v_mfma_f32_16x16x32_bf16 v[116:119], a[20:23], a[164:167], v[116:119]
  s_add_u32 s97, s97, s95
  s_add_u32 s72, s93, 3
  s_and_b32 s72, s72, 31
  s_mul_i32 s72, s72, 40960
  ds_read_b64_tr_b16 a[84:85], v234 offset:40960
  v_mfma_f32_16x16x32_bf16 v[116:119], a[24:27], a[168:171], v[116:119]
  ds_read_b64_tr_b16 a[86:87], v234 offset:45056
  v_exp_f32 v113, v113
  v_mfma_f32_16x16x32_bf16 v[116:119], a[28:31], a[172:175], v[116:119]
  s_lshr_b32 s83, s97, 4
  v_cvt_pk_bf16_f32 v166, v112, v113
  v_mfma_f32_16x16x32_bf16 v[76:79], a[88:91], v[148:151], v[76:79]
  v_exp_f32 v114, v114
  v_mfma_f32_16x16x32_bf16 v[76:79], a[92:95], v[152:155], v[76:79]
  s_add_u32 s94, s84, s96
  s_add_u32 m0, s62, 65536
  ds_read_b128 a[16:19], v228 offset:4096
  v_mfma_f32_16x16x32_bf16 v[16:19], a[96:99], v[132:135], v[16:19]
  ds_read_b128 a[20:23], v229 offset:4096
  v_exp_f32 v115, v115
  v_mfma_f32_16x16x32_bf16 v[16:19], a[100:103], v[136:139], v[16:19]
  s_max_u32 s94, s94, s83
  ds_read_b128 a[24:27], v230 offset:4096
  v_cvt_pk_bf16_f32 v167, v114, v115
  v_mfma_f32_16x16x32_bf16 v[108:111], a[32:35], a[128:131], v[96:99]
  ds_read_b128 a[28:31], v231 offset:4096
  v_exp_f32 v116, v116
  v_mfma_f32_16x16x32_bf16 v[108:111], a[36:39], a[132:135], v[108:111]
  s_sub_u32 s83, s84, s66
  buffer_load_dwordx4 v180, s[52:55], s72 offen offset:0 lds
  v_mfma_f32_16x16x32_bf16 v[108:111], a[40:43], a[136:139], v[108:111]
  ds_read_b64_tr_b16 a[88:89], v235 offset:32768
  v_exp_f32 v117, v117
  v_mfma_f32_16x16x32_bf16 v[108:111], a[44:47], a[140:143], v[108:111]
  s_min_u32 s83, s83, 200
  ds_read_b64_tr_b16 a[90:91], v235 offset:36864
  v_cvt_pk_bf16_f32 v174, v116, v117
  v_mfma_f32_16x16x32_bf16 v[48:51], a[96:99], v[140:143], v[48:51]
  ds_read_b64_tr_b16 a[92:93], v235 offset:40960
  v_exp_f32 v118, v118
  v_mfma_f32_16x16x32_bf16 v[48:51], a[100:103], v[144:147], v[48:51]
  s_add_u32 s65, s65, s83
  s_add_u32 s68, s60, 212992
  v_mfma_f32_16x16x32_bf16 v[80:83], a[96:99], v[148:151], v[80:83]
  v_exp_f32 v119, v119
  v_mfma_f32_16x16x32_bf16 v[80:83], a[100:103], v[152:155], v[80:83]
  s_lshr_b32 s65, s65, 1
  v_cvt_pk_bf16_f32 v175, v118, v119
  v_mfma_f32_16x16x32_bf16 v[20:23], a[104:107], v[132:135], v[20:23]
  s_add_u32 s83, s65, s84
  s_add_u32 m0, s62, 16384
  v_mfma_f32_16x16x32_bf16 v[20:23], a[108:111], v[136:139], v[20:23]
  s_sub_u32 s83, s83, 28
  buffer_load_dwordx4 v244, s[44:47], s68 offen offset:0 nt lds
  v_mfma_f32_16x16x32_bf16 v[112:115], a[32:35], a[144:147], v[100:103]
  v_exp_f32 v108, v108
  v_mfma_f32_16x16x32_bf16 v[112:115], a[36:39], a[148:151], v[112:115]
  s_max_u32 s94, s94, s83
  ds_read_b64_tr_b16 a[94:95], v235 offset:45056
  v_mfma_f32_16x16x32_bf16 v[112:115], a[40:43], a[152:155], v[112:115]
  v_exp_f32 v109, v109
  v_mfma_f32_16x16x32_bf16 v[112:115], a[44:47], a[156:159], v[112:115]
  s_mov_b32 s66, s84
  v_cvt_pk_bf16_f32 v160, v108, v109
  v_mfma_f32_16x16x32_bf16 v[52:55], a[104:107], v[140:143], v[52:55]
  v_exp_f32 v110, v110
  v_mfma_f32_16x16x32_bf16 v[52:55], a[108:111], v[144:147], v[52:55]
  ds_read_b64_tr_b16 a[96:97], v236 offset:32768
  ds_read_b64_tr_b16 a[98:99], v236 offset:36864
  v_mfma_f32_16x16x32_bf16 v[84:87], a[104:107], v[148:151], v[84:87]
  v_exp_f32 v111, v111
  v_mfma_f32_16x16x32_bf16 v[84:87], a[108:111], v[152:155], v[84:87]
  ds_read_b64_tr_b16 a[100:101], v236 offset:40960
  v_cvt_pk_bf16_f32 v161, v110, v111
  v_mfma_f32_16x16x32_bf16 v[116:119], a[32:35], a[160:163], v[104:107]
  v_exp_f32 v112, v112
  v_mfma_f32_16x16x32_bf16 v[116:119], a[36:39], a[164:167], v[116:119]
  buffer_load_dwordx4 v245, s[44:47], s68 offen offset:1024 nt lds
  v_mfma_f32_16x16x32_bf16 v[116:119], a[40:43], a[168:171], v[116:119]
  v_exp_f32 v113, v113
  v_mfma_f32_16x16x32_bf16 v[116:119], a[44:47], a[172:175], v[116:119]
  ds_read_b64_tr_b16 a[102:103], v236 offset:45056
  v_cvt_pk_bf16_f32 v168, v112, v113
  v_mfma_f32_16x16x32_bf16 v[24:27], a[112:115], v[132:135], v[24:27]
  v_exp_f32 v114, v114
  v_mfma_f32_16x16x32_bf16 v[24:27], a[116:119], v[136:139], v[24:27]
  ds_read_b64_tr_b16 a[104:105], v237 offset:32768
  ds_read_b64_tr_b16 a[106:107], v237 offset:36864
  v_mfma_f32_16x16x32_bf16 v[56:59], a[112:115], v[140:143], v[56:59]
  v_exp_f32 v115, v115
  v_mfma_f32_16x16x32_bf16 v[56:59], a[116:119], v[144:147], v[56:59]
  ds_read_b64_tr_b16 a[108:109], v237 offset:40960
  v_cvt_pk_bf16_f32 v169, v114, v115
  v_mfma_f32_16x16x32_bf16 v[108:111], a[48:51], a[128:131], v[96:99]
  v_exp_f32 v116, v116
  v_mfma_f32_16x16x32_bf16 v[108:111], a[52:55], a[132:135], v[108:111]
  buffer_load_dwordx4 v244, s[44:47], s68 offen offset:2048 nt lds
  v_mfma_f32_16x16x32_bf16 v[108:111], a[56:59], a[136:139], v[108:111]
  v_exp_f32 v117, v117
  v_mfma_f32_16x16x32_bf16 v[108:111], a[60:63], a[140:143], v[108:111]
  ds_read_b64_tr_b16 a[110:111], v237 offset:45056
  v_cvt_pk_bf16_f32 v176, v116, v117
  v_mfma_f32_16x16x32_bf16 v[88:91], a[112:115], v[148:151], v[88:91]
  v_exp_f32 v118, v118
  v_mfma_f32_16x16x32_bf16 v[88:91], a[116:119], v[152:155], v[88:91]
  ds_read_b128 a[32:35], v228 offset:8192
  ds_read_b128 a[36:39], v229 offset:8192
  v_mfma_f32_16x16x32_bf16 v[28:31], a[120:123], v[132:135], v[28:31]
  v_exp_f32 v119, v119
  v_mfma_f32_16x16x32_bf16 v[28:31], a[124:127], v[136:139], v[28:31]
  ds_read_b128 a[40:43], v230 offset:8192
  v_cvt_pk_bf16_f32 v177, v118, v119
  v_mfma_f32_16x16x32_bf16 v[60:63], a[120:123], v[140:143], v[60:63]
  buffer_load_dwordx4 v245, s[44:47], s68 offen offset:3072 nt lds
  v_mfma_f32_16x16x32_bf16 v[60:63], a[124:127], v[144:147], v[60:63]
  ds_read_b128 a[44:47], v231 offset:8192
  ds_read_b64_tr_b16 a[112:113], v238 offset:32768
  v_mfma_f32_16x16x32_bf16 v[112:115], a[48:51], a[144:147], v[100:103]
  v_exp_f32 v108, v108
  v_mfma_f32_16x16x32_bf16 v[112:115], a[52:55], a[148:151], v[112:115]
  ds_read_b64_tr_b16 a[114:115], v238 offset:36864
  ds_read_b64_tr_b16 a[116:117], v238 offset:40960
  v_mfma_f32_16x16x32_bf16 v[112:115], a[56:59], a[152:155], v[112:115]
  v_exp_f32 v109, v109
  v_mfma_f32_16x16x32_bf16 v[112:115], a[60:63], a[156:159], v[112:115]
  s_add_u32 m0, s62, 65536
  v_cvt_pk_bf16_f32 v162, v108, v109
  v_mfma_f32_16x16x32_bf16 v[92:95], a[120:123], v[148:151], v[92:95]
  v_exp_f32 v110, v110
  v_mfma_f32_16x16x32_bf16 v[92:95], a[124:127], v[152:155], v[92:95]
  buffer_load_dwordx4 v181, s[52:55], s72 offen offset:1024 lds
  v_mfma_f32_16x16x32_bf16 v[120:123], a[240:243], v[132:135], v[120:123]
  v_exp_f32 v111, v111
  v_mfma_f32_16x16x32_bf16 v[120:123], a[240:243], v[136:139], v[120:123]
  ds_read_b64_tr_b16 a[118:119], v238 offset:45056
  v_cvt_pk_bf16_f32 v163, v110, v111
  v_mfma_f32_16x16x32_bf16 v[116:119], a[48:51], a[160:163], v[104:107]
  v_exp_f32 v112, v112
  v_mfma_f32_16x16x32_bf16 v[116:119], a[52:55], a[164:167], v[116:119]
  ds_read_b64_tr_b16 a[120:121], v239 offset:32768
  ds_read_b64_tr_b16 a[122:123], v239 offset:36864
  v_mfma_f32_16x16x32_bf16 v[116:119], a[56:59], a[168:171], v[116:119]
  v_exp_f32 v113, v113
  v_mfma_f32_16x16x32_bf16 v[116:119], a[60:63], a[172:175], v[116:119]
  ds_read_b64_tr_b16 a[124:125], v239 offset:40960
  v_cvt_pk_bf16_f32 v170, v112, v113
  v_mfma_f32_16x16x32_bf16 v[124:127], a[240:243], v[140:143], v[124:127]
  v_exp_f32 v114, v114
  v_mfma_f32_16x16x32_bf16 v[124:127], a[240:243], v[144:147], v[124:127]
  ds_read_b64_tr_b16 a[126:127], v239 offset:45056
  v_mfma_f32_16x16x32_bf16 v[128:131], a[240:243], v[148:151], v[128:131]
  v_exp_f32 v115, v115
  v_mfma_f32_16x16x32_bf16 v[128:131], a[240:243], v[152:155], v[128:131]
  v_cvt_pk_bf16_f32 v171, v114, v115
  s_waitcnt lgkmcnt(8)
  v_mfma_f32_16x16x32_bf16 v[108:111], a[0:3], a[128:131], v[96:99]
  v_exp_f32 v116, v116
  v_mfma_f32_16x16x32_bf16 v[108:111], a[4:7], a[132:135], v[108:111]
  s_memrealtime s[82:83]
  ds_read_b128 a[48:51], v228 offset:12288
  v_mfma_f32_16x16x32_bf16 v[108:111], a[8:11], a[136:139], v[108:111]
  v_exp_f32 v117, v117
  v_mfma_f32_16x16x32_bf16 v[108:111], a[12:15], a[140:143], v[108:111]
  ds_read_b128 a[52:55], v229 offset:12288
  v_cvt_pk_bf16_f32 v178, v116, v117
  v_mfma_f32_16x16x32_bf16 v[0:3], a[64:67], v[156:159], v[0:3]
  v_exp_f32 v118, v118
  v_mfma_f32_16x16x32_bf16 v[0:3], a[68:71], v[160:163], v[0:3]
  ds_read_b128 a[56:59], v230 offset:12288
  ds_read_b128 a[60:63], v231 offset:12288
  v_mfma_f32_16x16x32_bf16 v[32:35], a[64:67], v[164:167], v[32:35]
  v_exp_f32 v119, v119
  v_mfma_f32_16x16x32_bf16 v[32:35], a[68:71], v[168:171], v[32:35]
  v_cvt_pk_bf16_f32 v179, v118, v119
  v_mfma_f32_16x16x32_bf16 v[112:115], a[0:3], a[144:147], v[100:103]
  v_exp_f32 v108, v108
  v_mfma_f32_16x16x32_bf16 v[112:115], a[4:7], a[148:151], v[112:115]
  v_mfma_f32_16x16x32_bf16 v[112:115], a[8:11], a[152:155], v[112:115]
  v_exp_f32 v109, v109
  v_mfma_f32_16x16x32_bf16 v[112:115], a[12:15], a[156:159], v[112:115]
  v_cvt_pk_bf16_f32 v132, v108, v109
  v_mfma_f32_16x16x32_bf16 v[64:67], a[64:67], v[172:175], v[64:67]
  v_exp_f32 v110, v110
  v_mfma_f32_16x16x32_bf16 v[64:67], a[68:71], v[176:179], v[64:67]
  s_waitcnt vmcnt(10) lgkmcnt(0)
  s_barrier
  v_mfma_f32_16x16x32_bf16 v[4:7], a[72:75], v[156:159], v[4:7]
  v_exp_f32 v111, v111
  v_mfma_f32_16x16x32_bf16 v[4:7], a[76:79], v[160:163], v[4:7]
  s_sub_u32 s84, s82, s85
  v_cvt_pk_bf16_f32 v133, v110, v111
  v_mfma_f32_16x16x32_bf16 v[116:119], a[0:3], a[160:163], v[104:107]
  v_exp_f32 v112, v112
  v_mfma_f32_16x16x32_bf16 v[116:119], a[4:7], a[164:167], v[116:119]
  s_cmp_lt_u32 s84, s94
  s_cbranch_scc1 PC11_%=
PC11_%=e:
  v_mfma_f32_16x16x32_bf16 v[116:119], a[8:11], a[168:171], v[116:119]
  v_exp_f32 v113, v113
  v_mfma_f32_16x16x32_bf16 v[116:119], a[12:15], a[172:175], v[116:119]
  s_add_u32 s68, s60, 245760
  ds_read_b64_tr_b16 a[208:209], v184 offset:0
  v_cvt_pk_bf16_f32 v140, v112, v113
  v_mfma_f32_16x16x32_bf16 v[36:39], a[72:75], v[164:167], v[36:39]
  v_exp_f32 v114, v114
  v_mfma_f32_16x16x32_bf16 v[36:39], a[76:79], v[168:171], v[36:39]
  s_add_u32 m0, s61, 0
  ds_read_b64_tr_b16 a[64:65], v232 offset:0
  v_mfma_f32_16x16x32_bf16 v[68:71], a[72:75], v[172:175], v[68:71]
  v_exp_f32 v115, v115
  v_mfma_f32_16x16x32_bf16 v[68:71], a[76:79], v[176:179], v[68:71]
  ds_read_b64_tr_b16 a[66:67], v232 offset:4096
  v_cvt_pk_bf16_f32 v141, v114, v115
  v_mfma_f32_16x16x32_bf16 v[8:11], a[80:83], v[156:159], v[8:11]
  buffer_load_dwordx4 v240, s[40:43], s68 offen offset:0 nt lds
  v_mfma_f32_16x16x32_bf16 v[8:11], a[84:87], v[160:163], v[8:11]
  buffer_load_dwordx4 v241, s[40:43], s68 offen offset:1024 nt lds
  ds_read_b64_tr_b16 a[210:211], v184 offset:2048
  v_mfma_f32_16x16x32_bf16 v[108:111], a[16:19], a[128:131], v[96:99]
  v_exp_f32 v116, v116
  v_mfma_f32_16x16x32_bf16 v[108:111], a[20:23], a[132:135], v[108:111]
  ds_read_b64_tr_b16 a[68:69], v232 offset:8192
  ds_read_b64_tr_b16 a[70:71], v232 offset:12288
  v_mfma_f32_16x16x32_bf16 v[108:111], a[24:27], a[136:139], v[108:111]
  v_exp_f32 v117, v117
  v_mfma_f32_16x16x32_bf16 v[108:111], a[28:31], a[140:143], v[108:111]
  ds_read_b128 a[0:3], v228 offset:49152
  v_cvt_pk_bf16_f32 v148, v116, v117
  v_mfma_f32_16x16x32_bf16 v[40:43], a[80:83], v[164:167], v[40:43]
  v_exp_f32 v118, v118
  v_mfma_f32_16x16x32_bf16 v[40:43], a[84:87], v[168:171], v[40:43]
  ds_read_b128 a[4:7], v229 offset:49152
  ds_read_b128 a[8:11], v230 offset:49152
  ds_read_b64_tr_b16 a[212:213], v185 offset:0
  v_mfma_f32_16x16x32_bf16 v[72:75], a[80:83], v[172:175], v[72:75]
  v_exp_f32 v119, v119
  v_mfma_f32_16x16x32_bf16 v[72:75], a[84:87], v[176:179], v[72:75]
  ds_read_b128 a[12:15], v231 offset:49152
  v_cvt_pk_bf16_f32 v149, v118, v119
  v_mfma_f32_16x16x32_bf16 v[112:115], a[16:19], a[144:147], v[100:103]
  v_exp_f32 v108, v108
  v_mfma_f32_16x16x32_bf16 v[112:115], a[20:23], a[148:151], v[112:115]
  buffer_load_dwordx4 v242, s[40:43], s68 offen offset:2048 nt lds
  v_mfma_f32_16x16x32_bf16 v[112:115], a[24:27], a[152:155], v[112:115]
  v_exp_f32 v109, v109
  v_mfma_f32_16x16x32_bf16 v[112:115], a[28:31], a[156:159], v[112:115]
  ds_read_b64_tr_b16 a[72:73], v233 offset:0
  ds_read_b64_tr_b16 a[214:215], v185 offset:2048
  v_cvt_pk_bf16_f32 v134, v108, v109
  v_mfma_f32_16x16x32_bf16 v[12:15], a[88:91], v[156:159], v[12:15]
  v_exp_f32 v110, v110
  v_mfma_f32_16x16x32_bf16 v[12:15], a[92:95], v[160:163], v[12:15]
  ds_read_b64_tr_b16 a[74:75], v233 offset:4096
  ds_read_b64_tr_b16 a[76:77], v233 offset:8192
  v_mfma_f32_16x16x32_bf16 v[44:47], a[88:91], v[164:167], v[44:47]
  v_exp_f32 v111, v111
  v_mfma_f32_16x16x32_bf16 v[44:47], a[92:95], v[168:171], v[44:47]
  buffer_load_dwordx4 v243, s[40:43], s68 offen offset:3072 nt lds
  v_cvt_pk_bf16_f32 v135, v110, v111
  v_mfma_f32_16x16x32_bf16 v[116:119], a[16:19], a[160:163], v[104:107]
  v_exp_f32 v112, v112
  v_mfma_f32_16x16x32_bf16 v[116:119], a[20:23], a[164:167], v[116:119]
  s_add_u32 s97, s97, s95
  ds_read_b64_tr_b16 a[78:79], v233 offset:12288
  ds_read_b64_tr_b16 a[80:81], v234 offset:0
  ds_read_b64_tr_b16 a[216:217], v186 offset:0
  v_mfma_f32_16x16x32_bf16 v[116:119], a[24:27], a[168:171], v[116:119]
  ds_read_b64_tr_b16 a[82:83], v234 offset:4096
  v_exp_f32 v113, v113
  v_mfma_f32_16x16x32_bf16 v[116:119], a[28:31], a[172:175], v[116:119]
  s_lshr_b32 s83, s97, 4
  ds_read_b64_tr_b16 a[84:85], v234 offset:8192
  v_cvt_pk_bf16_f32 v142, v112, v113
  v_mfma_f32_16x16x32_bf16 v[76:79], a[88:91], v[172:175], v[76:79]
  ds_read_b64_tr_b16 a[86:87], v234 offset:12288
  v_exp_f32 v114, v114
  v_mfma_f32_16x16x32_bf16 v[76:79], a[92:95], v[176:179], v[76:79]
  s_add_u32 s94, s84, s96
  s_add_u32 m0, s62, 65536
  ds_read_b128 a[16:19], v228 offset:53248
  v_mfma_f32_16x16x32_bf16 v[16:19], a[96:99], v[156:159], v[16:19]
  ds_read_b128 a[20:23], v229 offset:53248
  v_exp_f32 v115, v115
  v_mfma_f32_16x16x32_bf16 v[16:19], a[100:103], v[160:163], v[16:19]
  s_max_u32 s94, s94, s83
  ds_read_b128 a[24:27], v230 offset:53248
  v_cvt_pk_bf16_f32 v143, v114, v115
  v_mfma_f32_16x16x32_bf16 v[108:111], a[32:35], a[128:131], v[96:99]
  ds_read_b128 a[28:31], v231 offset:53248
  v_exp_f32 v116, v116
  v_mfma_f32_16x16x32_bf16 v[108:111], a[36:39], a[132:135], v[108:111]
  s_sub_u32 s83, s84, s66
  buffer_load_dwordx4 v182, s[52:55], s72 offen offset:2048 lds
  v_mfma_f32_16x16x32_bf16 v[108:111], a[40:43], a[136:139], v[108:111]
  ds_read_b64_tr_b16 a[88:89], v235 offset:0
  v_exp_f32 v117, v117
  v_mfma_f32_16x16x32_bf16 v[108:111], a[44:47], a[140:143], v[108:111]
  s_min_u32 s83, s83, 200
  ds_read_b64_tr_b16 a[90:91], v235 offset:4096
  v_cvt_pk_bf16_f32 v150, v116, v117
  v_mfma_f32_16x16x32_bf16 v[48:51], a[96:99], v[164:167], v[48:51]
  ds_read_b64_tr_b16 a[92:93], v235 offset:8192
  v_exp_f32 v118, v118
  v_mfma_f32_16x16x32_bf16 v[48:51], a[100:103], v[168:171], v[48:51]
  s_add_u32 s65, s65, s83
  s_add_u32 s68, s60, 229376
  ds_read_b64_tr_b16 a[218:219], v186 offset:2048
  v_mfma_f32_16x16x32_bf16 v[80:83], a[96:99], v[172:175], v[80:83]
  v_exp_f32 v119, v119
  v_mfma_f32_16x16x32_bf16 v[80:83], a[100:103], v[176:179], v[80:83]
  s_lshr_b32 s65, s65, 1
  v_cvt_pk_bf16_f32 v151, v118, v119
  v_mfma_f32_16x16x32_bf16 v[20:23], a[104:107], v[156:159], v[20:23]
  s_add_u32 s83, s65, s84
  s_add_u32 m0, s62, 32768
  v_mfma_f32_16x16x32_bf16 v[20:23], a[108:111], v[160:163], v[20:23]
  s_sub_u32 s83, s83, 28
  buffer_load_dwordx4 v244, s[44:47], s68 offen offset:0 lds
  v_mfma_f32_16x16x32_bf16 v[112:115], a[32:35], a[144:147], v[100:103]
  v_exp_f32 v108, v108
  v_mfma_f32_16x16x32_bf16 v[112:115], a[36:39], a[148:151], v[112:115]
  s_max_u32 s94, s94, s83
  ds_read_b64_tr_b16 a[94:95], v235 offset:12288
  ds_read_b64_tr_b16 a[220:221], v187 offset:0
  v_mfma_f32_16x16x32_bf16 v[112:115], a[40:43], a[152:155], v[112:115]
  v_exp_f32 v109, v109
  v_mfma_f32_16x16x32_bf16 v[112:115], a[44:47], a[156:159], v[112:115]
  s_mov_b32 s66, s84
  v_cvt_pk_bf16_f32 v136, v108, v109
  v_mfma_f32_16x16x32_bf16 v[52:55], a[104:107], v[164:167], v[52:55]
  v_exp_f32 v110, v110
  v_mfma_f32_16x16x32_bf16 v[52:55], a[108:111], v[168:171], v[52:55]
  ds_read_b64_tr_b16 a[96:97], v236 offset:0
  ds_read_b64_tr_b16 a[98:99], v236 offset:4096
  v_mfma_f32_16x16x32_bf16 v[84:87], a[104:107], v[172:175], v[84:87]
  v_exp_f32 v111, v111
  v_mfma_f32_16x16x32_bf16 v[84:87], a[108:111], v[176:179], v[84:87]
  ds_read_b64_tr_b16 a[100:101], v236 offset:8192
  ds_read_b64_tr_b16 a[222:223], v187 offset:2048
  v_cvt_pk_bf16_f32 v137, v110, v111
  v_mfma_f32_16x16x32_bf16 v[116:119], a[32:35], a[160:163], v[104:107]
  v_exp_f32 v112, v112
  v_mfma_f32_16x16x32_bf16 v[116:119], a[36:39], a[164:167], v[116:119]
  buffer_load_dwordx4 v245, s[44:47], s68 offen offset:1024 lds
  v_mfma_f32_16x16x32_bf16 v[116:119], a[40:43], a[168:171], v[116:119]
  v_exp_f32 v113, v113
  v_mfma_f32_16x16x32_bf16 v[116:119], a[44:47], a[172:175], v[116:119]
  ds_read_b64_tr_b16 a[102:103], v236 offset:12288
  v_cvt_pk_bf16_f32 v144, v112, v113
  v_mfma_f32_16x16x32_bf16 v[24:27], a[112:115], v[156:159], v[24:27]
  v_exp_f32 v114, v114
  v_mfma_f32_16x16x32_bf16 v[24:27], a[116:119], v[160:163], v[24:27]
  ds_read_b64_tr_b16 a[104:105], v237 offset:0
  ds_read_b64_tr_b16 a[106:107], v237 offset:4096
  v_mfma_f32_16x16x32_bf16 v[56:59], a[112:115], v[164:167], v[56:59]
  v_exp_f32 v115, v115
  v_mfma_f32_16x16x32_bf16 v[56:59], a[116:119], v[168:171], v[56:59]
  ds_read_b64_tr_b16 a[108:109], v237 offset:8192
  v_cvt_pk_bf16_f32 v145, v114, v115
  v_mfma_f32_16x16x32_bf16 v[108:111], a[48:51], a[128:131], v[96:99]
  v_exp_f32 v116, v116
  v_mfma_f32_16x16x32_bf16 v[108:111], a[52:55], a[132:135], v[108:111]
  buffer_load_dwordx4 v244, s[44:47], s68 offen offset:2048 lds
  v_mfma_f32_16x16x32_bf16 v[108:111], a[56:59], a[136:139], v[108:111]
  v_exp_f32 v117, v117
  v_mfma_f32_16x16x32_bf16 v[108:111], a[60:63], a[140:143], v[108:111]
  ds_read_b64_tr_b16 a[110:111], v237 offset:12288
  v_cvt_pk_bf16_f32 v152, v116, v117
  v_mfma_f32_16x16x32_bf16 v[88:91], a[112:115], v[172:175], v[88:91]
  v_exp_f32 v118, v118
  v_mfma_f32_16x16x32_bf16 v[88:91], a[116:119], v[176:179], v[88:91]
  ds_read_b128 a[32:35], v228 offset:57344
  ds_read_b128 a[36:39], v229 offset:57344
  v_mfma_f32_16x16x32_bf16 v[28:31], a[120:123], v[156:159], v[28:31]
  v_exp_f32 v119, v119
  v_mfma_f32_16x16x32_bf16 v[28:31], a[124:127], v[160:163], v[28:31]
  ds_read_b128 a[40:43], v230 offset:57344
  v_cvt_pk_bf16_f32 v153, v118, v119
  v_mfma_f32_16x16x32_bf16 v[60:63], a[120:123], v[164:167], v[60:63]
  buffer_load_dwordx4 v245, s[44:47], s68 offen offset:3072 lds
  v_mfma_f32_16x16x32_bf16 v[60:63], a[124:127], v[168:171], v[60:63]
  ds_read_b128 a[44:47], v231 offset:57344
  ds_read_b64_tr_b16 a[112:113], v238 offset:0
  v_mfma_f32_16x16x32_bf16 v[112:115], a[48:51], a[144:147], v[100:103]
  v_exp_f32 v108, v108
  v_mfma_f32_16x16x32_bf16 v[112:115], a[52:55], a[148:151], v[112:115]
  ds_read_b64_tr_b16 a[114:115], v238 offset:4096
  ds_read_b64_tr_b16 a[116:117], v238 offset:8192
  v_mfma_f32_16x16x32_bf16 v[112:115], a[56:59], a[152:155], v[112:115]
  v_exp_f32 v109, v109
  v_mfma_f32_16x16x32_bf16 v[112:115], a[60:63], a[156:159], v[112:115]
  s_add_u32 m0, s62, 65536
  v_cvt_pk_bf16_f32 v138, v108, v109
  v_mfma_f32_16x16x32_bf16 v[92:95], a[120:123], v[172:175], v[92:95]
  v_exp_f32 v110, v110
  v_mfma_f32_16x16x32_bf16 v[92:95], a[124:127], v[176:179], v[92:95]
  buffer_load_dwordx4 v183, s[52:55], s72 offen offset:3072 lds
  v_mfma_f32_16x16x32_bf16 v[120:123], a[240:243], v[156:159], v[120:123]
  v_exp_f32 v111, v111
  v_mfma_f32_16x16x32_bf16 v[120:123], a[240:243], v[160:163], v[120:123]
  ds_read_b64_tr_b16 a[118:119], v238 offset:12288
  v_cvt_pk_bf16_f32 v139, v110, v111
  v_mfma_f32_16x16x32_bf16 v[116:119], a[48:51], a[160:163], v[104:107]
  v_exp_f32 v112, v112
  v_mfma_f32_16x16x32_bf16 v[116:119], a[52:55], a[164:167], v[116:119]
  ds_read_b64_tr_b16 a[120:121], v239 offset:0
  ds_read_b64_tr_b16 a[122:123], v239 offset:4096
  v_mfma_f32_16x16x32_bf16 v[116:119], a[56:59], a[168:171], v[116:119]
  v_exp_f32 v113, v113
  v_mfma_f32_16x16x32_bf16 v[116:119], a[60:63], a[172:175], v[116:119]
  ds_read_b64_tr_b16 a[124:125], v239 offset:8192
  v_cvt_pk_bf16_f32 v146, v112, v113
  v_mfma_f32_16x16x32_bf16 v[124:127], a[240:243], v[164:167], v[124:127]
  v_exp_f32 v114, v114
  v_mfma_f32_16x16x32_bf16 v[124:127], a[240:243], v[168:171], v[124:127]
  ds_read_b64_tr_b16 a[126:127], v239 offset:12288
  v_mfma_f32_16x16x32_bf16 v[128:131], a[240:243], v[172:175], v[128:131]
  v_exp_f32 v115, v115
  v_mfma_f32_16x16x32_bf16 v[128:131], a[240:243], v[176:179], v[128:131]
  v_cvt_pk_bf16_f32 v147, v114, v115
  s_waitcnt lgkmcnt(8)
  v_mfma_f32_16x16x32_bf16 v[108:111], a[0:3], a[128:131], v[96:99]
  v_exp_f32 v116, v116
  v_mfma_f32_16x16x32_bf16 v[108:111], a[4:7], a[132:135], v[108:111]
  s_memrealtime s[82:83]
  ds_read_b128 a[48:51], v228 offset:61440
  v_mfma_f32_16x16x32_bf16 v[108:111], a[8:11], a[136:139], v[108:111]
  v_exp_f32 v117, v117
  v_mfma_f32_16x16x32_bf16 v[108:111], a[12:15], a[140:143], v[108:111]
  ds_read_b128 a[52:55], v229 offset:61440
  v_cvt_pk_bf16_f32 v154, v116, v117
  v_mfma_f32_16x16x32_bf16 v[0:3], a[64:67], v[132:135], v[0:3]
  v_exp_f32 v118, v118
  v_mfma_f32_16x16x32_bf16 v[0:3], a[68:71], v[136:139], v[0:3]
  ds_read_b128 a[56:59], v230 offset:61440
  ds_read_b128 a[60:63], v231 offset:61440
  v_mfma_f32_16x16x32_bf16 v[32:35], a[64:67], v[140:143], v[32:35]
  v_exp_f32 v119, v119
  v_mfma_f32_16x16x32_bf16 v[32:35], a[68:71], v[144:147], v[32:35]
  v_cvt_pk_bf16_f32 v155, v118, v119
  v_mfma_f32_16x16x32_bf16 v[112:115], a[0:3], a[144:147], v[100:103]
  v_exp_f32 v108, v108
  v_mfma_f32_16x16x32_bf16 v[112:115], a[4:7], a[148:151], v[112:115]
  v_mfma_f32_16x16x32_bf16 v[112:115], a[8:11], a[152:155], v[112:115]
  v_exp_f32 v109, v109
  v_mfma_f32_16x16x32_bf16 v[112:115], a[12:15], a[156:159], v[112:115]
  v_cvt_pk_bf16_f32 v156, v108, v109
  v_mfma_f32_16x16x32_bf16 v[64:67], a[64:67], v[148:151], v[64:67]
  v_exp_f32 v110, v110
  v_mfma_f32_16x16x32_bf16 v[64:67], a[68:71], v[152:155], v[64:67]
  s_waitcnt vmcnt(11) lgkmcnt(0)
  s_barrier
  v_mfma_f32_16x16x32_bf16 v[4:7], a[72:75], v[132:135], v[4:7]
  v_exp_f32 v111, v111
  v_mfma_f32_16x16x32_bf16 v[4:7], a[76:79], v[136:139], v[4:7]
  s_sub_u32 s84, s82, s85
  v_cvt_pk_bf16_f32 v157, v110, v111
  v_mfma_f32_16x16x32_bf16 v[116:119], a[0:3], a[160:163], v[104:107]
  v_exp_f32 v112, v112
  v_mfma_f32_16x16x32_bf16 v[116:119], a[4:7], a[164:167], v[116:119]
  s_cmp_lt_u32 s84, s94
  s_cbranch_scc1 PC12_%=
PC12_%=e:
  v_mfma_f32_16x16x32_bf16 v[116:119], a[8:11], a[168:171], v[116:119]
  v_exp_f32 v113, v113
  v_mfma_f32_16x16x32_bf16 v[116:119], a[12:15], a[172:175], v[116:119]
  s_add_u32 s68, s60, 262144
  v_cvt_pk_bf16_f32 v164, v112, v113
  v_mfma_f32_16x16x32_bf16 v[36:39], a[72:75], v[140:143], v[36:39]
  v_exp_f32 v114, v114
  v_mfma_f32_16x16x32_bf16 v[36:39], a[76:79], v[144:147], v[36:39]
  s_add_u32 m0, s61, 49152
  ds_read_b64_tr_b16 a[64:65], v232 offset:16384
  v_mfma_f32_16x16x32_bf16 v[68:71], a[72:75], v[148:151], v[68:71]
  v_exp_f32 v115, v115
  v_mfma_f32_16x16x32_bf16 v[68:71], a[76:79], v[152:155], v[68:71]
  ds_read_b64_tr_b16 a[66:67], v232 offset:20480
  v_cvt_pk_bf16_f32 v165, v114, v115
  v_mfma_f32_16x16x32_bf16 v[8:11], a[80:83], v[132:135], v[8:11]
  buffer_load_dwordx4 v240, s[40:43], s68 offen offset:0 nt lds
  v_mfma_f32_16x16x32_bf16 v[8:11], a[84:87], v[136:139], v[8:11]
  buffer_load_dwordx4 v241, s[40:43], s68 offen offset:1024 nt lds
  v_mfma_f32_16x16x32_bf16 v[108:111], a[16:19], a[128:131], v[96:99]
  v_exp_f32 v116, v116
  v_mfma_f32_16x16x32_bf16 v[108:111], a[20:23], a[132:135], v[108:111]
  ds_read_b64_tr_b16 a[68:69], v232 offset:24576
  ds_read_b64_tr_b16 a[70:71], v232 offset:28672
  v_mfma_f32_16x16x32_bf16 v[108:111], a[24:27], a[136:139], v[108:111]
  v_exp_f32 v117, v117
  v_mfma_f32_16x16x32_bf16 v[108:111], a[28:31], a[140:143], v[108:111]
  ds_read_b128 a[0:3], v228 offset:16384
  v_cvt_pk_bf16_f32 v172, v116, v117
  v_mfma_f32_16x16x32_bf16 v[40:43], a[80:83], v[140:143], v[40:43]
  v_exp_f32 v118, v118
  v_mfma_f32_16x16x32_bf16 v[40:43], a[84:87], v[144:147], v[40:43]
  ds_read_b128 a[4:7], v229 offset:16384
  ds_read_b128 a[8:11], v230 offset:16384
  v_mfma_f32_16x16x32_bf16 v[72:75], a[80:83], v[148:151], v[72:75]
  v_exp_f32 v119, v119
  v_mfma_f32_16x16x32_bf16 v[72:75], a[84:87], v[152:155], v[72:75]
  ds_read_b128 a[12:15], v231 offset:16384
  v_cvt_pk_bf16_f32 v173, v118, v119
  v_mfma_f32_16x16x32_bf16 v[112:115], a[16:19], a[144:147], v[100:103]
  v_exp_f32 v108, v108
  v_mfma_f32_16x16x32_bf16 v[112:115], a[20:23], a[148:151], v[112:115]
  buffer_load_dwordx4 v242, s[40:43], s68 offen offset:2048 nt lds
  v_mfma_f32_16x16x32_bf16 v[112:115], a[24:27], a[152:155], v[112:115]
  v_exp_f32 v109, v109
  v_mfma_f32_16x16x32_bf16 v[112:115], a[28:31], a[156:159], v[112:115]
  ds_read_b64_tr_b16 a[72:73], v233 offset:16384
  v_cvt_pk_bf16_f32 v158, v108, v109
  v_mfma_f32_16x16x32_bf16 v[12:15], a[88:91], v[132:135], v[12:15]
  v_exp_f32 v110, v110
  v_mfma_f32_16x16x32_bf16 v[12:15], a[92:95], v[136:139], v[12:15]
  ds_read_b64_tr_b16 a[74:75], v233 offset:20480
  ds_read_b64_tr_b16 a[76:77], v233 offset:24576
  ds_read_b64_tr_b16 a[78:79], v233 offset:28672
  v_mfma_f32_16x16x32_bf16 v[44:47], a[88:91], v[140:143], v[44:47]
  ds_read_b64_tr_b16 a[80:81], v234 offset:16384
  v_exp_f32 v111, v111
  v_mfma_f32_16x16x32_bf16 v[44:47], a[92:95], v[144:147], v[44:47]
  buffer_load_dwordx4 v243, s[40:43], s68 offen offset:3072 nt lds
  v_cvt_pk_bf16_f32 v159, v110, v111
  v_mfma_f32_16x16x32_bf16 v[116:119], a[16:19], a[160:163], v[104:107]
  ds_read_b64_tr_b16 a[82:83], v234 offset:20480
  v_exp_f32 v112, v112
  v_mfma_f32_16x16x32_bf16 v[116:119], a[20:23], a[164:167], v[116:119]
  s_add_u32 s97, s97, s95
  s_add_u32 s72, s93, 4
  s_and_b32 s72, s72, 31
  s_mul_i32 s72, s72, 40960
  ds_read_b64_tr_b16 a[84:85], v234 offset:24576
  v_mfma_f32_16x16x32_bf16 v[116:119], a[24:27], a[168:171], v[116:119]
  ds_read_b64_tr_b16 a[86:87], v234 offset:28672
  v_exp_f32 v113, v113
  v_mfma_f32_16x16x32_bf16 v[116:119], a[28:31], a[172:175], v[116:119]
  s_lshr_b32 s83, s97, 4
  v_cvt_pk_bf16_f32 v166, v112, v113
  v_mfma_f32_16x16x32_bf16 v[76:79], a[88:91], v[148:151], v[76:79]
  v_exp_f32 v114, v114
  v_mfma_f32_16x16x32_bf16 v[76:79], a[92:95], v[152:155], v[76:79]
  s_add_u32 s94, s84, s96
  s_add_u32 m0, s62, 49152
  ds_read_b128 a[16:19], v228 offset:20480
  v_mfma_f32_16x16x32_bf16 v[16:19], a[96:99], v[132:135], v[16:19]
  ds_read_b128 a[20:23], v229 offset:20480
  v_exp_f32 v115, v115
  v_mfma_f32_16x16x32_bf16 v[16:19], a[100:103], v[136:139], v[16:19]
  s_max_u32 s94, s94, s83
  ds_read_b128 a[24:27], v230 offset:20480
  v_cvt_pk_bf16_f32 v167, v114, v115
  v_mfma_f32_16x16x32_bf16 v[108:111], a[32:35], a[128:131], v[96:99]
  ds_read_b128 a[28:31], v231 offset:20480
  v_exp_f32 v116, v116
  v_mfma_f32_16x16x32_bf16 v[108:111], a[36:39], a[132:135], v[108:111]
  s_sub_u32 s83, s84, s66
  buffer_load_dwordx4 v180, s[52:55], s72 offen offset:0 lds
  v_mfma_f32_16x16x32_bf16 v[108:111], a[40:43], a[136:139], v[108:111]
  ds_read_b64_tr_b16 a[88:89], v235 offset:16384
  v_exp_f32 v117, v117
  v_mfma_f32_16x16x32_bf16 v[108:111], a[44:47], a[140:143], v[108:111]
  s_min_u32 s83, s83, 200
  ds_read_b64_tr_b16 a[90:91], v235 offset:20480
  v_cvt_pk_bf16_f32 v174, v116, v117
  v_mfma_f32_16x16x32_bf16 v[48:51], a[96:99], v[140:143], v[48:51]
  ds_read_b64_tr_b16 a[92:93], v235 offset:24576
  v_exp_f32 v118, v118
  v_mfma_f32_16x16x32_bf16 v[48:51], a[100:103], v[144:147], v[48:51]
  s_add_u32 s65, s65, s83
  s_add_u32 s68, s60, 245760
  v_mfma_f32_16x16x32_bf16 v[80:83], a[96:99], v[148:151], v[80:83]
  v_exp_f32 v119, v119
  v_mfma_f32_16x16x32_bf16 v[80:83], a[100:103], v[152:155], v[80:83]
  s_lshr_b32 s65, s65, 1
  v_cvt_pk_bf16_f32 v175, v118, v119
  v_mfma_f32_16x16x32_bf16 v[20:23], a[104:107], v[132:135], v[20:23]
  s_add_u32 s83, s65, s84
  s_add_u32 m0, s62, 0
  v_mfma_f32_16x16x32_bf16 v[20:23], a[108:111], v[136:139], v[20:23]
  s_sub_u32 s83, s83, 28
  buffer_load_dwordx4 v244, s[44:47], s68 offen offset:0 nt lds
  v_mfma_f32_16x16x32_bf16 v[112:115], a[32:35], a[144:147], v[100:103]
  v_exp_f32 v108, v108
  v_mfma_f32_16x16x32_bf16 v[112:115], a[36:39], a[148:151], v[112:115]
  s_max_u32 s94, s94, s83
  ds_read_b64_tr_b16 a[94:95], v235 offset:28672
  v_mfma_f32_16x16x32_bf16 v[112:115], a[40:43], a[152:155], v[112:115]
  v_exp_f32 v109, v109
  v_mfma_f32_16x16x32_bf16 v[112:115], a[44:47], a[156:159], v[112:115]
  s_mov_b32 s66, s84
  v_cvt_pk_bf16_f32 v160, v108, v109
  v_mfma_f32_16x16x32_bf16 v[52:55], a[104:107], v[140:143], v[52:55]
  v_exp_f32 v110, v110
  v_mfma_f32_16x16x32_bf16 v[52:55], a[108:111], v[144:147], v[52:55]
  ds_read_b64_tr_b16 a[96:97], v236 offset:16384
  ds_read_b64_tr_b16 a[98:99], v236 offset:20480
  v_mfma_f32_16x16x32_bf16 v[84:87], a[104:107], v[148:151], v[84:87]
  v_exp_f32 v111, v111
  v_mfma_f32_16x16x32_bf16 v[84:87], a[108:111], v[152:155], v[84:87]
  ds_read_b64_tr_b16 a[100:101], v236 offset:24576
  v_cvt_pk_bf16_f32 v161, v110, v111
  v_mfma_f32_16x16x32_bf16 v[116:119], a[32:35], a[160:163], v[104:107]
  v_exp_f32 v112, v112
  v_mfma_f32_16x16x32_bf16 v[116:119], a[36:39], a[164:167], v[116:119]
  buffer_load_dwordx4 v245, s[44:47], s68 offen offset:1024 nt lds
  v_mfma_f32_16x16x32_bf16 v[116:119], a[40:43], a[168:171], v[116:119]
  v_exp_f32 v113, v113
  v_mfma_f32_16x16x32_bf16 v[116:119], a[44:47], a[172:175], v[116:119]
  ds_read_b64_tr_b16 a[102:103], v236 offset:28672
  v_cvt_pk_bf16_f32 v168, v112, v113
  v_mfma_f32_16x16x32_bf16 v[24:27], a[112:115], v[132:135], v[24:27]
  v_exp_f32 v114, v114
  v_mfma_f32_16x16x32_bf16 v[24:27], a[116:119], v[136:139], v[24:27]
  ds_read_b64_tr_b16 a[104:105], v237 offset:16384
  ds_read_b64_tr_b16 a[106:107], v237 offset:20480
  v_mfma_f32_16x16x32_bf16 v[56:59], a[112:115], v[140:143], v[56:59]
  v_exp_f32 v115, v115
  v_mfma_f32_16x16x32_bf16 v[56:59], a[116:119], v[144:147], v[56:59]
  ds_read_b64_tr_b16 a[108:109], v237 offset:24576
  v_cvt_pk_bf16_f32 v169, v114, v115
  v_mfma_f32_16x16x32_bf16 v[108:111], a[48:51], a[128:131], v[96:99]
  v_exp_f32 v116, v116
  v_mfma_f32_16x16x32_bf16 v[108:111], a[52:55], a[132:135], v[108:111]
  buffer_load_dwordx4 v244, s[44:47], s68 offen offset:2048 nt lds
  v_mfma_f32_16x16x32_bf16 v[108:111], a[56:59], a[136:139], v[108:111]
  v_exp_f32 v117, v117
  v_mfma_f32_16x16x32_bf16 v[108:111], a[60:63], a[140:143], v[108:111]
  ds_read_b64_tr_b16 a[110:111], v237 offset:28672
  v_cvt_pk_bf16_f32 v176, v116, v117
  v_mfma_f32_16x16x32_bf16 v[88:91], a[112:115], v[148:151], v[88:91]
  v_exp_f32 v118, v118
  v_mfma_f32_16x16x32_bf16 v[88:91], a[116:119], v[152:155], v[88:91]
  ds_read_b128 a[32:35], v228 offset:24576
  ds_read_b128 a[36:39], v229 offset:24576
  v_mfma_f32_16x16x32_bf16 v[28:31], a[120:123], v[132:135], v[28:31]
  v_exp_f32 v119, v119
  v_mfma_f32_16x16x32_bf16 v[28:31], a[124:127], v[136:139], v[28:31]
  ds_read_b128 a[40:43], v230 offset:24576
  v_cvt_pk_bf16_f32 v177, v118, v119
  v_mfma_f32_16x16x32_bf16 v[60:63], a[120:123], v[140:143], v[60:63]
  buffer_load_dwordx4 v245, s[44:47], s68 offen offset:3072 nt lds
  v_mfma_f32_16x16x32_bf16 v[60:63], a[124:127], v[144:147], v[60:63]
  ds_read_b128 a[44:47], v231 offset:24576
  ds_read_b64_tr_b16 a[112:113], v238 offset:16384
  v_mfma_f32_16x16x32_bf16 v[112:115], a[48:51], a[144:147], v[100:103]
  v_exp_f32 v108, v108
  v_mfma_f32_16x16x32_bf16 v[112:115], a[52:55], a[148:151], v[112:115]
  ds_read_b64_tr_b16 a[114:115], v238 offset:20480
  ds_read_b64_tr_b16 a[116:117], v238 offset:24576
  v_mfma_f32_16x16x32_bf16 v[112:115], a[56:59], a[152:155], v[112:115]
  v_exp_f32 v109, v109
  v_mfma_f32_16x16x32_bf16 v[112:115], a[60:63], a[156:159], v[112:115]
  s_add_u32 m0, s62, 49152
  v_cvt_pk_bf16_f32 v162, v108, v109
  v_mfma_f32_16x16x32_bf16 v[92:95], a[120:123], v[148:151], v[92:95]
  v_exp_f32 v110, v110
  v_mfma_f32_16x16x32_bf16 v[92:95], a[124:127], v[152:155], v[92:95]
  buffer_load_dwordx4 v181, s[52:55], s72 offen offset:1024 lds
  v_mfma_f32_16x16x32_bf16 v[120:123], a[240:243], v[132:135], v[120:123]
  v_exp_f32 v111, v111
  v_mfma_f32_16x16x32_bf16 v[120:123], a[240:243], v[136:139], v[120:123]
  ds_read_b64_tr_b16 a[118:119], v238 offset:28672
  v_cvt_pk_bf16_f32 v163, v110, v111
  v_mfma_f32_16x16x32_bf16 v[116:119], a[48:51], a[160:163], v[104:107]
  v_exp_f32 v112, v112
  v_mfma_f32_16x16x32_bf16 v[116:119], a[52:55], a[164:167], v[116:119]
  ds_read_b64_tr_b16 a[120:121], v239 offset:16384
  ds_read_b64_tr_b16 a[122:123], v239 offset:20480
  v_mfma_f32_16x16x32_bf16 v[116:119], a[56:59], a[168:171], v[116:119]
  v_exp_f32 v113, v113
  v_mfma_f32_16x16x32_bf16 v[116:119], a[60:63], a[172:175], v[116:119]
  ds_read_b64_tr_b16 a[124:125], v239 offset:24576
  v_cvt_pk_bf16_f32 v170, v112, v113
  v_mfma_f32_16x16x32_bf16 v[124:127], a[240:243], v[140:143], v[124:127]
  v_exp_f32 v114, v114
  v_mfma_f32_16x16x32_bf16 v[124:127], a[240:243], v[144:147], v[124:127]
  ds_read_b64_tr_b16 a[126:127], v239 offset:28672
  v_mfma_f32_16x16x32_bf16 v[128:131], a[240:243], v[148:151], v[128:131]
  v_exp_f32 v115, v115
  v_mfma_f32_16x16x32_bf16 v[128:131], a[240:243], v[152:155], v[128:131]
  v_cvt_pk_bf16_f32 v171, v114, v115
  s_waitcnt lgkmcnt(8)
  v_mfma_f32_16x16x32_bf16 v[108:111], a[0:3], a[128:131], v[96:99]
  v_exp_f32 v116, v116
  v_mfma_f32_16x16x32_bf16 v[108:111], a[4:7], a[132:135], v[108:111]
  s_memrealtime s[82:83]
  ds_read_b128 a[48:51], v228 offset:28672
  v_mfma_f32_16x16x32_bf16 v[108:111], a[8:11], a[136:139], v[108:111]
  v_exp_f32 v117, v117
  v_mfma_f32_16x16x32_bf16 v[108:111], a[12:15], a[140:143], v[108:111]
  ds_read_b128 a[52:55], v229 offset:28672
  v_cvt_pk_bf16_f32 v178, v116, v117
  v_mfma_f32_16x16x32_bf16 v[0:3], a[64:67], v[156:159], v[0:3]
  v_exp_f32 v118, v118
  v_mfma_f32_16x16x32_bf16 v[0:3], a[68:71], v[160:163], v[0:3]
  ds_read_b128 a[56:59], v230 offset:28672
  ds_read_b128 a[60:63], v231 offset:28672
  v_mfma_f32_16x16x32_bf16 v[32:35], a[64:67], v[164:167], v[32:35]
  v_exp_f32 v119, v119
  v_mfma_f32_16x16x32_bf16 v[32:35], a[68:71], v[168:171], v[32:35]
  v_cvt_pk_bf16_f32 v179, v118, v119
  v_mfma_f32_16x16x32_bf16 v[112:115], a[0:3], a[144:147], v[100:103]
  v_exp_f32 v108, v108
  v_mfma_f32_16x16x32_bf16 v[112:115], a[4:7], a[148:151], v[112:115]
  v_mfma_f32_16x16x32_bf16 v[112:115], a[8:11], a[152:155], v[112:115]
  v_exp_f32 v109, v109
  v_mfma_f32_16x16x32_bf16 v[112:115], a[12:15], a[156:159], v[112:115]
  v_cvt_pk_bf16_f32 v132, v108, v109
  v_mfma_f32_16x16x32_bf16 v[64:67], a[64:67], v[172:175], v[64:67]
  v_exp_f32 v110, v110
  v_mfma_f32_16x16x32_bf16 v[64:67], a[68:71], v[176:179], v[64:67]
  s_waitcnt vmcnt(10) lgkmcnt(0)
  s_barrier
  v_mfma_f32_16x16x32_bf16 v[4:7], a[72:75], v[156:159], v[4:7]
  v_exp_f32 v111, v111
  v_mfma_f32_16x16x32_bf16 v[4:7], a[76:79], v[160:163], v[4:7]
  s_sub_u32 s84, s82, s85
  v_cvt_pk_bf16_f32 v133, v110, v111
  v_mfma_f32_16x16x32_bf16 v[116:119], a[0:3], a[160:163], v[104:107]
  v_exp_f32 v112, v112
  v_mfma_f32_16x16x32_bf16 v[116:119], a[4:7], a[164:167], v[116:119]
  s_cmp_lt_u32 s84, s94
  s_cbranch_scc1 PC13_%=
PC13_%=e:
  v_mfma_f32_16x16x32_bf16 v[116:119], a[8:11], a[168:171], v[116:119]
  v_exp_f32 v113, v113
  v_mfma_f32_16x16x32_bf16 v[116:119], a[12:15], a[172:175], v[116:119]
  s_add_u32 s68, s60, 278528
  ds_read_b64_tr_b16 a[224:225], v184 offset:16384
  v_cvt_pk_bf16_f32 v140, v112, v113
  v_mfma_f32_16x16x32_bf16 v[36:39], a[72:75], v[164:167], v[36:39]
  v_exp_f32 v114, v114
  v_mfma_f32_16x16x32_bf16 v[36:39], a[76:79], v[168:171], v[36:39]
  s_add_u32 m0, s61, 16384
  ds_read_b64_tr_b16 a[64:65], v232 offset:32768
  v_mfma_f32_16x16x32_bf16 v[68:71], a[72:75], v[172:175], v[68:71]
  v_exp_f32 v115, v115
  v_mfma_f32_16x16x32_bf16 v[68:71], a[76:79], v[176:179], v[68:71]
  ds_read_b64_tr_b16 a[66:67], v232 offset:36864
  v_cvt_pk_bf16_f32 v141, v114, v115
  v_mfma_f32_16x16x32_bf16 v[8:11], a[80:83], v[156:159], v[8:11]
  buffer_load_dwordx4 v240, s[40:43], s68 offen offset:0 nt lds
  v_mfma_f32_16x16x32_bf16 v[8:11], a[84:87], v[160:163], v[8:11]
  buffer_load_dwordx4 v241, s[40:43], s68 offen offset:1024 nt lds
  ds_read_b64_tr_b16 a[226:227], v184 offset:18432
  v_mfma_f32_16x16x32_bf16 v[108:111], a[16:19], a[128:131], v[96:99]
  v_exp_f32 v116, v116
  v_mfma_f32_16x16x32_bf16 v[108:111], a[20:23], a[132:135], v[108:111]
  ds_read_b64_tr_b16 a[68:69], v232 offset:40960
  ds_read_b64_tr_b16 a[70:71], v232 offset:45056
  v_mfma_f32_16x16x32_bf16 v[108:111], a[24:27], a[136:139], v[108:111]
  v_exp_f32 v117, v117
  v_mfma_f32_16x16x32_bf16 v[108:111], a[28:31], a[140:143], v[108:111]
  ds_read_b128 a[0:3], v228 offset:0
  v_cvt_pk_bf16_f32 v148, v116, v117
  v_mfma_f32_16x16x32_bf16 v[40:43], a[80:83], v[164:167], v[40:43]
  v_exp_f32 v118, v118
  v_mfma_f32_16x16x32_bf16 v[40:43], a[84:87], v[168:171], v[40:43]
  ds_read_b128 a[4:7], v229 offset:0
  ds_read_b128 a[8:11], v230 offset:0
  ds_read_b64_tr_b16 a[228:229], v185 offset:16384
  v_mfma_f32_16x16x32_bf16 v[72:75], a[80:83], v[172:175], v[72:75]
  v_exp_f32 v119, v119
  v_mfma_f32_16x16x32_bf16 v[72:75], a[84:87], v[176:179], v[72:75]
  ds_read_b128 a[12:15], v231 offset:0
  v_cvt_pk_bf16_f32 v149, v118, v119
  v_mfma_f32_16x16x32_bf16 v[112:115], a[16:19], a[144:147], v[100:103]
  v_exp_f32 v108, v108
  v_mfma_f32_16x16x32_bf16 v[112:115], a[20:23], a[148:151], v[112:115]
  buffer_load_dwordx4 v242, s[40:43], s68 offen offset:2048 nt lds
  v_mfma_f32_16x16x32_bf16 v[112:115], a[24:27], a[152:155], v[112:115]
  v_exp_f32 v109, v109
  v_mfma_f32_16x16x32_bf16 v[112:115], a[28:31], a[156:159], v[112:115]
  ds_read_b64_tr_b16 a[72:73], v233 offset:32768
  ds_read_b64_tr_b16 a[230:231], v185 offset:18432
  v_cvt_pk_bf16_f32 v134, v108, v109
  v_mfma_f32_16x16x32_bf16 v[12:15], a[88:91], v[156:159], v[12:15]
  v_exp_f32 v110, v110
  v_mfma_f32_16x16x32_bf16 v[12:15], a[92:95], v[160:163], v[12:15]
  ds_read_b64_tr_b16 a[74:75], v233 offset:36864
  ds_read_b64_tr_b16 a[76:77], v233 offset:40960
  v_mfma_f32_16x16x32_bf16 v[44:47], a[88:91], v[164:167], v[44:47]
  v_exp_f32 v111, v111
  v_mfma_f32_16x16x32_bf16 v[44:47], a[92:95], v[168:171], v[44:47]
  buffer_load_dwordx4 v243, s[40:43], s68 offen offset:3072 nt lds
  v_cvt_pk_bf16_f32 v135, v110, v111
  v_mfma_f32_16x16x32_bf16 v[116:119], a[16:19], a[160:163], v[104:107]
  v_exp_f32 v112, v112
  v_mfma_f32_16x16x32_bf16 v[116:119], a[20:23], a[164:167], v[116:119]
  s_add_u32 s97, s97, s95
  ds_read_b64_tr_b16 a[78:79], v233 offset:45056
  ds_read_b64_tr_b16 a[80:81], v234 offset:32768
  ds_read_b64_tr_b16 a[232:233], v186 offset:16384
  v_mfma_f32_16x16x32_bf16 v[116:119], a[24:27], a[168:171], v[116:119]
  ds_read_b64_tr_b16 a[82:83], v234 offset:36864
  v_exp_f32 v113, v113
  v_mfma_f32_16x16x32_bf16 v[116:119], a[28:31], a[172:175], v[116:119]
  s_lshr_b32 s83, s97, 4
  ds_read_b64_tr_b16 a[84:85], v234 offset:40960
  v_cvt_pk_bf16_f32 v142, v112, v113
  v_mfma_f32_16x16x32_bf16 v[76:79], a[88:91], v[172:175], v[76:79]
  ds_read_b64_tr_b16 a[86:87], v234 offset:45056
  v_exp_f32 v114, v114
  v_mfma_f32_16x16x32_bf16 v[76:79], a[92:95], v[176:179], v[76:79]
  s_add_u32 s94, s84, s96
  s_add_u32 m0, s62, 49152
  ds_read_b128 a[16:19], v228 offset:4096
  v_mfma_f32_16x16x32_bf16 v[16:19], a[96:99], v[156:159], v[16:19]
  ds_read_b128 a[20:23], v229 offset:4096
  v_exp_f32 v115, v115
  v_mfma_f32_16x16x32_bf16 v[16:19], a[100:103], v[160:163], v[16:19]
  s_max_u32 s94, s94, s83
  ds_read_b128 a[24:27], v230 offset:4096
  v_cvt_pk_bf16_f32 v143, v114, v115
  v_mfma_f32_16x16x32_bf16 v[108:111], a[32:35], a[128:131], v[96:99]
  ds_read_b128 a[28:31], v231 offset:4096
  v_exp_f32 v116, v116
  v_mfma_f32_16x16x32_bf16 v[108:111], a[36:39], a[132:135], v[108:111]
  s_sub_u32 s83, s84, s66
  buffer_load_dwordx4 v182, s[52:55], s72 offen offset:2048 lds
  v_mfma_f32_16x16x32_bf16 v[108:111], a[40:43], a[136:139], v[108:111]
  ds_read_b64_tr_b16 a[88:89], v235 offset:32768
  v_exp_f32 v117, v117
  v_mfma_f32_16x16x32_bf16 v[108:111], a[44:47], a[140:143], v[108:111]
  s_min_u32 s83, s83, 200
  ds_read_b64_tr_b16 a[90:91], v235 offset:36864
  v_cvt_pk_bf16_f32 v150, v116, v117
  v_mfma_f32_16x16x32_bf16 v[48:51], a[96:99], v[164:167], v[48:51]
  ds_read_b64_tr_b16 a[92:93], v235 offset:40960
  v_exp_f32 v118, v118
  v_mfma_f32_16x16x32_bf16 v[48:51], a[100:103], v[168:171], v[48:51]
  s_add_u32 s65, s65, s83
  s_add_u32 s68, s60, 262144
  ds_read_b64_tr_b16 a[234:235], v186 offset:18432
  v_mfma_f32_16x16x32_bf16 v[80:83], a[96:99], v[172:175], v[80:83]
  v_exp_f32 v119, v119
  v_mfma_f32_16x16x32_bf16 v[80:83], a[100:103], v[176:179], v[80:83]
  s_lshr_b32 s65, s65, 1
  v_cvt_pk_bf16_f32 v151, v118, v119
  v_mfma_f32_16x16x32_bf16 v[20:23], a[104:107], v[156:159], v[20:23]
  s_add_u32 s83, s65, s84
  s_add_u32 m0, s62, 16384
  v_mfma_f32_16x16x32_bf16 v[20:23], a[108:111], v[160:163], v[20:23]
  s_sub_u32 s83, s83, 28
  buffer_load_dwordx4 v244, s[44:47], s68 offen offset:0 nt lds
  v_mfma_f32_16x16x32_bf16 v[112:115], a[32:35], a[144:147], v[100:103]
  v_exp_f32 v108, v108
  v_mfma_f32_16x16x32_bf16 v[112:115], a[36:39], a[148:151], v[112:115]
  s_max_u32 s94, s94, s83
  ds_read_b64_tr_b16 a[94:95], v235 offset:45056
  ds_read_b64_tr_b16 a[236:237], v187 offset:16384
  v_mfma_f32_16x16x32_bf16 v[112:115], a[40:43], a[152:155], v[112:115]
  v_exp_f32 v109, v109
  v_mfma_f32_16x16x32_bf16 v[112:115], a[44:47], a[156:159], v[112:115]
  s_mov_b32 s66, s84
  v_cvt_pk_bf16_f32 v136, v108, v109
  v_mfma_f32_16x16x32_bf16 v[52:55], a[104:107], v[164:167], v[52:55]
  v_exp_f32 v110, v110
  v_mfma_f32_16x16x32_bf16 v[52:55], a[108:111], v[168:171], v[52:55]
  ds_read_b64_tr_b16 a[96:97], v236 offset:32768
  ds_read_b64_tr_b16 a[98:99], v236 offset:36864
  v_mfma_f32_16x16x32_bf16 v[84:87], a[104:107], v[172:175], v[84:87]
  v_exp_f32 v111, v111
  v_mfma_f32_16x16x32_bf16 v[84:87], a[108:111], v[176:179], v[84:87]
  ds_read_b64_tr_b16 a[100:101], v236 offset:40960
  ds_read_b64_tr_b16 a[238:239], v187 offset:18432
  v_cvt_pk_bf16_f32 v137, v110, v111
  v_mfma_f32_16x16x32_bf16 v[116:119], a[32:35], a[160:163], v[104:107]
  v_exp_f32 v112, v112
  v_mfma_f32_16x16x32_bf16 v[116:119], a[36:39], a[164:167], v[116:119]
  buffer_load_dwordx4 v245, s[44:47], s68 offen offset:1024 nt lds
  v_mfma_f32_16x16x32_bf16 v[116:119], a[40:43], a[168:171], v[116:119]
  v_exp_f32 v113, v113
  v_mfma_f32_16x16x32_bf16 v[116:119], a[44:47], a[172:175], v[116:119]
  ds_read_b64_tr_b16 a[102:103], v236 offset:45056
  v_cvt_pk_bf16_f32 v144, v112, v113
  v_mfma_f32_16x16x32_bf16 v[24:27], a[112:115], v[156:159], v[24:27]
  v_exp_f32 v114, v114
  v_mfma_f32_16x16x32_bf16 v[24:27], a[116:119], v[160:163], v[24:27]
  ds_read_b64_tr_b16 a[104:105], v237 offset:32768
  ds_read_b64_tr_b16 a[106:107], v237 offset:36864
  v_mfma_f32_16x16x32_bf16 v[56:59], a[112:115], v[164:167], v[56:59]
  v_exp_f32 v115, v115
  v_mfma_f32_16x16x32_bf16 v[56:59], a[116:119], v[168:171], v[56:59]
  ds_read_b64_tr_b16 a[108:109], v237 offset:40960
  v_cvt_pk_bf16_f32 v145, v114, v115
  v_mfma_f32_16x16x32_bf16 v[108:111], a[48:51], a[128:131], v[96:99]
  v_exp_f32 v116, v116
  v_mfma_f32_16x16x32_bf16 v[108:111], a[52:55], a[132:135], v[108:111]
  buffer_load_dwordx4 v244, s[44:47], s68 offen offset:2048 nt lds
  v_mfma_f32_16x16x32_bf16 v[108:111], a[56:59], a[136:139], v[108:111]
  v_exp_f32 v117, v117
  v_mfma_f32_16x16x32_bf16 v[108:111], a[60:63], a[140:143], v[108:111]
  ds_read_b64_tr_b16 a[110:111], v237 offset:45056
  v_cvt_pk_bf16_f32 v152, v116, v117
  v_mfma_f32_16x16x32_bf16 v[88:91], a[112:115], v[172:175], v[88:91]
  v_exp_f32 v118, v118
  v_mfma_f32_16x16x32_bf16 v[88:91], a[116:119], v[176:179], v[88:91]
  ds_read_b128 a[32:35], v228 offset:8192
  ds_read_b128 a[36:39], v229 offset:8192
  v_mfma_f32_16x16x32_bf16 v[28:31], a[120:123], v[156:159], v[28:31]
  v_exp_f32 v119, v119
  v_mfma_f32_16x16x32_bf16 v[28:31], a[124:127], v[160:163], v[28:31]
  ds_read_b128 a[40:43], v230 offset:8192
  v_cvt_pk_bf16_f32 v153, v118, v119
  v_mfma_f32_16x16x32_bf16 v[60:63], a[120:123], v[164:167], v[60:63]
  buffer_load_dwordx4 v245, s[44:47], s68 offen offset:3072 nt lds
  v_mfma_f32_16x16x32_bf16 v[60:63], a[124:127], v[168:171], v[60:63]
  ds_read_b128 a[44:47], v231 offset:8192
  ds_read_b64_tr_b16 a[112:113], v238 offset:32768
  v_mfma_f32_16x16x32_bf16 v[112:115], a[48:51], a[144:147], v[100:103]
  v_exp_f32 v108, v108
  v_mfma_f32_16x16x32_bf16 v[112:115], a[52:55], a[148:151], v[112:115]
  ds_read_b64_tr_b16 a[114:115], v238 offset:36864
  ds_read_b64_tr_b16 a[116:117], v238 offset:40960
  v_mfma_f32_16x16x32_bf16 v[112:115], a[56:59], a[152:155], v[112:115]
  v_exp_f32 v109, v109
  v_mfma_f32_16x16x32_bf16 v[112:115], a[60:63], a[156:159], v[112:115]
  s_add_u32 m0, s62, 49152
  v_cvt_pk_bf16_f32 v138, v108, v109
  v_mfma_f32_16x16x32_bf16 v[92:95], a[120:123], v[172:175], v[92:95]
  v_exp_f32 v110, v110
  v_mfma_f32_16x16x32_bf16 v[92:95], a[124:127], v[176:179], v[92:95]
  buffer_load_dwordx4 v183, s[52:55], s72 offen offset:3072 lds
  v_mfma_f32_16x16x32_bf16 v[120:123], a[240:243], v[156:159], v[120:123]
  v_exp_f32 v111, v111
  v_mfma_f32_16x16x32_bf16 v[120:123], a[240:243], v[160:163], v[120:123]
  ds_read_b64_tr_b16 a[118:119], v238 offset:45056
  v_cvt_pk_bf16_f32 v139, v110, v111
  v_mfma_f32_16x16x32_bf16 v[116:119], a[48:51], a[160:163], v[104:107]
  v_exp_f32 v112, v112
  v_mfma_f32_16x16x32_bf16 v[116:119], a[52:55], a[164:167], v[116:119]
  ds_read_b64_tr_b16 a[120:121], v239 offset:32768
  ds_read_b64_tr_b16 a[122:123], v239 offset:36864
  v_mfma_f32_16x16x32_bf16 v[116:119], a[56:59], a[168:171], v[116:119]
  v_exp_f32 v113, v113
  v_mfma_f32_16x16x32_bf16 v[116:119], a[60:63], a[172:175], v[116:119]
  ds_read_b64_tr_b16 a[124:125], v239 offset:40960
  v_cvt_pk_bf16_f32 v146, v112, v113
  v_mfma_f32_16x16x32_bf16 v[124:127], a[240:243], v[164:167], v[124:127]
  v_exp_f32 v114, v114
  v_mfma_f32_16x16x32_bf16 v[124:127], a[240:243], v[168:171], v[124:127]
  ds_read_b64_tr_b16 a[126:127], v239 offset:45056
  v_mfma_f32_16x16x32_bf16 v[128:131], a[240:243], v[172:175], v[128:131]
  v_exp_f32 v115, v115
  v_mfma_f32_16x16x32_bf16 v[128:131], a[240:243], v[176:179], v[128:131]
  v_cvt_pk_bf16_f32 v147, v114, v115
  s_waitcnt lgkmcnt(8)
  v_mfma_f32_16x16x32_bf16 v[108:111], a[0:3], a[128:131], v[96:99]
  v_exp_f32 v116, v116
  v_mfma_f32_16x16x32_bf16 v[108:111], a[4:7], a[132:135], v[108:111]
  s_memrealtime s[82:83]
  ds_read_b128 a[48:51], v228 offset:12288
  v_mfma_f32_16x16x32_bf16 v[108:111], a[8:11], a[136:139], v[108:111]
  v_exp_f32 v117, v117
  v_mfma_f32_16x16x32_bf16 v[108:111], a[12:15], a[140:143], v[108:111]
  ds_read_b128 a[52:55], v229 offset:12288
  v_cvt_pk_bf16_f32 v154, v116, v117
  v_mfma_f32_16x16x32_bf16 v[0:3], a[64:67], v[132:135], v[0:3]
  v_exp_f32 v118, v118
  v_mfma_f32_16x16x32_bf16 v[0:3], a[68:71], v[136:139], v[0:3]
  ds_read_b128 a[56:59], v230 offset:12288
  ds_read_b128 a[60:63], v231 offset:12288
  v_mfma_f32_16x16x32_bf16 v[32:35], a[64:67], v[140:143], v[32:35]
  v_exp_f32 v119, v119
  v_mfma_f32_16x16x32_bf16 v[32:35], a[68:71], v[144:147], v[32:35]
  v_cvt_pk_bf16_f32 v155, v118, v119
  v_mfma_f32_16x16x32_bf16 v[112:115], a[0:3], a[144:147], v[100:103]
  v_exp_f32 v108, v108
  v_mfma_f32_16x16x32_bf16 v[112:115], a[4:7], a[148:151], v[112:115]
  v_mfma_f32_16x16x32_bf16 v[112:115], a[8:11], a[152:155], v[112:115]
  v_exp_f32 v109, v109
  v_mfma_f32_16x16x32_bf16 v[112:115], a[12:15], a[156:159], v[112:115]
  v_cvt_pk_bf16_f32 v156, v108, v109
  v_mfma_f32_16x16x32_bf16 v[64:67], a[64:67], v[148:151], v[64:67]
  v_exp_f32 v110, v110
  v_mfma_f32_16x16x32_bf16 v[64:67], a[68:71], v[152:155], v[64:67]
  s_waitcnt vmcnt(11) lgkmcnt(0)
  s_barrier
  v_mfma_f32_16x16x32_bf16 v[4:7], a[72:75], v[132:135], v[4:7]
  v_exp_f32 v111, v111
  v_mfma_f32_16x16x32_bf16 v[4:7], a[76:79], v[136:139], v[4:7]
  s_sub_u32 s84, s82, s85
  v_cvt_pk_bf16_f32 v157, v110, v111
  v_mfma_f32_16x16x32_bf16 v[116:119], a[0:3], a[160:163], v[104:107]
  v_exp_f32 v112, v112
  v_mfma_f32_16x16x32_bf16 v[116:119], a[4:7], a[164:167], v[116:119]
  s_cmp_lt_u32 s84, s94
  s_cbranch_scc1 PC14_%=
PC14_%=e:
  v_mfma_f32_16x16x32_bf16 v[116:119], a[8:11], a[168:171], v[116:119]
  v_exp_f32 v113, v113
  v_mfma_f32_16x16x32_bf16 v[116:119], a[12:15], a[172:175], v[116:119]
  s_add_u32 s68, s60, 294912
  v_cvt_pk_bf16_f32 v164, v112, v113
  v_mfma_f32_16x16x32_bf16 v[36:39], a[72:75], v[140:143], v[36:39]
  v_exp_f32 v114, v114
  v_mfma_f32_16x16x32_bf16 v[36:39], a[76:79], v[144:147], v[36:39]
  s_add_u32 m0, s61, 0
  ds_read_b64_tr_b16 a[64:65], v232 offset:0
  v_mfma_f32_16x16x32_bf16 v[68:71], a[72:75], v[148:151], v[68:71]
  v_exp_f32 v115, v115
  v_mfma_f32_16x16x32_bf16 v[68:71], a[76:79], v[152:155], v[68:71]
  ds_read_b64_tr_b16 a[66:67], v232 offset:4096
  v_cvt_pk_bf16_f32 v165, v114, v115
  v_mfma_f32_16x16x32_bf16 v[8:11], a[80:83], v[132:135], v[8:11]
  buffer_load_dwordx4 v240, s[40:43], s68 offen offset:0 nt lds
  v_mfma_f32_16x16x32_bf16 v[8:11], a[84:87], v[136:139], v[8:11]
  buffer_load_dwordx4 v241, s[40:43], s68 offen offset:1024 nt lds
  v_mfma_f32_16x16x32_bf16 v[108:111], a[16:19], a[128:131], v[96:99]
  v_exp_f32 v116, v116
  v_mfma_f32_16x16x32_bf16 v[108:111], a[20:23], a[132:135], v[108:111]
  ds_read_b64_tr_b16 a[68:69], v232 offset:8192
  ds_read_b64_tr_b16 a[70:71], v232 offset:12288
  v_mfma_f32_16x16x32_bf16 v[108:111], a[24:27], a[136:139], v[108:111]
  v_exp_f32 v117, v117
  v_mfma_f32_16x16x32_bf16 v[108:111], a[28:31], a[140:143], v[108:111]
  ds_read_b128 a[0:3], v228 offset:49152
  v_cvt_pk_bf16_f32 v172, v116, v117
  v_mfma_f32_16x16x32_bf16 v[40:43], a[80:83], v[140:143], v[40:43]
  v_exp_f32 v118, v118
  v_mfma_f32_16x16x32_bf16 v[40:43], a[84:87], v[144:147], v[40:43]
  ds_read_b128 a[4:7], v229 offset:49152
  ds_read_b128 a[8:11], v230 offset:49152
  v_mfma_f32_16x16x32_bf16 v[72:75], a[80:83], v[148:151], v[72:75]
  v_exp_f32 v119, v119
  v_mfma_f32_16x16x32_bf16 v[72:75], a[84:87], v[152:155], v[72:75]
  ds_read_b128 a[12:15], v231 offset:49152
  v_cvt_pk_bf16_f32 v173, v118, v119
  v_mfma_f32_16x16x32_bf16 v[112:115], a[16:19], a[144:147], v[100:103]
  v_exp_f32 v108, v108
  v_mfma_f32_16x16x32_bf16 v[112:115], a[20:23], a[148:151], v[112:115]
  buffer_load_dwordx4 v242, s[40:43], s68 offen offset:2048 nt lds
  v_mfma_f32_16x16x32_bf16 v[112:115], a[24:27], a[152:155], v[112:115]
  v_exp_f32 v109, v109
  v_mfma_f32_16x16x32_bf16 v[112:115], a[28:31], a[156:159], v[112:115]
  ds_read_b64_tr_b16 a[72:73], v233 offset:0
  v_cvt_pk_bf16_f32 v158, v108, v109
  v_mfma_f32_16x16x32_bf16 v[12:15], a[88:91], v[132:135], v[12:15]
  v_exp_f32 v110, v110
  v_mfma_f32_16x16x32_bf16 v[12:15], a[92:95], v[136:139], v[12:15]
  ds_read_b64_tr_b16 a[74:75], v233 offset:4096
  ds_read_b64_tr_b16 a[76:77], v233 offset:8192
  ds_read_b64_tr_b16 a[78:79], v233 offset:12288
  v_mfma_f32_16x16x32_bf16 v[44:47], a[88:91], v[140:143], v[44:47]
  ds_read_b64_tr_b16 a[80:81], v234 offset:0
  v_exp_f32 v111, v111
  v_mfma_f32_16x16x32_bf16 v[44:47], a[92:95], v[144:147], v[44:47]
  buffer_load_dwordx4 v243, s[40:43], s68 offen offset:3072 nt lds
  v_cvt_pk_bf16_f32 v159, v110, v111
  v_mfma_f32_16x16x32_bf16 v[116:119], a[16:19], a[160:163], v[104:107]
  ds_read_b64_tr_b16 a[82:83], v234 offset:4096
  v_exp_f32 v112, v112
  v_mfma_f32_16x16x32_bf16 v[116:119], a[20:23], a[164:167], v[116:119]
  s_add_u32 s97, s97, s95
  s_add_u32 s72, s93, 5
  s_and_b32 s72, s72, 31
  s_mul_i32 s72, s72, 40960
  ds_read_b64_tr_b16 a[84:85], v234 offset:8192
  v_mfma_f32_16x16x32_bf16 v[116:119], a[24:27], a[168:171], v[116:119]
  ds_read_b64_tr_b16 a[86:87], v234 offset:12288
  v_exp_f32 v113, v113
  v_mfma_f32_16x16x32_bf16 v[116:119], a[28:31], a[172:175], v[116:119]
  s_lshr_b32 s83, s97, 4
  v_cvt_pk_bf16_f32 v166, v112, v113
  v_mfma_f32_16x16x32_bf16 v[76:79], a[88:91], v[148:151], v[76:79]
  v_exp_f32 v114, v114
  v_mfma_f32_16x16x32_bf16 v[76:79], a[92:95], v[152:155], v[76:79]
  s_add_u32 s94, s84, s96
  s_add_u32 m0, s62, 65536
  ds_read_b128 a[16:19], v228 offset:53248
  v_mfma_f32_16x16x32_bf16 v[16:19], a[96:99], v[132:135], v[16:19]
  ds_read_b128 a[20:23], v229 offset:53248
  v_exp_f32 v115, v115
  v_mfma_f32_16x16x32_bf16 v[16:19], a[100:103], v[136:139], v[16:19]
  s_max_u32 s94, s94, s83
  ds_read_b128 a[24:27], v230 offset:53248
  v_cvt_pk_bf16_f32 v167, v114, v115
  v_mfma_f32_16x16x32_bf16 v[108:111], a[32:35], a[128:131], v[96:99]
  ds_read_b128 a[28:31], v231 offset:53248
  v_exp_f32 v116, v116
  v_mfma_f32_16x16x32_bf16 v[108:111], a[36:39], a[132:135], v[108:111]
  s_sub_u32 s83, s84, s66
  buffer_load_dwordx4 v180, s[52:55], s72 offen offset:0 lds
  v_mfma_f32_16x16x32_bf16 v[108:111], a[40:43], a[136:139], v[108:111]
  ds_read_b64_tr_b16 a[88:89], v235 offset:0
  v_exp_f32 v117, v117
  v_mfma_f32_16x16x32_bf16 v[108:111], a[44:47], a[140:143], v[108:111]
  s_min_u32 s83, s83, 200
  ds_read_b64_tr_b16 a[90:91], v235 offset:4096
  v_cvt_pk_bf16_f32 v174, v116, v117
  v_mfma_f32_16x16x32_bf16 v[48:51], a[96:99], v[140:143], v[48:51]
  ds_read_b64_tr_b16 a[92:93], v235 offset:8192
  v_exp_f32 v118, v118
  v_mfma_f32_16x16x32_bf16 v[48:51], a[100:103], v[144:147], v[48:51]
  s_add_u32 s65, s65, s83
  s_add_u32 s68, s60, 278528
  v_mfma_f32_16x16x32_bf16 v[80:83], a[96:99], v[148:151], v[80:83]
  v_exp_f32 v119, v119
  v_mfma_f32_16x16x32_bf16 v[80:83], a[100:103], v[152:155], v[80:83]
  s_lshr_b32 s65, s65, 1
  v_cvt_pk_bf16_f32 v175, v118, v119
  v_mfma_f32_16x16x32_bf16 v[20:23], a[104:107], v[132:135], v[20:23]
  s_add_u32 s83, s65, s84
  s_add_u32 m0, s62, 32768
  v_mfma_f32_16x16x32_bf16 v[20:23], a[108:111], v[136:139], v[20:23]
  s_sub_u32 s83, s83, 28
  buffer_load_dwordx4 v244, s[44:47], s68 offen offset:0 lds
  v_mfma_f32_16x16x32_bf16 v[112:115], a[32:35], a[144:147], v[100:103]
  v_exp_f32 v108, v108
  v_mfma_f32_16x16x32_bf16 v[112:115], a[36:39], a[148:151], v[112:115]
  s_max_u32 s94, s94, s83
  ds_read_b64_tr_b16 a[94:95], v235 offset:12288
  v_mfma_f32_16x16x32_bf16 v[112:115], a[40:43], a[152:155], v[112:115]
  v_exp_f32 v109, v109
  v_mfma_f32_16x16x32_bf16 v[112:115], a[44:47], a[156:159], v[112:115]
  s_mov_b32 s66, s84
  v_cvt_pk_bf16_f32 v160, v108, v109
  v_mfma_f32_16x16x32_bf16 v[52:55], a[104:107], v[140:143], v[52:55]
  v_exp_f32 v110, v110
  v_mfma_f32_16x16x32_bf16 v[52:55], a[108:111], v[144:147], v[52:55]
  ds_read_b64_tr_b16 a[96:97], v236 offset:0
  ds_read_b64_tr_b16 a[98:99], v236 offset:4096
  v_mfma_f32_16x16x32_bf16 v[84:87], a[104:107], v[148:151], v[84:87]
  v_exp_f32 v111, v111
  v_mfma_f32_16x16x32_bf16 v[84:87], a[108:111], v[152:155], v[84:87]
  ds_read_b64_tr_b16 a[100:101], v236 offset:8192
  v_cvt_pk_bf16_f32 v161, v110, v111
  v_mfma_f32_16x16x32_bf16 v[116:119], a[32:35], a[160:163], v[104:107]
  v_exp_f32 v112, v112
  v_mfma_f32_16x16x32_bf16 v[116:119], a[36:39], a[164:167], v[116:119]
  buffer_load_dwordx4 v245, s[44:47], s68 offen offset:1024 lds
  v_mfma_f32_16x16x32_bf16 v[116:119], a[40:43], a[168:171], v[116:119]
  v_exp_f32 v113, v113
  v_mfma_f32_16x16x32_bf16 v[116:119], a[44:47], a[172:175], v[116:119]
  ds_read_b64_tr_b16 a[102:103], v236 offset:12288
  v_cvt_pk_bf16_f32 v168, v112, v113
  v_mfma_f32_16x16x32_bf16 v[24:27], a[112:115], v[132:135], v[24:27]
  v_exp_f32 v114, v114
  v_mfma_f32_16x16x32_bf16 v[24:27], a[116:119], v[136:139], v[24:27]
  ds_read_b64_tr_b16 a[104:105], v237 offset:0
  ds_read_b64_tr_b16 a[106:107], v237 offset:4096
  v_mfma_f32_16x16x32_bf16 v[56:59], a[112:115], v[140:143], v[56:59]
  v_exp_f32 v115, v115
  v_mfma_f32_16x16x32_bf16 v[56:59], a[116:119], v[144:147], v[56:59]
  ds_read_b64_tr_b16 a[108:109], v237 offset:8192
  v_cvt_pk_bf16_f32 v169, v114, v115
  v_mfma_f32_16x16x32_bf16 v[108:111], a[48:51], a[128:131], v[96:99]
  v_exp_f32 v116, v116
  v_mfma_f32_16x16x32_bf16 v[108:111], a[52:55], a[132:135], v[108:111]
  buffer_load_dwordx4 v244, s[44:47], s68 offen offset:2048 lds
  v_mfma_f32_16x16x32_bf16 v[108:111], a[56:59], a[136:139], v[108:111]
  v_exp_f32 v117, v117
  v_mfma_f32_16x16x32_bf16 v[108:111], a[60:63], a[140:143], v[108:111]
  ds_read_b64_tr_b16 a[110:111], v237 offset:12288
  v_cvt_pk_bf16_f32 v176, v116, v117
  v_mfma_f32_16x16x32_bf16 v[88:91], a[112:115], v[148:151], v[88:91]
  v_exp_f32 v118, v118
  v_mfma_f32_16x16x32_bf16 v[88:91], a[116:119], v[152:155], v[88:91]
  ds_read_b128 a[32:35], v228 offset:57344
  ds_read_b128 a[36:39], v229 offset:57344
  v_mfma_f32_16x16x32_bf16 v[28:31], a[120:123], v[132:135], v[28:31]
  v_exp_f32 v119, v119
  v_mfma_f32_16x16x32_bf16 v[28:31], a[124:127], v[136:139], v[28:31]
  ds_read_b128 a[40:43], v230 offset:57344
  v_cvt_pk_bf16_f32 v177, v118, v119
  v_mfma_f32_16x16x32_bf16 v[60:63], a[120:123], v[140:143], v[60:63]
  buffer_load_dwordx4 v245, s[44:47], s68 offen offset:3072 lds
  v_mfma_f32_16x16x32_bf16 v[60:63], a[124:127], v[144:147], v[60:63]
  ds_read_b128 a[44:47], v231 offset:57344
  ds_read_b64_tr_b16 a[112:113], v238 offset:0
  v_mfma_f32_16x16x32_bf16 v[112:115], a[48:51], a[144:147], v[100:103]
  v_exp_f32 v108, v108
  v_mfma_f32_16x16x32_bf16 v[112:115], a[52:55], a[148:151], v[112:115]
  ds_read_b64_tr_b16 a[114:115], v238 offset:4096
  ds_read_b64_tr_b16 a[116:117], v238 offset:8192
  v_mfma_f32_16x16x32_bf16 v[112:115], a[56:59], a[152:155], v[112:115]
  v_exp_f32 v109, v109
  v_mfma_f32_16x16x32_bf16 v[112:115], a[60:63], a[156:159], v[112:115]
  s_add_u32 m0, s62, 65536
  v_cvt_pk_bf16_f32 v162, v108, v109
  v_mfma_f32_16x16x32_bf16 v[92:95], a[120:123], v[148:151], v[92:95]
  v_exp_f32 v110, v110
  v_mfma_f32_16x16x32_bf16 v[92:95], a[124:127], v[152:155], v[92:95]
  buffer_load_dwordx4 v181, s[52:55], s72 offen offset:1024 lds
  v_mfma_f32_16x16x32_bf16 v[120:123], a[240:243], v[132:135], v[120:123]
  v_exp_f32 v111, v111
  v_mfma_f32_16x16x32_bf16 v[120:123], a[240:243], v[136:139], v[120:123]
  ds_read_b64_tr_b16 a[118:119], v238 offset:12288
  v_cvt_pk_bf16_f32 v163, v110, v111
  v_mfma_f32_16x16x32_bf16 v[116:119], a[48:51], a[160:163], v[104:107]
  v_exp_f32 v112, v112
  v_mfma_f32_16x16x32_bf16 v[116:119], a[52:55], a[164:167], v[116:119]
  ds_read_b64_tr_b16 a[120:121], v239 offset:0
  ds_read_b64_tr_b16 a[122:123], v239 offset:4096
  v_mfma_f32_16x16x32_bf16 v[116:119], a[56:59], a[168:171], v[116:119]
  v_exp_f32 v113, v113
  v_mfma_f32_16x16x32_bf16 v[116:119], a[60:63], a[172:175], v[116:119]
  ds_read_b64_tr_b16 a[124:125], v239 offset:8192
  v_cvt_pk_bf16_f32 v170, v112, v113
  v_mfma_f32_16x16x32_bf16 v[124:127], a[240:243], v[140:143], v[124:127]
  v_exp_f32 v114, v114
  v_mfma_f32_16x16x32_bf16 v[124:127], a[240:243], v[144:147], v[124:127]
  ds_read_b64_tr_b16 a[126:127], v239 offset:12288
  v_mfma_f32_16x16x32_bf16 v[128:131], a[240:243], v[148:151], v[128:131]
  v_exp_f32 v115, v115
  v_mfma_f32_16x16x32_bf16 v[128:131], a[240:243], v[152:155], v[128:131]
  v_cvt_pk_bf16_f32 v171, v114, v115
  s_waitcnt lgkmcnt(8)
  v_mfma_f32_16x16x32_bf16 v[108:111], a[0:3], a[128:131], v[96:99]
  v_exp_f32 v116, v116
  v_mfma_f32_16x16x32_bf16 v[108:111], a[4:7], a[132:135], v[108:111]
  s_memrealtime s[82:83]
  ds_read_b128 a[48:51], v228 offset:61440
  v_mfma_f32_16x16x32_bf16 v[108:111], a[8:11], a[136:139], v[108:111]
  v_exp_f32 v117, v117
  v_mfma_f32_16x16x32_bf16 v[108:111], a[12:15], a[140:143], v[108:111]
  ds_read_b128 a[52:55], v229 offset:61440
  v_cvt_pk_bf16_f32 v178, v116, v117
  v_mfma_f32_16x16x32_bf16 v[0:3], a[64:67], v[156:159], v[0:3]
  v_exp_f32 v118, v118
  v_mfma_f32_16x16x32_bf16 v[0:3], a[68:71], v[160:163], v[0:3]
  ds_read_b128 a[56:59], v230 offset:61440
  ds_read_b128 a[60:63], v231 offset:61440
  v_mfma_f32_16x16x32_bf16 v[32:35], a[64:67], v[164:167], v[32:35]
  v_exp_f32 v119, v119
  v_mfma_f32_16x16x32_bf16 v[32:35], a[68:71], v[168:171], v[32:35]
  v_cvt_pk_bf16_f32 v179, v118, v119
  v_mfma_f32_16x16x32_bf16 v[112:115], a[0:3], a[144:147], v[100:103]
  v_exp_f32 v108, v108
  v_mfma_f32_16x16x32_bf16 v[112:115], a[4:7], a[148:151], v[112:115]
  v_mfma_f32_16x16x32_bf16 v[112:115], a[8:11], a[152:155], v[112:115]
  v_exp_f32 v109, v109
  v_mfma_f32_16x16x32_bf16 v[112:115], a[12:15], a[156:159], v[112:115]
  v_cvt_pk_bf16_f32 v132, v108, v109
  v_mfma_f32_16x16x32_bf16 v[64:67], a[64:67], v[172:175], v[64:67]
  v_exp_f32 v110, v110
  v_mfma_f32_16x16x32_bf16 v[64:67], a[68:71], v[176:179], v[64:67]
  s_waitcnt vmcnt(10) lgkmcnt(0)
  s_barrier
  v_mfma_f32_16x16x32_bf16 v[4:7], a[72:75], v[156:159], v[4:7]
  v_exp_f32 v111, v111
  v_mfma_f32_16x16x32_bf16 v[4:7], a[76:79], v[160:163], v[4:7]
  s_sub_u32 s84, s82, s85
  v_cvt_pk_bf16_f32 v133, v110, v111
  v_mfma_f32_16x16x32_bf16 v[116:119], a[0:3], a[160:163], v[104:107]
  v_exp_f32 v112, v112
  v_mfma_f32_16x16x32_bf16 v[116:119], a[4:7], a[164:167], v[116:119]
  s_cmp_lt_u32 s84, s94
  s_cbranch_scc1 PC15_%=
PC15_%=e:
  v_mfma_f32_16x16x32_bf16 v[116:119], a[8:11], a[168:171], v[116:119]
  v_exp_f32 v113, v113
  v_mfma_f32_16x16x32_bf16 v[116:119], a[12:15], a[172:175], v[116:119]
  s_add_u32 s68, s60, 311296
  ds_read_b64_tr_b16 a[244:245], v184 offset:0
  v_cvt_pk_bf16_f32 v140, v112, v113
  v_mfma_f32_16x16x32_bf16 v[36:39], a[72:75], v[164:167], v[36:39]
  v_exp_f32 v114, v114
  v_mfma_f32_16x16x32_bf16 v[36:39], a[76:79], v[168:171], v[36:39]
  s_add_u32 m0, s61, 49152
  ds_read_b64_tr_b16 a[64:65], v232 offset:16384
  v_mfma_f32_16x16x32_bf16 v[68:71], a[72:75], v[172:175], v[68:71]
  v_exp_f32 v115, v115
  v_mfma_f32_16x16x32_bf16 v[68:71], a[76:79], v[176:179], v[68:71]
  ds_read_b64_tr_b16 a[66:67], v232 offset:20480
  v_cvt_pk_bf16_f32 v141, v114, v115
  v_mfma_f32_16x16x32_bf16 v[8:11], a[80:83], v[156:159], v[8:11]
  buffer_load_dwordx4 v240, s[40:43], s68 offen offset:0 nt lds
  v_mfma_f32_16x16x32_bf16 v[8:11], a[84:87], v[160:163], v[8:11]
  buffer_load_dwordx4 v241, s[40:43], s68 offen offset:1024 nt lds
  ds_read_b64_tr_b16 a[246:247], v184 offset:2048
  v_mfma_f32_16x16x32_bf16 v[108:111], a[16:19], a[128:131], v[96:99]
  v_exp_f32 v116, v116
  v_mfma_f32_16x16x32_bf16 v[108:111], a[20:23], a[132:135], v[108:111]
  ds_read_b64_tr_b16 a[68:69], v232 offset:24576
  ds_read_b64_tr_b16 a[70:71], v232 offset:28672
  v_mfma_f32_16x16x32_bf16 v[108:111], a[24:27], a[136:139], v[108:111]
  v_exp_f32 v117, v117
  v_mfma_f32_16x16x32_bf16 v[108:111], a[28:31], a[140:143], v[108:111]
  ds_read_b128 a[0:3], v228 offset:16384
  v_cvt_pk_bf16_f32 v148, v116, v117
  v_mfma_f32_16x16x32_bf16 v[40:43], a[80:83], v[164:167], v[40:43]
  v_exp_f32 v118, v118
  v_mfma_f32_16x16x32_bf16 v[40:43], a[84:87], v[168:171], v[40:43]
  ds_read_b128 a[4:7], v229 offset:16384
  ds_read_b128 a[8:11], v230 offset:16384
  ds_read_b64_tr_b16 a[248:249], v185 offset:0
  v_mfma_f32_16x16x32_bf16 v[72:75], a[80:83], v[172:175], v[72:75]
  v_exp_f32 v119, v119
  v_mfma_f32_16x16x32_bf16 v[72:75], a[84:87], v[176:179], v[72:75]
  ds_read_b128 a[12:15], v231 offset:16384
  v_cvt_pk_bf16_f32 v149, v118, v119
  v_mfma_f32_16x16x32_bf16 v[112:115], a[16:19], a[144:147], v[100:103]
  v_exp_f32 v108, v108
  v_mfma_f32_16x16x32_bf16 v[112:115], a[20:23], a[148:151], v[112:115]
  buffer_load_dwordx4 v242, s[40:43], s68 offen offset:2048 nt lds
  v_mfma_f32_16x16x32_bf16 v[112:115], a[24:27], a[152:155], v[112:115]
  v_exp_f32 v109, v109
  v_mfma_f32_16x16x32_bf16 v[112:115], a[28:31], a[156:159], v[112:115]
  ds_read_b64_tr_b16 a[72:73], v233 offset:16384
  ds_read_b64_tr_b16 a[250:251], v185 offset:2048
  v_cvt_pk_bf16_f32 v134, v108, v109
  v_mfma_f32_16x16x32_bf16 v[12:15], a[88:91], v[156:159], v[12:15]
  v_exp_f32 v110, v110
  v_mfma_f32_16x16x32_bf16 v[12:15], a[92:95], v[160:163], v[12:15]
  ds_read_b64_tr_b16 a[74:75], v233 offset:20480
  ds_read_b64_tr_b16 a[76:77], v233 offset:24576
  v_mfma_f32_16x16x32_bf16 v[44:47], a[88:91], v[164:167], v[44:47]
  v_exp_f32 v111, v111
  v_mfma_f32_16x16x32_bf16 v[44:47], a[92:95], v[168:171], v[44:47]
  buffer_load_dwordx4 v243, s[40:43], s68 offen offset:3072 nt lds
  v_cvt_pk_bf16_f32 v135, v110, v111
  v_mfma_f32_16x16x32_bf16 v[116:119], a[16:19], a[160:163], v[104:107]
  v_exp_f32 v112, v112
  v_mfma_f32_16x16x32_bf16 v[116:119], a[20:23], a[164:167], v[116:119]
  s_add_u32 s97, s97, s95
  ds_read_b64_tr_b16 a[78:79], v233 offset:28672
  ds_read_b64_tr_b16 a[80:81], v234 offset:16384
  ds_read_b64_tr_b16 a[252:253], v186 offset:0
  v_mfma_f32_16x16x32_bf16 v[116:119], a[24:27], a[168:171], v[116:119]
  ds_read_b64_tr_b16 a[82:83], v234 offset:20480
  v_exp_f32 v113, v113
  v_mfma_f32_16x16x32_bf16 v[116:119], a[28:31], a[172:175], v[116:119]
  s_lshr_b32 s83, s97, 4
  ds_read_b64_tr_b16 a[84:85], v234 offset:24576
  v_cvt_pk_bf16_f32 v142, v112, v113
  v_mfma_f32_16x16x32_bf16 v[76:79], a[88:91], v[172:175], v[76:79]
  ds_read_b64_tr_b16 a[86:87], v234 offset:28672
  v_exp_f32 v114, v114
  v_mfma_f32_16x16x32_bf16 v[76:79], a[92:95], v[176:179], v[76:79]
  s_add_u32 s94, s84, s96
  s_add_u32 m0, s62, 65536
  ds_read_b128 a[16:19], v228 offset:20480
  v_mfma_f32_16x16x32_bf16 v[16:19], a[96:99], v[156:159], v[16:19]
  ds_read_b128 a[20:23], v229 offset:20480
  v_exp_f32 v115, v115
  v_mfma_f32_16x16x32_bf16 v[16:19], a[100:103], v[160:163], v[16:19]
  s_max_u32 s94, s94, s83
  ds_read_b128 a[24:27], v230 offset:20480
  v_cvt_pk_bf16_f32 v143, v114, v115
  v_mfma_f32_16x16x32_bf16 v[108:111], a[32:35], a[128:131], v[96:99]
  ds_read_b128 a[28:31], v231 offset:20480
  v_exp_f32 v116, v116
  v_mfma_f32_16x16x32_bf16 v[108:111], a[36:39], a[132:135], v[108:111]
  s_sub_u32 s83, s84, s66
  buffer_load_dwordx4 v182, s[52:55], s72 offen offset:2048 lds
  v_mfma_f32_16x16x32_bf16 v[108:111], a[40:43], a[136:139], v[108:111]
  ds_read_b64_tr_b16 a[88:89], v235 offset:16384
  v_exp_f32 v117, v117
  v_mfma_f32_16x16x32_bf16 v[108:111], a[44:47], a[140:143], v[108:111]
  s_min_u32 s83, s83, 200
  ds_read_b64_tr_b16 a[90:91], v235 offset:20480
  v_cvt_pk_bf16_f32 v150, v116, v117
  v_mfma_f32_16x16x32_bf16 v[48:51], a[96:99], v[164:167], v[48:51]
  ds_read_b64_tr_b16 a[92:93], v235 offset:24576
  v_exp_f32 v118, v118
  v_mfma_f32_16x16x32_bf16 v[48:51], a[100:103], v[168:171], v[48:51]
  s_add_u32 s65, s65, s83
  s_add_u32 s68, s60, 294912
  ds_read_b64_tr_b16 a[254:255], v186 offset:2048
  v_mfma_f32_16x16x32_bf16 v[80:83], a[96:99], v[172:175], v[80:83]
  v_exp_f32 v119, v119
  v_mfma_f32_16x16x32_bf16 v[80:83], a[100:103], v[176:179], v[80:83]
  s_lshr_b32 s65, s65, 1
  v_cvt_pk_bf16_f32 v151, v118, v119
  v_mfma_f32_16x16x32_bf16 v[20:23], a[104:107], v[156:159], v[20:23]
  s_add_u32 s83, s65, s84
  s_add_u32 m0, s62, 0
  v_mfma_f32_16x16x32_bf16 v[20:23], a[108:111], v[160:163], v[20:23]
  s_sub_u32 s83, s83, 28
  buffer_load_dwordx4 v244, s[44:47], s68 offen offset:0 nt lds
  v_mfma_f32_16x16x32_bf16 v[112:115], a[32:35], a[144:147], v[100:103]
  v_exp_f32 v108, v108
  v_mfma_f32_16x16x32_bf16 v[112:115], a[36:39], a[148:151], v[112:115]
  s_max_u32 s94, s94, s83
  ds_read_b64_tr_b16 a[94:95], v235 offset:28672
  ds_read_b64_tr_b16 v[188:189], v187 offset:0
  v_mfma_f32_16x16x32_bf16 v[112:115], a[40:43], a[152:155], v[112:115]
  v_exp_f32 v109, v109
  v_mfma_f32_16x16x32_bf16 v[112:115], a[44:47], a[156:159], v[112:115]
  s_mov_b32 s66, s84
  v_cvt_pk_bf16_f32 v136, v108, v109
  v_mfma_f32_16x16x32_bf16 v[52:55], a[104:107], v[164:167], v[52:55]
  v_exp_f32 v110, v110
  v_mfma_f32_16x16x32_bf16 v[52:55], a[108:111], v[168:171], v[52:55]
  ds_read_b64_tr_b16 a[96:97], v236 offset:16384
  ds_read_b64_tr_b16 a[98:99], v236 offset:20480
  v_mfma_f32_16x16x32_bf16 v[84:87], a[104:107], v[172:175], v[84:87]
  v_exp_f32 v111, v111
  v_mfma_f32_16x16x32_bf16 v[84:87], a[108:111], v[176:179], v[84:87]
  ds_read_b64_tr_b16 a[100:101], v236 offset:24576
  ds_read_b64_tr_b16 v[190:191], v187 offset:2048
  v_cvt_pk_bf16_f32 v137, v110, v111
  v_mfma_f32_16x16x32_bf16 v[116:119], a[32:35], a[160:163], v[104:107]
  v_exp_f32 v112, v112
  v_mfma_f32_16x16x32_bf16 v[116:119], a[36:39], a[164:167], v[116:119]
  buffer_load_dwordx4 v245, s[44:47], s68 offen offset:1024 nt lds
  v_mfma_f32_16x16x32_bf16 v[116:119], a[40:43], a[168:171], v[116:119]
  v_exp_f32 v113, v113
  v_mfma_f32_16x16x32_bf16 v[116:119], a[44:47], a[172:175], v[116:119]
  ds_read_b64_tr_b16 a[102:103], v236 offset:28672
  v_cvt_pk_bf16_f32 v144, v112, v113
  v_mfma_f32_16x16x32_bf16 v[24:27], a[112:115], v[156:159], v[24:27]
  v_exp_f32 v114, v114
  v_mfma_f32_16x16x32_bf16 v[24:27], a[116:119], v[160:163], v[24:27]
  ds_read_b64_tr_b16 a[104:105], v237 offset:16384
  ds_read_b64_tr_b16 a[106:107], v237 offset:20480
  v_mfma_f32_16x16x32_bf16 v[56:59], a[112:115], v[164:167], v[56:59]
  v_exp_f32 v115, v115
  v_mfma_f32_16x16x32_bf16 v[56:59], a[116:119], v[168:171], v[56:59]
  ds_read_b64_tr_b16 a[108:109], v237 offset:24576
  v_cvt_pk_bf16_f32 v145, v114, v115
  v_mfma_f32_16x16x32_bf16 v[108:111], a[48:51], a[128:131], v[96:99]
  v_exp_f32 v116, v116
  v_mfma_f32_16x16x32_bf16 v[108:111], a[52:55], a[132:135], v[108:111]
  buffer_load_dwordx4 v244, s[44:47], s68 offen offset:2048 nt lds
  v_mfma_f32_16x16x32_bf16 v[108:111], a[56:59], a[136:139], v[108:111]
  v_exp_f32 v117, v117
  v_mfma_f32_16x16x32_bf16 v[108:111], a[60:63], a[140:143], v[108:111]
  ds_read_b64_tr_b16 a[110:111], v237 offset:28672
  v_cvt_pk_bf16_f32 v152, v116, v117
  v_mfma_f32_16x16x32_bf16 v[88:91], a[112:115], v[172:175], v[88:91]
  v_exp_f32 v118, v118
  v_mfma_f32_16x16x32_bf16 v[88:91], a[116:119], v[176:179], v[88:91]
  ds_read_b128 a[32:35], v228 offset:24576
  ds_read_b128 a[36:39], v229 offset:24576
  v_mfma_f32_16x16x32_bf16 v[28:31], a[120:123], v[156:159], v[28:31]
  v_exp_f32 v119, v119
  v_mfma_f32_16x16x32_bf16 v[28:31], a[124:127], v[160:163], v[28:31]
  ds_read_b128 a[40:43], v230 offset:24576
  v_cvt_pk_bf16_f32 v153, v118, v119
  v_mfma_f32_16x16x32_bf16 v[60:63], a[120:123], v[164:167], v[60:63]
  buffer_load_dwordx4 v245, s[44:47], s68 offen offset:3072 nt lds
  v_mfma_f32_16x16x32_bf16 v[60:63], a[124:127], v[168:171], v[60:63]
  ds_read_b128 a[44:47], v231 offset:24576
  ds_read_b64_tr_b16 a[112:113], v238 offset:16384
  v_mfma_f32_16x16x32_bf16 v[112:115], a[48:51], a[144:147], v[100:103]
  v_exp_f32 v108, v108
  v_mfma_f32_16x16x32_bf16 v[112:115], a[52:55], a[148:151], v[112:115]
  ds_read_b64_tr_b16 a[114:115], v238 offset:20480
  ds_read_b64_tr_b16 a[116:117], v238 offset:24576
  v_mfma_f32_16x16x32_bf16 v[112:115], a[56:59], a[152:155], v[112:115]
  v_exp_f32 v109, v109
  v_mfma_f32_16x16x32_bf16 v[112:115], a[60:63], a[156:159], v[112:115]
  s_add_u32 m0, s62, 65536
  v_cvt_pk_bf16_f32 v138, v108, v109
  v_mfma_f32_16x16x32_bf16 v[92:95], a[120:123], v[172:175], v[92:95]
  v_exp_f32 v110, v110
  v_mfma_f32_16x16x32_bf16 v[92:95], a[124:127], v[176:179], v[92:95]
  buffer_load_dwordx4 v183, s[52:55], s72 offen offset:3072 lds
  v_mfma_f32_16x16x32_bf16 v[120:123], a[240:243], v[156:159], v[120:123]
  v_exp_f32 v111, v111
  v_mfma_f32_16x16x32_bf16 v[120:123], a[240:243], v[160:163], v[120:123]
  ds_read_b64_tr_b16 a[118:119], v238 offset:28672
  v_cvt_pk_bf16_f32 v139, v110, v111
  v_mfma_f32_16x16x32_bf16 v[116:119], a[48:51], a[160:163], v[104:107]
  v_exp_f32 v112, v112
  v_mfma_f32_16x16x32_bf16 v[116:119], a[52:55], a[164:167], v[116:119]
  ds_read_b64_tr_b16 a[120:121], v239 offset:16384
  ds_read_b64_tr_b16 a[122:123], v239 offset:20480
  v_mfma_f32_16x16x32_bf16 v[116:119], a[56:59], a[168:171], v[116:119]
  v_exp_f32 v113, v113
  v_mfma_f32_16x16x32_bf16 v[116:119], a[60:63], a[172:175], v[116:119]
  ds_read_b64_tr_b16 a[124:125], v239 offset:24576
  v_cvt_pk_bf16_f32 v146, v112, v113
  v_mfma_f32_16x16x32_bf16 v[124:127], a[240:243], v[164:167], v[124:127]
  v_exp_f32 v114, v114
  v_mfma_f32_16x16x32_bf16 v[124:127], a[240:243], v[168:171], v[124:127]
  ds_read_b64_tr_b16 a[126:127], v239 offset:28672
  v_mfma_f32_16x16x32_bf16 v[128:131], a[240:243], v[172:175], v[128:131]
  v_exp_f32 v115, v115
  v_mfma_f32_16x16x32_bf16 v[128:131], a[240:243], v[176:179], v[128:131]
  v_cvt_pk_bf16_f32 v147, v114, v115
  s_waitcnt lgkmcnt(8)
  v_mfma_f32_16x16x32_bf16 v[108:111], a[0:3], a[128:131], v[96:99]
  v_exp_f32 v116, v116
  v_mfma_f32_16x16x32_bf16 v[108:111], a[4:7], a[132:135], v[108:111]
  s_memrealtime s[82:83]
  ds_read_b128 a[48:51], v228 offset:28672
  v_mfma_f32_16x16x32_bf16 v[108:111], a[8:11], a[136:139], v[108:111]
  v_exp_f32 v117, v117
  v_mfma_f32_16x16x32_bf16 v[108:111], a[12:15], a[140:143], v[108:111]
  ds_read_b128 a[52:55], v229 offset:28672
  v_cvt_pk_bf16_f32 v154, v116, v117
  v_mfma_f32_16x16x32_bf16 v[0:3], a[64:67], v[132:135], v[0:3]
  v_exp_f32 v118, v118
  v_mfma_f32_16x16x32_bf16 v[0:3], a[68:71], v[136:139], v[0:3]
  ds_read_b128 a[56:59], v230 offset:28672
  ds_read_b128 a[60:63], v231 offset:28672
  v_mfma_f32_16x16x32_bf16 v[32:35], a[64:67], v[140:143], v[32:35]
  v_exp_f32 v119, v119
  v_mfma_f32_16x16x32_bf16 v[32:35], a[68:71], v[144:147], v[32:35]
  v_cvt_pk_bf16_f32 v155, v118, v119
  v_mfma_f32_16x16x32_bf16 v[112:115], a[0:3], a[144:147], v[100:103]
  v_exp_f32 v108, v108
  v_mfma_f32_16x16x32_bf16 v[112:115], a[4:7], a[148:151], v[112:115]
  v_mfma_f32_16x16x32_bf16 v[112:115], a[8:11], a[152:155], v[112:115]
  v_exp_f32 v109, v109
  v_mfma_f32_16x16x32_bf16 v[112:115], a[12:15], a[156:159], v[112:115]
  v_cvt_pk_bf16_f32 v156, v108, v109
  v_mfma_f32_16x16x32_bf16 v[64:67], a[64:67], v[148:151], v[64:67]
  v_exp_f32 v110, v110
  v_mfma_f32_16x16x32_bf16 v[64:67], a[68:71], v[152:155], v[64:67]
  s_waitcnt vmcnt(11) lgkmcnt(0)
  s_barrier
  v_mfma_f32_16x16x32_bf16 v[4:7], a[72:75], v[132:135], v[4:7]
  v_exp_f32 v111, v111
  v_mfma_f32_16x16x32_bf16 v[4:7], a[76:79], v[136:139], v[4:7]
  s_sub_u32 s84, s82, s85
  v_cvt_pk_bf16_f32 v157, v110, v111
  v_mfma_f32_16x16x32_bf16 v[116:119], a[0:3], a[160:163], v[104:107]
  v_exp_f32 v112, v112
  v_mfma_f32_16x16x32_bf16 v[116:119], a[4:7], a[164:167], v[116:119]
  s_cmp_lt_u32 s84, s94
  s_cbranch_scc1 PC16_%=
PC16_%=e:
  v_mfma_f32_16x16x32_bf16 v[116:119], a[8:11], a[168:171], v[116:119]
  v_exp_f32 v113, v113
  v_mfma_f32_16x16x32_bf16 v[116:119], a[12:15], a[172:175], v[116:119]
  s_add_u32 s68, s60, 327680
  v_cvt_pk_bf16_f32 v164, v112, v113
  v_mfma_f32_16x16x32_bf16 v[36:39], a[72:75], v[140:143], v[36:39]
  v_exp_f32 v114, v114
  v_mfma_f32_16x16x32_bf16 v[36:39], a[76:79], v[144:147], v[36:39]
  s_add_u32 m0, s61, 16384
  ds_read_b64_tr_b16 a[64:65], v232 offset:32768
  v_mfma_f32_16x16x32_bf16 v[68:71], a[72:75], v[148:151], v[68:71]
  v_exp_f32 v115, v115
  v_mfma_f32_16x16x32_bf16 v[68:71], a[76:79], v[152:155], v[68:71]
  ds_read_b64_tr_b16 a[66:67], v232 offset:36864
  v_cvt_pk_bf16_f32 v165, v114, v115
  v_mfma_f32_16x16x32_bf16 v[8:11], a[80:83], v[132:135], v[8:11]
  buffer_load_dwordx4 v240, s[40:43], s68 offen offset:0 nt lds
  v_mfma_f32_16x16x32_bf16 v[8:11], a[84:87], v[136:139], v[8:11]
  buffer_load_dwordx4 v241, s[40:43], s68 offen offset:1024 nt lds
  v_mfma_f32_16x16x32_bf16 v[108:111], a[16:19], a[128:131], v[96:99]
  v_exp_f32 v116, v116
  v_mfma_f32_16x16x32_bf16 v[108:111], a[20:23], a[132:135], v[108:111]
  ds_read_b64_tr_b16 a[68:69], v232 offset:40960
  ds_read_b64_tr_b16 a[70:71], v232 offset:45056
  v_mfma_f32_16x16x32_bf16 v[108:111], a[24:27], a[136:139], v[108:111]
  v_exp_f32 v117, v117
  v_mfma_f32_16x16x32_bf16 v[108:111], a[28:31], a[140:143], v[108:111]
  ds_read_b128 a[0:3], v228 offset:0
  v_cvt_pk_bf16_f32 v172, v116, v117
  v_mfma_f32_16x16x32_bf16 v[40:43], a[80:83], v[140:143], v[40:43]
  v_exp_f32 v118, v118
  v_mfma_f32_16x16x32_bf16 v[40:43], a[84:87], v[144:147], v[40:43]
  ds_read_b128 a[4:7], v229 offset:0
  ds_read_b128 a[8:11], v230 offset:0
  v_mfma_f32_16x16x32_bf16 v[72:75], a[80:83], v[148:151], v[72:75]
  v_exp_f32 v119, v119
  v_mfma_f32_16x16x32_bf16 v[72:75], a[84:87], v[152:155], v[72:75]
  ds_read_b128 a[12:15], v231 offset:0
  v_cvt_pk_bf16_f32 v173, v118, v119
  v_mfma_f32_16x16x32_bf16 v[112:115], a[16:19], a[144:147], v[100:103]
  v_exp_f32 v108, v108
  v_mfma_f32_16x16x32_bf16 v[112:115], a[20:23], a[148:151], v[112:115]
  buffer_load_dwordx4 v242, s[40:43], s68 offen offset:2048 nt lds
  v_mfma_f32_16x16x32_bf16 v[112:115], a[24:27], a[152:155], v[112:115]
  v_exp_f32 v109, v109
  v_mfma_f32_16x16x32_bf16 v[112:115], a[28:31], a[156:159], v[112:115]
  ds_read_b64_tr_b16 a[72:73], v233 offset:32768
  v_cvt_pk_bf16_f32 v158, v108, v109
  v_mfma_f32_16x16x32_bf16 v[12:15], a[88:91], v[132:135], v[12:15]
  v_exp_f32 v110, v110
  v_mfma_f32_16x16x32_bf16 v[12:15], a[92:95], v[136:139], v[12:15]
  ds_read_b64_tr_b16 a[74:75], v233 offset:36864
  ds_read_b64_tr_b16 a[76:77], v233 offset:40960
  ds_read_b64_tr_b16 a[78:79], v233 offset:45056
  v_mfma_f32_16x16x32_bf16 v[44:47], a[88:91], v[140:143], v[44:47]
  ds_read_b64_tr_b16 a[80:81], v234 offset:32768
  v_exp_f32 v111, v111
  v_mfma_f32_16x16x32_bf16 v[44:47], a[92:95], v[144:147], v[44:47]
  buffer_load_dwordx4 v243, s[40:43], s68 offen offset:3072 nt lds
  v_cvt_pk_bf16_f32 v159, v110, v111
  v_mfma_f32_16x16x32_bf16 v[116:119], a[16:19], a[160:163], v[104:107]
  ds_read_b64_tr_b16 a[82:83], v234 offset:36864
  v_exp_f32 v112, v112
  v_mfma_f32_16x16x32_bf16 v[116:119], a[20:23], a[164:167], v[116:119]
  s_add_u32 s97, s97, s95
  s_add_u32 s72, s93, 6
  s_and_b32 s72, s72, 31
  s_mul_i32 s72, s72, 40960
  ds_read_b64_tr_b16 a[84:85], v234 offset:40960
  v_mfma_f32_16x16x32_bf16 v[116:119], a[24:27], a[168:171], v[116:119]
  ds_read_b64_tr_b16 a[86:87], v234 offset:45056
  v_exp_f32 v113, v113
  v_mfma_f32_16x16x32_bf16 v[116:119], a[28:31], a[172:175], v[116:119]
  s_lshr_b32 s83, s97, 4
  v_cvt_pk_bf16_f32 v166, v112, v113
  v_mfma_f32_16x16x32_bf16 v[76:79], a[88:91], v[148:151], v[76:79]
  v_exp_f32 v114, v114
  v_mfma_f32_16x16x32_bf16 v[76:79], a[92:95], v[152:155], v[76:79]
  s_add_u32 s94, s84, s96
  s_add_u32 m0, s62, 49152
  ds_read_b128 a[16:19], v228 offset:4096
  v_mfma_f32_16x16x32_bf16 v[16:19], a[96:99], v[132:135], v[16:19]
  ds_read_b128 a[20:23], v229 offset:4096
  v_exp_f32 v115, v115
  v_mfma_f32_16x16x32_bf16 v[16:19], a[100:103], v[136:139], v[16:19]
  s_max_u32 s94, s94, s83
  ds_read_b128 a[24:27], v230 offset:4096
  v_cvt_pk_bf16_f32 v167, v114, v115
  v_mfma_f32_16x16x32_bf16 v[108:111], a[32:35], a[128:131], v[96:99]
  ds_read_b128 a[28:31], v231 offset:4096
  v_exp_f32 v116, v116
  v_mfma_f32_16x16x32_bf16 v[108:111], a[36:39], a[132:135], v[108:111]
  s_sub_u32 s83, s84, s66
  buffer_load_dwordx4 v180, s[52:55], s72 offen offset:0 lds
  v_mfma_f32_16x16x32_bf16 v[108:111], a[40:43], a[136:139], v[108:111]
  ds_read_b64_tr_b16 a[88:89], v235 offset:32768
  v_exp_f32 v117, v117
  v_mfma_f32_16x16x32_bf16 v[108:111], a[44:47], a[140:143], v[108:111]
  s_min_u32 s83, s83, 200
  ds_read_b64_tr_b16 a[90:91], v235 offset:36864
  v_cvt_pk_bf16_f32 v174, v116, v117
  v_mfma_f32_16x16x32_bf16 v[48:51], a[96:99], v[140:143], v[48:51]
  ds_read_b64_tr_b16 a[92:93], v235 offset:40960
  v_exp_f32 v118, v118
  v_mfma_f32_16x16x32_bf16 v[48:51], a[100:103], v[144:147], v[48:51]
  s_add_u32 s65, s65, s83
  s_add_u32 s68, s60, 311296
  v_mfma_f32_16x16x32_bf16 v[80:83], a[96:99], v[148:151], v[80:83]
  v_exp_f32 v119, v119
  v_mfma_f32_16x16x32_bf16 v[80:83], a[100:103], v[152:155], v[80:83]
  s_lshr_b32 s65, s65, 1
  v_cvt_pk_bf16_f32 v175, v118, v119
  v_mfma_f32_16x16x32_bf16 v[20:23], a[104:107], v[132:135], v[20:23]
  s_add_u32 s83, s65, s84
  s_add_u32 m0, s62, 16384
  v_mfma_f32_16x16x32_bf16 v[20:23], a[108:111], v[136:139], v[20:23]
  s_sub_u32 s83, s83, 28
  buffer_load_dwordx4 v244, s[44:47], s68 offen offset:0 nt lds
  v_mfma_f32_16x16x32_bf16 v[112:115], a[32:35], a[144:147], v[100:103]
  v_exp_f32 v108, v108
  v_mfma_f32_16x16x32_bf16 v[112:115], a[36:39], a[148:151], v[112:115]
  s_max_u32 s94, s94, s83
  ds_read_b64_tr_b16 a[94:95], v235 offset:45056
  v_mfma_f32_16x16x32_bf16 v[112:115], a[40:43], a[152:155], v[112:115]
  v_exp_f32 v109, v109
  v_mfma_f32_16x16x32_bf16 v[112:115], a[44:47], a[156:159], v[112:115]
  s_mov_b32 s66, s84
  v_cvt_pk_bf16_f32 v160, v108, v109
  v_mfma_f32_16x16x32_bf16 v[52:55], a[104:107], v[140:143], v[52:55]
  v_exp_f32 v110, v110
  v_mfma_f32_16x16x32_bf16 v[52:55], a[108:111], v[144:147], v[52:55]
  ds_read_b64_tr_b16 a[96:97], v236 offset:32768
  ds_read_b64_tr_b16 a[98:99], v236 offset:36864
  v_mfma_f32_16x16x32_bf16 v[84:87], a[104:107], v[148:151], v[84:87]
  v_exp_f32 v111, v111
  v_mfma_f32_16x16x32_bf16 v[84:87], a[108:111], v[152:155], v[84:87]
  ds_read_b64_tr_b16 a[100:101], v236 offset:40960
  v_cvt_pk_bf16_f32 v161, v110, v111
  v_mfma_f32_16x16x32_bf16 v[116:119], a[32:35], a[160:163], v[104:107]
  v_exp_f32 v112, v112
  v_mfma_f32_16x16x32_bf16 v[116:119], a[36:39], a[164:167], v[116:119]
  buffer_load_dwordx4 v245, s[44:47], s68 offen offset:1024 nt lds
  v_mfma_f32_16x16x32_bf16 v[116:119], a[40:43], a[168:171], v[116:119]
  v_exp_f32 v113, v113
  v_mfma_f32_16x16x32_bf16 v[116:119], a[44:47], a[172:175], v[116:119]
  ds_read_b64_tr_b16 a[102:103], v236 offset:45056
  v_cvt_pk_bf16_f32 v168, v112, v113
  v_mfma_f32_16x16x32_bf16 v[24:27], a[112:115], v[132:135], v[24:27]
  v_exp_f32 v114, v114
  v_mfma_f32_16x16x32_bf16 v[24:27], a[116:119], v[136:139], v[24:27]
  ds_read_b64_tr_b16 a[104:105], v237 offset:32768
  ds_read_b64_tr_b16 a[106:107], v237 offset:36864
  v_mfma_f32_16x16x32_bf16 v[56:59], a[112:115], v[140:143], v[56:59]
  v_exp_f32 v115, v115
  v_mfma_f32_16x16x32_bf16 v[56:59], a[116:119], v[144:147], v[56:59]
  ds_read_b64_tr_b16 a[108:109], v237 offset:40960
  v_cvt_pk_bf16_f32 v169, v114, v115
  v_mfma_f32_16x16x32_bf16 v[108:111], a[48:51], a[128:131], v[96:99]
  v_exp_f32 v116, v116
  v_mfma_f32_16x16x32_bf16 v[108:111], a[52:55], a[132:135], v[108:111]
  buffer_load_dwordx4 v244, s[44:47], s68 offen offset:2048 nt lds
  v_mfma_f32_16x16x32_bf16 v[108:111], a[56:59], a[136:139], v[108:111]
  v_exp_f32 v117, v117
  v_mfma_f32_16x16x32_bf16 v[108:111], a[60:63], a[140:143], v[108:111]
  ds_read_b64_tr_b16 a[110:111], v237 offset:45056
  v_cvt_pk_bf16_f32 v176, v116, v117
  v_mfma_f32_16x16x32_bf16 v[88:91], a[112:115], v[148:151], v[88:91]
  v_exp_f32 v118, v118
  v_mfma_f32_16x16x32_bf16 v[88:91], a[116:119], v[152:155], v[88:91]
  ds_read_b128 a[32:35], v228 offset:8192
  ds_read_b128 a[36:39], v229 offset:8192
  v_mfma_f32_16x16x32_bf16 v[28:31], a[120:123], v[132:135], v[28:31]
  v_exp_f32 v119, v119
  v_mfma_f32_16x16x32_bf16 v[28:31], a[124:127], v[136:139], v[28:31]
  ds_read_b128 a[40:43], v230 offset:8192
  v_cvt_pk_bf16_f32 v177, v118, v119
  v_mfma_f32_16x16x32_bf16 v[60:63], a[120:123], v[140:143], v[60:63]
  buffer_load_dwordx4 v245, s[44:47], s68 offen offset:3072 nt lds
  v_mfma_f32_16x16x32_bf16 v[60:63], a[124:127], v[144:147], v[60:63]
  ds_read_b128 a[44:47], v231 offset:8192
  ds_read_b64_tr_b16 a[112:113], v238 offset:32768
  v_mfma_f32_16x16x32_bf16 v[112:115], a[48:51], a[144:147], v[100:103]
  v_exp_f32 v108, v108
  v_mfma_f32_16x16x32_bf16 v[112:115], a[52:55], a[148:151], v[112:115]
  ds_read_b64_tr_b16 a[114:115], v238 offset:36864
  ds_read_b64_tr_b16 a[116:117], v238 offset:40960
  v_mfma_f32_16x16x32_bf16 v[112:115], a[56:59], a[152:155], v[112:115]
  v_exp_f32 v109, v109
  v_mfma_f32_16x16x32_bf16 v[112:115], a[60:63], a[156:159], v[112:115]
  s_add_u32 m0, s62, 49152
  v_cvt_pk_bf16_f32 v162, v108, v109
  v_mfma_f32_16x16x32_bf16 v[92:95], a[120:123], v[148:151], v[92:95]
  v_exp_f32 v110, v110
  v_mfma_f32_16x16x32_bf16 v[92:95], a[124:127], v[152:155], v[92:95]
  buffer_load_dwordx4 v181, s[52:55], s72 offen offset:1024 lds
  v_mfma_f32_16x16x32_bf16 v[120:123], a[240:243], v[132:135], v[120:123]
  v_exp_f32 v111, v111
  v_mfma_f32_16x16x32_bf16 v[120:123], a[240:243], v[136:139], v[120:123]
  ds_read_b64_tr_b16 a[118:119], v238 offset:45056
  v_cvt_pk_bf16_f32 v163, v110, v111
  v_mfma_f32_16x16x32_bf16 v[116:119], a[48:51], a[160:163], v[104:107]
  v_exp_f32 v112, v112
  v_mfma_f32_16x16x32_bf16 v[116:119], a[52:55], a[164:167], v[116:119]
  ds_read_b64_tr_b16 a[120:121], v239 offset:32768
  ds_read_b64_tr_b16 a[122:123], v239 offset:36864
  v_mfma_f32_16x16x32_bf16 v[116:119], a[56:59], a[168:171], v[116:119]
  v_exp_f32 v113, v113
  v_mfma_f32_16x16x32_bf16 v[116:119], a[60:63], a[172:175], v[116:119]
  ds_read_b64_tr_b16 a[124:125], v239 offset:40960
  v_cvt_pk_bf16_f32 v170, v112, v113
  v_mfma_f32_16x16x32_bf16 v[124:127], a[240:243], v[140:143], v[124:127]
  v_exp_f32 v114, v114
  v_mfma_f32_16x16x32_bf16 v[124:127], a[240:243], v[144:147], v[124:127]
  ds_read_b64_tr_b16 a[126:127], v239 offset:45056
  v_mfma_f32_16x16x32_bf16 v[128:131], a[240:243], v[148:151], v[128:131]
  v_exp_f32 v115, v115
  v_mfma_f32_16x16x32_bf16 v[128:131], a[240:243], v[152:155], v[128:131]
  v_cvt_pk_bf16_f32 v171, v114, v115
  s_waitcnt lgkmcnt(8)
  v_mfma_f32_16x16x32_bf16 v[108:111], a[0:3], a[128:131], v[96:99]
  v_exp_f32 v116, v116
  v_mfma_f32_16x16x32_bf16 v[108:111], a[4:7], a[132:135], v[108:111]
  s_memrealtime s[82:83]
  ds_read_b128 a[48:51], v228 offset:12288
  v_mfma_f32_16x16x32_bf16 v[108:111], a[8:11], a[136:139], v[108:111]
  v_exp_f32 v117, v117
  v_mfma_f32_16x16x32_bf16 v[108:111], a[12:15], a[140:143], v[108:111]
  ds_read_b128 a[52:55], v229 offset:12288
  v_cvt_pk_bf16_f32 v178, v116, v117
  v_mfma_f32_16x16x32_bf16 v[0:3], a[64:67], v[156:159], v[0:3]
  v_exp_f32 v118, v118
  v_mfma_f32_16x16x32_bf16 v[0:3], a[68:71], v[160:163], v[0:3]
  ds_read_b128 a[56:59], v230 offset:12288
  ds_read_b128 a[60:63], v231 offset:12288
  v_mfma_f32_16x16x32_bf16 v[32:35], a[64:67], v[164:167], v[32:35]
  v_exp_f32 v119, v119
  v_mfma_f32_16x16x32_bf16 v[32:35], a[68:71], v[168:171], v[32:35]
  v_cvt_pk_bf16_f32 v179, v118, v119
  v_mfma_f32_16x16x32_bf16 v[112:115], a[0:3], a[144:147], v[100:103]
  v_exp_f32 v108, v108
  v_mfma_f32_16x16x32_bf16 v[112:115], a[4:7], a[148:151], v[112:115]
  v_mfma_f32_16x16x32_bf16 v[112:115], a[8:11], a[152:155], v[112:115]
  v_exp_f32 v109, v109
  v_mfma_f32_16x16x32_bf16 v[112:115], a[12:15], a[156:159], v[112:115]
  v_cvt_pk_bf16_f32 v132, v108, v109
  v_mfma_f32_16x16x32_bf16 v[64:67], a[64:67], v[172:175], v[64:67]
  v_exp_f32 v110, v110
  v_mfma_f32_16x16x32_bf16 v[64:67], a[68:71], v[176:179], v[64:67]
  s_waitcnt vmcnt(10) lgkmcnt(0)
  s_barrier
  v_mfma_f32_16x16x32_bf16 v[4:7], a[72:75], v[156:159], v[4:7]
  v_exp_f32 v111, v111
  v_mfma_f32_16x16x32_bf16 v[4:7], a[76:79], v[160:163], v[4:7]
  s_sub_u32 s84, s82, s85
  v_cvt_pk_bf16_f32 v133, v110, v111
  v_mfma_f32_16x16x32_bf16 v[116:119], a[0:3], a[160:163], v[104:107]
  v_exp_f32 v112, v112
  v_mfma_f32_16x16x32_bf16 v[116:119], a[4:7], a[164:167], v[116:119]
  s_cmp_lt_u32 s84, s94
  s_cbranch_scc1 PC17_%=
PC17_%=e:
  v_mfma_f32_16x16x32_bf16 v[116:119], a[8:11], a[168:171], v[116:119]
  v_exp_f32 v113, v113
  v_mfma_f32_16x16x32_bf16 v[116:119], a[12:15], a[172:175], v[116:119]
  s_add_u32 s68, s60, 344064
  ds_read_b64_tr_b16 v[192:193], v184 offset:16384
  v_cvt_pk_bf16_f32 v140, v112, v113
  v_mfma_f32_16x16x32_bf16 v[36:39], a[72:75], v[164:167], v[36:39]
  v_exp_f32 v114, v114
  v_mfma_f32_16x16x32_bf16 v[36:39], a[76:79], v[168:171], v[36:39]
  s_add_u32 m0, s61, 0
  ds_read_b64_tr_b16 a[64:65], v232 offset:0
  v_mfma_f32_16x16x32_bf16 v[68:71], a[72:75], v[172:175], v[68:71]
  v_exp_f32 v115, v115
  v_mfma_f32_16x16x32_bf16 v[68:71], a[76:79], v[176:179], v[68:71]
  ds_read_b64_tr_b16 a[66:67], v232 offset:4096
  v_cvt_pk_bf16_f32 v141, v114, v115
  v_mfma_f32_16x16x32_bf16 v[8:11], a[80:83], v[156:159], v[8:11]
  buffer_load_dwordx4 v240, s[40:43], s68 offen offset:0 nt lds
  v_mfma_f32_16x16x32_bf16 v[8:11], a[84:87], v[160:163], v[8:11]
  buffer_load_dwordx4 v241, s[40:43], s68 offen offset:1024 nt lds
  ds_read_b64_tr_b16 v[194:195], v184 offset:18432
  v_mfma_f32_16x16x32_bf16 v[108:111], a[16:19], a[128:131], v[96:99]
  v_exp_f32 v116, v116
  v_mfma_f32_16x16x32_bf16 v[108:111], a[20:23], a[132:135], v[108:111]
  ds_read_b64_tr_b16 a[68:69], v232 offset:8192
  ds_read_b64_tr_b16 a[70:71], v232 offset:12288
  v_mfma_f32_16x16x32_bf16 v[108:111], a[24:27], a[136:139], v[108:111]
  v_exp_f32 v117, v117
  v_mfma_f32_16x16x32_bf16 v[108:111], a[28:31], a[140:143], v[108:111]
  ds_read_b128 a[0:3], v228 offset:49152
  v_cvt_pk_bf16_f32 v148, v116, v117
  v_mfma_f32_16x16x32_bf16 v[40:43], a[80:83], v[164:167], v[40:43]
  v_exp_f32 v118, v118
  v_mfma_f32_16x16x32_bf16 v[40:43], a[84:87], v[168:171], v[40:43]
  ds_read_b128 a[4:7], v229 offset:49152
  ds_read_b128 a[8:11], v230 offset:49152
  ds_read_b64_tr_b16 v[196:197], v185 offset:16384
  v_mfma_f32_16x16x32_bf16 v[72:75], a[80:83], v[172:175], v[72:75]
  v_exp_f32 v119, v119
  v_mfma_f32_16x16x32_bf16 v[72:75], a[84:87], v[176:179], v[72:75]
  ds_read_b128 a[12:15], v231 offset:49152
  v_cvt_pk_bf16_f32 v149, v118, v119
  v_mfma_f32_16x16x32_bf16 v[112:115], a[16:19], a[144:147], v[100:103]
  v_exp_f32 v108, v108
  v_mfma_f32_16x16x32_bf16 v[112:115], a[20:23], a[148:151], v[112:115]
  buffer_load_dwordx4 v242, s[40:43], s68 offen offset:2048 nt lds
  v_mfma_f32_16x16x32_bf16 v[112:115], a[24:27], a[152:155], v[112:115]
  v_exp_f32 v109, v109
  v_mfma_f32_16x16x32_bf16 v[112:115], a[28:31], a[156:159], v[112:115]
  ds_read_b64_tr_b16 a[72:73], v233 offset:0
  ds_read_b64_tr_b16 v[198:199], v185 offset:18432
  v_cvt_pk_bf16_f32 v134, v108, v109
  v_mfma_f32_16x16x32_bf16 v[12:15], a[88:91], v[156:159], v[12:15]
  v_exp_f32 v110, v110
  v_mfma_f32_16x16x32_bf16 v[12:15], a[92:95], v[160:163], v[12:15]
  ds_read_b64_tr_b16 a[74:75], v233 offset:4096
  ds_read_b64_tr_b16 a[76:77], v233 offset:8192
  v_mfma_f32_16x16x32_bf16 v[44:47], a[88:91], v[164:167], v[44:47]
  v_exp_f32 v111, v111
  v_mfma_f32_16x16x32_bf16 v[44:47], a[92:95], v[168:171], v[44:47]
  buffer_load_dwordx4 v243, s[40:43], s68 offen offset:3072 nt lds
  v_cvt_pk_bf16_f32 v135, v110, v111
  v_mfma_f32_16x16x32_bf16 v[116:119], a[16:19], a[160:163], v[104:107]
  v_exp_f32 v112, v112
  v_mfma_f32_16x16x32_bf16 v[116:119], a[20:23], a[164:167], v[116:119]
  s_add_u32 s97, s97, s95
  ds_read_b64_tr_b16 a[78:79], v233 offset:12288
  ds_read_b64_tr_b16 a[80:81], v234 offset:0
  ds_read_b64_tr_b16 v[200:201], v186 offset:16384
  v_mfma_f32_16x16x32_bf16 v[116:119], a[24:27], a[168:171], v[116:119]
  ds_read_b64_tr_b16 a[82:83], v234 offset:4096
  v_exp_f32 v113, v113
  v_mfma_f32_16x16x32_bf16 v[116:119], a[28:31], a[172:175], v[116:119]
  s_lshr_b32 s83, s97, 4
  ds_read_b64_tr_b16 a[84:85], v234 offset:8192
  v_cvt_pk_bf16_f32 v142, v112, v113
  v_mfma_f32_16x16x32_bf16 v[76:79], a[88:91], v[172:175], v[76:79]
  ds_read_b64_tr_b16 a[86:87], v234 offset:12288
  v_exp_f32 v114, v114
  v_mfma_f32_16x16x32_bf16 v[76:79], a[92:95], v[176:179], v[76:79]
  s_add_u32 s94, s84, s96
  s_add_u32 m0, s62, 49152
  ds_read_b128 a[16:19], v228 offset:53248
  v_mfma_f32_16x16x32_bf16 v[16:19], a[96:99], v[156:159], v[16:19]
  ds_read_b128 a[20:23], v229 offset:53248
  v_exp_f32 v115, v115
  v_mfma_f32_16x16x32_bf16 v[16:19], a[100:103], v[160:163], v[16:19]
  s_max_u32 s94, s94, s83
  ds_read_b128 a[24:27], v230 offset:53248
  v_cvt_pk_bf16_f32 v143, v114, v115
  v_mfma_f32_16x16x32_bf16 v[108:111], a[32:35], a[128:131], v[96:99]
  ds_read_b128 a[28:31], v231 offset:53248
  v_exp_f32 v116, v116
  v_mfma_f32_16x16x32_bf16 v[108:111], a[36:39], a[132:135], v[108:111]
  s_sub_u32 s83, s84, s66
  buffer_load_dwordx4 v182, s[52:55], s72 offen offset:2048 lds
  v_mfma_f32_16x16x32_bf16 v[108:111], a[40:43], a[136:139], v[108:111]
  ds_read_b64_tr_b16 a[88:89], v235 offset:0
  v_exp_f32 v117, v117
  v_mfma_f32_16x16x32_bf16 v[108:111], a[44:47], a[140:143], v[108:111]
  s_min_u32 s83, s83, 200
  ds_read_b64_tr_b16 a[90:91], v235 offset:4096
  v_cvt_pk_bf16_f32 v150, v116, v117
  v_mfma_f32_16x16x32_bf16 v[48:51], a[96:99], v[164:167], v[48:51]
  ds_read_b64_tr_b16 a[92:93], v235 offset:8192
  v_exp_f32 v118, v118
  v_mfma_f32_16x16x32_bf16 v[48:51], a[100:103], v[168:171], v[48:51]
  s_add_u32 s65, s65, s83
  s_add_u32 s68, s60, 327680
  ds_read_b64_tr_b16 v[202:203], v186 offset:18432
  v_mfma_f32_16x16x32_bf16 v[80:83], a[96:99], v[172:175], v[80:83]
  v_exp_f32 v119, v119
  v_mfma_f32_16x16x32_bf16 v[80:83], a[100:103], v[176:179], v[80:83]
  s_lshr_b32 s65, s65, 1
  v_cvt_pk_bf16_f32 v151, v118, v119
  v_mfma_f32_16x16x32_bf16 v[20:23], a[104:107], v[156:159], v[20:23]
  s_add_u32 s83, s65, s84
  s_add_u32 m0, s62, 32768
  v_mfma_f32_16x16x32_bf16 v[20:23], a[108:111], v[160:163], v[20:23]
  s_sub_u32 s83, s83, 28
  buffer_load_dwordx4 v244, s[44:47], s68 offen offset:0 lds
  v_mfma_f32_16x16x32_bf16 v[112:115], a[32:35], a[144:147], v[100:103]
  v_exp_f32 v108, v108
  v_mfma_f32_16x16x32_bf16 v[112:115], a[36:39], a[148:151], v[112:115]
  s_max_u32 s94, s94, s83
  ds_read_b64_tr_b16 a[94:95], v235 offset:12288
  ds_read_b64_tr_b16 v[204:205], v187 offset:16384
  v_mfma_f32_16x16x32_bf16 v[112:115], a[40:43], a[152:155], v[112:115]
  v_exp_f32 v109, v109
  v_mfma_f32_16x16x32_bf16 v[112:115], a[44:47], a[156:159], v[112:115]
  s_mov_b32 s66, s84
  v_cvt_pk_bf16_f32 v136, v108, v109
  v_mfma_f32_16x16x32_bf16 v[52:55], a[104:107], v[164:167], v[52:55]
  v_exp_f32 v110, v110
  v_mfma_f32_16x16x32_bf16 v[52:55], a[108:111], v[168:171], v[52:55]
  ds_read_b64_tr_b16 a[96:97], v236 offset:0
  ds_read_b64_tr_b16 a[98:99], v236 offset:4096
  v_mfma_f32_16x16x32_bf16 v[84:87], a[104:107], v[172:175], v[84:87]
  v_exp_f32 v111, v111
  v_mfma_f32_16x16x32_bf16 v[84:87], a[108:111], v[176:179], v[84:87]
  ds_read_b64_tr_b16 a[100:101], v236 offset:8192
  ds_read_b64_tr_b16 v[206:207], v187 offset:18432
  v_cvt_pk_bf16_f32 v137, v110, v111
  v_mfma_f32_16x16x32_bf16 v[116:119], a[32:35], a[160:163], v[104:107]
  v_exp_f32 v112, v112
  v_mfma_f32_16x16x32_bf16 v[116:119], a[36:39], a[164:167], v[116:119]
  buffer_load_dwordx4 v245, s[44:47], s68 offen offset:1024 lds
  v_mfma_f32_16x16x32_bf16 v[116:119], a[40:43], a[168:171], v[116:119]
  v_exp_f32 v113, v113
  v_mfma_f32_16x16x32_bf16 v[116:119], a[44:47], a[172:175], v[116:119]
  ds_read_b64_tr_b16 a[102:103], v236 offset:12288
  v_cvt_pk_bf16_f32 v144, v112, v113
  v_mfma_f32_16x16x32_bf16 v[24:27], a[112:115], v[156:159], v[24:27]
  v_exp_f32 v114, v114
  v_mfma_f32_16x16x32_bf16 v[24:27], a[116:119], v[160:163], v[24:27]
  ds_read_b64_tr_b16 a[104:105], v237 offset:0
  ds_read_b64_tr_b16 a[106:107], v237 offset:4096
  v_mfma_f32_16x16x32_bf16 v[56:59], a[112:115], v[164:167], v[56:59]
  v_exp_f32 v115, v115
  v_mfma_f32_16x16x32_bf16 v[56:59], a[116:119], v[168:171], v[56:59]
  ds_read_b64_tr_b16 a[108:109], v237 offset:8192
  v_cvt_pk_bf16_f32 v145, v114, v115
  v_mfma_f32_16x16x32_bf16 v[108:111], a[48:51], a[128:131], v[96:99]
  v_exp_f32 v116, v116
  v_mfma_f32_16x16x32_bf16 v[108:111], a[52:55], a[132:135], v[108:111]
  buffer_load_dwordx4 v244, s[44:47], s68 offen offset:2048 lds
  v_mfma_f32_16x16x32_bf16 v[108:111], a[56:59], a[136:139], v[108:111]
  v_exp_f32 v117, v117
  v_mfma_f32_16x16x32_bf16 v[108:111], a[60:63], a[140:143], v[108:111]
  ds_read_b64_tr_b16 a[110:111], v237 offset:12288
  v_cvt_pk_bf16_f32 v152, v116, v117
  v_mfma_f32_16x16x32_bf16 v[88:91], a[112:115], v[172:175], v[88:91]
  v_exp_f32 v118, v118
  v_mfma_f32_16x16x32_bf16 v[88:91], a[116:119], v[176:179], v[88:91]
  ds_read_b128 a[32:35], v228 offset:57344
  ds_read_b128 a[36:39], v229 offset:57344
  v_mfma_f32_16x16x32_bf16 v[28:31], a[120:123], v[156:159], v[28:31]
  v_exp_f32 v119, v119
  v_mfma_f32_16x16x32_bf16 v[28:31], a[124:127], v[160:163], v[28:31]
  ds_read_b128 a[40:43], v230 offset:57344
  v_cvt_pk_bf16_f32 v153, v118, v119
  v_mfma_f32_16x16x32_bf16 v[60:63], a[120:123], v[164:167], v[60:63]
  buffer_load_dwordx4 v245, s[44:47], s68 offen offset:3072 lds
  v_mfma_f32_16x16x32_bf16 v[60:63], a[124:127], v[168:171], v[60:63]
  ds_read_b128 a[44:47], v231 offset:57344
  ds_read_b64_tr_b16 a[112:113], v238 offset:0
  v_mfma_f32_16x16x32_bf16 v[112:115], a[48:51], a[144:147], v[100:103]
  v_exp_f32 v108, v108
  v_mfma_f32_16x16x32_bf16 v[112:115], a[52:55], a[148:151], v[112:115]
  ds_read_b64_tr_b16 a[114:115], v238 offset:4096
  ds_read_b64_tr_b16 a[116:117], v238 offset:8192
  v_mfma_f32_16x16x32_bf16 v[112:115], a[56:59], a[152:155], v[112:115]
  v_exp_f32 v109, v109
  v_mfma_f32_16x16x32_bf16 v[112:115], a[60:63], a[156:159], v[112:115]
  s_add_u32 m0, s62, 49152
  v_cvt_pk_bf16_f32 v138, v108, v109
  v_mfma_f32_16x16x32_bf16 v[92:95], a[120:123], v[172:175], v[92:95]
  v_exp_f32 v110, v110
  v_mfma_f32_16x16x32_bf16 v[92:95], a[124:127], v[176:179], v[92:95]
  buffer_load_dwordx4 v183, s[52:55], s72 offen offset:3072 lds
  v_mfma_f32_16x16x32_bf16 v[120:123], a[240:243], v[156:159], v[120:123]
  v_exp_f32 v111, v111
  v_mfma_f32_16x16x32_bf16 v[120:123], a[240:243], v[160:163], v[120:123]
  ds_read_b64_tr_b16 a[118:119], v238 offset:12288
  v_cvt_pk_bf16_f32 v139, v110, v111
  v_mfma_f32_16x16x32_bf16 v[116:119], a[48:51], a[160:163], v[104:107]
  v_exp_f32 v112, v112
  v_mfma_f32_16x16x32_bf16 v[116:119], a[52:55], a[164:167], v[116:119]
  ds_read_b64_tr_b16 a[120:121], v239 offset:0
  ds_read_b64_tr_b16 a[122:123], v239 offset:4096
  v_mfma_f32_16x16x32_bf16 v[116:119], a[56:59], a[168:171], v[116:119]
  v_exp_f32 v113, v113
  v_mfma_f32_16x16x32_bf16 v[116:119], a[60:63], a[172:175], v[116:119]
  ds_read_b64_tr_b16 a[124:125], v239 offset:8192
  v_cvt_pk_bf16_f32 v146, v112, v113
  v_mfma_f32_16x16x32_bf16 v[124:127], a[240:243], v[164:167], v[124:127]
  v_exp_f32 v114, v114
  v_mfma_f32_16x16x32_bf16 v[124:127], a[240:243], v[168:171], v[124:127]
  ds_read_b64_tr_b16 a[126:127], v239 offset:12288
  v_mfma_f32_16x16x32_bf16 v[128:131], a[240:243], v[172:175], v[128:131]
  v_exp_f32 v115, v115
  v_mfma_f32_16x16x32_bf16 v[128:131], a[240:243], v[176:179], v[128:131]
  v_cvt_pk_bf16_f32 v147, v114, v115
  s_waitcnt lgkmcnt(8)
  v_mfma_f32_16x16x32_bf16 v[108:111], a[0:3], a[128:131], v[96:99]
  v_exp_f32 v116, v116
  v_mfma_f32_16x16x32_bf16 v[108:111], a[4:7], a[132:135], v[108:111]
  s_memrealtime s[82:83]
  ds_read_b128 a[48:51], v228 offset:61440
  v_mfma_f32_16x16x32_bf16 v[108:111], a[8:11], a[136:139], v[108:111]
  v_exp_f32 v117, v117
  v_mfma_f32_16x16x32_bf16 v[108:111], a[12:15], a[140:143], v[108:111]
  ds_read_b128 a[52:55], v229 offset:61440
  v_cvt_pk_bf16_f32 v154, v116, v117
  v_mfma_f32_16x16x32_bf16 v[0:3], a[64:67], v[132:135], v[0:3]
  v_exp_f32 v118, v118
  v_mfma_f32_16x16x32_bf16 v[0:3], a[68:71], v[136:139], v[0:3]
  ds_read_b128 a[56:59], v230 offset:61440
  ds_read_b128 a[60:63], v231 offset:61440
  v_mfma_f32_16x16x32_bf16 v[32:35], a[64:67], v[140:143], v[32:35]
  v_exp_f32 v119, v119
  v_mfma_f32_16x16x32_bf16 v[32:35], a[68:71], v[144:147], v[32:35]
  v_cvt_pk_bf16_f32 v155, v118, v119
  v_mfma_f32_16x16x32_bf16 v[112:115], a[0:3], a[144:147], v[100:103]
  v_exp_f32 v108, v108
  v_mfma_f32_16x16x32_bf16 v[112:115], a[4:7], a[148:151], v[112:115]
  v_mfma_f32_16x16x32_bf16 v[112:115], a[8:11], a[152:155], v[112:115]
  v_exp_f32 v109, v109
  v_mfma_f32_16x16x32_bf16 v[112:115], a[12:15], a[156:159], v[112:115]
  v_cvt_pk_bf16_f32 v156, v108, v109
  v_mfma_f32_16x16x32_bf16 v[64:67], a[64:67], v[148:151], v[64:67]
  v_exp_f32 v110, v110
  v_mfma_f32_16x16x32_bf16 v[64:67], a[68:71], v[152:155], v[64:67]
  s_waitcnt vmcnt(11) lgkmcnt(0)
  s_barrier
  v_mfma_f32_16x16x32_bf16 v[4:7], a[72:75], v[132:135], v[4:7]
  v_exp_f32 v111, v111
  v_mfma_f32_16x16x32_bf16 v[4:7], a[76:79], v[136:139], v[4:7]
  s_sub_u32 s84, s82, s85
  v_cvt_pk_bf16_f32 v157, v110, v111
  v_mfma_f32_16x16x32_bf16 v[116:119], a[0:3], a[160:163], v[104:107]
  v_exp_f32 v112, v112
  v_mfma_f32_16x16x32_bf16 v[116:119], a[4:7], a[164:167], v[116:119]
  s_cmp_lt_u32 s84, s94
  s_cbranch_scc1 PC18_%=
PC18_%=e:
  v_mfma_f32_16x16x32_bf16 v[116:119], a[8:11], a[168:171], v[116:119]
  v_exp_f32 v113, v113
  v_mfma_f32_16x16x32_bf16 v[116:119], a[12:15], a[172:175], v[116:119]
  s_add_u32 s68, s60, 360448
  v_cvt_pk_bf16_f32 v164, v112, v113
  v_mfma_f32_16x16x32_bf16 v[36:39], a[72:75], v[140:143], v[36:39]
  v_exp_f32 v114, v114
  v_mfma_f32_16x16x32_bf16 v[36:39], a[76:79], v[144:147], v[36:39]
  s_add_u32 m0, s61, 49152
  ds_read_b64_tr_b16 a[64:65], v232 offset:16384
  v_mfma_f32_16x16x32_bf16 v[68:71], a[72:75], v[148:151], v[68:71]
  v_exp_f32 v115, v115
  v_mfma_f32_16x16x32_bf16 v[68:71], a[76:79], v[152:155], v[68:71]
  ds_read_b64_tr_b16 a[66:67], v232 offset:20480
  v_cvt_pk_bf16_f32 v165, v114, v115
  v_mfma_f32_16x16x32_bf16 v[8:11], a[80:83], v[132:135], v[8:11]
  buffer_load_dwordx4 v240, s[40:43], s68 offen offset:0 nt lds
  v_mfma_f32_16x16x32_bf16 v[8:11], a[84:87], v[136:139], v[8:11]
  buffer_load_dwordx4 v241, s[40:43], s68 offen offset:1024 nt lds
  v_mfma_f32_16x16x32_bf16 v[108:111], a[16:19], a[128:131], v[96:99]
  v_exp_f32 v116, v116
  v_mfma_f32_16x16x32_bf16 v[108:111], a[20:23], a[132:135], v[108:111]
  ds_read_b64_tr_b16 a[68:69], v232 offset:24576
  ds_read_b64_tr_b16 a[70:71], v232 offset:28672
  v_mfma_f32_16x16x32_bf16 v[108:111], a[24:27], a[136:139], v[108:111]
  v_exp_f32 v117, v117
  v_mfma_f32_16x16x32_bf16 v[108:111], a[28:31], a[140:143], v[108:111]
  ds_read_b128 a[0:3], v228 offset:16384
  v_cvt_pk_bf16_f32 v172, v116, v117
  v_mfma_f32_16x16x32_bf16 v[40:43], a[80:83], v[140:143], v[40:43]
  v_exp_f32 v118, v118
  v_mfma_f32_16x16x32_bf16 v[40:43], a[84:87], v[144:147], v[40:43]
  ds_read_b128 a[4:7], v229 offset:16384
  ds_read_b128 a[8:11], v230 offset:16384
  v_mfma_f32_16x16x32_bf16 v[72:75], a[80:83], v[148:151], v[72:75]
  v_exp_f32 v119, v119
  v_mfma_f32_16x16x32_bf16 v[72:75], a[84:87], v[152:155], v[72:75]
  ds_read_b128 a[12:15], v231 offset:16384
  v_cvt_pk_bf16_f32 v173, v118, v119
  v_mfma_f32_16x16x32_bf16 v[112:115], a[16:19], a[144:147], v[100:103]
  v_exp_f32 v108, v108
  v_mfma_f32_16x16x32_bf16 v[112:115], a[20:23], a[148:151], v[112:115]
  buffer_load_dwordx4 v242, s[40:43], s68 offen offset:2048 nt lds
  v_mfma_f32_16x16x32_bf16 v[112:115], a[24:27], a[152:155], v[112:115]
  v_exp_f32 v109, v109
  v_mfma_f32_16x16x32_bf16 v[112:115], a[28:31], a[156:159], v[112:115]
  ds_read_b64_tr_b16 a[72:73], v233 offset:16384
  v_cvt_pk_bf16_f32 v158, v108, v109
  v_mfma_f32_16x16x32_bf16 v[12:15], a[88:91], v[132:135], v[12:15]
  v_exp_f32 v110, v110
  v_mfma_f32_16x16x32_bf16 v[12:15], a[92:95], v[136:139], v[12:15]
  ds_read_b64_tr_b16 a[74:75], v233 offset:20480
  ds_read_b64_tr_b16 a[76:77], v233 offset:24576
  ds_read_b64_tr_b16 a[78:79], v233 offset:28672
  v_mfma_f32_16x16x32_bf16 v[44:47], a[88:91], v[140:143], v[44:47]
  ds_read_b64_tr_b16 a[80:81], v234 offset:16384
  v_exp_f32 v111, v111
  v_mfma_f32_16x16x32_bf16 v[44:47], a[92:95], v[144:147], v[44:47]
  buffer_load_dwordx4 v243, s[40:43], s68 offen offset:3072 nt lds
  v_cvt_pk_bf16_f32 v159, v110, v111
  v_mfma_f32_16x16x32_bf16 v[116:119], a[16:19], a[160:163], v[104:107]
  ds_read_b64_tr_b16 a[82:83], v234 offset:20480
  v_exp_f32 v112, v112
  v_mfma_f32_16x16x32_bf16 v[116:119], a[20:23], a[164:167], v[116:119]
  s_add_u32 s97, s97, s95
  s_add_u32 s72, s93, 7
  s_and_b32 s72, s72, 31
  s_mul_i32 s72, s72, 40960
  ds_read_b64_tr_b16 a[84:85], v234 offset:24576
  v_mfma_f32_16x16x32_bf16 v[116:119], a[24:27], a[168:171], v[116:119]
  ds_read_b64_tr_b16 a[86:87], v234 offset:28672
  v_exp_f32 v113, v113
  v_mfma_f32_16x16x32_bf16 v[116:119], a[28:31], a[172:175], v[116:119]
  s_lshr_b32 s83, s97, 4
  v_cvt_pk_bf16_f32 v166, v112, v113
  v_mfma_f32_16x16x32_bf16 v[76:79], a[88:91], v[148:151], v[76:79]
  v_exp_f32 v114, v114
  v_mfma_f32_16x16x32_bf16 v[76:79], a[92:95], v[152:155], v[76:79]
  s_add_u32 s94, s84, s96
  s_add_u32 m0, s62, 65536
  ds_read_b128 a[16:19], v228 offset:20480
  v_mfma_f32_16x16x32_bf16 v[16:19], a[96:99], v[132:135], v[16:19]
  ds_read_b128 a[20:23], v229 offset:20480
  v_exp_f32 v115, v115
  v_mfma_f32_16x16x32_bf16 v[16:19], a[100:103], v[136:139], v[16:19]
  s_max_u32 s94, s94, s83
  ds_read_b128 a[24:27], v230 offset:20480
  v_cvt_pk_bf16_f32 v167, v114, v115
  v_mfma_f32_16x16x32_bf16 v[108:111], a[32:35], a[128:131], v[96:99]
  ds_read_b128 a[28:31], v231 offset:20480
  v_exp_f32 v116, v116
  v_mfma_f32_16x16x32_bf16 v[108:111], a[36:39], a[132:135], v[108:111]
  s_sub_u32 s83, s84, s66
  buffer_load_dwordx4 v180, s[52:55], s72 offen offset:0 lds
  v_mfma_f32_16x16x32_bf16 v[108:111], a[40:43], a[136:139], v[108:111]
  ds_read_b64_tr_b16 a[88:89], v235 offset:16384
  v_exp_f32 v117, v117
  v_mfma_f32_16x16x32_bf16 v[108:111], a[44:47], a[140:143], v[108:111]
  s_min_u32 s83, s83, 200
  ds_read_b64_tr_b16 a[90:91], v235 offset:20480
  v_cvt_pk_bf16_f32 v174, v116, v117
  v_mfma_f32_16x16x32_bf16 v[48:51], a[96:99], v[140:143], v[48:51]
  ds_read_b64_tr_b16 a[92:93], v235 offset:24576
  v_exp_f32 v118, v118
  v_mfma_f32_16x16x32_bf16 v[48:51], a[100:103], v[144:147], v[48:51]
  s_add_u32 s65, s65, s83
  s_add_u32 s68, s60, 344064
  v_mfma_f32_16x16x32_bf16 v[80:83], a[96:99], v[148:151], v[80:83]
  v_exp_f32 v119, v119
  v_mfma_f32_16x16x32_bf16 v[80:83], a[100:103], v[152:155], v[80:83]
  s_lshr_b32 s65, s65, 1
  v_cvt_pk_bf16_f32 v175, v118, v119
  v_mfma_f32_16x16x32_bf16 v[20:23], a[104:107], v[132:135], v[20:23]
  s_add_u32 s83, s65, s84
  s_add_u32 m0, s62, 0
  v_mfma_f32_16x16x32_bf16 v[20:23], a[108:111], v[136:139], v[20:23]
  s_sub_u32 s83, s83, 28
  buffer_load_dwordx4 v244, s[44:47], s68 offen offset:0 nt lds
  v_mfma_f32_16x16x32_bf16 v[112:115], a[32:35], a[144:147], v[100:103]
  v_exp_f32 v108, v108
  v_mfma_f32_16x16x32_bf16 v[112:115], a[36:39], a[148:151], v[112:115]
  s_max_u32 s94, s94, s83
  ds_read_b64_tr_b16 a[94:95], v235 offset:28672
  v_mfma_f32_16x16x32_bf16 v[112:115], a[40:43], a[152:155], v[112:115]
  v_exp_f32 v109, v109
  v_mfma_f32_16x16x32_bf16 v[112:115], a[44:47], a[156:159], v[112:115]
  s_mov_b32 s66, s84
  v_cvt_pk_bf16_f32 v160, v108, v109
  v_mfma_f32_16x16x32_bf16 v[52:55], a[104:107], v[140:143], v[52:55]
  v_exp_f32 v110, v110
  v_mfma_f32_16x16x32_bf16 v[52:55], a[108:111], v[144:147], v[52:55]
  ds_read_b64_tr_b16 a[96:97], v236 offset:16384
  ds_read_b64_tr_b16 a[98:99], v236 offset:20480
  v_mfma_f32_16x16x32_bf16 v[84:87], a[104:107], v[148:151], v[84:87]
  v_exp_f32 v111, v111
  v_mfma_f32_16x16x32_bf16 v[84:87], a[108:111], v[152:155], v[84:87]
  ds_read_b64_tr_b16 a[100:101], v236 offset:24576
  v_cvt_pk_bf16_f32 v161, v110, v111
  v_mfma_f32_16x16x32_bf16 v[116:119], a[32:35], a[160:163], v[104:107]
  v_exp_f32 v112, v112
  v_mfma_f32_16x16x32_bf16 v[116:119], a[36:39], a[164:167], v[116:119]
  buffer_load_dwordx4 v245, s[44:47], s68 offen offset:1024 nt lds
  v_mfma_f32_16x16x32_bf16 v[116:119], a[40:43], a[168:171], v[116:119]
  v_exp_f32 v113, v113
  v_mfma_f32_16x16x32_bf16 v[116:119], a[44:47], a[172:175], v[116:119]
  ds_read_b64_tr_b16 a[102:103], v236 offset:28672
  v_cvt_pk_bf16_f32 v168, v112, v113
  v_mfma_f32_16x16x32_bf16 v[24:27], a[112:115], v[132:135], v[24:27]
  v_exp_f32 v114, v114
  v_mfma_f32_16x16x32_bf16 v[24:27], a[116:119], v[136:139], v[24:27]
  ds_read_b64_tr_b16 a[104:105], v237 offset:16384
  ds_read_b64_tr_b16 a[106:107], v237 offset:20480
  v_mfma_f32_16x16x32_bf16 v[56:59], a[112:115], v[140:143], v[56:59]
  v_exp_f32 v115, v115
  v_mfma_f32_16x16x32_bf16 v[56:59], a[116:119], v[144:147], v[56:59]
  ds_read_b64_tr_b16 a[108:109], v237 offset:24576
  v_cvt_pk_bf16_f32 v169, v114, v115
  v_mfma_f32_16x16x32_bf16 v[108:111], a[48:51], a[128:131], v[96:99]
  v_exp_f32 v116, v116
  v_mfma_f32_16x16x32_bf16 v[108:111], a[52:55], a[132:135], v[108:111]
  buffer_load_dwordx4 v244, s[44:47], s68 offen offset:2048 nt lds
  v_mfma_f32_16x16x32_bf16 v[108:111], a[56:59], a[136:139], v[108:111]
  v_exp_f32 v117, v117
  v_mfma_f32_16x16x32_bf16 v[108:111], a[60:63], a[140:143], v[108:111]
  ds_read_b64_tr_b16 a[110:111], v237 offset:28672
  v_cvt_pk_bf16_f32 v176, v116, v117
  v_mfma_f32_16x16x32_bf16 v[88:91], a[112:115], v[148:151], v[88:91]
  v_exp_f32 v118, v118
  v_mfma_f32_16x16x32_bf16 v[88:91], a[116:119], v[152:155], v[88:91]
  ds_read_b128 a[32:35], v228 offset:24576
  ds_read_b128 a[36:39], v229 offset:24576
  v_mfma_f32_16x16x32_bf16 v[28:31], a[120:123], v[132:135], v[28:31]
  v_exp_f32 v119, v119
  v_mfma_f32_16x16x32_bf16 v[28:31], a[124:127], v[136:139], v[28:31]
  ds_read_b128 a[40:43], v230 offset:24576
  v_cvt_pk_bf16_f32 v177, v118, v119
  v_mfma_f32_16x16x32_bf16 v[60:63], a[120:123], v[140:143], v[60:63]
  buffer_load_dwordx4 v245, s[44:47], s68 offen offset:3072 nt lds
  v_mfma_f32_16x16x32_bf16 v[60:63], a[124:127], v[144:147], v[60:63]
  ds_read_b128 a[44:47], v231 offset:24576
  ds_read_b64_tr_b16 a[112:113], v238 offset:16384
  v_mfma_f32_16x16x32_bf16 v[112:115], a[48:51], a[144:147], v[100:103]
  v_exp_f32 v108, v108
  v_mfma_f32_16x16x32_bf16 v[112:115], a[52:55], a[148:151], v[112:115]
  ds_read_b64_tr_b16 a[114:115], v238 offset:20480
  ds_read_b64_tr_b16 a[116:117], v238 offset:24576
  v_mfma_f32_16x16x32_bf16 v[112:115], a[56:59], a[152:155], v[112:115]
  v_exp_f32 v109, v109
  v_mfma_f32_16x16x32_bf16 v[112:115], a[60:63], a[156:159], v[112:115]
  s_add_u32 m0, s62, 65536
  v_cvt_pk_bf16_f32 v162, v108, v109
  v_mfma_f32_16x16x32_bf16 v[92:95], a[120:123], v[148:151], v[92:95]
  v_exp_f32 v110, v110
  v_mfma_f32_16x16x32_bf16 v[92:95], a[124:127], v[152:155], v[92:95]
  buffer_load_dwordx4 v181, s[52:55], s72 offen offset:1024 lds
  v_mfma_f32_16x16x32_bf16 v[120:123], a[240:243], v[132:135], v[120:123]
  v_exp_f32 v111, v111
  v_mfma_f32_16x16x32_bf16 v[120:123], a[240:243], v[136:139], v[120:123]
  ds_read_b64_tr_b16 a[118:119], v238 offset:28672
  v_cvt_pk_bf16_f32 v163, v110, v111
  v_mfma_f32_16x16x32_bf16 v[116:119], a[48:51], a[160:163], v[104:107]
  v_exp_f32 v112, v112
  v_mfma_f32_16x16x32_bf16 v[116:119], a[52:55], a[164:167], v[116:119]
  ds_read_b64_tr_b16 a[120:121], v239 offset:16384
  ds_read_b64_tr_b16 a[122:123], v239 offset:20480
  v_mfma_f32_16x16x32_bf16 v[116:119], a[56:59], a[168:171], v[116:119]
  v_exp_f32 v113, v113
  v_mfma_f32_16x16x32_bf16 v[116:119], a[60:63], a[172:175], v[116:119]
  ds_read_b64_tr_b16 a[124:125], v239 offset:24576
  v_cvt_pk_bf16_f32 v170, v112, v113
  v_mfma_f32_16x16x32_bf16 v[124:127], a[240:243], v[140:143], v[124:127]
  v_exp_f32 v114, v114
  v_mfma_f32_16x16x32_bf16 v[124:127], a[240:243], v[144:147], v[124:127]
  ds_read_b64_tr_b16 a[126:127], v239 offset:28672
  v_mfma_f32_16x16x32_bf16 v[128:131], a[240:243], v[148:151], v[128:131]
  v_exp_f32 v115, v115
  v_mfma_f32_16x16x32_bf16 v[128:131], a[240:243], v[152:155], v[128:131]
  v_cvt_pk_bf16_f32 v171, v114, v115
  s_waitcnt lgkmcnt(8)
  v_mfma_f32_16x16x32_bf16 v[108:111], a[0:3], a[128:131], v[96:99]
  v_exp_f32 v116, v116
  v_mfma_f32_16x16x32_bf16 v[108:111], a[4:7], a[132:135], v[108:111]
  s_memrealtime s[82:83]
  ds_read_b128 a[48:51], v228 offset:28672
  v_mfma_f32_16x16x32_bf16 v[108:111], a[8:11], a[136:139], v[108:111]
  v_exp_f32 v117, v117
  v_mfma_f32_16x16x32_bf16 v[108:111], a[12:15], a[140:143], v[108:111]
  ds_read_b128 a[52:55], v229 offset:28672
  v_cvt_pk_bf16_f32 v178, v116, v117
  v_mfma_f32_16x16x32_bf16 v[0:3], a[64:67], v[156:159], v[0:3]
  v_exp_f32 v118, v118
  v_mfma_f32_16x16x32_bf16 v[0:3], a[68:71], v[160:163], v[0:3]
  ds_read_b128 a[56:59], v230 offset:28672
  ds_read_b128 a[60:63], v231 offset:28672
  v_mfma_f32_16x16x32_bf16 v[32:35], a[64:67], v[164:167], v[32:35]
  v_exp_f32 v119, v119
  v_mfma_f32_16x16x32_bf16 v[32:35], a[68:71], v[168:171], v[32:35]
  v_cvt_pk_bf16_f32 v179, v118, v119
  v_mfma_f32_16x16x32_bf16 v[112:115], a[0:3], a[144:147], v[100:103]
  v_exp_f32 v108, v108
  v_mfma_f32_16x16x32_bf16 v[112:115], a[4:7], a[148:151], v[112:115]
  v_mfma_f32_16x16x32_bf16 v[112:115], a[8:11], a[152:155], v[112:115]
  v_exp_f32 v109, v109
  v_mfma_f32_16x16x32_bf16 v[112:115], a[12:15], a[156:159], v[112:115]
  v_cvt_pk_bf16_f32 v132, v108, v109
  v_mfma_f32_16x16x32_bf16 v[64:67], a[64:67], v[172:175], v[64:67]
  v_exp_f32 v110, v110
  v_mfma_f32_16x16x32_bf16 v[64:67], a[68:71], v[176:179], v[64:67]
  s_waitcnt vmcnt(10) lgkmcnt(0)
  s_barrier
  v_mfma_f32_16x16x32_bf16 v[4:7], a[72:75], v[156:159], v[4:7]
  v_exp_f32 v111, v111
  v_mfma_f32_16x16x32_bf16 v[4:7], a[76:79], v[160:163], v[4:7]
  s_sub_u32 s84, s82, s85
  v_cvt_pk_bf16_f32 v133, v110, v111
  v_mfma_f32_16x16x32_bf16 v[116:119], a[0:3], a[160:163], v[104:107]
  v_exp_f32 v112, v112
  v_mfma_f32_16x16x32_bf16 v[116:119], a[4:7], a[164:167], v[116:119]
  s_cmp_lt_u32 s84, s94
  s_cbranch_scc1 PC19_%=
PC19_%=e:
  v_mfma_f32_16x16x32_bf16 v[116:119], a[8:11], a[168:171], v[116:119]
  v_exp_f32 v113, v113
  v_mfma_f32_16x16x32_bf16 v[116:119], a[12:15], a[172:175], v[116:119]
  s_add_u32 s68, s60, 376832
  ds_read_b64_tr_b16 v[208:209], v184 offset:0
  v_cvt_pk_bf16_f32 v140, v112, v113
  v_mfma_f32_16x16x32_bf16 v[36:39], a[72:75], v[164:167], v[36:39]
  v_exp_f32 v114, v114
  v_mfma_f32_16x16x32_bf16 v[36:39], a[76:79], v[168:171], v[36:39]
  s_add_u32 m0, s61, 16384
  ds_read_b64_tr_b16 a[64:65], v232 offset:32768
  v_mfma_f32_16x16x32_bf16 v[68:71], a[72:75], v[172:175], v[68:71]
  v_exp_f32 v115, v115
  v_mfma_f32_16x16x32_bf16 v[68:71], a[76:79], v[176:179], v[68:71]
  ds_read_b64_tr_b16 a[66:67], v232 offset:36864
  v_cvt_pk_bf16_f32 v141, v114, v115
  v_mfma_f32_16x16x32_bf16 v[8:11], a[80:83], v[156:159], v[8:11]
  buffer_load_dwordx4 v240, s[40:43], s68 offen offset:0 nt lds
  v_mfma_f32_16x16x32_bf16 v[8:11], a[84:87], v[160:163], v[8:11]
  buffer_load_dwordx4 v241, s[40:43], s68 offen offset:1024 nt lds
  ds_read_b64_tr_b16 v[210:211], v184 offset:2048
  v_mfma_f32_16x16x32_bf16 v[108:111], a[16:19], a[128:131], v[96:99]
  v_exp_f32 v116, v116
  v_mfma_f32_16x16x32_bf16 v[108:111], a[20:23], a[132:135], v[108:111]
  ds_read_b64_tr_b16 a[68:69], v232 offset:40960
  ds_read_b64_tr_b16 a[70:71], v232 offset:45056
  v_mfma_f32_16x16x32_bf16 v[108:111], a[24:27], a[136:139], v[108:111]
  v_exp_f32 v117, v117
  v_mfma_f32_16x16x32_bf16 v[108:111], a[28:31], a[140:143], v[108:111]
  ds_read_b128 a[0:3], v228 offset:0
  v_cvt_pk_bf16_f32 v148, v116, v117
  v_mfma_f32_16x16x32_bf16 v[40:43], a[80:83], v[164:167], v[40:43]
  v_exp_f32 v118, v118
  v_mfma_f32_16x16x32_bf16 v[40:43], a[84:87], v[168:171], v[40:43]
  ds_read_b128 a[4:7], v229 offset:0
  ds_read_b128 a[8:11], v230 offset:0
  ds_read_b64_tr_b16 v[212:213], v185 offset:0
  v_mfma_f32_16x16x32_bf16 v[72:75], a[80:83], v[172:175], v[72:75]
  v_exp_f32 v119, v119
  v_mfma_f32_16x16x32_bf16 v[72:75], a[84:87], v[176:179], v[72:75]
  ds_read_b128 a[12:15], v231 offset:0
  v_cvt_pk_bf16_f32 v149, v118, v119
  v_mfma_f32_16x16x32_bf16 v[112:115], a[16:19], a[144:147], v[100:103]
  v_exp_f32 v108, v108
  v_mfma_f32_16x16x32_bf16 v[112:115], a[20:23], a[148:151], v[112:115]
  buffer_load_dwordx4 v242, s[40:43], s68 offen offset:2048 nt lds
  v_mfma_f32_16x16x32_bf16 v[112:115], a[24:27], a[152:155], v[112:115]
  v_exp_f32 v109, v109
  v_mfma_f32_16x16x32_bf16 v[112:115], a[28:31], a[156:159], v[112:115]
  ds_read_b64_tr_b16 a[72:73], v233 offset:32768
  ds_read_b64_tr_b16 v[214:215], v185 offset:2048
  v_cvt_pk_bf16_f32 v134, v108, v109
  v_mfma_f32_16x16x32_bf16 v[12:15], a[88:91], v[156:159], v[12:15]
  v_exp_f32 v110, v110
  v_mfma_f32_16x16x32_bf16 v[12:15], a[92:95], v[160:163], v[12:15]
  ds_read_b64_tr_b16 a[74:75], v233 offset:36864
  ds_read_b64_tr_b16 a[76:77], v233 offset:40960
  v_mfma_f32_16x16x32_bf16 v[44:47], a[88:91], v[164:167], v[44:47]
  v_exp_f32 v111, v111
  v_mfma_f32_16x16x32_bf16 v[44:47], a[92:95], v[168:171], v[44:47]
  buffer_load_dwordx4 v243, s[40:43], s68 offen offset:3072 nt lds
  v_cvt_pk_bf16_f32 v135, v110, v111
  v_mfma_f32_16x16x32_bf16 v[116:119], a[16:19], a[160:163], v[104:107]
  v_exp_f32 v112, v112
  v_mfma_f32_16x16x32_bf16 v[116:119], a[20:23], a[164:167], v[116:119]
  s_add_u32 s97, s97, s95
  ds_read_b64_tr_b16 a[78:79], v233 offset:45056
  ds_read_b64_tr_b16 a[80:81], v234 offset:32768
  ds_read_b64_tr_b16 v[216:217], v186 offset:0
  v_mfma_f32_16x16x32_bf16 v[116:119], a[24:27], a[168:171], v[116:119]
  ds_read_b64_tr_b16 a[82:83], v234 offset:36864
  v_exp_f32 v113, v113
  v_mfma_f32_16x16x32_bf16 v[116:119], a[28:31], a[172:175], v[116:119]
  s_lshr_b32 s83, s97, 4
  ds_read_b64_tr_b16 a[84:85], v234 offset:40960
  v_cvt_pk_bf16_f32 v142, v112, v113
  v_mfma_f32_16x16x32_bf16 v[76:79], a[88:91], v[172:175], v[76:79]
  ds_read_b64_tr_b16 a[86:87], v234 offset:45056
  v_exp_f32 v114, v114
  v_mfma_f32_16x16x32_bf16 v[76:79], a[92:95], v[176:179], v[76:79]
  s_add_u32 s94, s84, s96
  s_add_u32 m0, s62, 65536
  ds_read_b128 a[16:19], v228 offset:4096
  v_mfma_f32_16x16x32_bf16 v[16:19], a[96:99], v[156:159], v[16:19]
  ds_read_b128 a[20:23], v229 offset:4096
  v_exp_f32 v115, v115
  v_mfma_f32_16x16x32_bf16 v[16:19], a[100:103], v[160:163], v[16:19]
  s_max_u32 s94, s94, s83
  ds_read_b128 a[24:27], v230 offset:4096
  v_cvt_pk_bf16_f32 v143, v114, v115
  v_mfma_f32_16x16x32_bf16 v[108:111], a[32:35], a[128:131], v[96:99]
  ds_read_b128 a[28:31], v231 offset:4096
  v_exp_f32 v116, v116
  v_mfma_f32_16x16x32_bf16 v[108:111], a[36:39], a[132:135], v[108:111]
  s_sub_u32 s83, s84, s66
  buffer_load_dwordx4 v182, s[52:55], s72 offen offset:2048 lds
  v_mfma_f32_16x16x32_bf16 v[108:111], a[40:43], a[136:139], v[108:111]
  ds_read_b64_tr_b16 a[88:89], v235 offset:32768
  v_exp_f32 v117, v117
  v_mfma_f32_16x16x32_bf16 v[108:111], a[44:47], a[140:143], v[108:111]
  s_min_u32 s83, s83, 200
  ds_read_b64_tr_b16 a[90:91], v235 offset:36864
  v_cvt_pk_bf16_f32 v150, v116, v117
  v_mfma_f32_16x16x32_bf16 v[48:51], a[96:99], v[164:167], v[48:51]
  ds_read_b64_tr_b16 a[92:93], v235 offset:40960
  v_exp_f32 v118, v118
  v_mfma_f32_16x16x32_bf16 v[48:51], a[100:103], v[168:171], v[48:51]
  s_add_u32 s65, s65, s83
  s_add_u32 s68, s60, 360448
  ds_read_b64_tr_b16 v[218:219], v186 offset:2048
  v_mfma_f32_16x16x32_bf16 v[80:83], a[96:99], v[172:175], v[80:83]
  v_exp_f32 v119, v119
  v_mfma_f32_16x16x32_bf16 v[80:83], a[100:103], v[176:179], v[80:83]
  s_lshr_b32 s65, s65, 1
  v_cvt_pk_bf16_f32 v151, v118, v119
  v_mfma_f32_16x16x32_bf16 v[20:23], a[104:107], v[156:159], v[20:23]
  s_add_u32 s83, s65, s84
  s_add_u32 m0, s62, 16384
  v_mfma_f32_16x16x32_bf16 v[20:23], a[108:111], v[160:163], v[20:23]
  s_sub_u32 s83, s83, 28
  buffer_load_dwordx4 v244, s[44:47], s68 offen offset:0 lds
  v_mfma_f32_16x16x32_bf16 v[112:115], a[32:35], a[144:147], v[100:103]
  v_exp_f32 v108, v108
  v_mfma_f32_16x16x32_bf16 v[112:115], a[36:39], a[148:151], v[112:115]
  s_max_u32 s94, s94, s83
  ds_read_b64_tr_b16 a[94:95], v235 offset:45056
  ds_read_b64_tr_b16 v[220:221], v187 offset:0
  v_mfma_f32_16x16x32_bf16 v[112:115], a[40:43], a[152:155], v[112:115]
  v_exp_f32 v109, v109
  v_mfma_f32_16x16x32_bf16 v[112:115], a[44:47], a[156:159], v[112:115]
  s_mov_b32 s66, s84
  v_cvt_pk_bf16_f32 v136, v108, v109
  v_mfma_f32_16x16x32_bf16 v[52:55], a[104:107], v[164:167], v[52:55]
  v_exp_f32 v110, v110
  v_mfma_f32_16x16x32_bf16 v[52:55], a[108:111], v[168:171], v[52:55]
  ds_read_b64_tr_b16 a[96:97], v236 offset:32768
  ds_read_b64_tr_b16 a[98:99], v236 offset:36864
  v_mfma_f32_16x16x32_bf16 v[84:87], a[104:107], v[172:175], v[84:87]
  v_exp_f32 v111, v111
  v_mfma_f32_16x16x32_bf16 v[84:87], a[108:111], v[176:179], v[84:87]
  ds_read_b64_tr_b16 a[100:101], v236 offset:40960
  ds_read_b64_tr_b16 v[222:223], v187 offset:2048
  v_cvt_pk_bf16_f32 v137, v110, v111
  v_mfma_f32_16x16x32_bf16 v[116:119], a[32:35], a[160:163], v[104:107]
  v_exp_f32 v112, v112
  v_mfma_f32_16x16x32_bf16 v[116:119], a[36:39], a[164:167], v[116:119]
  buffer_load_dwordx4 v245, s[44:47], s68 offen offset:1024 lds
  v_mfma_f32_16x16x32_bf16 v[116:119], a[40:43], a[168:171], v[116:119]
  v_exp_f32 v113, v113
  v_mfma_f32_16x16x32_bf16 v[116:119], a[44:47], a[172:175], v[116:119]
  ds_read_b64_tr_b16 a[102:103], v236 offset:45056
  v_cvt_pk_bf16_f32 v144, v112, v113
  v_mfma_f32_16x16x32_bf16 v[24:27], a[112:115], v[156:159], v[24:27]
  v_exp_f32 v114, v114
  v_mfma_f32_16x16x32_bf16 v[24:27], a[116:119], v[160:163], v[24:27]
  ds_read_b64_tr_b16 a[104:105], v237 offset:32768
  ds_read_b64_tr_b16 a[106:107], v237 offset:36864
  v_mfma_f32_16x16x32_bf16 v[56:59], a[112:115], v[164:167], v[56:59]
  v_exp_f32 v115, v115
  v_mfma_f32_16x16x32_bf16 v[56:59], a[116:119], v[168:171], v[56:59]
  ds_read_b64_tr_b16 a[108:109], v237 offset:40960
  v_cvt_pk_bf16_f32 v145, v114, v115
  v_mfma_f32_16x16x32_bf16 v[108:111], a[48:51], a[128:131], v[96:99]
  v_exp_f32 v116, v116
  v_mfma_f32_16x16x32_bf16 v[108:111], a[52:55], a[132:135], v[108:111]
  buffer_load_dwordx4 v244, s[44:47], s68 offen offset:2048 lds
  v_mfma_f32_16x16x32_bf16 v[108:111], a[56:59], a[136:139], v[108:111]
  v_exp_f32 v117, v117
  v_mfma_f32_16x16x32_bf16 v[108:111], a[60:63], a[140:143], v[108:111]
  ds_read_b64_tr_b16 a[110:111], v237 offset:45056
  v_cvt_pk_bf16_f32 v152, v116, v117
  v_mfma_f32_16x16x32_bf16 v[88:91], a[112:115], v[172:175], v[88:91]
  v_exp_f32 v118, v118
  v_mfma_f32_16x16x32_bf16 v[88:91], a[116:119], v[176:179], v[88:91]
  ds_read_b128 a[32:35], v228 offset:8192
  ds_read_b128 a[36:39], v229 offset:8192
  v_mfma_f32_16x16x32_bf16 v[28:31], a[120:123], v[156:159], v[28:31]
  v_exp_f32 v119, v119
  v_mfma_f32_16x16x32_bf16 v[28:31], a[124:127], v[160:163], v[28:31]
  ds_read_b128 a[40:43], v230 offset:8192
  v_cvt_pk_bf16_f32 v153, v118, v119
  v_mfma_f32_16x16x32_bf16 v[60:63], a[120:123], v[164:167], v[60:63]
  buffer_load_dwordx4 v245, s[44:47], s68 offen offset:3072 lds
  v_mfma_f32_16x16x32_bf16 v[60:63], a[124:127], v[168:171], v[60:63]
  ds_read_b128 a[44:47], v231 offset:8192
  ds_read_b64_tr_b16 a[112:113], v238 offset:32768
  v_mfma_f32_16x16x32_bf16 v[112:115], a[48:51], a[144:147], v[100:103]
  v_exp_f32 v108, v108
  v_mfma_f32_16x16x32_bf16 v[112:115], a[52:55], a[148:151], v[112:115]
  ds_read_b64_tr_b16 a[114:115], v238 offset:36864
  ds_read_b64_tr_b16 a[116:117], v238 offset:40960
  v_mfma_f32_16x16x32_bf16 v[112:115], a[56:59], a[152:155], v[112:115]
  v_exp_f32 v109, v109
  v_mfma_f32_16x16x32_bf16 v[112:115], a[60:63], a[156:159], v[112:115]
  s_add_u32 m0, s62, 65536
  v_cvt_pk_bf16_f32 v138, v108, v109
  v_mfma_f32_16x16x32_bf16 v[92:95], a[120:123], v[172:175], v[92:95]
  v_exp_f32 v110, v110
  v_mfma_f32_16x16x32_bf16 v[92:95], a[124:127], v[176:179], v[92:95]
  buffer_load_dwordx4 v183, s[52:55], s72 offen offset:3072 lds
  v_mfma_f32_16x16x32_bf16 v[120:123], a[240:243], v[156:159], v[120:123]
  v_exp_f32 v111, v111
  v_mfma_f32_16x16x32_bf16 v[120:123], a[240:243], v[160:163], v[120:123]
  ds_read_b64_tr_b16 a[118:119], v238 offset:45056
  v_cvt_pk_bf16_f32 v139, v110, v111
  v_mfma_f32_16x16x32_bf16 v[116:119], a[48:51], a[160:163], v[104:107]
  v_exp_f32 v112, v112
  v_mfma_f32_16x16x32_bf16 v[116:119], a[52:55], a[164:167], v[116:119]
  ds_read_b64_tr_b16 a[120:121], v239 offset:32768
  ds_read_b64_tr_b16 a[122:123], v239 offset:36864
  v_mfma_f32_16x16x32_bf16 v[116:119], a[56:59], a[168:171], v[116:119]
  v_exp_f32 v113, v113
  v_mfma_f32_16x16x32_bf16 v[116:119], a[60:63], a[172:175], v[116:119]
  ds_read_b64_tr_b16 a[124:125], v239 offset:40960
  v_cvt_pk_bf16_f32 v146, v112, v113
  v_mfma_f32_16x16x32_bf16 v[124:127], a[240:243], v[164:167], v[124:127]
  v_exp_f32 v114, v114
  v_mfma_f32_16x16x32_bf16 v[124:127], a[240:243], v[168:171], v[124:127]
  ds_read_b64_tr_b16 a[126:127], v239 offset:45056
  v_mfma_f32_16x16x32_bf16 v[128:131], a[240:243], v[172:175], v[128:131]
  v_exp_f32 v115, v115
  v_mfma_f32_16x16x32_bf16 v[128:131], a[240:243], v[176:179], v[128:131]
  v_cvt_pk_bf16_f32 v147, v114, v115
  s_waitcnt lgkmcnt(8)
  v_mfma_f32_16x16x32_bf16 v[108:111], a[0:3], a[128:131], v[96:99]
  v_exp_f32 v116, v116
  v_mfma_f32_16x16x32_bf16 v[108:111], a[4:7], a[132:135], v[108:111]
  s_memrealtime s[82:83]
  ds_read_b128 a[48:51], v228 offset:12288
  v_mfma_f32_16x16x32_bf16 v[108:111], a[8:11], a[136:139], v[108:111]
  v_exp_f32 v117, v117
  v_mfma_f32_16x16x32_bf16 v[108:111], a[12:15], a[140:143], v[108:111]
  ds_read_b128 a[52:55], v229 offset:12288
  v_cvt_pk_bf16_f32 v154, v116, v117
  v_mfma_f32_16x16x32_bf16 v[0:3], a[64:67], v[132:135], v[0:3]
  v_exp_f32 v118, v118
  v_mfma_f32_16x16x32_bf16 v[0:3], a[68:71], v[136:139], v[0:3]
  ds_read_b128 a[56:59], v230 offset:12288
  ds_read_b128 a[60:63], v231 offset:12288
  v_mfma_f32_16x16x32_bf16 v[32:35], a[64:67], v[140:143], v[32:35]
  v_exp_f32 v119, v119
  v_mfma_f32_16x16x32_bf16 v[32:35], a[68:71], v[144:147], v[32:35]
  v_cvt_pk_bf16_f32 v155, v118, v119
  v_mfma_f32_16x16x32_bf16 v[112:115], a[0:3], a[144:147], v[100:103]
  v_exp_f32 v108, v108
  v_mfma_f32_16x16x32_bf16 v[112:115], a[4:7], a[148:151], v[112:115]
  v_mfma_f32_16x16x32_bf16 v[112:115], a[8:11], a[152:155], v[112:115]
  v_exp_f32 v109, v109
  v_mfma_f32_16x16x32_bf16 v[112:115], a[12:15], a[156:159], v[112:115]
  v_cvt_pk_bf16_f32 v156, v108, v109
  v_mfma_f32_16x16x32_bf16 v[64:67], a[64:67], v[148:151], v[64:67]
  v_exp_f32 v110, v110
  v_mfma_f32_16x16x32_bf16 v[64:67], a[68:71], v[152:155], v[64:67]
  s_waitcnt vmcnt(11) lgkmcnt(0)
  s_barrier
  v_mfma_f32_16x16x32_bf16 v[4:7], a[72:75], v[132:135], v[4:7]
  v_exp_f32 v111, v111
  v_mfma_f32_16x16x32_bf16 v[4:7], a[76:79], v[136:139], v[4:7]
  s_sub_u32 s84, s82, s85
  v_cvt_pk_bf16_f32 v157, v110, v111
  v_mfma_f32_16x16x32_bf16 v[116:119], a[0:3], a[160:163], v[104:107]
  v_exp_f32 v112, v112
  v_mfma_f32_16x16x32_bf16 v[116:119], a[4:7], a[164:167], v[116:119]
  s_cmp_lt_u32 s84, s94
  s_cbranch_scc1 PC20_%=
PC20_%=e:
  v_mfma_f32_16x16x32_bf16 v[116:119], a[8:11], a[168:171], v[116:119]
  v_exp_f32 v113, v113
  v_mfma_f32_16x16x32_bf16 v[116:119], a[12:15], a[172:175], v[116:119]
  s_add_u32 s68, s60, 393216
  v_cvt_pk_bf16_f32 v164, v112, v113
  v_mfma_f32_16x16x32_bf16 v[36:39], a[72:75], v[140:143], v[36:39]
  v_exp_f32 v114, v114
  v_mfma_f32_16x16x32_bf16 v[36:39], a[76:79], v[144:147], v[36:39]
  s_add_u32 m0, s61, 0
  ds_read_b64_tr_b16 a[64:65], v232 offset:0
  v_mfma_f32_16x16x32_bf16 v[68:71], a[72:75], v[148:151], v[68:71]
  v_exp_f32 v115, v115
  v_mfma_f32_16x16x32_bf16 v[68:71], a[76:79], v[152:155], v[68:71]
  ds_read_b64_tr_b16 a[66:67], v232 offset:4096
  v_cvt_pk_bf16_f32 v165, v114, v115
  v_mfma_f32_16x16x32_bf16 v[8:11], a[80:83], v[132:135], v[8:11]
  buffer_load_dwordx4 v240, s[40:43], s68 offen offset:0 nt lds
  v_mfma_f32_16x16x32_bf16 v[8:11], a[84:87], v[136:139], v[8:11]
  buffer_load_dwordx4 v241, s[40:43], s68 offen offset:1024 nt lds
  v_mfma_f32_16x16x32_bf16 v[108:111], a[16:19], a[128:131], v[96:99]
  v_exp_f32 v116, v116
  v_mfma_f32_16x16x32_bf16 v[108:111], a[20:23], a[132:135], v[108:111]
  ds_read_b64_tr_b16 a[68:69], v232 offset:8192
  ds_read_b64_tr_b16 a[70:71], v232 offset:12288
  v_mfma_f32_16x16x32_bf16 v[108:111], a[24:27], a[136:139], v[108:111]
  v_exp_f32 v117, v117
  v_mfma_f32_16x16x32_bf16 v[108:111], a[28:31], a[140:143], v[108:111]
  ds_read_b128 a[0:3], v228 offset:49152
  v_cvt_pk_bf16_f32 v172, v116, v117
  v_mfma_f32_16x16x32_bf16 v[40:43], a[80:83], v[140:143], v[40:43]
  v_exp_f32 v118, v118
  v_mfma_f32_16x16x32_bf16 v[40:43], a[84:87], v[144:147], v[40:43]
  ds_read_b128 a[4:7], v229 offset:49152
  ds_read_b128 a[8:11], v230 offset:49152
  v_mfma_f32_16x16x32_bf16 v[72:75], a[80:83], v[148:151], v[72:75]
  v_exp_f32 v119, v119
  v_mfma_f32_16x16x32_bf16 v[72:75], a[84:87], v[152:155], v[72:75]
  ds_read_b128 a[12:15], v231 offset:49152
  v_cvt_pk_bf16_f32 v173, v118, v119
  v_mfma_f32_16x16x32_bf16 v[112:115], a[16:19], a[144:147], v[100:103]
  v_exp_f32 v108, v108
  v_mfma_f32_16x16x32_bf16 v[112:115], a[20:23], a[148:151], v[112:115]
  buffer_load_dwordx4 v242, s[40:43], s68 offen offset:2048 nt lds
  v_mfma_f32_16x16x32_bf16 v[112:115], a[24:27], a[152:155], v[112:115]
  v_exp_f32 v109, v109
  v_mfma_f32_16x16x32_bf16 v[112:115], a[28:31], a[156:159], v[112:115]
  ds_read_b64_tr_b16 a[72:73], v233 offset:0
  v_cvt_pk_bf16_f32 v158, v108, v109
  v_mfma_f32_16x16x32_bf16 v[12:15], a[88:91], v[132:135], v[12:15]
  v_exp_f32 v110, v110
  v_mfma_f32_16x16x32_bf16 v[12:15], a[92:95], v[136:139], v[12:15]
  ds_read_b64_tr_b16 a[74:75], v233 offset:4096
  ds_read_b64_tr_b16 a[76:77], v233 offset:8192
  ds_read_b64_tr_b16 a[78:79], v233 offset:12288
  v_mfma_f32_16x16x32_bf16 v[44:47], a[88:91], v[140:143], v[44:47]
  ds_read_b64_tr_b16 a[80:81], v234 offset:0
  v_exp_f32 v111, v111
  v_mfma_f32_16x16x32_bf16 v[44:47], a[92:95], v[144:147], v[44:47]
  buffer_load_dwordx4 v243, s[40:43], s68 offen offset:3072 nt lds
  v_cvt_pk_bf16_f32 v159, v110, v111
  v_mfma_f32_16x16x32_bf16 v[116:119], a[16:19], a[160:163], v[104:107]
  ds_read_b64_tr_b16 a[82:83], v234 offset:4096
  v_exp_f32 v112, v112
  v_mfma_f32_16x16x32_bf16 v[116:119], a[20:23], a[164:167], v[116:119]
  s_add_u32 s97, s97, s95
  s_add_u32 s72, s93, 8
  s_and_b32 s72, s72, 31
  s_mul_i32 s72, s72, 40960
  ds_read_b64_tr_b16 a[84:85], v234 offset:8192
  v_mfma_f32_16x16x32_bf16 v[116:119], a[24:27], a[168:171], v[116:119]
  ds_read_b64_tr_b16 a[86:87], v234 offset:12288
  v_exp_f32 v113, v113
  v_mfma_f32_16x16x32_bf16 v[116:119], a[28:31], a[172:175], v[116:119]
  s_lshr_b32 s83, s97, 4
  v_cvt_pk_bf16_f32 v166, v112, v113
  v_mfma_f32_16x16x32_bf16 v[76:79], a[88:91], v[148:151], v[76:79]
  v_exp_f32 v114, v114
  v_mfma_f32_16x16x32_bf16 v[76:79], a[92:95], v[152:155], v[76:79]
  s_add_u32 s94, s84, s96
  s_add_u32 m0, s62, 49152
  ds_read_b128 a[16:19], v228 offset:53248
  v_mfma_f32_16x16x32_bf16 v[16:19], a[96:99], v[132:135], v[16:19]
  ds_read_b128 a[20:23], v229 offset:53248
  v_exp_f32 v115, v115
  v_mfma_f32_16x16x32_bf16 v[16:19], a[100:103], v[136:139], v[16:19]
  s_max_u32 s94, s94, s83
  ds_read_b128 a[24:27], v230 offset:53248
  v_cvt_pk_bf16_f32 v167, v114, v115
  v_mfma_f32_16x16x32_bf16 v[108:111], a[32:35], a[128:131], v[96:99]
  ds_read_b128 a[28:31], v231 offset:53248
  v_exp_f32 v116, v116
  v_mfma_f32_16x16x32_bf16 v[108:111], a[36:39], a[132:135], v[108:111]
  s_sub_u32 s83, s84, s66
  buffer_load_dwordx4 v180, s[52:55], s72 offen offset:0 lds
  v_mfma_f32_16x16x32_bf16 v[108:111], a[40:43], a[136:139], v[108:111]
  ds_read_b64_tr_b16 a[88:89], v235 offset:0
  v_exp_f32 v117, v117
  v_mfma_f32_16x16x32_bf16 v[108:111], a[44:47], a[140:143], v[108:111]
  s_min_u32 s83, s83, 200
  ds_read_b64_tr_b16 a[90:91], v235 offset:4096
  v_cvt_pk_bf16_f32 v174, v116, v117
  v_mfma_f32_16x16x32_bf16 v[48:51], a[96:99], v[140:143], v[48:51]
  ds_read_b64_tr_b16 a[92:93], v235 offset:8192
  v_exp_f32 v118, v118
  v_mfma_f32_16x16x32_bf16 v[48:51], a[100:103], v[144:147], v[48:51]
  s_add_u32 s65, s65, s83
  s_add_u32 s68, s60, 376832
  v_mfma_f32_16x16x32_bf16 v[80:83], a[96:99], v[148:151], v[80:83]
  v_exp_f32 v119, v119
  v_mfma_f32_16x16x32_bf16 v[80:83], a[100:103], v[152:155], v[80:83]
  s_lshr_b32 s65, s65, 1
  v_cvt_pk_bf16_f32 v175, v118, v119
  v_mfma_f32_16x16x32_bf16 v[20:23], a[104:107], v[132:135], v[20:23]
  s_add_u32 s83, s65, s84
  s_add_u32 m0, s62, 32768
  v_mfma_f32_16x16x32_bf16 v[20:23], a[108:111], v[136:139], v[20:23]
  s_sub_u32 s83, s83, 28
  buffer_load_dwordx4 v244, s[44:47], s68 offen offset:0 lds
  v_mfma_f32_16x16x32_bf16 v[112:115], a[32:35], a[144:147], v[100:103]
  v_exp_f32 v108, v108
  v_mfma_f32_16x16x32_bf16 v[112:115], a[36:39], a[148:151], v[112:115]
  s_max_u32 s94, s94, s83
  ds_read_b64_tr_b16 a[94:95], v235 offset:12288
  v_mfma_f32_16x16x32_bf16 v[112:115], a[40:43], a[152:155], v[112:115]
  v_exp_f32 v109, v109
  v_mfma_f32_16x16x32_bf16 v[112:115], a[44:47], a[156:159], v[112:115]
  s_mov_b32 s66, s84
  v_cvt_pk_bf16_f32 v160, v108, v109
  v_mfma_f32_16x16x32_bf16 v[52:55], a[104:107], v[140:143], v[52:55]
  v_exp_f32 v110, v110
  v_mfma_f32_16x16x32_bf16 v[52:55], a[108:111], v[144:147], v[52:55]
  ds_read_b64_tr_b16 a[96:97], v236 offset:0
  ds_read_b64_tr_b16 a[98:99], v236 offset:4096
  v_mfma_f32_16x16x32_bf16 v[84:87], a[104:107], v[148:151], v[84:87]
  v_exp_f32 v111, v111
  v_mfma_f32_16x16x32_bf16 v[84:87], a[108:111], v[152:155], v[84:87]
  ds_read_b64_tr_b16 a[100:101], v236 offset:8192
  v_cvt_pk_bf16_f32 v161, v110, v111
  v_mfma_f32_16x16x32_bf16 v[116:119], a[32:35], a[160:163], v[104:107]
  v_exp_f32 v112, v112
  v_mfma_f32_16x16x32_bf16 v[116:119], a[36:39], a[164:167], v[116:119]
  buffer_load_dwordx4 v245, s[44:47], s68 offen offset:1024 lds
  v_mfma_f32_16x16x32_bf16 v[116:119], a[40:43], a[168:171], v[116:119]
  v_exp_f32 v113, v113
  v_mfma_f32_16x16x32_bf16 v[116:119], a[44:47], a[172:175], v[116:119]
  ds_read_b64_tr_b16 a[102:103], v236 offset:12288
  v_cvt_pk_bf16_f32 v168, v112, v113
  v_mfma_f32_16x16x32_bf16 v[24:27], a[112:115], v[132:135], v[24:27]
  v_exp_f32 v114, v114
  v_mfma_f32_16x16x32_bf16 v[24:27], a[116:119], v[136:139], v[24:27]
  ds_read_b64_tr_b16 a[104:105], v237 offset:0
  ds_read_b64_tr_b16 a[106:107], v237 offset:4096
  v_mfma_f32_16x16x32_bf16 v[56:59], a[112:115], v[140:143], v[56:59]
  v_exp_f32 v115, v115
  v_mfma_f32_16x16x32_bf16 v[56:59], a[116:119], v[144:147], v[56:59]
  ds_read_b64_tr_b16 a[108:109], v237 offset:8192
  v_cvt_pk_bf16_f32 v169, v114, v115
  v_mfma_f32_16x16x32_bf16 v[108:111], a[48:51], a[128:131], v[96:99]
  v_exp_f32 v116, v116
  v_mfma_f32_16x16x32_bf16 v[108:111], a[52:55], a[132:135], v[108:111]
  buffer_load_dwordx4 v244, s[44:47], s68 offen offset:2048 lds
  v_mfma_f32_16x16x32_bf16 v[108:111], a[56:59], a[136:139], v[108:111]
  v_exp_f32 v117, v117
  v_mfma_f32_16x16x32_bf16 v[108:111], a[60:63], a[140:143], v[108:111]
  ds_read_b64_tr_b16 a[110:111], v237 offset:12288
  v_cvt_pk_bf16_f32 v176, v116, v117
  v_mfma_f32_16x16x32_bf16 v[88:91], a[112:115], v[148:151], v[88:91]
  v_exp_f32 v118, v118
  v_mfma_f32_16x16x32_bf16 v[88:91], a[116:119], v[152:155], v[88:91]
  ds_read_b128 a[32:35], v228 offset:57344
  ds_read_b128 a[36:39], v229 offset:57344
  v_mfma_f32_16x16x32_bf16 v[28:31], a[120:123], v[132:135], v[28:31]
  v_exp_f32 v119, v119
  v_mfma_f32_16x16x32_bf16 v[28:31], a[124:127], v[136:139], v[28:31]
  ds_read_b128 a[40:43], v230 offset:57344
  v_cvt_pk_bf16_f32 v177, v118, v119
  v_mfma_f32_16x16x32_bf16 v[60:63], a[120:123], v[140:143], v[60:63]
  buffer_load_dwordx4 v245, s[44:47], s68 offen offset:3072 lds
  v_mfma_f32_16x16x32_bf16 v[60:63], a[124:127], v[144:147], v[60:63]
  ds_read_b128 a[44:47], v231 offset:57344
  ds_read_b64_tr_b16 a[112:113], v238 offset:0
  v_mfma_f32_16x16x32_bf16 v[112:115], a[48:51], a[144:147], v[100:103]
  v_exp_f32 v108, v108
  v_mfma_f32_16x16x32_bf16 v[112:115], a[52:55], a[148:151], v[112:115]
  ds_read_b64_tr_b16 a[114:115], v238 offset:4096
  ds_read_b64_tr_b16 a[116:117], v238 offset:8192
  v_mfma_f32_16x16x32_bf16 v[112:115], a[56:59], a[152:155], v[112:115]
  v_exp_f32 v109, v109
  v_mfma_f32_16x16x32_bf16 v[112:115], a[60:63], a[156:159], v[112:115]
  s_add_u32 m0, s62, 49152
  v_cvt_pk_bf16_f32 v162, v108, v109
  v_mfma_f32_16x16x32_bf16 v[92:95], a[120:123], v[148:151], v[92:95]
  v_exp_f32 v110, v110
  v_mfma_f32_16x16x32_bf16 v[92:95], a[124:127], v[152:155], v[92:95]
  buffer_load_dwordx4 v181, s[52:55], s72 offen offset:1024 lds
  v_mfma_f32_16x16x32_bf16 v[120:123], a[240:243], v[132:135], v[120:123]
  v_exp_f32 v111, v111
  v_mfma_f32_16x16x32_bf16 v[120:123], a[240:243], v[136:139], v[120:123]
  ds_read_b64_tr_b16 a[118:119], v238 offset:12288
  v_cvt_pk_bf16_f32 v163, v110, v111
  v_mfma_f32_16x16x32_bf16 v[116:119], a[48:51], a[160:163], v[104:107]
  v_exp_f32 v112, v112
  v_mfma_f32_16x16x32_bf16 v[116:119], a[52:55], a[164:167], v[116:119]
  ds_read_b64_tr_b16 a[120:121], v239 offset:0
  ds_read_b64_tr_b16 a[122:123], v239 offset:4096
  v_mfma_f32_16x16x32_bf16 v[116:119], a[56:59], a[168:171], v[116:119]
  v_exp_f32 v113, v113
  v_mfma_f32_16x16x32_bf16 v[116:119], a[60:63], a[172:175], v[116:119]
  ds_read_b64_tr_b16 a[124:125], v239 offset:8192
  v_cvt_pk_bf16_f32 v170, v112, v113
  v_mfma_f32_16x16x32_bf16 v[124:127], a[240:243], v[140:143], v[124:127]
  v_exp_f32 v114, v114
  v_mfma_f32_16x16x32_bf16 v[124:127], a[240:243], v[144:147], v[124:127]
  ds_read_b64_tr_b16 a[126:127], v239 offset:12288
  v_mfma_f32_16x16x32_bf16 v[128:131], a[240:243], v[148:151], v[128:131]
  v_exp_f32 v115, v115
  v_mfma_f32_16x16x32_bf16 v[128:131], a[240:243], v[152:155], v[128:131]
  v_cvt_pk_bf16_f32 v171, v114, v115
  s_waitcnt lgkmcnt(8)
  v_mfma_f32_16x16x32_bf16 v[108:111], a[0:3], a[128:131], v[96:99]
  v_exp_f32 v116, v116
  v_mfma_f32_16x16x32_bf16 v[108:111], a[4:7], a[132:135], v[108:111]
  s_memrealtime s[82:83]
  ds_read_b128 a[48:51], v228 offset:61440
  v_mfma_f32_16x16x32_bf16 v[108:111], a[8:11], a[136:139], v[108:111]
  v_exp_f32 v117, v117
  v_mfma_f32_16x16x32_bf16 v[108:111], a[12:15], a[140:143], v[108:111]
  ds_read_b128 a[52:55], v229 offset:61440
  v_cvt_pk_bf16_f32 v178, v116, v117
  v_mfma_f32_16x16x32_bf16 v[0:3], a[64:67], v[156:159], v[0:3]
  v_exp_f32 v118, v118
  v_mfma_f32_16x16x32_bf16 v[0:3], a[68:71], v[160:163], v[0:3]
  ds_read_b128 a[56:59], v230 offset:61440
  ds_read_b128 a[60:63], v231 offset:61440
  v_mfma_f32_16x16x32_bf16 v[32:35], a[64:67], v[164:167], v[32:35]
  v_exp_f32 v119, v119
  v_mfma_f32_16x16x32_bf16 v[32:35], a[68:71], v[168:171], v[32:35]
  v_cvt_pk_bf16_f32 v179, v118, v119
  v_mfma_f32_16x16x32_bf16 v[112:115], a[0:3], a[144:147], v[100:103]
  v_exp_f32 v108, v108
  v_mfma_f32_16x16x32_bf16 v[112:115], a[4:7], a[148:151], v[112:115]
  v_mfma_f32_16x16x32_bf16 v[112:115], a[8:11], a[152:155], v[112:115]
  v_exp_f32 v109, v109
  v_mfma_f32_16x16x32_bf16 v[112:115], a[12:15], a[156:159], v[112:115]
  v_cvt_pk_bf16_f32 v132, v108, v109
  v_mfma_f32_16x16x32_bf16 v[64:67], a[64:67], v[172:175], v[64:67]
  v_exp_f32 v110, v110
  v_mfma_f32_16x16x32_bf16 v[64:67], a[68:71], v[176:179], v[64:67]
  s_waitcnt vmcnt(11) lgkmcnt(0)
  s_barrier
  v_mfma_f32_16x16x32_bf16 v[4:7], a[72:75], v[156:159], v[4:7]
  v_exp_f32 v111, v111
  v_mfma_f32_16x16x32_bf16 v[4:7], a[76:79], v[160:163], v[4:7]
  s_sub_u32 s84, s82, s85
  v_cvt_pk_bf16_f32 v133, v110, v111
  v_mfma_f32_16x16x32_bf16 v[116:119], a[0:3], a[160:163], v[104:107]
  v_exp_f32 v112, v112
  v_mfma_f32_16x16x32_bf16 v[116:119], a[4:7], a[164:167], v[116:119]
  s_cmp_lt_u32 s84, s94
  s_cbranch_scc1 PC21_%=
PC21_%=e:
  v_mfma_f32_16x16x32_bf16 v[116:119], a[8:11], a[168:171], v[116:119]
  v_exp_f32 v113, v113
  v_mfma_f32_16x16x32_bf16 v[116:119], a[12:15], a[172:175], v[116:119]
  s_add_u32 s68, s60, 409600
  v_cvt_pk_bf16_f32 v140, v112, v113
  v_mfma_f32_16x16x32_bf16 v[36:39], a[72:75], v[164:167], v[36:39]
  v_exp_f32 v114, v114
  v_mfma_f32_16x16x32_bf16 v[36:39], a[76:79], v[168:171], v[36:39]
  s_add_u32 m0, s61, 49152
  ds_read_b64_tr_b16 a[64:65], v232 offset:16384
  v_mfma_f32_16x16x32_bf16 v[68:71], a[72:75], v[172:175], v[68:71]
  v_exp_f32 v115, v115
  v_mfma_f32_16x16x32_bf16 v[68:71], a[76:79], v[176:179], v[68:71]
  ds_read_b64_tr_b16 a[66:67], v232 offset:20480
  v_cvt_pk_bf16_f32 v141, v114, v115
  v_mfma_f32_16x16x32_bf16 v[8:11], a[80:83], v[156:159], v[8:11]
  buffer_load_dwordx4 v240, s[40:43], s68 offen offset:0 nt lds
  v_mfma_f32_16x16x32_bf16 v[8:11], a[84:87], v[160:163], v[8:11]
  buffer_load_dwordx4 v241, s[40:43], s68 offen offset:1024 nt lds
  v_mfma_f32_16x16x32_bf16 v[108:111], a[16:19], a[128:131], v[96:99]
  v_exp_f32 v116, v116
  v_mfma_f32_16x16x32_bf16 v[108:111], a[20:23], a[132:135], v[108:111]
  ds_read_b64_tr_b16 a[68:69], v232 offset:24576
  ds_read_b64_tr_b16 a[70:71], v232 offset:28672
  v_mfma_f32_16x16x32_bf16 v[108:111], a[24:27], a[136:139], v[108:111]
  v_exp_f32 v117, v117
  v_mfma_f32_16x16x32_bf16 v[108:111], a[28:31], a[140:143], v[108:111]
  ds_read_b128 a[0:3], v228 offset:16384
  v_cvt_pk_bf16_f32 v148, v116, v117
  v_mfma_f32_16x16x32_bf16 v[40:43], a[80:83], v[164:167], v[40:43]
  v_exp_f32 v118, v118
  v_mfma_f32_16x16x32_bf16 v[40:43], a[84:87], v[168:171], v[40:43]
  ds_read_b128 a[4:7], v229 offset:16384
  ds_read_b128 a[8:11], v230 offset:16384
  v_mfma_f32_16x16x32_bf16 v[72:75], a[80:83], v[172:175], v[72:75]
  v_exp_f32 v119, v119
  v_mfma_f32_16x16x32_bf16 v[72:75], a[84:87], v[176:179], v[72:75]
  ds_read_b128 a[12:15], v231 offset:16384
  v_cvt_pk_bf16_f32 v149, v118, v119
  v_mfma_f32_16x16x32_bf16 v[112:115], a[16:19], a[144:147], v[100:103]
  v_exp_f32 v108, v108
  v_mfma_f32_16x16x32_bf16 v[112:115], a[20:23], a[148:151], v[112:115]
  buffer_load_dwordx4 v242, s[40:43], s68 offen offset:2048 nt lds
  v_mfma_f32_16x16x32_bf16 v[112:115], a[24:27], a[152:155], v[112:115]
  v_exp_f32 v109, v109
  v_mfma_f32_16x16x32_bf16 v[112:115], a[28:31], a[156:159], v[112:115]
  ds_read_b64_tr_b16 a[72:73], v233 offset:16384
  v_cvt_pk_bf16_f32 v134, v108, v109
  v_mfma_f32_16x16x32_bf16 v[12:15], a[88:91], v[156:159], v[12:15]
  v_exp_f32 v110, v110
  v_mfma_f32_16x16x32_bf16 v[12:15], a[92:95], v[160:163], v[12:15]
  buffer_load_dwordx4 v243, s[40:43], s68 offen offset:3072 nt lds
  v_mfma_f32_16x16x32_bf16 v[44:47], a[88:91], v[164:167], v[44:47]
  v_exp_f32 v111, v111
  v_mfma_f32_16x16x32_bf16 v[44:47], a[92:95], v[168:171], v[44:47]
  ds_read_b64_tr_b16 a[74:75], v233 offset:20480
  v_cvt_pk_bf16_f32 v135, v110, v111
  v_mfma_f32_16x16x32_bf16 v[116:119], a[16:19], a[160:163], v[104:107]
  v_exp_f32 v112, v112
  v_mfma_f32_16x16x32_bf16 v[116:119], a[20:23], a[164:167], v[116:119]
  s_sub_u32 s95, s95, 48
  ds_read_b64_tr_b16 a[76:77], v233 offset:24576
  ds_read_b64_tr_b16 a[78:79], v233 offset:28672
  v_mfma_f32_16x16x32_bf16 v[116:119], a[24:27], a[168:171], v[116:119]
  ds_read_b64_tr_b16 a[80:81], v234 offset:16384
  v_exp_f32 v113, v113
  v_mfma_f32_16x16x32_bf16 v[116:119], a[28:31], a[172:175], v[116:119]
  s_max_u32 s95, s95, 1808
  ds_read_b64_tr_b16 a[82:83], v234 offset:20480
  v_cvt_pk_bf16_f32 v142, v112, v113
  v_mfma_f32_16x16x32_bf16 v[76:79], a[88:91], v[172:175], v[76:79]
  ds_read_b64_tr_b16 a[84:85], v234 offset:24576
  v_exp_f32 v114, v114
  v_mfma_f32_16x16x32_bf16 v[76:79], a[92:95], v[176:179], v[76:79]
  s_lshr_b32 s96, s95, 4
  s_add_u32 m0, s62, 49152
  ds_read_b64_tr_b16 a[86:87], v234 offset:28672
  v_mfma_f32_16x16x32_bf16 v[16:19], a[96:99], v[156:159], v[16:19]
  ds_read_b128 a[16:19], v228 offset:20480
  v_exp_f32 v115, v115
  v_mfma_f32_16x16x32_bf16 v[16:19], a[100:103], v[160:163], v[16:19]
  s_sub_u32 s96, s96, 4
  ds_read_b128 a[20:23], v229 offset:20480
  v_cvt_pk_bf16_f32 v143, v114, v115
  v_mfma_f32_16x16x32_bf16 v[108:111], a[32:35], a[128:131], v[96:99]
  ds_read_b128 a[24:27], v230 offset:20480
  v_exp_f32 v116, v116
  v_mfma_f32_16x16x32_bf16 v[108:111], a[36:39], a[132:135], v[108:111]
  s_add_u32 s97, s97, s95
  buffer_load_dwordx4 v182, s[52:55], s72 offen offset:2048 lds
  v_mfma_f32_16x16x32_bf16 v[108:111], a[40:43], a[136:139], v[108:111]
  ds_read_b128 a[28:31], v231 offset:20480
  v_exp_f32 v117, v117
  v_mfma_f32_16x16x32_bf16 v[108:111], a[44:47], a[140:143], v[108:111]
  s_lshr_b32 s83, s97, 4
  ds_read_b64_tr_b16 a[88:89], v235 offset:16384
  v_cvt_pk_bf16_f32 v150, v116, v117
  v_mfma_f32_16x16x32_bf16 v[48:51], a[96:99], v[164:167], v[48:51]
  ds_read_b64_tr_b16 a[90:91], v235 offset:20480
  v_exp_f32 v118, v118
  v_mfma_f32_16x16x32_bf16 v[48:51], a[100:103], v[168:171], v[48:51]
  s_add_u32 s94, s84, s96
  s_add_u32 s68, s60, 393216
  v_mfma_f32_16x16x32_bf16 v[80:83], a[96:99], v[172:175], v[80:83]
  v_exp_f32 v119, v119
  v_mfma_f32_16x16x32_bf16 v[80:83], a[100:103], v[176:179], v[80:83]
  s_max_u32 s94, s94, s83
  v_cvt_pk_bf16_f32 v151, v118, v119
  v_mfma_f32_16x16x32_bf16 v[20:23], a[104:107], v[156:159], v[20:23]
  s_sub_u32 s83, s84, s66
  s_add_u32 m0, s62, 0
  v_mfma_f32_16x16x32_bf16 v[20:23], a[108:111], v[160:163], v[20:23]
  s_min_u32 s83, s83, 200
  buffer_load_dwordx4 v244, s[44:47], s68 offen offset:0 lds
  v_mfma_f32_16x16x32_bf16 v[112:115], a[32:35], a[144:147], v[100:103]
  v_exp_f32 v108, v108
  v_mfma_f32_16x16x32_bf16 v[112:115], a[36:39], a[148:151], v[112:115]
  s_add_u32 s65, s65, s83
  ds_read_b64_tr_b16 a[92:93], v235 offset:24576
  ds_read_b64_tr_b16 a[94:95], v235 offset:28672
  v_mfma_f32_16x16x32_bf16 v[112:115], a[40:43], a[152:155], v[112:115]
  ds_read_b64_tr_b16 a[96:97], v236 offset:16384
  v_exp_f32 v109, v109
  v_mfma_f32_16x16x32_bf16 v[112:115], a[44:47], a[156:159], v[112:115]
  s_lshr_b32 s65, s65, 1
  v_cvt_pk_bf16_f32 v136, v108, v109
  v_mfma_f32_16x16x32_bf16 v[52:55], a[104:107], v[164:167], v[52:55]
  v_exp_f32 v110, v110
  v_mfma_f32_16x16x32_bf16 v[52:55], a[108:111], v[168:171], v[52:55]
  s_add_u32 s83, s65, s84
  buffer_load_dwordx4 v245, s[44:47], s68 offen offset:1024 lds
  v_mfma_f32_16x16x32_bf16 v[84:87], a[104:107], v[172:175], v[84:87]
  v_exp_f32 v111, v111
  v_mfma_f32_16x16x32_bf16 v[84:87], a[108:111], v[176:179], v[84:87]
  s_sub_u32 s83, s83, 28
  v_cvt_pk_bf16_f32 v137, v110, v111
  v_mfma_f32_16x16x32_bf16 v[116:119], a[32:35], a[160:163], v[104:107]
  v_exp_f32 v112, v112
  v_mfma_f32_16x16x32_bf16 v[116:119], a[36:39], a[164:167], v[116:119]
  s_max_u32 s94, s94, s83
  ds_read_b64_tr_b16 a[98:99], v236 offset:20480
  v_mfma_f32_16x16x32_bf16 v[116:119], a[40:43], a[168:171], v[116:119]
  v_exp_f32 v113, v113
  v_mfma_f32_16x16x32_bf16 v[116:119], a[44:47], a[172:175], v[116:119]
  s_mov_b32 s66, s84
  v_cvt_pk_bf16_f32 v144, v112, v113
  v_mfma_f32_16x16x32_bf16 v[24:27], a[112:115], v[156:159], v[24:27]
  v_exp_f32 v114, v114
  v_mfma_f32_16x16x32_bf16 v[24:27], a[116:119], v[160:163], v[24:27]
  buffer_load_dwordx4 v244, s[44:47], s68 offen offset:2048 lds
  v_mfma_f32_16x16x32_bf16 v[56:59], a[112:115], v[164:167], v[56:59]
  v_exp_f32 v115, v115
  v_mfma_f32_16x16x32_bf16 v[56:59], a[116:119], v[168:171], v[56:59]
  ds_read_b64_tr_b16 a[100:101], v236 offset:24576
  v_cvt_pk_bf16_f32 v145, v114, v115
  v_mfma_f32_16x16x32_bf16 v[108:111], a[48:51], a[128:131], v[96:99]
  v_exp_f32 v116, v116
  v_mfma_f32_16x16x32_bf16 v[108:111], a[52:55], a[132:135], v[108:111]
  buffer_load_dwordx4 v245, s[44:47], s68 offen offset:3072 lds
  v_mfma_f32_16x16x32_bf16 v[108:111], a[56:59], a[136:139], v[108:111]
  v_exp_f32 v117, v117
  v_mfma_f32_16x16x32_bf16 v[108:111], a[60:63], a[140:143], v[108:111]
  v_cvt_pk_bf16_f32 v152, v116, v117
  s_getpc_b64 s[48:49]
RW21_%=:
  s_and_b32 s49, s49, 0xffff
  s_mov_b32 s50, CEND%=-RW21_%=
  s_mov_b32 s51, 0x20000
  s_lshl_b32 s70, s28, 2
  s_add_u32 s70, s70, s64
  s_lshl_b32 s70, s70, 10
  v_mfma_f32_16x16x32_bf16 v[88:91], a[112:115], v[172:175], v[88:91]
  v_exp_f32 v118, v118
  v_mfma_f32_16x16x32_bf16 v[88:91], a[116:119], v[176:179], v[88:91]
  ds_read_b64_tr_b16 a[102:103], v236 offset:28672
  ds_read_b64_tr_b16 a[104:105], v237 offset:16384
  v_mfma_f32_16x16x32_bf16 v[28:31], a[120:123], v[156:159], v[28:31]
  v_exp_f32 v119, v119
  v_mfma_f32_16x16x32_bf16 v[28:31], a[124:127], v[160:163], v[28:31]
  ds_read_b64_tr_b16 a[106:107], v237 offset:20480
  v_cvt_pk_bf16_f32 v153, v118, v119
  v_mfma_f32_16x16x32_bf16 v[60:63], a[120:123], v[164:167], v[60:63]
  buffer_load_dwordx4 v[224:227], v248, s[48:51], s70 offen offset:0
  v_mfma_f32_16x16x32_bf16 v[60:63], a[124:127], v[168:171], v[60:63]
  ds_read_b64_tr_b16 a[108:109], v237 offset:24576
  ds_read_b64_tr_b16 a[110:111], v237 offset:28672
  v_mfma_f32_16x16x32_bf16 v[112:115], a[48:51], a[144:147], v[100:103]
  v_exp_f32 v108, v108
  v_mfma_f32_16x16x32_bf16 v[112:115], a[52:55], a[148:151], v[112:115]
  ds_read_b128 a[32:35], v228 offset:24576
  ds_read_b128 a[36:39], v229 offset:24576
  v_mfma_f32_16x16x32_bf16 v[112:115], a[56:59], a[152:155], v[112:115]
  v_exp_f32 v109, v109
  v_mfma_f32_16x16x32_bf16 v[112:115], a[60:63], a[156:159], v[112:115]
  s_add_u32 m0, s62, 49152
  v_cvt_pk_bf16_f32 v138, v108, v109
  v_mfma_f32_16x16x32_bf16 v[92:95], a[120:123], v[172:175], v[92:95]
  v_exp_f32 v110, v110
  v_mfma_f32_16x16x32_bf16 v[92:95], a[124:127], v[176:179], v[92:95]
  ds_read_b128 a[40:43], v230 offset:24576
  ds_read_b128 a[44:47], v231 offset:24576
  v_mfma_f32_16x16x32_bf16 v[120:123], a[240:243], v[156:159], v[120:123]
  v_exp_f32 v111, v111
  v_mfma_f32_16x16x32_bf16 v[120:123], a[240:243], v[160:163], v[120:123]
  ds_read_b64_tr_b16 a[112:113], v238 offset:16384
  v_cvt_pk_bf16_f32 v139, v110, v111
  v_mfma_f32_16x16x32_bf16 v[116:119], a[48:51], a[160:163], v[104:107]
  v_exp_f32 v112, v112
  v_mfma_f32_16x16x32_bf16 v[116:119], a[52:55], a[164:167], v[116:119]
  buffer_load_dwordx4 v183, s[52:55], s72 offen offset:3072 lds
  v_mfma_f32_16x16x32_bf16 v[116:119], a[56:59], a[168:171], v[116:119]
  v_exp_f32 v113, v113
  v_mfma_f32_16x16x32_bf16 v[116:119], a[60:63], a[172:175], v[116:119]
  ds_read_b64_tr_b16 a[114:115], v238 offset:20480
  v_cvt_pk_bf16_f32 v146, v112, v113
  v_mfma_f32_16x16x32_bf16 v[124:127], a[240:243], v[164:167], v[124:127]
  v_exp_f32 v114, v114
  v_mfma_f32_16x16x32_bf16 v[124:127], a[240:243], v[168:171], v[124:127]
  ds_read_b64_tr_b16 a[116:117], v238 offset:24576
  ds_read_b64_tr_b16 a[118:119], v238 offset:28672
  ds_read_b64_tr_b16 a[120:121], v239 offset:16384
  v_mfma_f32_16x16x32_bf16 v[128:131], a[240:243], v[172:175], v[128:131]
  ds_read_b64_tr_b16 a[122:123], v239 offset:20480
  ds_read_b64_tr_b16 a[124:125], v239 offset:24576
  ds_read_b64_tr_b16 a[126:127], v239 offset:28672
  v_exp_f32 v115, v115
  v_mfma_f32_16x16x32_bf16 v[128:131], a[240:243], v[176:179], v[128:131]
  v_cvt_pk_bf16_f32 v147, v114, v115
  s_waitcnt lgkmcnt(8)
  v_mfma_f32_16x16x32_bf16 v[108:111], a[0:3], a[128:131], v[96:99]
  v_exp_f32 v116, v116
  v_mfma_f32_16x16x32_bf16 v[108:111], a[4:7], a[132:135], v[108:111]
  s_memrealtime s[82:83]
  ds_read_b128 a[48:51], v228 offset:28672
  v_mfma_f32_16x16x32_bf16 v[108:111], a[8:11], a[136:139], v[108:111]
  v_exp_f32 v117, v117
  v_mfma_f32_16x16x32_bf16 v[108:111], a[12:15], a[140:143], v[108:111]
  ds_read_b128 a[52:55], v229 offset:28672
  v_cvt_pk_bf16_f32 v154, v116, v117
  v_mfma_f32_16x16x32_bf16 v[0:3], a[64:67], v[132:135], v[0:3]
  v_exp_f32 v118, v118
  v_mfma_f32_16x16x32_bf16 v[0:3], a[68:71], v[136:139], v[0:3]
  ds_read_b128 a[56:59], v230 offset:28672
  ds_read_b128 a[60:63], v231 offset:28672
  v_mfma_f32_16x16x32_bf16 v[32:35], a[64:67], v[140:143], v[32:35]
  v_exp_f32 v119, v119
  v_mfma_f32_16x16x32_bf16 v[32:35], a[68:71], v[144:147], v[32:35]
  v_cvt_pk_bf16_f32 v155, v118, v119
  v_mfma_f32_16x16x32_bf16 v[112:115], a[0:3], a[144:147], v[100:103]
  v_exp_f32 v108, v108
  v_mfma_f32_16x16x32_bf16 v[112:115], a[4:7], a[148:151], v[112:115]
  v_mfma_f32_16x16x32_bf16 v[112:115], a[8:11], a[152:155], v[112:115]
  v_exp_f32 v109, v109
  v_mfma_f32_16x16x32_bf16 v[112:115], a[12:15], a[156:159], v[112:115]
  v_cvt_pk_bf16_f32 v156, v108, v109
  v_mfma_f32_16x16x32_bf16 v[64:67], a[64:67], v[148:151], v[64:67]
  v_exp_f32 v110, v110
  v_mfma_f32_16x16x32_bf16 v[64:67], a[68:71], v[152:155], v[64:67]
  s_waitcnt vmcnt(12) lgkmcnt(0)
  s_barrier
  v_mfma_f32_16x16x32_bf16 v[4:7], a[72:75], v[132:135], v[4:7]
  v_exp_f32 v111, v111
  v_mfma_f32_16x16x32_bf16 v[4:7], a[76:79], v[136:139], v[4:7]
  s_sub_u32 s84, s82, s85
  v_cvt_pk_bf16_f32 v157, v110, v111
  v_mfma_f32_16x16x32_bf16 v[116:119], a[0:3], a[160:163], v[104:107]
  v_exp_f32 v112, v112
  v_mfma_f32_16x16x32_bf16 v[116:119], a[4:7], a[164:167], v[116:119]
  s_cmp_lt_u32 s84, s94
  s_cbranch_scc1 PC22_%=
PC22_%=e:
  v_mfma_f32_16x16x32_bf16 v[116:119], a[8:11], a[168:171], v[116:119]
  v_exp_f32 v113, v113
  v_mfma_f32_16x16x32_bf16 v[116:119], a[12:15], a[172:175], v[116:119]
  s_add_u32 s68, s60, 425984
  v_cvt_pk_bf16_f32 v164, v112, v113
  v_mfma_f32_16x16x32_bf16 v[36:39], a[72:75], v[140:143], v[36:39]
  v_exp_f32 v114, v114
  v_mfma_f32_16x16x32_bf16 v[36:39], a[76:79], v[144:147], v[36:39]
  s_add_u32 m0, s61, 16384
  ds_read_b64_tr_b16 a[64:65], v232 offset:32768
  v_mfma_f32_16x16x32_bf16 v[68:71], a[72:75], v[148:151], v[68:71]
  v_exp_f32 v115, v115
  v_mfma_f32_16x16x32_bf16 v[68:71], a[76:79], v[152:155], v[68:71]
  ds_read_b64_tr_b16 a[66:67], v232 offset:36864
  v_cvt_pk_bf16_f32 v165, v114, v115
  v_mfma_f32_16x16x32_bf16 v[8:11], a[80:83], v[132:135], v[8:11]
  buffer_load_dwordx4 v240, s[40:43], s68 offen offset:0 nt lds
  v_mfma_f32_16x16x32_bf16 v[8:11], a[84:87], v[136:139], v[8:11]
  buffer_load_dwordx4 v241, s[40:43], s68 offen offset:1024 nt lds
  v_mfma_f32_16x16x32_bf16 v[108:111], a[16:19], a[128:131], v[96:99]
  v_exp_f32 v116, v116
  v_mfma_f32_16x16x32_bf16 v[108:111], a[20:23], a[132:135], v[108:111]
  ds_read_b64_tr_b16 a[68:69], v232 offset:40960
  ds_read_b64_tr_b16 a[70:71], v232 offset:45056
  v_mfma_f32_16x16x32_bf16 v[108:111], a[24:27], a[136:139], v[108:111]
  v_exp_f32 v117, v117
  v_mfma_f32_16x16x32_bf16 v[108:111], a[28:31], a[140:143], v[108:111]
  ds_read_b128 a[0:3], v228 offset:0
  v_cvt_pk_bf16_f32 v172, v116, v117
  v_mfma_f32_16x16x32_bf16 v[40:43], a[80:83], v[140:143], v[40:43]
  v_exp_f32 v118, v118
  v_mfma_f32_16x16x32_bf16 v[40:43], a[84:87], v[144:147], v[40:43]
  ds_read_b128 a[4:7], v229 offset:0
  ds_read_b128 a[8:11], v230 offset:0
  v_mfma_f32_16x16x32_bf16 v[72:75], a[80:83], v[148:151], v[72:75]
  v_exp_f32 v119, v119
  v_mfma_f32_16x16x32_bf16 v[72:75], a[84:87], v[152:155], v[72:75]
  ds_read_b128 a[12:15], v231 offset:0
  v_cvt_pk_bf16_f32 v173, v118, v119
  v_mfma_f32_16x16x32_bf16 v[112:115], a[16:19], a[144:147], v[100:103]
  v_exp_f32 v108, v108
  v_mfma_f32_16x16x32_bf16 v[112:115], a[20:23], a[148:151], v[112:115]
  buffer_load_dwordx4 v242, s[40:43], s68 offen offset:2048 nt lds
  v_mfma_f32_16x16x32_bf16 v[112:115], a[24:27], a[152:155], v[112:115]
  v_exp_f32 v109, v109
  v_mfma_f32_16x16x32_bf16 v[112:115], a[28:31], a[156:159], v[112:115]
  ds_read_b64_tr_b16 a[72:73], v233 offset:32768
  v_cvt_pk_bf16_f32 v158, v108, v109
  v_mfma_f32_16x16x32_bf16 v[12:15], a[88:91], v[132:135], v[12:15]
  v_exp_f32 v110, v110
  v_mfma_f32_16x16x32_bf16 v[12:15], a[92:95], v[136:139], v[12:15]
  ds_read_b64_tr_b16 a[74:75], v233 offset:36864
  ds_read_b64_tr_b16 a[76:77], v233 offset:40960
  ds_read_b64_tr_b16 a[78:79], v233 offset:45056
  v_mfma_f32_16x16x32_bf16 v[44:47], a[88:91], v[140:143], v[44:47]
  ds_read_b64_tr_b16 a[80:81], v234 offset:32768
  v_exp_f32 v111, v111
  v_mfma_f32_16x16x32_bf16 v[44:47], a[92:95], v[144:147], v[44:47]
  buffer_load_dwordx4 v243, s[40:43], s68 offen offset:3072 nt lds
  v_cvt_pk_bf16_f32 v159, v110, v111
  v_mfma_f32_16x16x32_bf16 v[116:119], a[16:19], a[160:163], v[104:107]
  ds_read_b64_tr_b16 a[82:83], v234 offset:36864
  v_exp_f32 v112, v112
  v_mfma_f32_16x16x32_bf16 v[116:119], a[20:23], a[164:167], v[116:119]
  s_add_u32 s97, s97, s95
  s_add_u32 s72, s93, 9
  s_and_b32 s72, s72, 31
  s_mul_i32 s72, s72, 40960
  ds_read_b64_tr_b16 a[84:85], v234 offset:40960
  v_mfma_f32_16x16x32_bf16 v[116:119], a[24:27], a[168:171], v[116:119]
  ds_read_b64_tr_b16 a[86:87], v234 offset:45056
  v_exp_f32 v113, v113
  v_mfma_f32_16x16x32_bf16 v[116:119], a[28:31], a[172:175], v[116:119]
  s_lshr_b32 s83, s97, 4
  v_cvt_pk_bf16_f32 v166, v112, v113
  v_mfma_f32_16x16x32_bf16 v[76:79], a[88:91], v[148:151], v[76:79]
  v_exp_f32 v114, v114
  v_mfma_f32_16x16x32_bf16 v[76:79], a[92:95], v[152:155], v[76:79]
  s_add_u32 s94, s84, s96
  s_add_u32 m0, s62, 122880
  ds_read_b128 a[16:19], v228 offset:4096
  v_mfma_f32_16x16x32_bf16 v[16:19], a[96:99], v[132:135], v[16:19]
  ds_read_b128 a[20:23], v229 offset:4096
  v_exp_f32 v115, v115
  v_mfma_f32_16x16x32_bf16 v[16:19], a[100:103], v[136:139], v[16:19]
  s_max_u32 s94, s94, s83
  ds_read_b128 a[24:27], v230 offset:4096
  v_cvt_pk_bf16_f32 v167, v114, v115
  v_mfma_f32_16x16x32_bf16 v[108:111], a[32:35], a[128:131], v[96:99]
  ds_read_b128 a[28:31], v231 offset:4096
  v_exp_f32 v116, v116
  v_mfma_f32_16x16x32_bf16 v[108:111], a[36:39], a[132:135], v[108:111]
  s_sub_u32 s83, s84, s66
  buffer_load_dwordx4 v180, s[52:55], s72 offen offset:0 lds
  v_mfma_f32_16x16x32_bf16 v[108:111], a[40:43], a[136:139], v[108:111]
  ds_read_b64_tr_b16 a[88:89], v235 offset:32768
  v_exp_f32 v117, v117
  v_mfma_f32_16x16x32_bf16 v[108:111], a[44:47], a[140:143], v[108:111]
  s_min_u32 s83, s83, 200
  ds_read_b64_tr_b16 a[90:91], v235 offset:36864
  v_cvt_pk_bf16_f32 v174, v116, v117
  v_mfma_f32_16x16x32_bf16 v[48:51], a[96:99], v[140:143], v[48:51]
  ds_read_b64_tr_b16 a[92:93], v235 offset:40960
  v_exp_f32 v118, v118
  v_mfma_f32_16x16x32_bf16 v[48:51], a[100:103], v[144:147], v[48:51]
  s_add_u32 s65, s65, s83
  s_add_u32 s68, s60, 409600
  v_mfma_f32_16x16x32_bf16 v[80:83], a[96:99], v[148:151], v[80:83]
  v_exp_f32 v119, v119
  v_mfma_f32_16x16x32_bf16 v[80:83], a[100:103], v[152:155], v[80:83]
  s_lshr_b32 s65, s65, 1
  v_cvt_pk_bf16_f32 v175, v118, v119
  v_mfma_f32_16x16x32_bf16 v[20:23], a[104:107], v[132:135], v[20:23]
  s_add_u32 s83, s65, s84
  s_add_u32 m0, s62, 16384
  v_mfma_f32_16x16x32_bf16 v[20:23], a[108:111], v[136:139], v[20:23]
  s_sub_u32 s83, s83, 28
  buffer_load_dwordx4 v244, s[44:47], s68 offen offset:0 lds
  v_mfma_f32_16x16x32_bf16 v[112:115], a[32:35], a[144:147], v[100:103]
  v_exp_f32 v108, v108
  v_mfma_f32_16x16x32_bf16 v[112:115], a[36:39], a[148:151], v[112:115]
  s_max_u32 s94, s94, s83
  ds_read_b64_tr_b16 a[94:95], v235 offset:45056
  v_mfma_f32_16x16x32_bf16 v[112:115], a[40:43], a[152:155], v[112:115]
  v_exp_f32 v109, v109
  v_mfma_f32_16x16x32_bf16 v[112:115], a[44:47], a[156:159], v[112:115]
  s_mov_b32 s66, s84
  v_cvt_pk_bf16_f32 v160, v108, v109
  v_mfma_f32_16x16x32_bf16 v[52:55], a[104:107], v[140:143], v[52:55]
  v_exp_f32 v110, v110
  v_mfma_f32_16x16x32_bf16 v[52:55], a[108:111], v[144:147], v[52:55]
  ds_read_b64_tr_b16 a[96:97], v236 offset:32768
  ds_read_b64_tr_b16 a[98:99], v236 offset:36864
  v_mfma_f32_16x16x32_bf16 v[84:87], a[104:107], v[148:151], v[84:87]
  v_exp_f32 v111, v111
  v_mfma_f32_16x16x32_bf16 v[84:87], a[108:111], v[152:155], v[84:87]
  ds_read_b64_tr_b16 a[100:101], v236 offset:40960
  v_cvt_pk_bf16_f32 v161, v110, v111
  v_mfma_f32_16x16x32_bf16 v[116:119], a[32:35], a[160:163], v[104:107]
  v_exp_f32 v112, v112
  v_mfma_f32_16x16x32_bf16 v[116:119], a[36:39], a[164:167], v[116:119]
  buffer_load_dwordx4 v245, s[44:47], s68 offen offset:1024 lds
  v_mfma_f32_16x16x32_bf16 v[116:119], a[40:43], a[168:171], v[116:119]
  v_exp_f32 v113, v113
  v_mfma_f32_16x16x32_bf16 v[116:119], a[44:47], a[172:175], v[116:119]
  ds_read_b64_tr_b16 a[102:103], v236 offset:45056
  v_cvt_pk_bf16_f32 v168, v112, v113
  v_mfma_f32_16x16x32_bf16 v[24:27], a[112:115], v[132:135], v[24:27]
  v_exp_f32 v114, v114
  v_mfma_f32_16x16x32_bf16 v[24:27], a[116:119], v[136:139], v[24:27]
  ds_read_b64_tr_b16 a[104:105], v237 offset:32768
  ds_read_b64_tr_b16 a[106:107], v237 offset:36864
  v_mfma_f32_16x16x32_bf16 v[56:59], a[112:115], v[140:143], v[56:59]
  v_exp_f32 v115, v115
  v_mfma_f32_16x16x32_bf16 v[56:59], a[116:119], v[144:147], v[56:59]
  ds_read_b64_tr_b16 a[108:109], v237 offset:40960
  v_cvt_pk_bf16_f32 v169, v114, v115
  v_mfma_f32_16x16x32_bf16 v[108:111], a[48:51], a[128:131], v[96:99]
  v_exp_f32 v116, v116
  v_mfma_f32_16x16x32_bf16 v[108:111], a[52:55], a[132:135], v[108:111]
  buffer_load_dwordx4 v244, s[44:47], s68 offen offset:2048 lds
  v_mfma_f32_16x16x32_bf16 v[108:111], a[56:59], a[136:139], v[108:111]
  v_exp_f32 v117, v117
  v_mfma_f32_16x16x32_bf16 v[108:111], a[60:63], a[140:143], v[108:111]
  ds_read_b64_tr_b16 a[110:111], v237 offset:45056
  v_cvt_pk_bf16_f32 v176, v116, v117
  v_mfma_f32_16x16x32_bf16 v[88:91], a[112:115], v[148:151], v[88:91]
  v_exp_f32 v118, v118
  v_mfma_f32_16x16x32_bf16 v[88:91], a[116:119], v[152:155], v[88:91]
  ds_read_b128 a[32:35], v228 offset:8192
  ds_read_b128 a[36:39], v229 offset:8192
  v_mfma_f32_16x16x32_bf16 v[28:31], a[120:123], v[132:135], v[28:31]
  v_exp_f32 v119, v119
  v_mfma_f32_16x16x32_bf16 v[28:31], a[124:127], v[136:139], v[28:31]
  ds_read_b128 a[40:43], v230 offset:8192
  v_cvt_pk_bf16_f32 v177, v118, v119
  v_mfma_f32_16x16x32_bf16 v[60:63], a[120:123], v[140:143], v[60:63]
  buffer_load_dwordx4 v245, s[44:47], s68 offen offset:3072 lds
  v_mfma_f32_16x16x32_bf16 v[60:63], a[124:127], v[144:147], v[60:63]
  ds_read_b128 a[44:47], v231 offset:8192
  ds_read_b64_tr_b16 a[112:113], v238 offset:32768
  v_mfma_f32_16x16x32_bf16 v[112:115], a[48:51], a[144:147], v[100:103]
  v_exp_f32 v108, v108
  v_mfma_f32_16x16x32_bf16 v[112:115], a[52:55], a[148:151], v[112:115]
  ds_read_b64_tr_b16 a[114:115], v238 offset:36864
  ds_read_b64_tr_b16 a[116:117], v238 offset:40960
  v_mfma_f32_16x16x32_bf16 v[112:115], a[56:59], a[152:155], v[112:115]
  v_exp_f32 v109, v109
  v_mfma_f32_16x16x32_bf16 v[112:115], a[60:63], a[156:159], v[112:115]
  s_add_u32 m0, s62, 122880
  v_cvt_pk_bf16_f32 v162, v108, v109
  v_mfma_f32_16x16x32_bf16 v[92:95], a[120:123], v[148:151], v[92:95]
  v_exp_f32 v110, v110
  v_mfma_f32_16x16x32_bf16 v[92:95], a[124:127], v[152:155], v[92:95]
  buffer_load_dwordx4 v181, s[52:55], s72 offen offset:1024 lds
  v_mfma_f32_16x16x32_bf16 v[120:123], a[240:243], v[132:135], v[120:123]
  v_exp_f32 v111, v111
  v_mfma_f32_16x16x32_bf16 v[120:123], a[240:243], v[136:139], v[120:123]
  ds_read_b64_tr_b16 a[118:119], v238 offset:45056
  v_cvt_pk_bf16_f32 v163, v110, v111
  v_mfma_f32_16x16x32_bf16 v[116:119], a[48:51], a[160:163], v[104:107]
  v_exp_f32 v112, v112
  v_mfma_f32_16x16x32_bf16 v[116:119], a[52:55], a[164:167], v[116:119]
  ds_read_b64_tr_b16 a[120:121], v239 offset:32768
  ds_read_b64_tr_b16 a[122:123], v239 offset:36864
  v_mfma_f32_16x16x32_bf16 v[116:119], a[56:59], a[168:171], v[116:119]
  v_exp_f32 v113, v113
  v_mfma_f32_16x16x32_bf16 v[116:119], a[60:63], a[172:175], v[116:119]
  ds_read_b64_tr_b16 a[124:125], v239 offset:40960
  v_cvt_pk_bf16_f32 v170, v112, v113
  v_mfma_f32_16x16x32_bf16 v[124:127], a[240:243], v[140:143], v[124:127]
  v_exp_f32 v114, v114
  v_mfma_f32_16x16x32_bf16 v[124:127], a[240:243], v[144:147], v[124:127]
  ds_read_b64_tr_b16 a[126:127], v239 offset:45056
  v_mfma_f32_16x16x32_bf16 v[128:131], a[240:243], v[148:151], v[128:131]
  v_exp_f32 v115, v115
  v_mfma_f32_16x16x32_bf16 v[128:131], a[240:243], v[152:155], v[128:131]
  v_cvt_pk_bf16_f32 v171, v114, v115
  s_waitcnt lgkmcnt(8)
  v_mfma_f32_16x16x32_bf16 v[108:111], a[0:3], a[128:131], v[96:99]
  v_exp_f32 v116, v116
  v_mfma_f32_16x16x32_bf16 v[108:111], a[4:7], a[132:135], v[108:111]
  s_memrealtime s[82:83]
  ds_read_b128 a[48:51], v228 offset:12288
  v_mfma_f32_16x16x32_bf16 v[108:111], a[8:11], a[136:139], v[108:111]
  v_exp_f32 v117, v117
  v_mfma_f32_16x16x32_bf16 v[108:111], a[12:15], a[140:143], v[108:111]
  ds_read_b128 a[52:55], v229 offset:12288
  v_cvt_pk_bf16_f32 v178, v116, v117
  v_mfma_f32_16x16x32_bf16 v[0:3], a[64:67], v[156:159], v[0:3]
  v_exp_f32 v118, v118
  v_mfma_f32_16x16x32_bf16 v[0:3], a[68:71], v[160:163], v[0:3]
  ds_read_b128 a[56:59], v230 offset:12288
  ds_read_b128 a[60:63], v231 offset:12288
  v_mfma_f32_16x16x32_bf16 v[32:35], a[64:67], v[164:167], v[32:35]
  v_exp_f32 v119, v119
  v_mfma_f32_16x16x32_bf16 v[32:35], a[68:71], v[168:171], v[32:35]
  v_cvt_pk_bf16_f32 v179, v118, v119
  v_mfma_f32_16x16x32_bf16 v[112:115], a[0:3], a[144:147], v[100:103]
  v_exp_f32 v108, v108
  v_mfma_f32_16x16x32_bf16 v[112:115], a[4:7], a[148:151], v[112:115]
  v_mfma_f32_16x16x32_bf16 v[112:115], a[8:11], a[152:155], v[112:115]
  v_exp_f32 v109, v109
  v_mfma_f32_16x16x32_bf16 v[112:115], a[12:15], a[156:159], v[112:115]
  v_cvt_pk_bf16_f32 v132, v108, v109
  v_mfma_f32_16x16x32_bf16 v[64:67], a[64:67], v[172:175], v[64:67]
  v_exp_f32 v110, v110
  v_mfma_f32_16x16x32_bf16 v[64:67], a[68:71], v[176:179], v[64:67]
  s_waitcnt vmcnt(12) lgkmcnt(0)
  s_barrier
  v_mfma_f32_16x16x32_bf16 v[4:7], a[72:75], v[156:159], v[4:7]
  v_exp_f32 v111, v111
  v_mfma_f32_16x16x32_bf16 v[4:7], a[76:79], v[160:163], v[4:7]
  s_sub_u32 s84, s82, s85
  v_cvt_pk_bf16_f32 v133, v110, v111
  v_mfma_f32_16x16x32_bf16 v[116:119], a[0:3], a[160:163], v[104:107]
  v_exp_f32 v112, v112
  v_mfma_f32_16x16x32_bf16 v[116:119], a[4:7], a[164:167], v[116:119]
  s_cmp_lt_u32 s84, s94
  s_cbranch_scc1 PC23_%=
PC23_%=e:
  v_mfma_f32_16x16x32_bf16 v[116:119], a[8:11], a[168:171], v[116:119]
  v_exp_f32 v113, v113
  v_mfma_f32_16x16x32_bf16 v[116:119], a[12:15], a[172:175], v[116:119]
  s_add_u32 s68, s60, 442368
  v_cvt_pk_bf16_f32 v140, v112, v113
  v_mfma_f32_16x16x32_bf16 v[36:39], a[72:75], v[164:167], v[36:39]
  v_exp_f32 v114, v114
  v_mfma_f32_16x16x32_bf16 v[36:39], a[76:79], v[168:171], v[36:39]
  s_add_u32 m0, s61, 0
  ds_read_b64_tr_b16 a[64:65], v232 offset:0
  v_mfma_f32_16x16x32_bf16 v[68:71], a[72:75], v[172:175], v[68:71]
  v_exp_f32 v115, v115
  v_mfma_f32_16x16x32_bf16 v[68:71], a[76:79], v[176:179], v[68:71]
  ds_read_b64_tr_b16 a[66:67], v232 offset:4096
  v_cvt_pk_bf16_f32 v141, v114, v115
  v_mfma_f32_16x16x32_bf16 v[8:11], a[80:83], v[156:159], v[8:11]
  buffer_load_dwordx4 v240, s[40:43], s68 offen offset:0 nt lds
  v_mfma_f32_16x16x32_bf16 v[8:11], a[84:87], v[160:163], v[8:11]
  buffer_load_dwordx4 v241, s[40:43], s68 offen offset:1024 nt lds
  v_mfma_f32_16x16x32_bf16 v[108:111], a[16:19], a[128:131], v[96:99]
  v_exp_f32 v116, v116
  v_mfma_f32_16x16x32_bf16 v[108:111], a[20:23], a[132:135], v[108:111]
  ds_read_b64_tr_b16 a[68:69], v232 offset:8192
  ds_read_b64_tr_b16 a[70:71], v232 offset:12288
  v_mfma_f32_16x16x32_bf16 v[108:111], a[24:27], a[136:139], v[108:111]
  v_exp_f32 v117, v117
  v_mfma_f32_16x16x32_bf16 v[108:111], a[28:31], a[140:143], v[108:111]
  ds_read_b128 a[0:3], v228 offset:49152
  v_cvt_pk_bf16_f32 v148, v116, v117
  v_mfma_f32_16x16x32_bf16 v[40:43], a[80:83], v[164:167], v[40:43]
  v_exp_f32 v118, v118
  v_mfma_f32_16x16x32_bf16 v[40:43], a[84:87], v[168:171], v[40:43]
  ds_read_b128 a[4:7], v229 offset:49152
  ds_read_b128 a[8:11], v230 offset:49152
  v_mfma_f32_16x16x32_bf16 v[72:75], a[80:83], v[172:175], v[72:75]
  v_exp_f32 v119, v119
  v_mfma_f32_16x16x32_bf16 v[72:75], a[84:87], v[176:179], v[72:75]
  ds_read_b128 a[12:15], v231 offset:49152
  v_cvt_pk_bf16_f32 v149, v118, v119
  v_mfma_f32_16x16x32_bf16 v[112:115], a[16:19], a[144:147], v[100:103]
  v_exp_f32 v108, v108
  v_mfma_f32_16x16x32_bf16 v[112:115], a[20:23], a[148:151], v[112:115]
  buffer_load_dwordx4 v242, s[40:43], s68 offen offset:2048 nt lds
  v_mfma_f32_16x16x32_bf16 v[112:115], a[24:27], a[152:155], v[112:115]
  v_exp_f32 v109, v109
  v_mfma_f32_16x16x32_bf16 v[112:115], a[28:31], a[156:159], v[112:115]
  ds_read_b64_tr_b16 a[72:73], v233 offset:0
  v_cvt_pk_bf16_f32 v134, v108, v109
  v_mfma_f32_16x16x32_bf16 v[12:15], a[88:91], v[156:159], v[12:15]
  v_exp_f32 v110, v110
  v_mfma_f32_16x16x32_bf16 v[12:15], a[92:95], v[160:163], v[12:15]
  ds_read_b64_tr_b16 a[74:75], v233 offset:4096
  ds_read_b64_tr_b16 a[76:77], v233 offset:8192
  v_mfma_f32_16x16x32_bf16 v[44:47], a[88:91], v[164:167], v[44:47]
  v_exp_f32 v111, v111
  v_mfma_f32_16x16x32_bf16 v[44:47], a[92:95], v[168:171], v[44:47]
  buffer_load_dwordx4 v243, s[40:43], s68 offen offset:3072 nt lds
  v_cvt_pk_bf16_f32 v135, v110, v111
  v_mfma_f32_16x16x32_bf16 v[116:119], a[16:19], a[160:163], v[104:107]
  v_exp_f32 v112, v112
  v_mfma_f32_16x16x32_bf16 v[116:119], a[20:23], a[164:167], v[116:119]
  s_add_u32 s97, s97, s95
  ds_read_b64_tr_b16 a[78:79], v233 offset:12288
  ds_read_b64_tr_b16 a[80:81], v234 offset:0
  v_mfma_f32_16x16x32_bf16 v[116:119], a[24:27], a[168:171], v[116:119]
  ds_read_b64_tr_b16 a[82:83], v234 offset:4096
  v_exp_f32 v113, v113
  v_mfma_f32_16x16x32_bf16 v[116:119], a[28:31], a[172:175], v[116:119]
  s_lshr_b32 s83, s97, 4
  ds_read_b64_tr_b16 a[84:85], v234 offset:8192
  v_cvt_pk_bf16_f32 v142, v112, v113
  v_mfma_f32_16x16x32_bf16 v[76:79], a[88:91], v[172:175], v[76:79]
  ds_read_b64_tr_b16 a[86:87], v234 offset:12288
  v_exp_f32 v114, v114
  v_mfma_f32_16x16x32_bf16 v[76:79], a[92:95], v[176:179], v[76:79]
  s_add_u32 s94, s84, s96
  s_add_u32 m0, s62, 122880
  ds_read_b128 a[16:19], v228 offset:53248
  v_mfma_f32_16x16x32_bf16 v[16:19], a[96:99], v[156:159], v[16:19]
  ds_read_b128 a[20:23], v229 offset:53248
  v_exp_f32 v115, v115
  v_mfma_f32_16x16x32_bf16 v[16:19], a[100:103], v[160:163], v[16:19]
  s_max_u32 s94, s94, s83
  ds_read_b128 a[24:27], v230 offset:53248
  v_cvt_pk_bf16_f32 v143, v114, v115
  v_mfma_f32_16x16x32_bf16 v[108:111], a[32:35], a[128:131], v[96:99]
  ds_read_b128 a[28:31], v231 offset:53248
  v_exp_f32 v116, v116
  v_mfma_f32_16x16x32_bf16 v[108:111], a[36:39], a[132:135], v[108:111]
  s_sub_u32 s83, s84, s66
  buffer_load_dwordx4 v182, s[52:55], s72 offen offset:2048 lds
  v_mfma_f32_16x16x32_bf16 v[108:111], a[40:43], a[136:139], v[108:111]
  ds_read_b64_tr_b16 a[88:89], v235 offset:0
  v_exp_f32 v117, v117
  v_mfma_f32_16x16x32_bf16 v[108:111], a[44:47], a[140:143], v[108:111]
  s_min_u32 s83, s83, 200
  ds_read_b64_tr_b16 a[90:91], v235 offset:4096
  v_cvt_pk_bf16_f32 v150, v116, v117
  v_mfma_f32_16x16x32_bf16 v[48:51], a[96:99], v[164:167], v[48:51]
  ds_read_b64_tr_b16 a[92:93], v235 offset:8192
  v_exp_f32 v118, v118
  v_mfma_f32_16x16x32_bf16 v[48:51], a[100:103], v[168:171], v[48:51]
  s_add_u32 s65, s65, s83
  s_add_u32 s68, s60, 425984
  v_mfma_f32_16x16x32_bf16 v[80:83], a[96:99], v[172:175], v[80:83]
  v_exp_f32 v119, v119
  v_mfma_f32_16x16x32_bf16 v[80:83], a[100:103], v[176:179], v[80:83]
  s_lshr_b32 s65, s65, 1
  v_cvt_pk_bf16_f32 v151, v118, v119
  v_mfma_f32_16x16x32_bf16 v[20:23], a[104:107], v[156:159], v[20:23]
  s_add_u32 s83, s65, s84
  s_add_u32 m0, s62, 32768
  v_mfma_f32_16x16x32_bf16 v[20:23], a[108:111], v[160:163], v[20:23]
  s_sub_u32 s83, s83, 28
  buffer_load_dwordx4 v244, s[44:47], s68 offen offset:0 lds
  v_mfma_f32_16x16x32_bf16 v[112:115], a[32:35], a[144:147], v[100:103]
  v_exp_f32 v108, v108
  v_mfma_f32_16x16x32_bf16 v[112:115], a[36:39], a[148:151], v[112:115]
  s_max_u32 s94, s94, s83
  ds_read_b64_tr_b16 a[94:95], v235 offset:12288
  v_mfma_f32_16x16x32_bf16 v[112:115], a[40:43], a[152:155], v[112:115]
  v_exp_f32 v109, v109
  v_mfma_f32_16x16x32_bf16 v[112:115], a[44:47], a[156:159], v[112:115]
  s_mov_b32 s66, s84
  v_cvt_pk_bf16_f32 v136, v108, v109
  v_mfma_f32_16x16x32_bf16 v[52:55], a[104:107], v[164:167], v[52:55]
  v_exp_f32 v110, v110
  v_mfma_f32_16x16x32_bf16 v[52:55], a[108:111], v[168:171], v[52:55]
  ds_read_b64_tr_b16 a[96:97], v236 offset:0
  ds_read_b64_tr_b16 a[98:99], v236 offset:4096
  v_mfma_f32_16x16x32_bf16 v[84:87], a[104:107], v[172:175], v[84:87]
  v_exp_f32 v111, v111
  v_mfma_f32_16x16x32_bf16 v[84:87], a[108:111], v[176:179], v[84:87]
  ds_read_b64_tr_b16 a[100:101], v236 offset:8192
  v_cvt_pk_bf16_f32 v137, v110, v111
  v_mfma_f32_16x16x32_bf16 v[116:119], a[32:35], a[160:163], v[104:107]
  v_exp_f32 v112, v112
  v_mfma_f32_16x16x32_bf16 v[116:119], a[36:39], a[164:167], v[116:119]
  buffer_load_dwordx4 v245, s[44:47], s68 offen offset:1024 lds
  v_mfma_f32_16x16x32_bf16 v[116:119], a[40:43], a[168:171], v[116:119]
  v_exp_f32 v113, v113
  v_mfma_f32_16x16x32_bf16 v[116:119], a[44:47], a[172:175], v[116:119]
  ds_read_b64_tr_b16 a[102:103], v236 offset:12288
  v_cvt_pk_bf16_f32 v144, v112, v113
  v_mfma_f32_16x16x32_bf16 v[24:27], a[112:115], v[156:159], v[24:27]
  v_exp_f32 v114, v114
  v_mfma_f32_16x16x32_bf16 v[24:27], a[116:119], v[160:163], v[24:27]
  ds_read_b64_tr_b16 a[104:105], v237 offset:0
  ds_read_b64_tr_b16 a[106:107], v237 offset:4096
  v_mfma_f32_16x16x32_bf16 v[56:59], a[112:115], v[164:167], v[56:59]
  v_exp_f32 v115, v115
  v_mfma_f32_16x16x32_bf16 v[56:59], a[116:119], v[168:171], v[56:59]
  ds_read_b64_tr_b16 a[108:109], v237 offset:8192
  v_cvt_pk_bf16_f32 v145, v114, v115
  v_mfma_f32_16x16x32_bf16 v[108:111], a[48:51], a[128:131], v[96:99]
  v_exp_f32 v116, v116
  v_mfma_f32_16x16x32_bf16 v[108:111], a[52:55], a[132:135], v[108:111]
  buffer_load_dwordx4 v244, s[44:47], s68 offen offset:2048 lds
  v_mfma_f32_16x16x32_bf16 v[108:111], a[56:59], a[136:139], v[108:111]
  v_exp_f32 v117, v117
  v_mfma_f32_16x16x32_bf16 v[108:111], a[60:63], a[140:143], v[108:111]
  ds_read_b64_tr_b16 a[110:111], v237 offset:12288
  v_cvt_pk_bf16_f32 v152, v116, v117
  v_mfma_f32_16x16x32_bf16 v[88:91], a[112:115], v[172:175], v[88:91]
  v_exp_f32 v118, v118
  v_mfma_f32_16x16x32_bf16 v[88:91], a[116:119], v[176:179], v[88:91]
  ds_read_b128 a[32:35], v228 offset:57344
  ds_read_b128 a[36:39], v229 offset:57344
  v_mfma_f32_16x16x32_bf16 v[28:31], a[120:123], v[156:159], v[28:31]
  v_exp_f32 v119, v119
  v_mfma_f32_16x16x32_bf16 v[28:31], a[124:127], v[160:163], v[28:31]
  ds_read_b128 a[40:43], v230 offset:57344
  v_cvt_pk_bf16_f32 v153, v118, v119
  v_mfma_f32_16x16x32_bf16 v[60:63], a[120:123], v[164:167], v[60:63]
  buffer_load_dwordx4 v245, s[44:47], s68 offen offset:3072 lds
  v_mfma_f32_16x16x32_bf16 v[60:63], a[124:127], v[168:171], v[60:63]
  ds_read_b128 a[44:47], v231 offset:57344
  ds_read_b64_tr_b16 a[112:113], v238 offset:0
  v_mfma_f32_16x16x32_bf16 v[112:115], a[48:51], a[144:147], v[100:103]
  v_exp_f32 v108, v108
  v_mfma_f32_16x16x32_bf16 v[112:115], a[52:55], a[148:151], v[112:115]
  ds_read_b64_tr_b16 a[114:115], v238 offset:4096
  ds_read_b64_tr_b16 a[116:117], v238 offset:8192
  v_mfma_f32_16x16x32_bf16 v[112:115], a[56:59], a[152:155], v[112:115]
  v_exp_f32 v109, v109
  v_mfma_f32_16x16x32_bf16 v[112:115], a[60:63], a[156:159], v[112:115]
  s_add_u32 m0, s62, 122880
  v_cvt_pk_bf16_f32 v138, v108, v109
  v_mfma_f32_16x16x32_bf16 v[92:95], a[120:123], v[172:175], v[92:95]
  v_exp_f32 v110, v110
  v_mfma_f32_16x16x32_bf16 v[92:95], a[124:127], v[176:179], v[92:95]
  buffer_load_dwordx4 v183, s[52:55], s72 offen offset:3072 lds
  v_mfma_f32_16x16x32_bf16 v[120:123], a[240:243], v[156:159], v[120:123]
  v_exp_f32 v111, v111
  v_mfma_f32_16x16x32_bf16 v[120:123], a[240:243], v[160:163], v[120:123]
  ds_read_b64_tr_b16 a[118:119], v238 offset:12288
  v_cvt_pk_bf16_f32 v139, v110, v111
  v_mfma_f32_16x16x32_bf16 v[116:119], a[48:51], a[160:163], v[104:107]
  v_exp_f32 v112, v112
  v_mfma_f32_16x16x32_bf16 v[116:119], a[52:55], a[164:167], v[116:119]
  ds_read_b64_tr_b16 a[120:121], v239 offset:0
  ds_read_b64_tr_b16 a[122:123], v239 offset:4096
  v_mfma_f32_16x16x32_bf16 v[116:119], a[56:59], a[168:171], v[116:119]
  v_exp_f32 v113, v113
  v_mfma_f32_16x16x32_bf16 v[116:119], a[60:63], a[172:175], v[116:119]
  ds_read_b64_tr_b16 a[124:125], v239 offset:8192
  v_cvt_pk_bf16_f32 v146, v112, v113
  v_mfma_f32_16x16x32_bf16 v[124:127], a[240:243], v[164:167], v[124:127]
  v_exp_f32 v114, v114
  v_mfma_f32_16x16x32_bf16 v[124:127], a[240:243], v[168:171], v[124:127]
  ds_read_b64_tr_b16 a[126:127], v239 offset:12288
  v_mfma_f32_16x16x32_bf16 v[128:131], a[240:243], v[172:175], v[128:131]
  v_exp_f32 v115, v115
  v_mfma_f32_16x16x32_bf16 v[128:131], a[240:243], v[176:179], v[128:131]
  v_cvt_pk_bf16_f32 v147, v114, v115
  s_waitcnt lgkmcnt(8)
  v_mfma_f32_16x16x32_bf16 v[108:111], a[0:3], a[128:131], v[96:99]
  v_exp_f32 v116, v116
  v_mfma_f32_16x16x32_bf16 v[108:111], a[4:7], a[132:135], v[108:111]
  s_memrealtime s[82:83]
  ds_read_b128 a[48:51], v228 offset:61440
  v_mfma_f32_16x16x32_bf16 v[108:111], a[8:11], a[136:139], v[108:111]
  v_exp_f32 v117, v117
  v_mfma_f32_16x16x32_bf16 v[108:111], a[12:15], a[140:143], v[108:111]
  ds_read_b128 a[52:55], v229 offset:61440
  v_cvt_pk_bf16_f32 v154, v116, v117
  v_mfma_f32_16x16x32_bf16 v[0:3], a[64:67], v[132:135], v[0:3]
  v_exp_f32 v118, v118
  v_mfma_f32_16x16x32_bf16 v[0:3], a[68:71], v[136:139], v[0:3]
  ds_read_b128 a[56:59], v230 offset:61440
  ds_read_b128 a[60:63], v231 offset:61440
  v_mfma_f32_16x16x32_bf16 v[32:35], a[64:67], v[140:143], v[32:35]
  v_exp_f32 v119, v119
  v_mfma_f32_16x16x32_bf16 v[32:35], a[68:71], v[144:147], v[32:35]
  v_cvt_pk_bf16_f32 v155, v118, v119
  v_mfma_f32_16x16x32_bf16 v[112:115], a[0:3], a[144:147], v[100:103]
  v_exp_f32 v108, v108
  v_mfma_f32_16x16x32_bf16 v[112:115], a[4:7], a[148:151], v[112:115]
  v_mfma_f32_16x16x32_bf16 v[112:115], a[8:11], a[152:155], v[112:115]
  v_exp_f32 v109, v109
  v_mfma_f32_16x16x32_bf16 v[112:115], a[12:15], a[156:159], v[112:115]
  v_cvt_pk_bf16_f32 v156, v108, v109
  v_mfma_f32_16x16x32_bf16 v[64:67], a[64:67], v[148:151], v[64:67]
  v_exp_f32 v110, v110
  v_mfma_f32_16x16x32_bf16 v[64:67], a[68:71], v[152:155], v[64:67]
  s_waitcnt vmcnt(11) lgkmcnt(0)
  s_barrier
  v_mfma_f32_16x16x32_bf16 v[4:7], a[72:75], v[132:135], v[4:7]
  v_exp_f32 v111, v111
  v_mfma_f32_16x16x32_bf16 v[4:7], a[76:79], v[136:139], v[4:7]
  s_sub_u32 s84, s82, s85
  v_cvt_pk_bf16_f32 v157, v110, v111
  v_mfma_f32_16x16x32_bf16 v[116:119], a[0:3], a[160:163], v[104:107]
  v_exp_f32 v112, v112
  v_mfma_f32_16x16x32_bf16 v[116:119], a[4:7], a[164:167], v[116:119]
  s_cmp_lt_u32 s84, s94
  s_cbranch_scc1 PC24_%=
PC24_%=e:
  v_mfma_f32_16x16x32_bf16 v[116:119], a[8:11], a[168:171], v[116:119]
  v_exp_f32 v113, v113
  v_mfma_f32_16x16x32_bf16 v[116:119], a[12:15], a[172:175], v[116:119]
  s_add_u32 s68, s60, 458752
  v_cvt_pk_bf16_f32 v164, v112, v113
  v_mfma_f32_16x16x32_bf16 v[36:39], a[72:75], v[140:143], v[36:39]
  v_exp_f32 v114, v114
  v_mfma_f32_16x16x32_bf16 v[36:39], a[76:79], v[144:147], v[36:39]
  s_add_u32 m0, s61, 49152
  ds_read_b64_tr_b16 a[64:65], v232 offset:16384
  v_mfma_f32_16x16x32_bf16 v[68:71], a[72:75], v[148:151], v[68:71]
  v_exp_f32 v115, v115
  v_mfma_f32_16x16x32_bf16 v[68:71], a[76:79], v[152:155], v[68:71]
  ds_read_b64_tr_b16 a[66:67], v232 offset:20480
  v_cvt_pk_bf16_f32 v165, v114, v115
  v_mfma_f32_16x16x32_bf16 v[8:11], a[80:83], v[132:135], v[8:11]
  buffer_load_dwordx4 v240, s[40:43], s68 offen offset:0 nt lds
  v_mfma_f32_16x16x32_bf16 v[8:11], a[84:87], v[136:139], v[8:11]
  ds_read_b64_tr_b16 a[68:69], v232 offset:24576
  ds_read_b64_tr_b16 a[70:71], v232 offset:28672
  v_mfma_f32_16x16x32_bf16 v[108:111], a[16:19], a[128:131], v[96:99]
  v_exp_f32 v116, v116
  v_mfma_f32_16x16x32_bf16 v[108:111], a[20:23], a[132:135], v[108:111]
  buffer_load_dwordx4 v241, s[40:43], s68 offen offset:1024 nt lds
  v_mfma_f32_16x16x32_bf16 v[108:111], a[24:27], a[136:139], v[108:111]
  v_exp_f32 v117, v117
  v_mfma_f32_16x16x32_bf16 v[108:111], a[28:31], a[140:143], v[108:111]
  ds_read_b128 a[0:3], v228 offset:16384
  v_cvt_pk_bf16_f32 v172, v116, v117
  v_mfma_f32_16x16x32_bf16 v[40:43], a[80:83], v[140:143], v[40:43]
  v_exp_f32 v118, v118
  v_mfma_f32_16x16x32_bf16 v[40:43], a[84:87], v[144:147], v[40:43]
  ds_read_b128 a[4:7], v229 offset:16384
  ds_read_b128 a[8:11], v230 offset:16384
  v_mfma_f32_16x16x32_bf16 v[72:75], a[80:83], v[148:151], v[72:75]
  v_exp_f32 v119, v119
  v_mfma_f32_16x16x32_bf16 v[72:75], a[84:87], v[152:155], v[72:75]
  ds_read_b128 a[12:15], v231 offset:16384
  v_cvt_pk_bf16_f32 v173, v118, v119
  v_mfma_f32_16x16x32_bf16 v[112:115], a[16:19], a[144:147], v[100:103]
  v_exp_f32 v108, v108
  v_mfma_f32_16x16x32_bf16 v[112:115], a[20:23], a[148:151], v[112:115]
  ds_read_b64_tr_b16 a[72:73], v233 offset:16384
  ds_read_b64_tr_b16 a[74:75], v233 offset:20480
  v_mfma_f32_16x16x32_bf16 v[112:115], a[24:27], a[152:155], v[112:115]
  v_exp_f32 v109, v109
  v_mfma_f32_16x16x32_bf16 v[112:115], a[28:31], a[156:159], v[112:115]
  ds_read_b64_tr_b16 a[76:77], v233 offset:24576
  v_cvt_pk_bf16_f32 v158, v108, v109
  v_mfma_f32_16x16x32_bf16 v[12:15], a[88:91], v[132:135], v[12:15]
  v_exp_f32 v110, v110
  v_mfma_f32_16x16x32_bf16 v[12:15], a[92:95], v[136:139], v[12:15]
  buffer_load_dwordx4 v242, s[40:43], s68 offen offset:2048 nt lds
  v_mfma_f32_16x16x32_bf16 v[44:47], a[88:91], v[140:143], v[44:47]
  v_exp_f32 v111, v111
  v_mfma_f32_16x16x32_bf16 v[44:47], a[92:95], v[144:147], v[44:47]
  ds_read_b64_tr_b16 a[78:79], v233 offset:28672
  v_cvt_pk_bf16_f32 v159, v110, v111
  v_mfma_f32_16x16x32_bf16 v[116:119], a[16:19], a[160:163], v[104:107]
  v_exp_f32 v112, v112
  v_mfma_f32_16x16x32_bf16 v[116:119], a[20:23], a[164:167], v[116:119]
  s_sub_u32 s95, s95, 80
  ds_read_b64_tr_b16 a[80:81], v234 offset:16384
  v_mfma_f32_16x16x32_bf16 v[116:119], a[24:27], a[168:171], v[116:119]
  v_exp_f32 v113, v113
  v_mfma_f32_16x16x32_bf16 v[116:119], a[28:31], a[172:175], v[116:119]
  s_max_u32 s95, s95, 1808
  v_cvt_pk_bf16_f32 v166, v112, v113
  v_mfma_f32_16x16x32_bf16 v[76:79], a[88:91], v[148:151], v[76:79]
  v_exp_f32 v114, v114
  v_mfma_f32_16x16x32_bf16 v[76:79], a[92:95], v[152:155], v[76:79]
  s_lshr_b32 s96, s95, 4
  ds_read_b64_tr_b16 a[82:83], v234 offset:20480
  ds_read_b64_tr_b16 a[84:85], v234 offset:24576
  v_mfma_f32_16x16x32_bf16 v[16:19], a[96:99], v[132:135], v[16:19]
  ds_read_b64_tr_b16 a[86:87], v234 offset:28672
  v_exp_f32 v115, v115
  v_mfma_f32_16x16x32_bf16 v[16:19], a[100:103], v[136:139], v[16:19]
  s_sub_u32 s96, s96, 4
  ds_read_b128 a[16:19], v228 offset:20480
  v_cvt_pk_bf16_f32 v167, v114, v115
  v_mfma_f32_16x16x32_bf16 v[108:111], a[32:35], a[128:131], v[96:99]
  ds_read_b128 a[20:23], v229 offset:20480
  v_exp_f32 v116, v116
  v_mfma_f32_16x16x32_bf16 v[108:111], a[36:39], a[132:135], v[108:111]
  s_add_u32 s97, s97, s95
  buffer_load_dwordx4 v243, s[40:43], s68 offen offset:3072 nt lds
  v_mfma_f32_16x16x32_bf16 v[108:111], a[40:43], a[136:139], v[108:111]
  ds_read_b128 a[24:27], v230 offset:20480
  v_exp_f32 v117, v117
  v_mfma_f32_16x16x32_bf16 v[108:111], a[44:47], a[140:143], v[108:111]
  s_lshr_b32 s83, s97, 4
  ds_read_b128 a[28:31], v231 offset:20480
  v_cvt_pk_bf16_f32 v174, v116, v117
  v_mfma_f32_16x16x32_bf16 v[48:51], a[96:99], v[140:143], v[48:51]
  ds_read_b64_tr_b16 a[88:89], v235 offset:16384
  v_exp_f32 v118, v118
  v_mfma_f32_16x16x32_bf16 v[48:51], a[100:103], v[144:147], v[48:51]
  s_add_u32 s94, s84, s96
  s_add_u32 s68, s60, 442368
  v_mfma_f32_16x16x32_bf16 v[80:83], a[96:99], v[148:151], v[80:83]
  v_exp_f32 v119, v119
  v_mfma_f32_16x16x32_bf16 v[80:83], a[100:103], v[152:155], v[80:83]
  s_max_u32 s94, s94, s83
  v_cvt_pk_bf16_f32 v175, v118, v119
  v_mfma_f32_16x16x32_bf16 v[20:23], a[104:107], v[132:135], v[20:23]
  s_sub_u32 s83, s84, s66
  s_add_u32 m0, s62, 0
  v_mfma_f32_16x16x32_bf16 v[20:23], a[108:111], v[136:139], v[20:23]
  s_min_u32 s83, s83, 200
  buffer_load_dwordx4 v244, s[44:47], s68 offen offset:0 lds
  v_mfma_f32_16x16x32_bf16 v[112:115], a[32:35], a[144:147], v[100:103]
  v_exp_f32 v108, v108
  v_mfma_f32_16x16x32_bf16 v[112:115], a[36:39], a[148:151], v[112:115]
  s_add_u32 s65, s65, s83
  ds_read_b64_tr_b16 a[90:91], v235 offset:20480
  v_mfma_f32_16x16x32_bf16 v[112:115], a[40:43], a[152:155], v[112:115]
  v_exp_f32 v109, v109
  v_mfma_f32_16x16x32_bf16 v[112:115], a[44:47], a[156:159], v[112:115]
  s_lshr_b32 s65, s65, 1
  v_cvt_pk_bf16_f32 v160, v108, v109
  v_mfma_f32_16x16x32_bf16 v[52:55], a[104:107], v[140:143], v[52:55]
  v_exp_f32 v110, v110
  v_mfma_f32_16x16x32_bf16 v[52:55], a[108:111], v[144:147], v[52:55]
  s_add_u32 s83, s65, s84
  ds_read_b64_tr_b16 a[92:93], v235 offset:24576
  v_mfma_f32_16x16x32_bf16 v[84:87], a[104:107], v[148:151], v[84:87]
  v_exp_f32 v111, v111
  v_mfma_f32_16x16x32_bf16 v[84:87], a[108:111], v[152:155], v[84:87]
  s_sub_u32 s83, s83, 28
  v_cvt_pk_bf16_f32 v161, v110, v111
  v_mfma_f32_16x16x32_bf16 v[116:119], a[32:35], a[160:163], v[104:107]
  v_exp_f32 v112, v112
  v_mfma_f32_16x16x32_bf16 v[116:119], a[36:39], a[164:167], v[116:119]
  s_max_u32 s94, s94, s83
  ds_read_b64_tr_b16 a[94:95], v235 offset:28672
  v_mfma_f32_16x16x32_bf16 v[116:119], a[40:43], a[168:171], v[116:119]
  v_exp_f32 v113, v113
  v_mfma_f32_16x16x32_bf16 v[116:119], a[44:47], a[172:175], v[116:119]
  s_mov_b32 s66, s84
  v_cvt_pk_bf16_f32 v168, v112, v113
  v_mfma_f32_16x16x32_bf16 v[24:27], a[112:115], v[132:135], v[24:27]
  v_exp_f32 v114, v114
  v_mfma_f32_16x16x32_bf16 v[24:27], a[116:119], v[136:139], v[24:27]
  buffer_load_dwordx4 v245, s[44:47], s68 offen offset:1024 lds
  v_mfma_f32_16x16x32_bf16 v[56:59], a[112:115], v[140:143], v[56:59]
  v_exp_f32 v115, v115
  v_mfma_f32_16x16x32_bf16 v[56:59], a[116:119], v[144:147], v[56:59]
  ds_read_b64_tr_b16 a[96:97], v236 offset:16384
  v_cvt_pk_bf16_f32 v169, v114, v115
  v_mfma_f32_16x16x32_bf16 v[108:111], a[48:51], a[128:131], v[96:99]
  v_exp_f32 v116, v116
  v_mfma_f32_16x16x32_bf16 v[108:111], a[52:55], a[132:135], v[108:111]
  ds_read_b64_tr_b16 a[98:99], v236 offset:20480
  ds_read_b64_tr_b16 a[100:101], v236 offset:24576
  v_mfma_f32_16x16x32_bf16 v[108:111], a[56:59], a[136:139], v[108:111]
  v_exp_f32 v117, v117
  v_mfma_f32_16x16x32_bf16 v[108:111], a[60:63], a[140:143], v[108:111]
  ds_read_b64_tr_b16 a[102:103], v236 offset:28672
  v_cvt_pk_bf16_f32 v176, v116, v117
  v_mfma_f32_16x16x32_bf16 v[88:91], a[112:115], v[148:151], v[88:91]
  v_exp_f32 v118, v118
  v_mfma_f32_16x16x32_bf16 v[88:91], a[116:119], v[152:155], v[88:91]
  buffer_load_dwordx4 v244, s[44:47], s68 offen offset:2048 lds
  v_mfma_f32_16x16x32_bf16 v[28:31], a[120:123], v[132:135], v[28:31]
  v_exp_f32 v119, v119
  v_mfma_f32_16x16x32_bf16 v[28:31], a[124:127], v[136:139], v[28:31]
  ds_read_b64_tr_b16 a[104:105], v237 offset:16384
  v_cvt_pk_bf16_f32 v177, v118, v119
  v_mfma_f32_16x16x32_bf16 v[60:63], a[120:123], v[140:143], v[60:63]
  ds_read_b64_tr_b16 a[106:107], v237 offset:20480
  ds_read_b64_tr_b16 a[108:109], v237 offset:24576
  v_mfma_f32_16x16x32_bf16 v[60:63], a[124:127], v[144:147], v[60:63]
  ds_read_b64_tr_b16 a[110:111], v237 offset:28672
  ds_read_b128 a[32:35], v228 offset:24576
  v_mfma_f32_16x16x32_bf16 v[112:115], a[48:51], a[144:147], v[100:103]
  v_exp_f32 v108, v108
  v_mfma_f32_16x16x32_bf16 v[112:115], a[52:55], a[148:151], v[112:115]
  ds_read_b128 a[36:39], v229 offset:24576
  ds_read_b128 a[40:43], v230 offset:24576
  v_mfma_f32_16x16x32_bf16 v[112:115], a[56:59], a[152:155], v[112:115]
  v_exp_f32 v109, v109
  v_mfma_f32_16x16x32_bf16 v[112:115], a[60:63], a[156:159], v[112:115]
  ds_read_b128 a[44:47], v231 offset:24576
  v_cvt_pk_bf16_f32 v162, v108, v109
  v_mfma_f32_16x16x32_bf16 v[92:95], a[120:123], v[148:151], v[92:95]
  v_exp_f32 v110, v110
  v_mfma_f32_16x16x32_bf16 v[92:95], a[124:127], v[152:155], v[92:95]
  buffer_load_dwordx4 v245, s[44:47], s68 offen offset:3072 lds
  v_mfma_f32_16x16x32_bf16 v[120:123], a[240:243], v[132:135], v[120:123]
  v_exp_f32 v111, v111
  v_mfma_f32_16x16x32_bf16 v[120:123], a[240:243], v[136:139], v[120:123]
  ds_read_b64_tr_b16 a[112:113], v238 offset:16384
  v_cvt_pk_bf16_f32 v163, v110, v111
  v_mfma_f32_16x16x32_bf16 v[116:119], a[48:51], a[160:163], v[104:107]
  v_exp_f32 v112, v112
  v_mfma_f32_16x16x32_bf16 v[116:119], a[52:55], a[164:167], v[116:119]
  ds_read_b64_tr_b16 a[114:115], v238 offset:20480
  ds_read_b64_tr_b16 a[116:117], v238 offset:24576
  v_mfma_f32_16x16x32_bf16 v[116:119], a[56:59], a[168:171], v[116:119]
  v_exp_f32 v113, v113
  v_mfma_f32_16x16x32_bf16 v[116:119], a[60:63], a[172:175], v[116:119]
  ds_read_b64_tr_b16 a[118:119], v238 offset:28672
  v_cvt_pk_bf16_f32 v170, v112, v113
  v_mfma_f32_16x16x32_bf16 v[124:127], a[240:243], v[140:143], v[124:127]
  v_exp_f32 v114, v114
  v_mfma_f32_16x16x32_bf16 v[124:127], a[240:243], v[144:147], v[124:127]
  ds_read_b64_tr_b16 a[120:121], v239 offset:16384
  ds_read_b64_tr_b16 a[122:123], v239 offset:20480
  ds_read_b64_tr_b16 a[124:125], v239 offset:24576
  v_mfma_f32_16x16x32_bf16 v[128:131], a[240:243], v[148:151], v[128:131]
  ds_read_b64_tr_b16 a[126:127], v239 offset:28672
  v_exp_f32 v115, v115
  v_mfma_f32_16x16x32_bf16 v[128:131], a[240:243], v[152:155], v[128:131]
  v_cvt_pk_bf16_f32 v171, v114, v115
  s_waitcnt lgkmcnt(8)
  v_mfma_f32_16x16x32_bf16 v[108:111], a[0:3], a[128:131], v[96:99]
  v_exp_f32 v116, v116
  v_mfma_f32_16x16x32_bf16 v[108:111], a[4:7], a[132:135], v[108:111]
  s_memrealtime s[82:83]
  ds_read_b128 a[48:51], v228 offset:28672
  v_mfma_f32_16x16x32_bf16 v[108:111], a[8:11], a[136:139], v[108:111]
  v_exp_f32 v117, v117
  v_mfma_f32_16x16x32_bf16 v[108:111], a[12:15], a[140:143], v[108:111]
  ds_read_b128 a[52:55], v229 offset:28672
  v_cvt_pk_bf16_f32 v178, v116, v117
  v_mfma_f32_16x16x32_bf16 v[0:3], a[64:67], v[156:159], v[0:3]
  v_exp_f32 v118, v118
  v_mfma_f32_16x16x32_bf16 v[0:3], a[68:71], v[160:163], v[0:3]
  ds_read_b128 a[56:59], v230 offset:28672
  ds_read_b128 a[60:63], v231 offset:28672
  v_mfma_f32_16x16x32_bf16 v[32:35], a[64:67], v[164:167], v[32:35]
  v_exp_f32 v119, v119
  v_mfma_f32_16x16x32_bf16 v[32:35], a[68:71], v[168:171], v[32:35]
  v_cvt_pk_bf16_f32 v179, v118, v119
  v_mfma_f32_16x16x32_bf16 v[112:115], a[0:3], a[144:147], v[100:103]
  v_exp_f32 v108, v108
  v_mfma_f32_16x16x32_bf16 v[112:115], a[4:7], a[148:151], v[112:115]
  v_mfma_f32_16x16x32_bf16 v[112:115], a[8:11], a[152:155], v[112:115]
  v_exp_f32 v109, v109
  v_mfma_f32_16x16x32_bf16 v[112:115], a[12:15], a[156:159], v[112:115]
  v_cvt_pk_bf16_f32 v132, v108, v109
  v_mfma_f32_16x16x32_bf16 v[64:67], a[64:67], v[172:175], v[64:67]
  v_exp_f32 v110, v110
  v_mfma_f32_16x16x32_bf16 v[64:67], a[68:71], v[176:179], v[64:67]
  s_waitcnt vmcnt(9) lgkmcnt(0)
  s_barrier
  v_mfma_f32_16x16x32_bf16 v[4:7], a[72:75], v[156:159], v[4:7]
  v_exp_f32 v111, v111
  v_mfma_f32_16x16x32_bf16 v[4:7], a[76:79], v[160:163], v[4:7]
  s_sub_u32 s84, s82, s85
  v_cvt_pk_bf16_f32 v133, v110, v111
  v_mfma_f32_16x16x32_bf16 v[116:119], a[0:3], a[160:163], v[104:107]
  v_exp_f32 v112, v112
  v_mfma_f32_16x16x32_bf16 v[116:119], a[4:7], a[164:167], v[116:119]
  s_cmp_lt_u32 s84, s94
  s_cbranch_scc1 PC25_%=
PC25_%=e:
  v_mfma_f32_16x16x32_bf16 v[116:119], a[8:11], a[168:171], v[116:119]
  v_exp_f32 v113, v113
  v_mfma_f32_16x16x32_bf16 v[116:119], a[12:15], a[172:175], v[116:119]
  s_add_u32 s68, s60, 475136
  v_cvt_pk_bf16_f32 v140, v112, v113
  v_mfma_f32_16x16x32_bf16 v[36:39], a[72:75], v[164:167], v[36:39]
  v_exp_f32 v114, v114
  v_mfma_f32_16x16x32_bf16 v[36:39], a[76:79], v[168:171], v[36:39]
  s_add_u32 m0, s61, 16384
  ds_read_b64_tr_b16 a[64:65], v232 offset:32768
  v_mfma_f32_16x16x32_bf16 v[68:71], a[72:75], v[172:175], v[68:71]
  v_exp_f32 v115, v115
  v_mfma_f32_16x16x32_bf16 v[68:71], a[76:79], v[176:179], v[68:71]
  ds_read_b64_tr_b16 a[66:67], v232 offset:36864
  v_cvt_pk_bf16_f32 v141, v114, v115
  v_mfma_f32_16x16x32_bf16 v[8:11], a[80:83], v[156:159], v[8:11]
  buffer_load_dwordx4 v240, s[40:43], s68 offen offset:0 nt lds
  v_mfma_f32_16x16x32_bf16 v[8:11], a[84:87], v[160:163], v[8:11]
  ds_read_b64_tr_b16 a[68:69], v232 offset:40960
  ds_read_b64_tr_b16 a[70:71], v232 offset:45056
  v_mfma_f32_16x16x32_bf16 v[108:111], a[16:19], a[128:131], v[96:99]
  v_exp_f32 v116, v116
  v_mfma_f32_16x16x32_bf16 v[108:111], a[20:23], a[132:135], v[108:111]
  buffer_load_dwordx4 v241, s[40:43], s68 offen offset:1024 nt lds
  v_mfma_f32_16x16x32_bf16 v[108:111], a[24:27], a[136:139], v[108:111]
  v_exp_f32 v117, v117
  v_mfma_f32_16x16x32_bf16 v[108:111], a[28:31], a[140:143], v[108:111]
  ds_read_b128 a[0:3], v228 offset:0
  v_cvt_pk_bf16_f32 v148, v116, v117
  v_mfma_f32_16x16x32_bf16 v[40:43], a[80:83], v[164:167], v[40:43]
  v_exp_f32 v118, v118
  v_mfma_f32_16x16x32_bf16 v[40:43], a[84:87], v[168:171], v[40:43]
  ds_read_b128 a[4:7], v229 offset:0
  ds_read_b128 a[8:11], v230 offset:0
  v_mfma_f32_16x16x32_bf16 v[72:75], a[80:83], v[172:175], v[72:75]
  v_exp_f32 v119, v119
  v_mfma_f32_16x16x32_bf16 v[72:75], a[84:87], v[176:179], v[72:75]
  ds_read_b128 a[12:15], v231 offset:0
  v_cvt_pk_bf16_f32 v149, v118, v119
  v_mfma_f32_16x16x32_bf16 v[112:115], a[16:19], a[144:147], v[100:103]
  v_exp_f32 v108, v108
  v_mfma_f32_16x16x32_bf16 v[112:115], a[20:23], a[148:151], v[112:115]
  ds_read_b64_tr_b16 a[72:73], v233 offset:32768
  ds_read_b64_tr_b16 a[74:75], v233 offset:36864
  v_mfma_f32_16x16x32_bf16 v[112:115], a[24:27], a[152:155], v[112:115]
  v_exp_f32 v109, v109
  v_mfma_f32_16x16x32_bf16 v[112:115], a[28:31], a[156:159], v[112:115]
  ds_read_b64_tr_b16 a[76:77], v233 offset:40960
  v_cvt_pk_bf16_f32 v134, v108, v109
  v_mfma_f32_16x16x32_bf16 v[12:15], a[88:91], v[156:159], v[12:15]
  v_exp_f32 v110, v110
  v_mfma_f32_16x16x32_bf16 v[12:15], a[92:95], v[160:163], v[12:15]
  buffer_load_dwordx4 v242, s[40:43], s68 offen offset:2048 nt lds
  v_mfma_f32_16x16x32_bf16 v[44:47], a[88:91], v[164:167], v[44:47]
  v_exp_f32 v111, v111
  v_mfma_f32_16x16x32_bf16 v[44:47], a[92:95], v[168:171], v[44:47]
  ds_read_b64_tr_b16 a[78:79], v233 offset:45056
  v_cvt_pk_bf16_f32 v135, v110, v111
  v_mfma_f32_16x16x32_bf16 v[116:119], a[16:19], a[160:163], v[104:107]
  v_exp_f32 v112, v112
  v_mfma_f32_16x16x32_bf16 v[116:119], a[20:23], a[164:167], v[116:119]
  s_add_u32 s97, s97, s95
  ds_read_b64_tr_b16 a[80:81], v234 offset:32768
  v_mfma_f32_16x16x32_bf16 v[116:119], a[24:27], a[168:171], v[116:119]
  v_exp_f32 v113, v113
  v_mfma_f32_16x16x32_bf16 v[116:119], a[28:31], a[172:175], v[116:119]
  s_lshr_b32 s83, s97, 4
  v_cvt_pk_bf16_f32 v142, v112, v113
  v_mfma_f32_16x16x32_bf16 v[76:79], a[88:91], v[172:175], v[76:79]
  v_exp_f32 v114, v114
  v_mfma_f32_16x16x32_bf16 v[76:79], a[92:95], v[176:179], v[76:79]
  s_add_u32 s94, s84, s96
  ds_read_b64_tr_b16 a[82:83], v234 offset:36864
  ds_read_b64_tr_b16 a[84:85], v234 offset:40960
  v_mfma_f32_16x16x32_bf16 v[16:19], a[96:99], v[156:159], v[16:19]
  ds_read_b64_tr_b16 a[86:87], v234 offset:45056
  v_exp_f32 v115, v115
  v_mfma_f32_16x16x32_bf16 v[16:19], a[100:103], v[160:163], v[16:19]
  s_max_u32 s94, s94, s83
  ds_read_b128 a[16:19], v228 offset:4096
  v_cvt_pk_bf16_f32 v143, v114, v115
  v_mfma_f32_16x16x32_bf16 v[108:111], a[32:35], a[128:131], v[96:99]
  ds_read_b128 a[20:23], v229 offset:4096
  v_exp_f32 v116, v116
  v_mfma_f32_16x16x32_bf16 v[108:111], a[36:39], a[132:135], v[108:111]
  s_sub_u32 s83, s84, s66
  buffer_load_dwordx4 v243, s[40:43], s68 offen offset:3072 nt lds
  v_mfma_f32_16x16x32_bf16 v[108:111], a[40:43], a[136:139], v[108:111]
  ds_read_b128 a[24:27], v230 offset:4096
  v_exp_f32 v117, v117
  v_mfma_f32_16x16x32_bf16 v[108:111], a[44:47], a[140:143], v[108:111]
  s_min_u32 s83, s83, 200
  ds_read_b128 a[28:31], v231 offset:4096
  v_cvt_pk_bf16_f32 v150, v116, v117
  v_mfma_f32_16x16x32_bf16 v[48:51], a[96:99], v[164:167], v[48:51]
  ds_read_b64_tr_b16 a[88:89], v235 offset:32768
  v_exp_f32 v118, v118
  v_mfma_f32_16x16x32_bf16 v[48:51], a[100:103], v[168:171], v[48:51]
  s_add_u32 s65, s65, s83
  s_add_u32 s68, s60, 458752
  v_mfma_f32_16x16x32_bf16 v[80:83], a[96:99], v[172:175], v[80:83]
  v_exp_f32 v119, v119
  v_mfma_f32_16x16x32_bf16 v[80:83], a[100:103], v[176:179], v[80:83]
  s_lshr_b32 s65, s65, 1
  v_cvt_pk_bf16_f32 v151, v118, v119
  v_mfma_f32_16x16x32_bf16 v[20:23], a[104:107], v[156:159], v[20:23]
  s_add_u32 s83, s65, s84
  s_add_u32 m0, s62, 16384
  v_mfma_f32_16x16x32_bf16 v[20:23], a[108:111], v[160:163], v[20:23]
  s_sub_u32 s83, s83, 28
  buffer_load_dwordx4 v244, s[44:47], s68 offen offset:0 lds
  v_mfma_f32_16x16x32_bf16 v[112:115], a[32:35], a[144:147], v[100:103]
  v_exp_f32 v108, v108
  v_mfma_f32_16x16x32_bf16 v[112:115], a[36:39], a[148:151], v[112:115]
  s_max_u32 s94, s94, s83
  ds_read_b64_tr_b16 a[90:91], v235 offset:36864
  v_mfma_f32_16x16x32_bf16 v[112:115], a[40:43], a[152:155], v[112:115]
  v_exp_f32 v109, v109
  v_mfma_f32_16x16x32_bf16 v[112:115], a[44:47], a[156:159], v[112:115]
  s_mov_b32 s66, s84
  v_cvt_pk_bf16_f32 v136, v108, v109
  v_mfma_f32_16x16x32_bf16 v[52:55], a[104:107], v[164:167], v[52:55]
  v_exp_f32 v110, v110
  v_mfma_f32_16x16x32_bf16 v[52:55], a[108:111], v[168:171], v[52:55]
  ds_read_b64_tr_b16 a[92:93], v235 offset:40960
  ds_read_b64_tr_b16 a[94:95], v235 offset:45056
  v_mfma_f32_16x16x32_bf16 v[84:87], a[104:107], v[172:175], v[84:87]
  v_exp_f32 v111, v111
  v_mfma_f32_16x16x32_bf16 v[84:87], a[108:111], v[176:179], v[84:87]
  ds_read_b64_tr_b16 a[96:97], v236 offset:32768
  v_cvt_pk_bf16_f32 v137, v110, v111
  v_mfma_f32_16x16x32_bf16 v[116:119], a[32:35], a[160:163], v[104:107]
  v_exp_f32 v112, v112
  v_mfma_f32_16x16x32_bf16 v[116:119], a[36:39], a[164:167], v[116:119]
  buffer_load_dwordx4 v245, s[44:47], s68 offen offset:1024 lds
  v_mfma_f32_16x16x32_bf16 v[116:119], a[40:43], a[168:171], v[116:119]
  v_exp_f32 v113, v113
  v_mfma_f32_16x16x32_bf16 v[116:119], a[44:47], a[172:175], v[116:119]
  ds_read_b64_tr_b16 a[98:99], v236 offset:36864
  v_cvt_pk_bf16_f32 v144, v112, v113
  v_mfma_f32_16x16x32_bf16 v[24:27], a[112:115], v[156:159], v[24:27]
  v_exp_f32 v114, v114
  v_mfma_f32_16x16x32_bf16 v[24:27], a[116:119], v[160:163], v[24:27]
  ds_read_b64_tr_b16 a[100:101], v236 offset:40960
  ds_read_b64_tr_b16 a[102:103], v236 offset:45056
  v_mfma_f32_16x16x32_bf16 v[56:59], a[112:115], v[164:167], v[56:59]
  v_exp_f32 v115, v115
  v_mfma_f32_16x16x32_bf16 v[56:59], a[116:119], v[168:171], v[56:59]
  ds_read_b64_tr_b16 a[104:105], v237 offset:32768
  v_cvt_pk_bf16_f32 v145, v114, v115
  v_mfma_f32_16x16x32_bf16 v[108:111], a[48:51], a[128:131], v[96:99]
  v_exp_f32 v116, v116
  v_mfma_f32_16x16x32_bf16 v[108:111], a[52:55], a[132:135], v[108:111]
  ds_read_b64_tr_b16 a[106:107], v237 offset:36864
  ds_read_b64_tr_b16 a[108:109], v237 offset:40960
  v_mfma_f32_16x16x32_bf16 v[108:111], a[56:59], a[136:139], v[108:111]
  v_exp_f32 v117, v117
  v_mfma_f32_16x16x32_bf16 v[108:111], a[60:63], a[140:143], v[108:111]
  ds_read_b64_tr_b16 a[110:111], v237 offset:45056
  v_cvt_pk_bf16_f32 v152, v116, v117
  v_mfma_f32_16x16x32_bf16 v[88:91], a[112:115], v[172:175], v[88:91]
  v_exp_f32 v118, v118
  v_mfma_f32_16x16x32_bf16 v[88:91], a[116:119], v[176:179], v[88:91]
  buffer_load_dwordx4 v244, s[44:47], s68 offen offset:2048 lds
  v_mfma_f32_16x16x32_bf16 v[28:31], a[120:123], v[156:159], v[28:31]
  v_exp_f32 v119, v119
  v_mfma_f32_16x16x32_bf16 v[28:31], a[124:127], v[160:163], v[28:31]
  ds_read_b128 a[32:35], v228 offset:8192
  v_cvt_pk_bf16_f32 v153, v118, v119
  v_mfma_f32_16x16x32_bf16 v[60:63], a[120:123], v[164:167], v[60:63]
  ds_read_b128 a[36:39], v229 offset:8192
  ds_read_b128 a[40:43], v230 offset:8192
  v_mfma_f32_16x16x32_bf16 v[60:63], a[124:127], v[168:171], v[60:63]
  ds_read_b128 a[44:47], v231 offset:8192
  ds_read_b64_tr_b16 a[112:113], v238 offset:32768
  v_mfma_f32_16x16x32_bf16 v[112:115], a[48:51], a[144:147], v[100:103]
  v_exp_f32 v108, v108
  v_mfma_f32_16x16x32_bf16 v[112:115], a[52:55], a[148:151], v[112:115]
  ds_read_b64_tr_b16 a[114:115], v238 offset:36864
  ds_read_b64_tr_b16 a[116:117], v238 offset:40960
  v_mfma_f32_16x16x32_bf16 v[112:115], a[56:59], a[152:155], v[112:115]
  v_exp_f32 v109, v109
  v_mfma_f32_16x16x32_bf16 v[112:115], a[60:63], a[156:159], v[112:115]
  ds_read_b64_tr_b16 a[118:119], v238 offset:45056
  v_cvt_pk_bf16_f32 v138, v108, v109
  v_mfma_f32_16x16x32_bf16 v[92:95], a[120:123], v[172:175], v[92:95]
  v_exp_f32 v110, v110
  v_mfma_f32_16x16x32_bf16 v[92:95], a[124:127], v[176:179], v[92:95]
  buffer_load_dwordx4 v245, s[44:47], s68 offen offset:3072 lds
  v_mfma_f32_16x16x32_bf16 v[120:123], a[240:243], v[156:159], v[120:123]
  v_exp_f32 v111, v111
  v_mfma_f32_16x16x32_bf16 v[120:123], a[240:243], v[160:163], v[120:123]
  ds_read_b64_tr_b16 a[120:121], v239 offset:32768
  v_cvt_pk_bf16_f32 v139, v110, v111
  v_mfma_f32_16x16x32_bf16 v[116:119], a[48:51], a[160:163], v[104:107]
  v_exp_f32 v112, v112
  v_mfma_f32_16x16x32_bf16 v[116:119], a[52:55], a[164:167], v[116:119]
  ds_read_b64_tr_b16 a[122:123], v239 offset:36864
  ds_read_b64_tr_b16 a[124:125], v239 offset:40960
  v_mfma_f32_16x16x32_bf16 v[116:119], a[56:59], a[168:171], v[116:119]
  v_exp_f32 v113, v113
  v_mfma_f32_16x16x32_bf16 v[116:119], a[60:63], a[172:175], v[116:119]
  ds_read_b64_tr_b16 a[126:127], v239 offset:45056
  v_cvt_pk_bf16_f32 v146, v112, v113
  v_mfma_f32_16x16x32_bf16 v[124:127], a[240:243], v[164:167], v[124:127]
  v_exp_f32 v114, v114
  v_mfma_f32_16x16x32_bf16 v[124:127], a[240:243], v[168:171], v[124:127]
  v_mfma_f32_16x16x32_bf16 v[128:131], a[240:243], v[172:175], v[128:131]
  v_exp_f32 v115, v115
  v_mfma_f32_16x16x32_bf16 v[128:131], a[240:243], v[176:179], v[128:131]
  v_cvt_pk_bf16_f32 v147, v114, v115
  s_waitcnt lgkmcnt(8)
  v_mfma_f32_16x16x32_bf16 v[108:111], a[0:3], a[128:131], v[96:99]
  v_exp_f32 v116, v116
  v_mfma_f32_16x16x32_bf16 v[108:111], a[4:7], a[132:135], v[108:111]
  s_memrealtime s[82:83]
  ds_read_b128 a[48:51], v228 offset:12288
  v_mfma_f32_16x16x32_bf16 v[108:111], a[8:11], a[136:139], v[108:111]
  v_exp_f32 v117, v117
  v_mfma_f32_16x16x32_bf16 v[108:111], a[12:15], a[140:143], v[108:111]
  ds_read_b128 a[52:55], v229 offset:12288
  v_cvt_pk_bf16_f32 v154, v116, v117
  v_mfma_f32_16x16x32_bf16 v[0:3], a[64:67], v[132:135], v[0:3]
  v_exp_f32 v118, v118
  v_mfma_f32_16x16x32_bf16 v[0:3], a[68:71], v[136:139], v[0:3]
  ds_read_b128 a[56:59], v230 offset:12288
  ds_read_b128 a[60:63], v231 offset:12288
  v_mfma_f32_16x16x32_bf16 v[32:35], a[64:67], v[140:143], v[32:35]
  v_exp_f32 v119, v119
  v_mfma_f32_16x16x32_bf16 v[32:35], a[68:71], v[144:147], v[32:35]
  v_cvt_pk_bf16_f32 v155, v118, v119
  v_mfma_f32_16x16x32_bf16 v[112:115], a[0:3], a[144:147], v[100:103]
  v_exp_f32 v108, v108
  v_mfma_f32_16x16x32_bf16 v[112:115], a[4:7], a[148:151], v[112:115]
  v_mfma_f32_16x16x32_bf16 v[112:115], a[8:11], a[152:155], v[112:115]
  v_exp_f32 v109, v109
  v_mfma_f32_16x16x32_bf16 v[112:115], a[12:15], a[156:159], v[112:115]
  v_cvt_pk_bf16_f32 v156, v108, v109
  v_mfma_f32_16x16x32_bf16 v[64:67], a[64:67], v[148:151], v[64:67]
  v_exp_f32 v110, v110
  v_mfma_f32_16x16x32_bf16 v[64:67], a[68:71], v[152:155], v[64:67]
  s_waitcnt vmcnt(8) lgkmcnt(0)
  s_barrier
  v_mfma_f32_16x16x32_bf16 v[4:7], a[72:75], v[132:135], v[4:7]
  v_exp_f32 v111, v111
  v_mfma_f32_16x16x32_bf16 v[4:7], a[76:79], v[136:139], v[4:7]
  s_sub_u32 s84, s82, s85
  v_cvt_pk_bf16_f32 v157, v110, v111
  v_mfma_f32_16x16x32_bf16 v[116:119], a[0:3], a[160:163], v[104:107]
  v_exp_f32 v112, v112
  v_mfma_f32_16x16x32_bf16 v[116:119], a[4:7], a[164:167], v[116:119]
  s_cmp_lt_u32 s84, s94
  s_cbranch_scc1 PC26_%=
PC26_%=e:
  v_mfma_f32_16x16x32_bf16 v[116:119], a[8:11], a[168:171], v[116:119]
  v_exp_f32 v113, v113
  v_mfma_f32_16x16x32_bf16 v[116:119], a[12:15], a[172:175], v[116:119]
  s_add_u32 s68, s60, 491520
  v_cvt_pk_bf16_f32 v164, v112, v113
  v_mfma_f32_16x16x32_bf16 v[36:39], a[72:75], v[140:143], v[36:39]
  v_exp_f32 v114, v114
  v_mfma_f32_16x16x32_bf16 v[36:39], a[76:79], v[144:147], v[36:39]
  s_add_u32 m0, s61, 0
  ds_read_b64_tr_b16 a[64:65], v232 offset:0
  v_mfma_f32_16x16x32_bf16 v[68:71], a[72:75], v[148:151], v[68:71]
  v_exp_f32 v115, v115
  v_mfma_f32_16x16x32_bf16 v[68:71], a[76:79], v[152:155], v[68:71]
  ds_read_b64_tr_b16 a[66:67], v232 offset:4096
  v_cvt_pk_bf16_f32 v165, v114, v115
  v_mfma_f32_16x16x32_bf16 v[8:11], a[80:83], v[132:135], v[8:11]
  buffer_load_dwordx4 v240, s[40:43], s68 offen offset:0 nt lds
  v_mfma_f32_16x16x32_bf16 v[8:11], a[84:87], v[136:139], v[8:11]
  ds_read_b64_tr_b16 a[68:69], v232 offset:8192
  ds_read_b64_tr_b16 a[70:71], v232 offset:12288
  v_mfma_f32_16x16x32_bf16 v[108:111], a[16:19], a[128:131], v[96:99]
  v_exp_f32 v116, v116
  v_mfma_f32_16x16x32_bf16 v[108:111], a[20:23], a[132:135], v[108:111]
  buffer_load_dwordx4 v241, s[40:43], s68 offen offset:1024 nt lds
  v_mfma_f32_16x16x32_bf16 v[108:111], a[24:27], a[136:139], v[108:111]
  v_exp_f32 v117, v117
  v_mfma_f32_16x16x32_bf16 v[108:111], a[28:31], a[140:143], v[108:111]
  ds_read_b128 a[0:3], v228 offset:49152
  v_cvt_pk_bf16_f32 v172, v116, v117
  v_mfma_f32_16x16x32_bf16 v[40:43], a[80:83], v[140:143], v[40:43]
  v_exp_f32 v118, v118
  v_mfma_f32_16x16x32_bf16 v[40:43], a[84:87], v[144:147], v[40:43]
  ds_read_b128 a[4:7], v229 offset:49152
  ds_read_b128 a[8:11], v230 offset:49152
  v_mfma_f32_16x16x32_bf16 v[72:75], a[80:83], v[148:151], v[72:75]
  v_exp_f32 v119, v119
  v_mfma_f32_16x16x32_bf16 v[72:75], a[84:87], v[152:155], v[72:75]
  ds_read_b128 a[12:15], v231 offset:49152
  v_cvt_pk_bf16_f32 v173, v118, v119
  v_mfma_f32_16x16x32_bf16 v[112:115], a[16:19], a[144:147], v[100:103]
  v_exp_f32 v108, v108
  v_mfma_f32_16x16x32_bf16 v[112:115], a[20:23], a[148:151], v[112:115]
  ds_read_b64_tr_b16 a[72:73], v233 offset:0
  ds_read_b64_tr_b16 a[74:75], v233 offset:4096
  v_mfma_f32_16x16x32_bf16 v[112:115], a[24:27], a[152:155], v[112:115]
  v_exp_f32 v109, v109
  v_mfma_f32_16x16x32_bf16 v[112:115], a[28:31], a[156:159], v[112:115]
  ds_read_b64_tr_b16 a[76:77], v233 offset:8192
  v_cvt_pk_bf16_f32 v158, v108, v109
  v_mfma_f32_16x16x32_bf16 v[12:15], a[88:91], v[132:135], v[12:15]
  v_exp_f32 v110, v110
  v_mfma_f32_16x16x32_bf16 v[12:15], a[92:95], v[136:139], v[12:15]
  buffer_load_dwordx4 v242, s[40:43], s68 offen offset:2048 nt lds
  v_mfma_f32_16x16x32_bf16 v[44:47], a[88:91], v[140:143], v[44:47]
  v_exp_f32 v111, v111
  v_mfma_f32_16x16x32_bf16 v[44:47], a[92:95], v[144:147], v[44:47]
  ds_read_b64_tr_b16 a[78:79], v233 offset:12288
  v_cvt_pk_bf16_f32 v159, v110, v111
  v_mfma_f32_16x16x32_bf16 v[116:119], a[16:19], a[160:163], v[104:107]
  v_exp_f32 v112, v112
  v_mfma_f32_16x16x32_bf16 v[116:119], a[20:23], a[164:167], v[116:119]
  s_add_u32 s97, s97, s95
  ds_read_b64_tr_b16 a[80:81], v234 offset:0
  v_mfma_f32_16x16x32_bf16 v[116:119], a[24:27], a[168:171], v[116:119]
  v_exp_f32 v113, v113
  v_mfma_f32_16x16x32_bf16 v[116:119], a[28:31], a[172:175], v[116:119]
  s_lshr_b32 s83, s97, 4
  v_cvt_pk_bf16_f32 v166, v112, v113
  v_mfma_f32_16x16x32_bf16 v[76:79], a[88:91], v[148:151], v[76:79]
  v_exp_f32 v114, v114
  v_mfma_f32_16x16x32_bf16 v[76:79], a[92:95], v[152:155], v[76:79]
  s_add_u32 s94, s84, s96
  ds_read_b64_tr_b16 a[82:83], v234 offset:4096
  ds_read_b64_tr_b16 a[84:85], v234 offset:8192
  v_mfma_f32_16x16x32_bf16 v[16:19], a[96:99], v[132:135], v[16:19]
  ds_read_b64_tr_b16 a[86:87], v234 offset:12288
  v_exp_f32 v115, v115
  v_mfma_f32_16x16x32_bf16 v[16:19], a[100:103], v[136:139], v[16:19]
  s_max_u32 s94, s94, s83
  ds_read_b128 a[16:19], v228 offset:53248
  v_cvt_pk_bf16_f32 v167, v114, v115
  v_mfma_f32_16x16x32_bf16 v[108:111], a[32:35], a[128:131], v[96:99]
  ds_read_b128 a[20:23], v229 offset:53248
  v_exp_f32 v116, v116
  v_mfma_f32_16x16x32_bf16 v[108:111], a[36:39], a[132:135], v[108:111]
  s_sub_u32 s83, s84, s66
  buffer_load_dwordx4 v243, s[40:43], s68 offen offset:3072 nt lds
  v_mfma_f32_16x16x32_bf16 v[108:111], a[40:43], a[136:139], v[108:111]
  ds_read_b128 a[24:27], v230 offset:53248
  v_exp_f32 v117, v117
  v_mfma_f32_16x16x32_bf16 v[108:111], a[44:47], a[140:143], v[108:111]
  s_min_u32 s83, s83, 200
  ds_read_b128 a[28:31], v231 offset:53248
  v_cvt_pk_bf16_f32 v174, v116, v117
  v_mfma_f32_16x16x32_bf16 v[48:51], a[96:99], v[140:143], v[48:51]
  ds_read_b64_tr_b16 a[88:89], v235 offset:0
  v_exp_f32 v118, v118
  v_mfma_f32_16x16x32_bf16 v[48:51], a[100:103], v[144:147], v[48:51]
  s_add_u32 s65, s65, s83
  s_add_u32 s68, s60, 475136
  v_mfma_f32_16x16x32_bf16 v[80:83], a[96:99], v[148:151], v[80:83]
  v_exp_f32 v119, v119
  v_mfma_f32_16x16x32_bf16 v[80:83], a[100:103], v[152:155], v[80:83]
  s_lshr_b32 s65, s65, 1
  v_cvt_pk_bf16_f32 v175, v118, v119
  v_mfma_f32_16x16x32_bf16 v[20:23], a[104:107], v[132:135], v[20:23]
  s_add_u32 s83, s65, s84
  s_add_u32 m0, s62, 32768
  v_mfma_f32_16x16x32_bf16 v[20:23], a[108:111], v[136:139], v[20:23]
  s_sub_u32 s83, s83, 28
  buffer_load_dwordx4 v244, s[44:47], s68 offen offset:0 lds
  v_mfma_f32_16x16x32_bf16 v[112:115], a[32:35], a[144:147], v[100:103]
  v_exp_f32 v108, v108
  v_mfma_f32_16x16x32_bf16 v[112:115], a[36:39], a[148:151], v[112:115]
  s_max_u32 s94, s94, s83
  ds_read_b64_tr_b16 a[90:91], v235 offset:4096
  v_mfma_f32_16x16x32_bf16 v[112:115], a[40:43], a[152:155], v[112:115]
  v_exp_f32 v109, v109
  v_mfma_f32_16x16x32_bf16 v[112:115], a[44:47], a[156:159], v[112:115]
  s_mov_b32 s66, s84
  v_cvt_pk_bf16_f32 v160, v108, v109
  v_mfma_f32_16x16x32_bf16 v[52:55], a[104:107], v[140:143], v[52:55]
  v_exp_f32 v110, v110
  v_mfma_f32_16x16x32_bf16 v[52:55], a[108:111], v[144:147], v[52:55]
  ds_read_b64_tr_b16 a[92:93], v235 offset:8192
  ds_read_b64_tr_b16 a[94:95], v235 offset:12288
  v_mfma_f32_16x16x32_bf16 v[84:87], a[104:107], v[148:151], v[84:87]
  v_exp_f32 v111, v111
  v_mfma_f32_16x16x32_bf16 v[84:87], a[108:111], v[152:155], v[84:87]
  ds_read_b64_tr_b16 a[96:97], v236 offset:0
  v_cvt_pk_bf16_f32 v161, v110, v111
  v_mfma_f32_16x16x32_bf16 v[116:119], a[32:35], a[160:163], v[104:107]
  v_exp_f32 v112, v112
  v_mfma_f32_16x16x32_bf16 v[116:119], a[36:39], a[164:167], v[116:119]
  buffer_load_dwordx4 v245, s[44:47], s68 offen offset:1024 lds
  v_mfma_f32_16x16x32_bf16 v[116:119], a[40:43], a[168:171], v[116:119]
  v_exp_f32 v113, v113
  v_mfma_f32_16x16x32_bf16 v[116:119], a[44:47], a[172:175], v[116:119]
  ds_read_b64_tr_b16 a[98:99], v236 offset:4096
  v_cvt_pk_bf16_f32 v168, v112, v113
  v_mfma_f32_16x16x32_bf16 v[24:27], a[112:115], v[132:135], v[24:27]
  v_exp_f32 v114, v114
  v_mfma_f32_16x16x32_bf16 v[24:27], a[116:119], v[136:139], v[24:27]
  ds_read_b64_tr_b16 a[100:101], v236 offset:8192
  ds_read_b64_tr_b16 a[102:103], v236 offset:12288
  v_mfma_f32_16x16x32_bf16 v[56:59], a[112:115], v[140:143], v[56:59]
  v_exp_f32 v115, v115
  v_mfma_f32_16x16x32_bf16 v[56:59], a[116:119], v[144:147], v[56:59]
  ds_read_b64_tr_b16 a[104:105], v237 offset:0
  v_cvt_pk_bf16_f32 v169, v114, v115
  v_mfma_f32_16x16x32_bf16 v[108:111], a[48:51], a[128:131], v[96:99]
  v_exp_f32 v116, v116
  v_mfma_f32_16x16x32_bf16 v[108:111], a[52:55], a[132:135], v[108:111]
  ds_read_b64_tr_b16 a[106:107], v237 offset:4096
  ds_read_b64_tr_b16 a[108:109], v237 offset:8192
  v_mfma_f32_16x16x32_bf16 v[108:111], a[56:59], a[136:139], v[108:111]
  v_exp_f32 v117, v117
  v_mfma_f32_16x16x32_bf16 v[108:111], a[60:63], a[140:143], v[108:111]
  ds_read_b64_tr_b16 a[110:111], v237 offset:12288
  v_cvt_pk_bf16_f32 v176, v116, v117
  v_mfma_f32_16x16x32_bf16 v[88:91], a[112:115], v[148:151], v[88:91]
  v_exp_f32 v118, v118
  v_mfma_f32_16x16x32_bf16 v[88:91], a[116:119], v[152:155], v[88:91]
  buffer_load_dwordx4 v244, s[44:47], s68 offen offset:2048 lds
  v_mfma_f32_16x16x32_bf16 v[28:31], a[120:123], v[132:135], v[28:31]
  v_exp_f32 v119, v119
  v_mfma_f32_16x16x32_bf16 v[28:31], a[124:127], v[136:139], v[28:31]
  ds_read_b128 a[32:35], v228 offset:57344
  v_cvt_pk_bf16_f32 v177, v118, v119
  v_mfma_f32_16x16x32_bf16 v[60:63], a[120:123], v[140:143], v[60:63]
  ds_read_b128 a[36:39], v229 offset:57344
  ds_read_b128 a[40:43], v230 offset:57344
  v_mfma_f32_16x16x32_bf16 v[60:63], a[124:127], v[144:147], v[60:63]
  ds_read_b128 a[44:47], v231 offset:57344
  ds_read_b64_tr_b16 a[112:113], v238 offset:0
  v_mfma_f32_16x16x32_bf16 v[112:115], a[48:51], a[144:147], v[100:103]
  v_exp_f32 v108, v108
  v_mfma_f32_16x16x32_bf16 v[112:115], a[52:55], a[148:151], v[112:115]
  ds_read_b64_tr_b16 a[114:115], v238 offset:4096
  ds_read_b64_tr_b16 a[116:117], v238 offset:8192
  v_mfma_f32_16x16x32_bf16 v[112:115], a[56:59], a[152:155], v[112:115]
  v_exp_f32 v109, v109
  v_mfma_f32_16x16x32_bf16 v[112:115], a[60:63], a[156:159], v[112:115]
  ds_read_b64_tr_b16 a[118:119], v238 offset:12288
  v_cvt_pk_bf16_f32 v162, v108, v109
  v_mfma_f32_16x16x32_bf16 v[92:95], a[120:123], v[148:151], v[92:95]
  v_exp_f32 v110, v110
  v_mfma_f32_16x16x32_bf16 v[92:95], a[124:127], v[152:155], v[92:95]
  buffer_load_dwordx4 v245, s[44:47], s68 offen offset:3072 lds
  v_mfma_f32_16x16x32_bf16 v[120:123], a[240:243], v[132:135], v[120:123]
  v_exp_f32 v111, v111
  v_mfma_f32_16x16x32_bf16 v[120:123], a[240:243], v[136:139], v[120:123]
  ds_read_b64_tr_b16 a[120:121], v239 offset:0
  v_cvt_pk_bf16_f32 v163, v110, v111
  v_mfma_f32_16x16x32_bf16 v[116:119], a[48:51], a[160:163], v[104:107]
  v_exp_f32 v112, v112
  v_mfma_f32_16x16x32_bf16 v[116:119], a[52:55], a[164:167], v[116:119]
  ds_read_b64_tr_b16 a[122:123], v239 offset:4096
  ds_read_b64_tr_b16 a[124:125], v239 offset:8192
  v_mfma_f32_16x16x32_bf16 v[116:119], a[56:59], a[168:171], v[116:119]
  v_exp_f32 v113, v113
  v_mfma_f32_16x16x32_bf16 v[116:119], a[60:63], a[172:175], v[116:119]
  ds_read_b64_tr_b16 a[126:127], v239 offset:12288
  v_cvt_pk_bf16_f32 v170, v112, v113
  v_mfma_f32_16x16x32_bf16 v[124:127], a[240:243], v[140:143], v[124:127]
  v_exp_f32 v114, v114
  v_mfma_f32_16x16x32_bf16 v[124:127], a[240:243], v[144:147], v[124:127]
  v_mfma_f32_16x16x32_bf16 v[128:131], a[240:243], v[148:151], v[128:131]
  v_exp_f32 v115, v115
  v_mfma_f32_16x16x32_bf16 v[128:131], a[240:243], v[152:155], v[128:131]
  v_cvt_pk_bf16_f32 v171, v114, v115
  s_waitcnt lgkmcnt(8)
  v_mfma_f32_16x16x32_bf16 v[108:111], a[0:3], a[128:131], v[96:99]
  v_exp_f32 v116, v116
  v_mfma_f32_16x16x32_bf16 v[108:111], a[4:7], a[132:135], v[108:111]
  s_memrealtime s[82:83]
  ds_read_b128 a[48:51], v228 offset:61440
  v_mfma_f32_16x16x32_bf16 v[108:111], a[8:11], a[136:139], v[108:111]
  v_exp_f32 v117, v117
  v_mfma_f32_16x16x32_bf16 v[108:111], a[12:15], a[140:143], v[108:111]
  ds_read_b128 a[52:55], v229 offset:61440
  v_cvt_pk_bf16_f32 v178, v116, v117
  v_mfma_f32_16x16x32_bf16 v[0:3], a[64:67], v[156:159], v[0:3]
  v_exp_f32 v118, v118
  v_mfma_f32_16x16x32_bf16 v[0:3], a[68:71], v[160:163], v[0:3]
  ds_read_b128 a[56:59], v230 offset:61440
  ds_read_b128 a[60:63], v231 offset:61440
  v_mfma_f32_16x16x32_bf16 v[32:35], a[64:67], v[164:167], v[32:35]
  v_exp_f32 v119, v119
  v_mfma_f32_16x16x32_bf16 v[32:35], a[68:71], v[168:171], v[32:35]
  v_cvt_pk_bf16_f32 v179, v118, v119
  v_mfma_f32_16x16x32_bf16 v[112:115], a[0:3], a[144:147], v[100:103]
  v_exp_f32 v108, v108
  v_mfma_f32_16x16x32_bf16 v[112:115], a[4:7], a[148:151], v[112:115]
  v_mfma_f32_16x16x32_bf16 v[112:115], a[8:11], a[152:155], v[112:115]
  v_exp_f32 v109, v109
  v_mfma_f32_16x16x32_bf16 v[112:115], a[12:15], a[156:159], v[112:115]
  v_cvt_pk_bf16_f32 v132, v108, v109
  v_mfma_f32_16x16x32_bf16 v[64:67], a[64:67], v[172:175], v[64:67]
  v_exp_f32 v110, v110
  v_mfma_f32_16x16x32_bf16 v[64:67], a[68:71], v[176:179], v[64:67]
  s_waitcnt vmcnt(8) lgkmcnt(0)
  s_barrier
  v_mfma_f32_16x16x32_bf16 v[4:7], a[72:75], v[156:159], v[4:7]
  v_exp_f32 v111, v111
  v_mfma_f32_16x16x32_bf16 v[4:7], a[76:79], v[160:163], v[4:7]
  s_sub_u32 s84, s82, s85
  v_cvt_pk_bf16_f32 v133, v110, v111
  v_mfma_f32_16x16x32_bf16 v[116:119], a[0:3], a[160:163], v[104:107]
  v_exp_f32 v112, v112
  v_mfma_f32_16x16x32_bf16 v[116:119], a[4:7], a[164:167], v[116:119]
  s_cmp_lt_u32 s84, s94
  s_cbranch_scc1 PC27_%=
PC27_%=e:
  v_mfma_f32_16x16x32_bf16 v[116:119], a[8:11], a[168:171], v[116:119]
  v_exp_f32 v113, v113
  v_mfma_f32_16x16x32_bf16 v[116:119], a[12:15], a[172:175], v[116:119]
  s_add_u32 s68, s60, 507904
  v_cvt_pk_bf16_f32 v140, v112, v113
  v_mfma_f32_16x16x32_bf16 v[36:39], a[72:75], v[164:167], v[36:39]
  v_exp_f32 v114, v114
  v_mfma_f32_16x16x32_bf16 v[36:39], a[76:79], v[168:171], v[36:39]
  s_add_u32 m0, s61, 49152
  ds_read_b64_tr_b16 a[64:65], v232 offset:16384
  v_mfma_f32_16x16x32_bf16 v[68:71], a[72:75], v[172:175], v[68:71]
  v_exp_f32 v115, v115
  v_mfma_f32_16x16x32_bf16 v[68:71], a[76:79], v[176:179], v[68:71]
  ds_read_b64_tr_b16 a[66:67], v232 offset:20480
  v_cvt_pk_bf16_f32 v141, v114, v115
  v_mfma_f32_16x16x32_bf16 v[8:11], a[80:83], v[156:159], v[8:11]
  buffer_load_dwordx4 v240, s[40:43], s68 offen offset:0 nt lds
  v_mfma_f32_16x16x32_bf16 v[8:11], a[84:87], v[160:163], v[8:11]
  ds_read_b64_tr_b16 a[68:69], v232 offset:24576
  ds_read_b64_tr_b16 a[70:71], v232 offset:28672
  v_mfma_f32_16x16x32_bf16 v[108:111], a[16:19], a[128:131], v[96:99]
  v_exp_f32 v116, v116
  v_mfma_f32_16x16x32_bf16 v[108:111], a[20:23], a[132:135], v[108:111]
  buffer_load_dwordx4 v241, s[40:43], s68 offen offset:1024 nt lds
  v_mfma_f32_16x16x32_bf16 v[108:111], a[24:27], a[136:139], v[108:111]
  v_exp_f32 v117, v117
  v_mfma_f32_16x16x32_bf16 v[108:111], a[28:31], a[140:143], v[108:111]
  ds_read_b128 a[0:3], v228 offset:16384
  v_cvt_pk_bf16_f32 v148, v116, v117
  v_mfma_f32_16x16x32_bf16 v[40:43], a[80:83], v[164:167], v[40:43]
  v_exp_f32 v118, v118
  v_mfma_f32_16x16x32_bf16 v[40:43], a[84:87], v[168:171], v[40:43]
  ds_read_b128 a[4:7], v229 offset:16384
  ds_read_b128 a[8:11], v230 offset:16384
  v_mfma_f32_16x16x32_bf16 v[72:75], a[80:83], v[172:175], v[72:75]
  v_exp_f32 v119, v119
  v_mfma_f32_16x16x32_bf16 v[72:75], a[84:87], v[176:179], v[72:75]
  ds_read_b128 a[12:15], v231 offset:16384
  v_cvt_pk_bf16_f32 v149, v118, v119
  v_mfma_f32_16x16x32_bf16 v[112:115], a[16:19], a[144:147], v[100:103]
  v_exp_f32 v108, v108
  v_mfma_f32_16x16x32_bf16 v[112:115], a[20:23], a[148:151], v[112:115]
  ds_read_b64_tr_b16 a[72:73], v233 offset:16384
  ds_read_b64_tr_b16 a[74:75], v233 offset:20480
  v_mfma_f32_16x16x32_bf16 v[112:115], a[24:27], a[152:155], v[112:115]
  v_exp_f32 v109, v109
  v_mfma_f32_16x16x32_bf16 v[112:115], a[28:31], a[156:159], v[112:115]
  ds_read_b64_tr_b16 a[76:77], v233 offset:24576
  v_cvt_pk_bf16_f32 v134, v108, v109
  v_mfma_f32_16x16x32_bf16 v[12:15], a[88:91], v[156:159], v[12:15]
  v_exp_f32 v110, v110
  v_mfma_f32_16x16x32_bf16 v[12:15], a[92:95], v[160:163], v[12:15]
  buffer_load_dwordx4 v242, s[40:43], s68 offen offset:2048 nt lds
  v_mfma_f32_16x16x32_bf16 v[44:47], a[88:91], v[164:167], v[44:47]
  v_exp_f32 v111, v111
  v_mfma_f32_16x16x32_bf16 v[44:47], a[92:95], v[168:171], v[44:47]
  ds_read_b64_tr_b16 a[78:79], v233 offset:28672
  v_cvt_pk_bf16_f32 v135, v110, v111
  v_mfma_f32_16x16x32_bf16 v[116:119], a[16:19], a[160:163], v[104:107]
  v_exp_f32 v112, v112
  v_mfma_f32_16x16x32_bf16 v[116:119], a[20:23], a[164:167], v[116:119]
  s_add_u32 s97, s97, s95
  ds_read_b64_tr_b16 a[80:81], v234 offset:16384
  v_mfma_f32_16x16x32_bf16 v[116:119], a[24:27], a[168:171], v[116:119]
  v_exp_f32 v113, v113
  v_mfma_f32_16x16x32_bf16 v[116:119], a[28:31], a[172:175], v[116:119]
  s_lshr_b32 s83, s97, 4
  v_cvt_pk_bf16_f32 v142, v112, v113
  v_mfma_f32_16x16x32_bf16 v[76:79], a[88:91], v[172:175], v[76:79]
  v_exp_f32 v114, v114
  v_mfma_f32_16x16x32_bf16 v[76:79], a[92:95], v[176:179], v[76:79]
  s_add_u32 s94, s84, s96
  ds_read_b64_tr_b16 a[82:83], v234 offset:20480
  ds_read_b64_tr_b16 a[84:85], v234 offset:24576
  v_mfma_f32_16x16x32_bf16 v[16:19], a[96:99], v[156:159], v[16:19]
  ds_read_b64_tr_b16 a[86:87], v234 offset:28672
  v_exp_f32 v115, v115
  v_mfma_f32_16x16x32_bf16 v[16:19], a[100:103], v[160:163], v[16:19]
  s_max_u32 s94, s94, s83
  ds_read_b128 a[16:19], v228 offset:20480
  v_cvt_pk_bf16_f32 v143, v114, v115
  v_mfma_f32_16x16x32_bf16 v[108:111], a[32:35], a[128:131], v[96:99]
  ds_read_b128 a[20:23], v229 offset:20480
  v_exp_f32 v116, v116
  v_mfma_f32_16x16x32_bf16 v[108:111], a[36:39], a[132:135], v[108:111]
  s_sub_u32 s83, s84, s66
  buffer_load_dwordx4 v243, s[40:43], s68 offen offset:3072 nt lds
  v_mfma_f32_16x16x32_bf16 v[108:111], a[40:43], a[136:139], v[108:111]
  ds_read_b128 a[24:27], v230 offset:20480
  v_exp_f32 v117, v117
  v_mfma_f32_16x16x32_bf16 v[108:111], a[44:47], a[140:143], v[108:111]
  s_min_u32 s83, s83, 200
  ds_read_b128 a[28:31], v231 offset:20480
  v_cvt_pk_bf16_f32 v150, v116, v117
  v_mfma_f32_16x16x32_bf16 v[48:51], a[96:99], v[164:167], v[48:51]
  ds_read_b64_tr_b16 a[88:89], v235 offset:16384
  v_exp_f32 v118, v118
  v_mfma_f32_16x16x32_bf16 v[48:51], a[100:103], v[168:171], v[48:51]
  s_add_u32 s65, s65, s83
  s_add_u32 s68, s60, 491520
  v_mfma_f32_16x16x32_bf16 v[80:83], a[96:99], v[172:175], v[80:83]
  v_exp_f32 v119, v119
  v_mfma_f32_16x16x32_bf16 v[80:83], a[100:103], v[176:179], v[80:83]
  s_lshr_b32 s65, s65, 1
  v_cvt_pk_bf16_f32 v151, v118, v119
  v_mfma_f32_16x16x32_bf16 v[20:23], a[104:107], v[156:159], v[20:23]
  s_add_u32 s83, s65, s84
  s_add_u32 m0, s62, 0
  v_mfma_f32_16x16x32_bf16 v[20:23], a[108:111], v[160:163], v[20:23]
  s_sub_u32 s83, s83, 28
  buffer_load_dwordx4 v244, s[44:47], s68 offen offset:0 lds
  v_mfma_f32_16x16x32_bf16 v[112:115], a[32:35], a[144:147], v[100:103]
  v_exp_f32 v108, v108
  v_mfma_f32_16x16x32_bf16 v[112:115], a[36:39], a[148:151], v[112:115]
  s_max_u32 s94, s94, s83
  ds_read_b64_tr_b16 a[90:91], v235 offset:20480
  v_mfma_f32_16x16x32_bf16 v[112:115], a[40:43], a[152:155], v[112:115]
  v_exp_f32 v109, v109
  v_mfma_f32_16x16x32_bf16 v[112:115], a[44:47], a[156:159], v[112:115]
  s_mov_b32 s66, s84
  v_cvt_pk_bf16_f32 v136, v108, v109
  v_mfma_f32_16x16x32_bf16 v[52:55], a[104:107], v[164:167], v[52:55]
  v_exp_f32 v110, v110
  v_mfma_f32_16x16x32_bf16 v[52:55], a[108:111], v[168:171], v[52:55]
  ds_read_b64_tr_b16 a[92:93], v235 offset:24576
  ds_read_b64_tr_b16 a[94:95], v235 offset:28672
  v_mfma_f32_16x16x32_bf16 v[84:87], a[104:107], v[172:175], v[84:87]
  v_exp_f32 v111, v111
  v_mfma_f32_16x16x32_bf16 v[84:87], a[108:111], v[176:179], v[84:87]
  ds_read_b64_tr_b16 a[96:97], v236 offset:16384
  v_cvt_pk_bf16_f32 v137, v110, v111
  v_mfma_f32_16x16x32_bf16 v[116:119], a[32:35], a[160:163], v[104:107]
  v_exp_f32 v112, v112
  v_mfma_f32_16x16x32_bf16 v[116:119], a[36:39], a[164:167], v[116:119]
  buffer_load_dwordx4 v245, s[44:47], s68 offen offset:1024 lds
  v_mfma_f32_16x16x32_bf16 v[116:119], a[40:43], a[168:171], v[116:119]
  v_exp_f32 v113, v113
  v_mfma_f32_16x16x32_bf16 v[116:119], a[44:47], a[172:175], v[116:119]
  ds_read_b64_tr_b16 a[98:99], v236 offset:20480
  v_cvt_pk_bf16_f32 v144, v112, v113
  v_mfma_f32_16x16x32_bf16 v[24:27], a[112:115], v[156:159], v[24:27]
  v_exp_f32 v114, v114
  v_mfma_f32_16x16x32_bf16 v[24:27], a[116:119], v[160:163], v[24:27]
  ds_read_b64_tr_b16 a[100:101], v236 offset:24576
  ds_read_b64_tr_b16 a[102:103], v236 offset:28672
  v_mfma_f32_16x16x32_bf16 v[56:59], a[112:115], v[164:167], v[56:59]
  v_exp_f32 v115, v115
  v_mfma_f32_16x16x32_bf16 v[56:59], a[116:119], v[168:171], v[56:59]
  ds_read_b64_tr_b16 a[104:105], v237 offset:16384
  v_cvt_pk_bf16_f32 v145, v114, v115
  v_mfma_f32_16x16x32_bf16 v[108:111], a[48:51], a[128:131], v[96:99]
  v_exp_f32 v116, v116
  v_mfma_f32_16x16x32_bf16 v[108:111], a[52:55], a[132:135], v[108:111]
  ds_read_b64_tr_b16 a[106:107], v237 offset:20480
  ds_read_b64_tr_b16 a[108:109], v237 offset:24576
  v_mfma_f32_16x16x32_bf16 v[108:111], a[56:59], a[136:139], v[108:111]
  v_exp_f32 v117, v117
  v_mfma_f32_16x16x32_bf16 v[108:111], a[60:63], a[140:143], v[108:111]
  ds_read_b64_tr_b16 a[110:111], v237 offset:28672
  v_cvt_pk_bf16_f32 v152, v116, v117
  v_mfma_f32_16x16x32_bf16 v[88:91], a[112:115], v[172:175], v[88:91]
  v_exp_f32 v118, v118
  v_mfma_f32_16x16x32_bf16 v[88:91], a[116:119], v[176:179], v[88:91]
  buffer_load_dwordx4 v244, s[44:47], s68 offen offset:2048 lds
  v_mfma_f32_16x16x32_bf16 v[28:31], a[120:123], v[156:159], v[28:31]
  v_exp_f32 v119, v119
  v_mfma_f32_16x16x32_bf16 v[28:31], a[124:127], v[160:163], v[28:31]
  ds_read_b128 a[32:35], v228 offset:24576
  v_cvt_pk_bf16_f32 v153, v118, v119
  v_mfma_f32_16x16x32_bf16 v[60:63], a[120:123], v[164:167], v[60:63]
  ds_read_b128 a[36:39], v229 offset:24576
  ds_read_b128 a[40:43], v230 offset:24576
  v_mfma_f32_16x16x32_bf16 v[60:63], a[124:127], v[168:171], v[60:63]
  ds_read_b128 a[44:47], v231 offset:24576
  ds_read_b64_tr_b16 a[112:113], v238 offset:16384
  v_mfma_f32_16x16x32_bf16 v[112:115], a[48:51], a[144:147], v[100:103]
  v_exp_f32 v108, v108
  v_mfma_f32_16x16x32_bf16 v[112:115], a[52:55], a[148:151], v[112:115]
  ds_read_b64_tr_b16 a[114:115], v238 offset:20480
  ds_read_b64_tr_b16 a[116:117], v238 offset:24576
  v_mfma_f32_16x16x32_bf16 v[112:115], a[56:59], a[152:155], v[112:115]
  v_exp_f32 v109, v109
  v_mfma_f32_16x16x32_bf16 v[112:115], a[60:63], a[156:159], v[112:115]
  ds_read_b64_tr_b16 a[118:119], v238 offset:28672
  v_cvt_pk_bf16_f32 v138, v108, v109
  v_mfma_f32_16x16x32_bf16 v[92:95], a[120:123], v[172:175], v[92:95]
  v_exp_f32 v110, v110
  v_mfma_f32_16x16x32_bf16 v[92:95], a[124:127], v[176:179], v[92:95]
  buffer_load_dwordx4 v245, s[44:47], s68 offen offset:3072 lds
  v_mfma_f32_16x16x32_bf16 v[120:123], a[240:243], v[156:159], v[120:123]
  v_exp_f32 v111, v111
  v_mfma_f32_16x16x32_bf16 v[120:123], a[240:243], v[160:163], v[120:123]
  ds_read_b64_tr_b16 a[120:121], v239 offset:16384
  v_cvt_pk_bf16_f32 v139, v110, v111
  v_mfma_f32_16x16x32_bf16 v[116:119], a[48:51], a[160:163], v[104:107]
  v_exp_f32 v112, v112
  v_mfma_f32_16x16x32_bf16 v[116:119], a[52:55], a[164:167], v[116:119]
  ds_read_b64_tr_b16 a[122:123], v239 offset:20480
  ds_read_b64_tr_b16 a[124:125], v239 offset:24576
  v_mfma_f32_16x16x32_bf16 v[116:119], a[56:59], a[168:171], v[116:119]
  v_exp_f32 v113, v113
  v_mfma_f32_16x16x32_bf16 v[116:119], a[60:63], a[172:175], v[116:119]
  ds_read_b64_tr_b16 a[126:127], v239 offset:28672
  v_cvt_pk_bf16_f32 v146, v112, v113
  v_mfma_f32_16x16x32_bf16 v[124:127], a[240:243], v[164:167], v[124:127]
  v_exp_f32 v114, v114
  v_mfma_f32_16x16x32_bf16 v[124:127], a[240:243], v[168:171], v[124:127]
  v_mfma_f32_16x16x32_bf16 v[128:131], a[240:243], v[172:175], v[128:131]
  v_exp_f32 v115, v115
  v_mfma_f32_16x16x32_bf16 v[128:131], a[240:243], v[176:179], v[128:131]
  v_cvt_pk_bf16_f32 v147, v114, v115
  s_waitcnt lgkmcnt(8)
  v_mfma_f32_16x16x32_bf16 v[108:111], a[0:3], a[128:131], v[96:99]
  v_exp_f32 v116, v116
  v_mfma_f32_16x16x32_bf16 v[108:111], a[4:7], a[132:135], v[108:111]
  s_memrealtime s[82:83]
  ds_read_b128 a[48:51], v228 offset:28672
  v_mfma_f32_16x16x32_bf16 v[108:111], a[8:11], a[136:139], v[108:111]
  v_exp_f32 v117, v117
  v_mfma_f32_16x16x32_bf16 v[108:111], a[12:15], a[140:143], v[108:111]
  ds_read_b128 a[52:55], v229 offset:28672
  v_cvt_pk_bf16_f32 v154, v116, v117
  v_mfma_f32_16x16x32_bf16 v[0:3], a[64:67], v[132:135], v[0:3]
  v_exp_f32 v118, v118
  v_mfma_f32_16x16x32_bf16 v[0:3], a[68:71], v[136:139], v[0:3]
  ds_read_b128 a[56:59], v230 offset:28672
  ds_read_b128 a[60:63], v231 offset:28672
  v_mfma_f32_16x16x32_bf16 v[32:35], a[64:67], v[140:143], v[32:35]
  v_exp_f32 v119, v119
  v_mfma_f32_16x16x32_bf16 v[32:35], a[68:71], v[144:147], v[32:35]
  v_cvt_pk_bf16_f32 v155, v118, v119
  v_mfma_f32_16x16x32_bf16 v[112:115], a[0:3], a[144:147], v[100:103]
  v_exp_f32 v108, v108
  v_mfma_f32_16x16x32_bf16 v[112:115], a[4:7], a[148:151], v[112:115]
  v_mfma_f32_16x16x32_bf16 v[112:115], a[8:11], a[152:155], v[112:115]
  v_exp_f32 v109, v109
  v_mfma_f32_16x16x32_bf16 v[112:115], a[12:15], a[156:159], v[112:115]
  v_cvt_pk_bf16_f32 v156, v108, v109
  v_mfma_f32_16x16x32_bf16 v[64:67], a[64:67], v[148:151], v[64:67]
  v_exp_f32 v110, v110
  v_mfma_f32_16x16x32_bf16 v[64:67], a[68:71], v[152:155], v[64:67]
  s_waitcnt vmcnt(8) lgkmcnt(0)
  s_barrier
  v_mfma_f32_16x16x32_bf16 v[4:7], a[72:75], v[132:135], v[4:7]
  v_exp_f32 v111, v111
  v_mfma_f32_16x16x32_bf16 v[4:7], a[76:79], v[136:139], v[4:7]
  s_sub_u32 s84, s82, s85
  v_cvt_pk_bf16_f32 v157, v110, v111
  v_mfma_f32_16x16x32_bf16 v[116:119], a[0:3], a[160:163], v[104:107]
  v_exp_f32 v112, v112
  v_mfma_f32_16x16x32_bf16 v[116:119], a[4:7], a[164:167], v[116:119]
  s_cmp_lt_u32 s84, s94
  s_cbranch_scc1 PC28_%=
PC28_%=e:
  v_mfma_f32_16x16x32_bf16 v[116:119], a[8:11], a[168:171], v[116:119]
  v_exp_f32 v113, v113
  v_mfma_f32_16x16x32_bf16 v[116:119], a[12:15], a[172:175], v[116:119]
  s_add_u32 s68, s60, 507904
  v_cvt_pk_bf16_f32 v164, v112, v113
  v_mfma_f32_16x16x32_bf16 v[36:39], a[72:75], v[140:143], v[36:39]
  v_exp_f32 v114, v114
  v_mfma_f32_16x16x32_bf16 v[36:39], a[76:79], v[144:147], v[36:39]
  s_add_u32 m0, s62, 16384
  ds_read_b64_tr_b16 a[64:65], v232 offset:32768
  v_mfma_f32_16x16x32_bf16 v[68:71], a[72:75], v[148:151], v[68:71]
  v_exp_f32 v115, v115
  v_mfma_f32_16x16x32_bf16 v[68:71], a[76:79], v[152:155], v[68:71]
  ds_read_b64_tr_b16 a[66:67], v232 offset:36864
  v_cvt_pk_bf16_f32 v165, v114, v115
  v_mfma_f32_16x16x32_bf16 v[8:11], a[80:83], v[132:135], v[8:11]
  buffer_load_dwordx4 v244, s[44:47], s68 offen offset:0 lds
  v_mfma_f32_16x16x32_bf16 v[8:11], a[84:87], v[136:139], v[8:11]
  ds_read_b64_tr_b16 a[68:69], v232 offset:40960
  ds_read_b64_tr_b16 a[70:71], v232 offset:45056
  v_mfma_f32_16x16x32_bf16 v[108:111], a[16:19], a[128:131], v[96:99]
  v_exp_f32 v116, v116
  v_mfma_f32_16x16x32_bf16 v[108:111], a[20:23], a[132:135], v[108:111]
  buffer_load_dwordx4 v245, s[44:47], s68 offen offset:1024 lds
  v_mfma_f32_16x16x32_bf16 v[108:111], a[24:27], a[136:139], v[108:111]
  v_exp_f32 v117, v117
  v_mfma_f32_16x16x32_bf16 v[108:111], a[28:31], a[140:143], v[108:111]
  ds_read_b128 a[0:3], v228 offset:0
  v_cvt_pk_bf16_f32 v172, v116, v117
  v_mfma_f32_16x16x32_bf16 v[40:43], a[80:83], v[140:143], v[40:43]
  v_exp_f32 v118, v118
  v_mfma_f32_16x16x32_bf16 v[40:43], a[84:87], v[144:147], v[40:43]
  ds_read_b128 a[4:7], v229 offset:0
  ds_read_b128 a[8:11], v230 offset:0
  v_mfma_f32_16x16x32_bf16 v[72:75], a[80:83], v[148:151], v[72:75]
  v_exp_f32 v119, v119
  v_mfma_f32_16x16x32_bf16 v[72:75], a[84:87], v[152:155], v[72:75]
  ds_read_b128 a[12:15], v231 offset:0
  v_cvt_pk_bf16_f32 v173, v118, v119
  v_mfma_f32_16x16x32_bf16 v[112:115], a[16:19], a[144:147], v[100:103]
  v_exp_f32 v108, v108
  v_mfma_f32_16x16x32_bf16 v[112:115], a[20:23], a[148:151], v[112:115]
  ds_read_b64_tr_b16 a[72:73], v233 offset:32768
  ds_read_b64_tr_b16 a[74:75], v233 offset:36864
  v_mfma_f32_16x16x32_bf16 v[112:115], a[24:27], a[152:155], v[112:115]
  v_exp_f32 v109, v109
  v_mfma_f32_16x16x32_bf16 v[112:115], a[28:31], a[156:159], v[112:115]
  ds_read_b64_tr_b16 a[76:77], v233 offset:40960
  v_cvt_pk_bf16_f32 v158, v108, v109
  v_mfma_f32_16x16x32_bf16 v[12:15], a[88:91], v[132:135], v[12:15]
  v_exp_f32 v110, v110
  v_mfma_f32_16x16x32_bf16 v[12:15], a[92:95], v[136:139], v[12:15]
  buffer_load_dwordx4 v244, s[44:47], s68 offen offset:2048 lds
  v_mfma_f32_16x16x32_bf16 v[44:47], a[88:91], v[140:143], v[44:47]
  v_exp_f32 v111, v111
  v_mfma_f32_16x16x32_bf16 v[44:47], a[92:95], v[144:147], v[44:47]
  ds_read_b64_tr_b16 a[78:79], v233 offset:45056
  v_cvt_pk_bf16_f32 v159, v110, v111
  v_mfma_f32_16x16x32_bf16 v[116:119], a[16:19], a[160:163], v[104:107]
  v_exp_f32 v112, v112
  v_mfma_f32_16x16x32_bf16 v[116:119], a[20:23], a[164:167], v[116:119]
  s_add_u32 s97, s97, s95
  ds_read_b64_tr_b16 a[80:81], v234 offset:32768
  v_mfma_f32_16x16x32_bf16 v[116:119], a[24:27], a[168:171], v[116:119]
  v_exp_f32 v113, v113
  v_mfma_f32_16x16x32_bf16 v[116:119], a[28:31], a[172:175], v[116:119]
  s_lshr_b32 s83, s97, 4
  v_cvt_pk_bf16_f32 v166, v112, v113
  v_mfma_f32_16x16x32_bf16 v[76:79], a[88:91], v[148:151], v[76:79]
  v_exp_f32 v114, v114
  v_mfma_f32_16x16x32_bf16 v[76:79], a[92:95], v[152:155], v[76:79]
  s_add_u32 s94, s84, s96
  ds_read_b64_tr_b16 a[82:83], v234 offset:36864
  ds_read_b64_tr_b16 a[84:85], v234 offset:40960
  v_mfma_f32_16x16x32_bf16 v[16:19], a[96:99], v[132:135], v[16:19]
  ds_read_b64_tr_b16 a[86:87], v234 offset:45056
  v_exp_f32 v115, v115
  v_mfma_f32_16x16x32_bf16 v[16:19], a[100:103], v[136:139], v[16:19]
  s_max_u32 s94, s94, s83
  ds_read_b128 a[16:19], v228 offset:4096
  v_cvt_pk_bf16_f32 v167, v114, v115
  v_mfma_f32_16x16x32_bf16 v[108:111], a[32:35], a[128:131], v[96:99]
  ds_read_b128 a[20:23], v229 offset:4096
  v_exp_f32 v116, v116
  v_mfma_f32_16x16x32_bf16 v[108:111], a[36:39], a[132:135], v[108:111]
  s_sub_u32 s83, s84, s66
  buffer_load_dwordx4 v245, s[44:47], s68 offen offset:3072 lds
  v_mfma_f32_16x16x32_bf16 v[108:111], a[40:43], a[136:139], v[108:111]
  ds_read_b128 a[24:27], v230 offset:4096
  v_exp_f32 v117, v117
  v_mfma_f32_16x16x32_bf16 v[108:111], a[44:47], a[140:143], v[108:111]
  s_min_u32 s83, s83, 200
  ds_read_b128 a[28:31], v231 offset:4096
  v_cvt_pk_bf16_f32 v174, v116, v117
  v_mfma_f32_16x16x32_bf16 v[48:51], a[96:99], v[140:143], v[48:51]
  ds_read_b64_tr_b16 a[88:89], v235 offset:32768
  v_exp_f32 v118, v118
  v_mfma_f32_16x16x32_bf16 v[48:51], a[100:103], v[144:147], v[48:51]
  s_add_u32 s65, s65, s83
  ds_read_b64_tr_b16 a[90:91], v235 offset:36864
  v_subrev_u32 v240, 49152, v184
  v_mfma_f32_16x16x32_bf16 v[80:83], a[96:99], v[148:151], v[80:83]
  ds_read_b64_tr_b16 a[92:93], v235 offset:40960
  v_exp_f32 v119, v119
  v_mfma_f32_16x16x32_bf16 v[80:83], a[100:103], v[152:155], v[80:83]
  s_lshr_b32 s65, s65, 1
  ds_read_b64_tr_b16 a[94:95], v235 offset:45056
  v_cvt_pk_bf16_f32 v175, v118, v119
  v_mfma_f32_16x16x32_bf16 v[20:23], a[104:107], v[132:135], v[20:23]
  s_add_u32 s83, s65, s84
  v_subrev_u32 v241, 49152, v185
  v_mfma_f32_16x16x32_bf16 v[20:23], a[108:111], v[136:139], v[20:23]
  s_sub_u32 s83, s83, 28
  ds_read_b64_tr_b16 a[96:97], v236 offset:32768
  v_subrev_u32 v242, 49152, v186
  v_mfma_f32_16x16x32_bf16 v[112:115], a[32:35], a[144:147], v[100:103]
  ds_read_b64_tr_b16 a[98:99], v236 offset:36864
  v_exp_f32 v108, v108
  v_mfma_f32_16x16x32_bf16 v[112:115], a[36:39], a[148:151], v[112:115]
  s_max_u32 s94, s94, s83
  v_subrev_u32 v243, 49152, v187
  v_mfma_f32_16x16x32_bf16 v[112:115], a[40:43], a[152:155], v[112:115]
  v_exp_f32 v109, v109
  v_mfma_f32_16x16x32_bf16 v[112:115], a[44:47], a[156:159], v[112:115]
  s_mov_b32 s66, s84
  v_cvt_pk_bf16_f32 v160, v108, v109
  v_mfma_f32_16x16x32_bf16 v[52:55], a[104:107], v[140:143], v[52:55]
  v_exp_f32 v110, v110
  v_mfma_f32_16x16x32_bf16 v[52:55], a[108:111], v[144:147], v[52:55]
  s_add_u32 s72, s93, 10
  s_and_b32 s72, s72, 31
  s_mul_i32 s72, s72, 40960
  s_add_u32 m0, s62, 106496
  v_mfma_f32_16x16x32_bf16 v[84:87], a[104:107], v[148:151], v[84:87]
  v_exp_f32 v111, v111
  v_mfma_f32_16x16x32_bf16 v[84:87], a[108:111], v[152:155], v[84:87]
  ds_read_b64_tr_b16 a[100:101], v236 offset:40960
  v_cvt_pk_bf16_f32 v161, v110, v111
  v_mfma_f32_16x16x32_bf16 v[116:119], a[32:35], a[160:163], v[104:107]
  v_exp_f32 v112, v112
  v_mfma_f32_16x16x32_bf16 v[116:119], a[36:39], a[164:167], v[116:119]
  buffer_load_dwordx4 v180, s[52:55], s72 offen offset:0 lds
  v_mfma_f32_16x16x32_bf16 v[116:119], a[40:43], a[168:171], v[116:119]
  v_exp_f32 v113, v113
  v_mfma_f32_16x16x32_bf16 v[116:119], a[44:47], a[172:175], v[116:119]
  ds_read_b64_tr_b16 a[102:103], v236 offset:45056
  v_cvt_pk_bf16_f32 v168, v112, v113
  v_mfma_f32_16x16x32_bf16 v[24:27], a[112:115], v[132:135], v[24:27]
  v_exp_f32 v114, v114
  v_mfma_f32_16x16x32_bf16 v[24:27], a[116:119], v[136:139], v[24:27]
  buffer_load_dwordx4 v181, s[52:55], s72 offen offset:1024 lds
  v_mfma_f32_16x16x32_bf16 v[56:59], a[112:115], v[140:143], v[56:59]
  v_exp_f32 v115, v115
  v_mfma_f32_16x16x32_bf16 v[56:59], a[116:119], v[144:147], v[56:59]
  ds_read_b64_tr_b16 a[104:105], v237 offset:32768
  v_cvt_pk_bf16_f32 v169, v114, v115
  v_mfma_f32_16x16x32_bf16 v[108:111], a[48:51], a[128:131], v[96:99]
  v_exp_f32 v116, v116
  v_mfma_f32_16x16x32_bf16 v[108:111], a[52:55], a[132:135], v[108:111]
  ds_read_b64_tr_b16 a[106:107], v237 offset:36864
  ds_read_b64_tr_b16 a[108:109], v237 offset:40960
  v_mfma_f32_16x16x32_bf16 v[108:111], a[56:59], a[136:139], v[108:111]
  v_exp_f32 v117, v117
  v_mfma_f32_16x16x32_bf16 v[108:111], a[60:63], a[140:143], v[108:111]
  ds_read_b64_tr_b16 a[110:111], v237 offset:45056
  v_cvt_pk_bf16_f32 v176, v116, v117
  v_mfma_f32_16x16x32_bf16 v[88:91], a[112:115], v[148:151], v[88:91]
  v_exp_f32 v118, v118
  v_mfma_f32_16x16x32_bf16 v[88:91], a[116:119], v[152:155], v[88:91]
  buffer_load_dwordx4 v182, s[52:55], s72 offen offset:2048 lds
  v_mfma_f32_16x16x32_bf16 v[28:31], a[120:123], v[132:135], v[28:31]
  v_exp_f32 v119, v119
  v_mfma_f32_16x16x32_bf16 v[28:31], a[124:127], v[136:139], v[28:31]
  ds_read_b128 a[32:35], v228 offset:8192
  v_cvt_pk_bf16_f32 v177, v118, v119
  v_mfma_f32_16x16x32_bf16 v[60:63], a[120:123], v[140:143], v[60:63]
  ds_read_b128 a[36:39], v229 offset:8192
  ds_read_b128 a[40:43], v230 offset:8192
  v_mfma_f32_16x16x32_bf16 v[60:63], a[124:127], v[144:147], v[60:63]
  ds_read_b128 a[44:47], v231 offset:8192
  ds_read_b64_tr_b16 a[112:113], v238 offset:32768
  v_mfma_f32_16x16x32_bf16 v[112:115], a[48:51], a[144:147], v[100:103]
  v_exp_f32 v108, v108
  v_mfma_f32_16x16x32_bf16 v[112:115], a[52:55], a[148:151], v[112:115]
  ds_read_b64_tr_b16 a[114:115], v238 offset:36864
  ds_read_b64_tr_b16 a[116:117], v238 offset:40960
  v_mfma_f32_16x16x32_bf16 v[112:115], a[56:59], a[152:155], v[112:115]
  v_exp_f32 v109, v109
  v_mfma_f32_16x16x32_bf16 v[112:115], a[60:63], a[156:159], v[112:115]
  ds_read_b64_tr_b16 a[118:119], v238 offset:45056
  v_cvt_pk_bf16_f32 v162, v108, v109
  v_mfma_f32_16x16x32_bf16 v[92:95], a[120:123], v[148:151], v[92:95]
  v_exp_f32 v110, v110
  v_mfma_f32_16x16x32_bf16 v[92:95], a[124:127], v[152:155], v[92:95]
  buffer_load_dwordx4 v183, s[52:55], s72 offen offset:3072 lds
  v_mfma_f32_16x16x32_bf16 v[120:123], a[240:243], v[132:135], v[120:123]
  v_exp_f32 v111, v111
  v_mfma_f32_16x16x32_bf16 v[120:123], a[240:243], v[136:139], v[120:123]
  ds_read_b64_tr_b16 a[120:121], v239 offset:32768
  v_cvt_pk_bf16_f32 v163, v110, v111
  v_mfma_f32_16x16x32_bf16 v[116:119], a[48:51], a[160:163], v[104:107]
  v_exp_f32 v112, v112
  v_mfma_f32_16x16x32_bf16 v[116:119], a[52:55], a[164:167], v[116:119]
  ds_read_b64_tr_b16 a[122:123], v239 offset:36864
  ds_read_b64_tr_b16 a[124:125], v239 offset:40960
  v_mfma_f32_16x16x32_bf16 v[116:119], a[56:59], a[168:171], v[116:119]
  v_exp_f32 v113, v113
  v_mfma_f32_16x16x32_bf16 v[116:119], a[60:63], a[172:175], v[116:119]
  ds_read_b64_tr_b16 a[126:127], v239 offset:45056
  v_cvt_pk_bf16_f32 v170, v112, v113
  v_mfma_f32_16x16x32_bf16 v[124:127], a[240:243], v[140:143], v[124:127]
  v_exp_f32 v114, v114
  v_mfma_f32_16x16x32_bf16 v[124:127], a[240:243], v[144:147], v[124:127]
  v_mfma_f32_16x16x32_bf16 v[128:131], a[240:243], v[148:151], v[128:131]
  v_exp_f32 v115, v115
  v_mfma_f32_16x16x32_bf16 v[128:131], a[240:243], v[152:155], v[128:131]
  v_cvt_pk_bf16_f32 v171, v114, v115
  s_waitcnt lgkmcnt(8)
  v_mfma_f32_16x16x32_bf16 v[108:111], a[0:3], a[128:131], v[96:99]
  v_exp_f32 v116, v116
  v_mfma_f32_16x16x32_bf16 v[108:111], a[4:7], a[132:135], v[108:111]
  ds_read_b128 a[48:51], v228 offset:12288
  ds_read_b128 a[52:55], v229 offset:12288
  v_mfma_f32_16x16x32_bf16 v[108:111], a[8:11], a[136:139], v[108:111]
  v_exp_f32 v117, v117
  v_mfma_f32_16x16x32_bf16 v[108:111], a[12:15], a[140:143], v[108:111]
  ds_read_b128 a[56:59], v230 offset:12288
  v_cvt_pk_bf16_f32 v178, v116, v117
  v_mfma_f32_16x16x32_bf16 v[0:3], a[64:67], v[156:159], v[0:3]
  v_exp_f32 v118, v118
  v_mfma_f32_16x16x32_bf16 v[0:3], a[68:71], v[160:163], v[0:3]
  ds_read_b128 a[60:63], v231 offset:12288
  s_mov_b64 s[48:49], s[22:23]
  v_mfma_f32_16x16x32_bf16 v[32:35], a[64:67], v[164:167], v[32:35]
  v_exp_f32 v119, v119
  v_mfma_f32_16x16x32_bf16 v[32:35], a[68:71], v[168:171], v[32:35]
  v_cvt_pk_bf16_f32 v179, v118, v119
  s_mov_b32 s50, 0
  v_mfma_f32_16x16x32_bf16 v[112:115], a[0:3], a[144:147], v[100:103]
  v_exp_f32 v108, v108
  v_mfma_f32_16x16x32_bf16 v[112:115], a[4:7], a[148:151], v[112:115]
  s_mul_i32 s51, s89, 49152
  v_mfma_f32_16x16x32_bf16 v[112:115], a[8:11], a[152:155], v[112:115]
  v_exp_f32 v109, v109
  v_mfma_f32_16x16x32_bf16 v[112:115], a[12:15], a[156:159], v[112:115]
  v_cvt_pk_bf16_f32 v132, v108, v109
  s_add_u32 s50, s50, s51
  v_mfma_f32_16x16x32_bf16 v[64:67], a[64:67], v[172:175], v[64:67]
  v_exp_f32 v110, v110
  s_add_u32 s48, s48, s50
  s_addc_u32 s49, s49, 0
  v_mfma_f32_16x16x32_bf16 v[64:67], a[68:71], v[176:179], v[64:67]
  s_waitcnt vmcnt(8) lgkmcnt(0)
  s_barrier
  s_and_b32 s49, s49, 0xffff
  v_mfma_f32_16x16x32_bf16 v[4:7], a[72:75], v[156:159], v[4:7]
  v_exp_f32 v111, v111
  s_mov_b32 s50, 49152
  v_mfma_f32_16x16x32_bf16 v[4:7], a[76:79], v[160:163], v[4:7]
  ds_read_b64_tr_b16 a[64:65], v232 offset:0
  v_cvt_pk_bf16_f32 v133, v110, v111
  s_mov_b32 s51, 0x20000
  v_mfma_f32_16x16x32_bf16 v[116:119], a[0:3], a[160:163], v[104:107]
  v_exp_f32 v112, v112
  s_mul_i32 s73, s64, 3072
  v_mfma_f32_16x16x32_bf16 v[116:119], a[4:7], a[164:167], v[116:119]
  s_add_u32 s72, s93, 11
  s_and_b32 s72, s72, 31
  s_mul_i32 s72, s72, 40960
  s_add_u32 m0, s62, 90112
  v_and_b32 v244, 15, v246
  v_mfma_f32_16x16x32_bf16 v[116:119], a[8:11], a[168:171], v[116:119]
  v_exp_f32 v113, v113
  v_lshlrev_b32 v244, 4, v244
  v_mfma_f32_16x16x32_bf16 v[116:119], a[12:15], a[172:175], v[116:119]
  ds_read_b64_tr_b16 a[66:67], v232 offset:4096
  v_cvt_pk_bf16_f32 v140, v112, v113
  v_add_u32 v244, 159744, v244
  v_mfma_f32_16x16x32_bf16 v[36:39], a[72:75], v[164:167], v[36:39]
  v_exp_f32 v114, v114
  s_lshl_b32 s68, s64, 2
  v_mfma_f32_16x16x32_bf16 v[36:39], a[76:79], v[168:171], v[36:39]
  buffer_load_dwordx4 v180, s[52:55], s72 offen offset:0 lds
  v_add_u32 v253, s68, v244
  v_mfma_f32_16x16x32_bf16 v[68:71], a[72:75], v[172:175], v[68:71]
  v_exp_f32 v115, v115
  v_mfma_f32_16x16x32_bf16 v[68:71], a[76:79], v[176:179], v[68:71]
  ds_read_b64_tr_b16 a[68:69], v232 offset:8192
  v_cvt_pk_bf16_f32 v141, v114, v115
  v_mfma_f32_16x16x32_bf16 v[8:11], a[80:83], v[156:159], v[8:11]
  ds_read_b64_tr_b16 a[70:71], v232 offset:12288
  ds_read_b128 a[0:3], v228 offset:49152
  v_mfma_f32_16x16x32_bf16 v[8:11], a[84:87], v[160:163], v[8:11]
  ds_read_b128 a[4:7], v229 offset:49152
  ds_read_b128 a[8:11], v230 offset:49152
  v_mfma_f32_16x16x32_bf16 v[108:111], a[16:19], a[128:131], v[96:99]
  v_exp_f32 v116, v116
  v_mfma_f32_16x16x32_bf16 v[108:111], a[20:23], a[132:135], v[108:111]
  buffer_load_dwordx4 v181, s[52:55], s72 offen offset:1024 lds
  v_mfma_f32_16x16x32_bf16 v[108:111], a[24:27], a[136:139], v[108:111]
  v_exp_f32 v117, v117
  v_mfma_f32_16x16x32_bf16 v[108:111], a[28:31], a[140:143], v[108:111]
  ds_read_b128 a[12:15], v231 offset:49152
  v_cvt_pk_bf16_f32 v148, v116, v117
  v_mfma_f32_16x16x32_bf16 v[40:43], a[80:83], v[164:167], v[40:43]
  v_exp_f32 v118, v118
  v_mfma_f32_16x16x32_bf16 v[40:43], a[84:87], v[168:171], v[40:43]
  ds_read_b64_tr_b16 a[72:73], v233 offset:0
  ds_read_b64_tr_b16 a[74:75], v233 offset:4096
  v_mfma_f32_16x16x32_bf16 v[72:75], a[80:83], v[172:175], v[72:75]
  v_exp_f32 v119, v119
  v_mfma_f32_16x16x32_bf16 v[72:75], a[84:87], v[176:179], v[72:75]
  ds_read_b64_tr_b16 a[76:77], v233 offset:8192
  v_cvt_pk_bf16_f32 v149, v118, v119
  v_mfma_f32_16x16x32_bf16 v[112:115], a[16:19], a[144:147], v[100:103]
  v_exp_f32 v108, v108
  v_mfma_f32_16x16x32_bf16 v[112:115], a[20:23], a[148:151], v[112:115]
  ds_read_b64_tr_b16 a[78:79], v233 offset:12288
  ds_read_b64_tr_b16 a[80:81], v234 offset:0
  v_mfma_f32_16x16x32_bf16 v[112:115], a[24:27], a[152:155], v[112:115]
  v_exp_f32 v109, v109
  v_mfma_f32_16x16x32_bf16 v[112:115], a[28:31], a[156:159], v[112:115]
  ds_read_b64_tr_b16 a[82:83], v234 offset:4096
  v_cvt_pk_bf16_f32 v134, v108, v109
  v_mfma_f32_16x16x32_bf16 v[12:15], a[88:91], v[156:159], v[12:15]
  v_exp_f32 v110, v110
  v_mfma_f32_16x16x32_bf16 v[12:15], a[92:95], v[160:163], v[12:15]
  buffer_load_dwordx4 v182, s[52:55], s72 offen offset:2048 lds
  v_mfma_f32_16x16x32_bf16 v[44:47], a[88:91], v[164:167], v[44:47]
  v_exp_f32 v111, v111
  v_mfma_f32_16x16x32_bf16 v[44:47], a[92:95], v[168:171], v[44:47]
  ds_read_b64_tr_b16 a[84:85], v234 offset:8192
  v_cvt_pk_bf16_f32 v135, v110, v111
  v_mfma_f32_16x16x32_bf16 v[116:119], a[16:19], a[160:163], v[104:107]
  v_exp_f32 v112, v112
  v_mfma_f32_16x16x32_bf16 v[116:119], a[20:23], a[164:167], v[116:119]
  ds_read_b64_tr_b16 a[86:87], v234 offset:12288
  v_mfma_f32_16x16x32_bf16 v[116:119], a[24:27], a[168:171], v[116:119]
  v_exp_f32 v113, v113
  v_mfma_f32_16x16x32_bf16 v[116:119], a[28:31], a[172:175], v[116:119]
  v_cvt_pk_bf16_f32 v142, v112, v113
  v_mfma_f32_16x16x32_bf16 v[76:79], a[88:91], v[172:175], v[76:79]
  v_exp_f32 v114, v114
  v_mfma_f32_16x16x32_bf16 v[76:79], a[92:95], v[176:179], v[76:79]
  ds_read_b128 a[16:19], v228 offset:53248
  ds_read_b128 a[20:23], v229 offset:53248
  v_mfma_f32_16x16x32_bf16 v[16:19], a[96:99], v[156:159], v[16:19]
  v_exp_f32 v115, v115
  v_mfma_f32_16x16x32_bf16 v[16:19], a[100:103], v[160:163], v[16:19]
  ds_read_b128 a[24:27], v230 offset:53248
  v_cvt_pk_bf16_f32 v143, v114, v115
  v_mfma_f32_16x16x32_bf16 v[108:111], a[32:35], a[128:131], v[96:99]
  v_exp_f32 v116, v116
  v_mfma_f32_16x16x32_bf16 v[108:111], a[36:39], a[132:135], v[108:111]
  buffer_load_dwordx4 v183, s[52:55], s72 offen offset:3072 lds
  v_mfma_f32_16x16x32_bf16 v[108:111], a[40:43], a[136:139], v[108:111]
  v_exp_f32 v117, v117
  v_mfma_f32_16x16x32_bf16 v[108:111], a[44:47], a[140:143], v[108:111]
  v_cvt_pk_bf16_f32 v150, v116, v117
  s_add_u32 s72, s93, 12
  s_and_b32 s72, s72, 31
  s_mul_i32 s72, s72, 40960
  v_mfma_f32_16x16x32_bf16 v[48:51], a[96:99], v[164:167], v[48:51]
  v_exp_f32 v118, v118
  v_mfma_f32_16x16x32_bf16 v[48:51], a[100:103], v[168:171], v[48:51]
  s_add_u32 m0, s62, 32768
  ds_read_b128 a[28:31], v231 offset:53248
  v_mfma_f32_16x16x32_bf16 v[80:83], a[96:99], v[172:175], v[80:83]
  v_exp_f32 v119, v119
  v_mfma_f32_16x16x32_bf16 v[80:83], a[100:103], v[176:179], v[80:83]
  ds_read_b64_tr_b16 a[88:89], v235 offset:0
  v_cvt_pk_bf16_f32 v151, v118, v119
  v_mfma_f32_16x16x32_bf16 v[20:23], a[104:107], v[156:159], v[20:23]
  buffer_load_dwordx4 v180, s[52:55], s72 offen offset:0 lds
  v_mfma_f32_16x16x32_bf16 v[20:23], a[108:111], v[160:163], v[20:23]
  ds_read_b64_tr_b16 a[90:91], v235 offset:4096
  ds_read_b64_tr_b16 a[92:93], v235 offset:8192
  v_mfma_f32_16x16x32_bf16 v[112:115], a[32:35], a[144:147], v[100:103]
  v_exp_f32 v108, v108
  v_mfma_f32_16x16x32_bf16 v[112:115], a[36:39], a[148:151], v[112:115]
  ds_read_b64_tr_b16 a[94:95], v235 offset:12288
  ds_read_b64_tr_b16 a[96:97], v236 offset:0
  v_mfma_f32_16x16x32_bf16 v[112:115], a[40:43], a[152:155], v[112:115]
  v_exp_f32 v109, v109
  v_mfma_f32_16x16x32_bf16 v[112:115], a[44:47], a[156:159], v[112:115]
  ds_read_b64_tr_b16 a[98:99], v236 offset:4096
  v_cvt_pk_bf16_f32 v136, v108, v109
  v_mfma_f32_16x16x32_bf16 v[52:55], a[104:107], v[164:167], v[52:55]
  v_exp_f32 v110, v110
  v_mfma_f32_16x16x32_bf16 v[52:55], a[108:111], v[168:171], v[52:55]
  ds_read_b64_tr_b16 a[100:101], v236 offset:8192
  ds_read_b64_tr_b16 a[102:103], v236 offset:12288
  v_mfma_f32_16x16x32_bf16 v[84:87], a[104:107], v[172:175], v[84:87]
  v_exp_f32 v111, v111
  v_mfma_f32_16x16x32_bf16 v[84:87], a[108:111], v[176:179], v[84:87]
  v_cvt_pk_bf16_f32 v137, v110, v111
  v_mfma_f32_16x16x32_bf16 v[116:119], a[32:35], a[160:163], v[104:107]
  v_exp_f32 v112, v112
  v_mfma_f32_16x16x32_bf16 v[116:119], a[36:39], a[164:167], v[116:119]
  buffer_load_dwordx4 v181, s[52:55], s72 offen offset:1024 lds
  v_mfma_f32_16x16x32_bf16 v[116:119], a[40:43], a[168:171], v[116:119]
  v_exp_f32 v113, v113
  v_mfma_f32_16x16x32_bf16 v[116:119], a[44:47], a[172:175], v[116:119]
  ds_read_b64_tr_b16 a[104:105], v237 offset:0
  v_cvt_pk_bf16_f32 v144, v112, v113
  v_mfma_f32_16x16x32_bf16 v[24:27], a[112:115], v[156:159], v[24:27]
  v_exp_f32 v114, v114
  v_mfma_f32_16x16x32_bf16 v[24:27], a[116:119], v[160:163], v[24:27]
  ds_read_b64_tr_b16 a[106:107], v237 offset:4096
  ds_read_b64_tr_b16 a[108:109], v237 offset:8192
  v_mfma_f32_16x16x32_bf16 v[56:59], a[112:115], v[164:167], v[56:59]
  v_exp_f32 v115, v115
  v_mfma_f32_16x16x32_bf16 v[56:59], a[116:119], v[168:171], v[56:59]
  ds_read_b64_tr_b16 a[110:111], v237 offset:12288
  v_cvt_pk_bf16_f32 v145, v114, v115
  v_mfma_f32_16x16x32_bf16 v[108:111], a[48:51], a[128:131], v[96:99]
  v_exp_f32 v116, v116
  v_mfma_f32_16x16x32_bf16 v[108:111], a[52:55], a[132:135], v[108:111]
  ds_read_b128 a[32:35], v228 offset:57344
  ds_read_b128 a[36:39], v229 offset:57344
  v_mfma_f32_16x16x32_bf16 v[108:111], a[56:59], a[136:139], v[108:111]
  v_exp_f32 v117, v117
  v_mfma_f32_16x16x32_bf16 v[108:111], a[60:63], a[140:143], v[108:111]
  ds_read_b128 a[40:43], v230 offset:57344
  v_cvt_pk_bf16_f32 v152, v116, v117
  v_mfma_f32_16x16x32_bf16 v[88:91], a[112:115], v[172:175], v[88:91]
  v_exp_f32 v118, v118
  v_mfma_f32_16x16x32_bf16 v[88:91], a[116:119], v[176:179], v[88:91]
  buffer_load_dwordx4 v182, s[52:55], s72 offen offset:2048 lds
  v_mfma_f32_16x16x32_bf16 v[28:31], a[120:123], v[156:159], v[28:31]
  v_exp_f32 v119, v119
  v_mfma_f32_16x16x32_bf16 v[28:31], a[124:127], v[160:163], v[28:31]
  ds_read_b128 a[44:47], v231 offset:57344
  v_cvt_pk_bf16_f32 v153, v118, v119
  v_mfma_f32_16x16x32_bf16 v[60:63], a[120:123], v[164:167], v[60:63]
  ds_read_b64_tr_b16 a[112:113], v238 offset:0
  ds_read_b64_tr_b16 a[114:115], v238 offset:4096
  v_mfma_f32_16x16x32_bf16 v[60:63], a[124:127], v[168:171], v[60:63]
  ds_read_b64_tr_b16 a[116:117], v238 offset:8192
  ds_read_b64_tr_b16 a[118:119], v238 offset:12288
  v_mfma_f32_16x16x32_bf16 v[112:115], a[48:51], a[144:147], v[100:103]
  v_exp_f32 v108, v108
  v_mfma_f32_16x16x32_bf16 v[112:115], a[52:55], a[148:151], v[112:115]
  v_mfma_f32_16x16x32_bf16 v[112:115], a[56:59], a[152:155], v[112:115]
  v_exp_f32 v109, v109
  v_mfma_f32_16x16x32_bf16 v[112:115], a[60:63], a[156:159], v[112:115]
  v_cvt_pk_bf16_f32 v138, v108, v109
  v_mfma_f32_16x16x32_bf16 v[92:95], a[120:123], v[172:175], v[92:95]
  v_exp_f32 v110, v110
  v_mfma_f32_16x16x32_bf16 v[92:95], a[124:127], v[176:179], v[92:95]
  buffer_load_dwordx4 v183, s[52:55], s72 offen offset:3072 lds
  v_mfma_f32_16x16x32_bf16 v[120:123], a[240:243], v[156:159], v[120:123]
  v_exp_f32 v111, v111
  v_mfma_f32_16x16x32_bf16 v[120:123], a[240:243], v[160:163], v[120:123]
  ds_read_b64_tr_b16 a[120:121], v239 offset:0
  v_cvt_pk_bf16_f32 v139, v110, v111
  v_mfma_f32_16x16x32_bf16 v[116:119], a[48:51], a[160:163], v[104:107]
  v_exp_f32 v112, v112
  v_mfma_f32_16x16x32_bf16 v[116:119], a[52:55], a[164:167], v[116:119]
  ds_read_b64_tr_b16 a[122:123], v239 offset:4096
  ds_read_b64_tr_b16 a[124:125], v239 offset:8192
  v_mfma_f32_16x16x32_bf16 v[116:119], a[56:59], a[168:171], v[116:119]
  v_exp_f32 v113, v113
  v_mfma_f32_16x16x32_bf16 v[116:119], a[60:63], a[172:175], v[116:119]
  ds_read_b64_tr_b16 a[126:127], v239 offset:12288
  v_cvt_pk_bf16_f32 v146, v112, v113
  v_mfma_f32_16x16x32_bf16 v[124:127], a[240:243], v[164:167], v[124:127]
  v_exp_f32 v114, v114
  v_mfma_f32_16x16x32_bf16 v[124:127], a[240:243], v[168:171], v[124:127]
  v_mfma_f32_16x16x32_bf16 v[128:131], a[240:243], v[172:175], v[128:131]
  v_exp_f32 v115, v115
  v_mfma_f32_16x16x32_bf16 v[128:131], a[240:243], v[176:179], v[128:131]
  v_cvt_pk_bf16_f32 v147, v114, v115
  s_waitcnt lgkmcnt(8)
  v_mfma_f32_16x16x32_bf16 v[108:111], a[0:3], a[128:131], v[96:99]
  ds_read_b128 a[48:51], v228 offset:61440
  v_exp_f32 v116, v116
  v_mfma_f32_16x16x32_bf16 v[108:111], a[4:7], a[132:135], v[108:111]
  ds_read_b128 a[52:55], v229 offset:61440
  v_exp_f32 v117, v117
  v_mfma_f32_16x16x32_bf16 v[108:111], a[8:11], a[136:139], v[108:111]
  ds_read_b128 a[56:59], v230 offset:61440
  v_exp_f32 v118, v118
  v_cvt_pk_bf16_f32 v154, v116, v117
  v_mfma_f32_16x16x32_bf16 v[108:111], a[12:15], a[140:143], v[108:111]
  ds_read_b128 a[60:63], v231 offset:61440
  v_exp_f32 v119, v119
  v_mfma_f32_16x16x32_bf16 v[120:123], a[240:243], v[132:135], v[120:123]
  v_cvt_pk_bf16_f32 v155, v118, v119
  v_mfma_f32_16x16x32_bf16 v[120:123], a[240:243], v[136:139], v[120:123]
  v_mfma_f32_16x16x32_bf16 v[112:115], a[0:3], a[144:147], v[100:103]
  s_nop 1
  v_exp_f32 v108, v108
  v_mfma_f32_16x16x32_bf16 v[112:115], a[4:7], a[148:151], v[112:115]
  v_exp_f32 v109, v109
  v_mfma_f32_16x16x32_bf16 v[112:115], a[8:11], a[152:155], v[112:115]
  v_exp_f32 v110, v110
  v_cvt_pk_bf16_f32 v156, v108, v109
  v_mfma_f32_16x16x32_bf16 v[112:115], a[12:15], a[156:159], v[112:115]
  v_exp_f32 v111, v111
  s_waitcnt vmcnt(4) lgkmcnt(0)
  s_barrier
  v_mfma_f32_16x16x32_bf16 v[116:119], a[0:3], a[160:163], v[104:107]
  v_cvt_pk_bf16_f32 v157, v110, v111
  s_add_u32 s72, s93, 13
  s_and_b32 s72, s72, 31
  s_mul_i32 s72, s72, 40960
  s_add_u32 m0, s62, 139264
  v_mfma_f32_16x16x32_bf16 v[116:119], a[4:7], a[164:167], v[116:119]
  buffer_load_dwordx4 v180, s[52:55], s72 offen offset:0 lds
  v_add_u32 v228, 73728, v184
  v_mfma_f32_16x16x32_bf16 v[116:119], a[8:11], a[168:171], v[116:119]
  v_exp_f32 v112, v112
  v_mfma_f32_16x16x32_bf16 v[116:119], a[12:15], a[172:175], v[116:119]
  v_exp_f32 v113, v113
  v_mfma_f32_16x16x32_bf16 v[124:127], a[240:243], v[140:143], v[124:127]
  buffer_load_dwordx4 v181, s[52:55], s72 offen offset:1024 lds
  v_exp_f32 v114, v114
  v_cvt_pk_bf16_f32 v164, v112, v113
  v_mfma_f32_16x16x32_bf16 v[124:127], a[240:243], v[144:147], v[124:127]
  ds_read_b64_tr_b16 a[0:1], v184 offset:16384
  v_exp_f32 v115, v115
  v_mfma_f32_16x16x32_bf16 v[108:111], a[16:19], a[128:131], v[96:99]
  ds_read_b64_tr_b16 a[2:3], v184 offset:18432
  v_cvt_pk_bf16_f32 v165, v114, v115
  v_exp_f32 v116, v116
  v_mfma_f32_16x16x32_bf16 v[108:111], a[20:23], a[132:135], v[108:111]
  buffer_load_dwordx4 v182, s[52:55], s72 offen offset:2048 lds
  ds_read_b64_tr_b16 a[4:5], v185 offset:16384
  v_exp_f32 v117, v117
  v_mfma_f32_16x16x32_bf16 v[108:111], a[24:27], a[136:139], v[108:111]
  ds_read_b64_tr_b16 a[6:7], v185 offset:18432
  v_exp_f32 v118, v118
  v_cvt_pk_bf16_f32 v172, v116, v117
  v_mfma_f32_16x16x32_bf16 v[108:111], a[28:31], a[140:143], v[108:111]
  ds_read_b64_tr_b16 a[8:9], v186 offset:16384
  v_exp_f32 v119, v119
  v_add_u32 v229, 73728, v185
  v_mfma_f32_16x16x32_bf16 v[112:115], a[16:19], a[144:147], v[100:103]
  buffer_load_dwordx4 v183, s[52:55], s72 offen offset:3072 lds
  ds_read_b64_tr_b16 a[10:11], v186 offset:18432
  v_cvt_pk_bf16_f32 v173, v118, v119
  v_mfma_f32_16x16x32_bf16 v[112:115], a[20:23], a[148:151], v[112:115]
  ds_read_b64_tr_b16 a[12:13], v187 offset:16384
  v_mfma_f32_16x16x32_bf16 v[112:115], a[24:27], a[152:155], v[112:115]
  v_exp_f32 v108, v108
  s_add_u32 s72, s93, 14
  s_and_b32 s72, s72, 31
  s_mul_i32 s72, s72, 40960
  s_add_u32 m0, s62, 0
  ds_read_b64_tr_b16 a[14:15], v187 offset:18432
  v_mfma_f32_16x16x32_bf16 v[112:115], a[28:31], a[156:159], v[112:115]
  buffer_load_dwordx4 v180, s[52:55], s72 offen offset:0 lds
  v_exp_f32 v109, v109
  v_mfma_f32_16x16x32_bf16 v[128:131], a[240:243], v[148:151], v[128:131]
  v_exp_f32 v110, v110
  v_cvt_pk_bf16_f32 v158, v108, v109
  v_mfma_f32_16x16x32_bf16 v[128:131], a[240:243], v[152:155], v[128:131]
  v_exp_f32 v111, v111
  v_mfma_f32_16x16x32_bf16 v[116:119], a[16:19], a[160:163], v[104:107]
  buffer_load_dwordx4 v181, s[52:55], s72 offen offset:1024 lds
  v_cvt_pk_bf16_f32 v159, v110, v111
  v_exp_f32 v112, v112
  v_mfma_f32_16x16x32_bf16 v[116:119], a[20:23], a[164:167], v[116:119]
  v_exp_f32 v113, v113
  v_add_u32 v230, 73728, v186
  v_mfma_f32_16x16x32_bf16 v[116:119], a[24:27], a[168:171], v[116:119]
  v_exp_f32 v114, v114
  v_cvt_pk_bf16_f32 v166, v112, v113
  v_mfma_f32_16x16x32_bf16 v[116:119], a[28:31], a[172:175], v[116:119]
  buffer_load_dwordx4 v182, s[52:55], s72 offen offset:2048 lds
  v_exp_f32 v115, v115
  v_mfma_f32_16x16x32_bf16 v[108:111], a[32:35], a[128:131], v[96:99]
  v_cvt_pk_bf16_f32 v167, v114, v115
  v_mfma_f32_16x16x32_bf16 v[108:111], a[36:39], a[132:135], v[108:111]
  ds_read_b64_tr_b16 a[16:17], v184 offset:0
  v_mfma_f32_16x16x32_bf16 v[108:111], a[40:43], a[136:139], v[108:111]
  buffer_load_dwordx4 v183, s[52:55], s72 offen offset:3072 lds
  ds_read_b64_tr_b16 a[18:19], v184 offset:2048
  v_exp_f32 v116, v116
  v_mfma_f32_16x16x32_bf16 v[108:111], a[44:47], a[140:143], v[108:111]
  ds_read_b64_tr_b16 a[20:21], v185 offset:0
  v_exp_f32 v117, v117
  v_mfma_f32_16x16x32_bf16 v[112:115], a[32:35], a[144:147], v[100:103]
  ds_read_b64_tr_b16 a[22:23], v185 offset:2048
  v_exp_f32 v118, v118
  v_cvt_pk_bf16_f32 v174, v116, v117
  s_waitcnt lgkmcnt(2)
  s_add_u32 s72, s93, 15
  s_and_b32 s72, s72, 31
  s_mul_i32 s72, s72, 40960
  s_add_u32 m0, s62, 65536
  v_mfma_f32_16x16x32_bf16 v[112:115], a[36:39], a[148:151], v[112:115]
  ds_read_b64_tr_b16 a[24:25], v186 offset:0
  buffer_load_dwordx4 v180, s[52:55], s72 offen offset:0 lds
  v_exp_f32 v119, v119
  v_add_u32 v231, 73728, v187
  v_mfma_f32_16x16x32_bf16 v[112:115], a[40:43], a[152:155], v[112:115]
  ds_read_b64_tr_b16 a[26:27], v186 offset:2048
  v_cvt_pk_bf16_f32 v175, v118, v119
  v_exp_f32 v108, v108
  v_mfma_f32_16x16x32_bf16 v[112:115], a[44:47], a[156:159], v[112:115]
  ds_read_b64_tr_b16 a[28:29], v187 offset:0
  v_exp_f32 v109, v109
  v_mfma_f32_16x16x32_bf16 v[116:119], a[32:35], a[160:163], v[104:107]
  ds_read_b64_tr_b16 a[30:31], v187 offset:2048
  buffer_load_dwordx4 v181, s[52:55], s72 offen offset:1024 lds
  v_exp_f32 v110, v110
  v_cvt_pk_bf16_f32 v160, v108, v109
  v_mfma_f32_16x16x32_bf16 v[116:119], a[36:39], a[164:167], v[116:119]
  v_exp_f32 v111, v111
  v_mfma_f32_16x16x32_bf16 v[116:119], a[40:43], a[168:171], v[116:119]
  v_cvt_pk_bf16_f32 v161, v110, v111
  v_exp_f32 v112, v112
  v_mfma_f32_16x16x32_bf16 v[116:119], a[44:47], a[172:175], v[116:119]
  buffer_load_dwordx4 v182, s[52:55], s72 offen offset:2048 lds
  v_exp_f32 v113, v113
  v_mfma_f32_16x16x32_bf16 v[108:111], a[48:51], a[128:131], v[96:99]
  v_exp_f32 v114, v114
  v_cvt_pk_bf16_f32 v168, v112, v113
  v_mfma_f32_16x16x32_bf16 v[108:111], a[52:55], a[132:135], v[108:111]
  ds_read_b64_tr_b16 a[32:33], v232 offset:16384
  v_exp_f32 v115, v115
  v_mfma_f32_16x16x32_bf16 v[108:111], a[56:59], a[136:139], v[108:111]
  ds_read_b64_tr_b16 a[34:35], v232 offset:20480
  buffer_load_dwordx4 v183, s[52:55], s72 offen offset:3072 lds
  v_cvt_pk_bf16_f32 v169, v114, v115
  v_exp_f32 v116, v116
  v_mfma_f32_16x16x32_bf16 v[108:111], a[60:63], a[140:143], v[108:111]
  ds_read_b64_tr_b16 a[36:37], v232 offset:24576
  v_exp_f32 v117, v117
  v_mfma_f32_16x16x32_bf16 v[112:115], a[48:51], a[144:147], v[100:103]
  ds_read_b64_tr_b16 a[38:39], v232 offset:28672
  v_exp_f32 v118, v118
  v_cvt_pk_bf16_f32 v176, v116, v117
  s_waitcnt lgkmcnt(2)
  s_add_u32 s72, s93, 16
  s_and_b32 s72, s72, 31
  s_mul_i32 s72, s72, 40960
  s_add_u32 m0, s62, 49152
  v_mfma_f32_16x16x32_bf16 v[112:115], a[52:55], a[148:151], v[112:115]
  ds_read_b64_tr_b16 a[40:41], v233 offset:16384
  buffer_load_dwordx4 v180, s[52:55], s72 offen offset:0 lds
  v_exp_f32 v119, v119
  v_mfma_f32_16x16x32_bf16 v[112:115], a[56:59], a[152:155], v[112:115]
  ds_read_b64_tr_b16 a[42:43], v233 offset:20480
  v_cvt_pk_bf16_f32 v177, v118, v119
  v_exp_f32 v108, v108
  v_mfma_f32_16x16x32_bf16 v[112:115], a[60:63], a[156:159], v[112:115]
  ds_read_b64_tr_b16 a[44:45], v233 offset:24576
  v_exp_f32 v109, v109
  v_mfma_f32_16x16x32_bf16 v[116:119], a[48:51], a[160:163], v[104:107]
  ds_read_b64_tr_b16 a[46:47], v233 offset:28672
  buffer_load_dwordx4 v181, s[52:55], s72 offen offset:1024 lds
  v_exp_f32 v110, v110
  v_cvt_pk_bf16_f32 v162, v108, v109
  v_mfma_f32_16x16x32_bf16 v[116:119], a[52:55], a[164:167], v[116:119]
  v_exp_f32 v111, v111
  v_mfma_f32_16x16x32_bf16 v[116:119], a[56:59], a[168:171], v[116:119]
  v_cvt_pk_bf16_f32 v163, v110, v111
  v_exp_f32 v112, v112
  v_mfma_f32_16x16x32_bf16 v[116:119], a[60:63], a[172:175], v[116:119]
  buffer_load_dwordx4 v182, s[52:55], s72 offen offset:2048 lds
  v_exp_f32 v113, v113
  v_mfma_f32_16x16x32_bf16 v[0:3], a[64:67], v[132:135], v[0:3]
  v_exp_f32 v114, v114
  v_cvt_pk_bf16_f32 v170, v112, v113
  v_mfma_f32_16x16x32_bf16 v[0:3], a[68:71], v[136:139], v[0:3]
  ds_read_b64_tr_b16 a[48:49], v234 offset:16384
  ds_read_b64_tr_b16 a[128:129], v228 offset:0
  ds_read_b64_tr_b16 a[130:131], v228 offset:2048
  v_exp_f32 v115, v115
  v_mfma_f32_16x16x32_bf16 v[0:3], a[32:35], v[156:159], v[0:3]
  ds_read_b64_tr_b16 a[50:51], v234 offset:20480
  buffer_load_dwordx4 v183, s[52:55], s72 offen offset:3072 lds
  ds_read_b64_tr_b16 a[132:133], v229 offset:0
  v_cvt_pk_bf16_f32 v171, v114, v115
  v_exp_f32 v116, v116
  s_waitcnt lgkmcnt(9)
  v_mfma_f32_16x16x32_bf16 v[0:3], a[36:39], v[160:163], v[0:3]
  ds_read_b64_tr_b16 a[52:53], v234 offset:24576
  ds_read_b64_tr_b16 a[134:135], v229 offset:2048
  ds_read_b64_tr_b16 a[136:137], v230 offset:0
  v_exp_f32 v117, v117
  v_mfma_f32_16x16x32_bf16 v[120:123], a[240:243], v[156:159], v[120:123]
  ds_read_b64_tr_b16 a[54:55], v234 offset:28672
  ds_read_b64_tr_b16 a[138:139], v230 offset:2048
  v_exp_f32 v118, v118
  v_cvt_pk_bf16_f32 v178, v116, v117
  v_mfma_f32_16x16x32_bf16 v[120:123], a[240:243], v[160:163], v[120:123]
  ds_read_b64_tr_b16 a[56:57], v235 offset:16384
  ds_read_b64_tr_b16 a[140:141], v231 offset:0
  ds_read_b64_tr_b16 a[142:143], v231 offset:2048
  v_exp_f32 v119, v119
  v_mfma_f32_16x16x32_bf16 v[4:7], a[72:75], v[132:135], v[4:7]
  ds_read_b64_tr_b16 a[58:59], v235 offset:20480
  ds_read_b64_tr_b16 a[144:145], v184 offset:57344
  v_cvt_pk_bf16_f32 v179, v118, v119
  v_mfma_f32_16x16x32_bf16 v[4:7], a[76:79], v[136:139], v[4:7]
  ds_read_b64_tr_b16 a[60:61], v235 offset:24576
  ds_read_b64_tr_b16 a[146:147], v184 offset:59392
  ds_read_b64_tr_b16 a[148:149], v185 offset:57344
  s_waitcnt lgkmcnt(14)
  v_mfma_f32_16x16x32_bf16 v[4:7], a[40:43], v[156:159], v[4:7]
  ds_read_b64_tr_b16 a[62:63], v235 offset:28672
  ds_read_b64_tr_b16 a[150:151], v185 offset:59392
  v_mfma_f32_16x16x32_bf16 v[4:7], a[44:47], v[160:163], v[4:7]
  ds_read_b64_tr_b16 a[152:153], v186 offset:57344
  ds_read_b64_tr_b16 a[154:155], v186 offset:59392
  s_waitcnt lgkmcnt(8)
  s_add_u32 s72, s93, 17
  s_and_b32 s72, s72, 31
  s_mul_i32 s72, s72, 40960
  s_add_u32 m0, s62, 122880
  v_mfma_f32_16x16x32_bf16 v[124:127], a[240:243], v[164:167], v[124:127]
  ds_read_b64_tr_b16 a[156:157], v187 offset:57344
  buffer_load_dwordx4 v180, s[52:55], s72 offen offset:0 lds
  v_mov_b32 v245, v120
  v_rcp_f32 v120, v120
  v_mfma_f32_16x16x32_bf16 v[124:127], a[240:243], v[168:171], v[124:127]
  ds_read_b64_tr_b16 a[158:159], v187 offset:59392
  ds_read_b64_tr_b16 a[160:161], v184 offset:40960
  v_mul_f32 v0, v120, v0
  v_mul_f32 v1, v120, v1
  v_mfma_f32_16x16x32_bf16 v[32:35], a[64:67], v[140:143], v[32:35]
  ds_read_b64_tr_b16 a[162:163], v184 offset:43008
  v_mul_f32 v2, v120, v2
  v_mul_f32 v3, v120, v3
  v_mfma_f32_16x16x32_bf16 v[32:35], a[68:71], v[144:147], v[32:35]
  ds_read_b64_tr_b16 a[164:165], v185 offset:40960
  ds_read_b64_tr_b16 a[166:167], v185 offset:43008
  buffer_load_dwordx4 v181, s[52:55], s72 offen offset:1024 lds
  v_cvt_pk_bf16_f32 v0, v0, v1
  v_cvt_pk_bf16_f32 v1, v2, v3
  v_mfma_f32_16x16x32_bf16 v[32:35], a[32:35], v[164:167], v[32:35]
  ds_read_b64_tr_b16 a[168:169], v186 offset:40960
  v_mul_f32 v4, v120, v4
  v_mul_f32 v5, v120, v5
  v_mfma_f32_16x16x32_bf16 v[32:35], a[36:39], v[168:171], v[32:35]
  ds_read_b64_tr_b16 a[170:171], v186 offset:43008
  ds_read_b64_tr_b16 a[172:173], v187 offset:40960
  v_mul_f32 v6, v120, v6
  v_mul_f32 v7, v120, v7
  v_mfma_f32_16x16x32_bf16 v[36:39], a[72:75], v[140:143], v[36:39]
  ds_read_b64_tr_b16 a[174:175], v187 offset:43008
  buffer_load_dwordx4 v182, s[52:55], s72 offen offset:2048 lds
  v_add_f32 v245, v245, v124
  v_rcp_f32 v124, v124
  v_cvt_pk_bf16_f32 v2, v4, v5
  v_cvt_pk_bf16_f32 v3, v6, v7
  v_mfma_f32_16x16x32_bf16 v[36:39], a[76:79], v[144:147], v[36:39]
  v_mfma_f32_16x16x32_bf16 v[36:39], a[40:43], v[164:167], v[36:39]
  v_mfma_f32_16x16x32_bf16 v[36:39], a[44:47], v[168:171], v[36:39]
  buffer_load_dwordx4 v183, s[52:55], s72 offen offset:3072 lds
  v_mfma_f32_16x16x32_bf16 v[128:131], a[240:243], v[172:175], v[128:131]
  v_mfma_f32_16x16x32_bf16 v[128:131], a[240:243], v[176:179], v[128:131]
  v_mul_f32 v32, v124, v32
  v_mul_f32 v33, v124, v33
  s_waitcnt lgkmcnt(0)
  s_add_u32 s72, s93, 18
  s_and_b32 s72, s72, 31
  s_mul_i32 s72, s72, 40960
  s_add_u32 m0, s62, 106496
  v_mfma_f32_16x16x32_bf16 v[64:67], a[64:67], v[148:151], v[64:67]
  buffer_load_dwordx4 v180, s[52:55], s72 offen offset:0 lds
  v_mul_f32 v34, v124, v34
  v_mul_f32 v35, v124, v35
  v_mfma_f32_16x16x32_bf16 v[64:67], a[68:71], v[152:155], v[64:67]
  v_cvt_pk_bf16_f32 v32, v32, v33
  v_cvt_pk_bf16_f32 v33, v34, v35
  v_mfma_f32_16x16x32_bf16 v[64:67], a[32:35], v[172:175], v[64:67]
  v_mul_f32 v36, v124, v36
  v_mul_f32 v37, v124, v37
  v_mfma_f32_16x16x32_bf16 v[64:67], a[36:39], v[176:179], v[64:67]
  buffer_load_dwordx4 v181, s[52:55], s72 offen offset:1024 lds
  v_mul_f32 v38, v124, v38
  v_mul_f32 v39, v124, v39
  v_mfma_f32_16x16x32_bf16 v[68:71], a[72:75], v[148:151], v[68:71]
  v_add_f32 v245, v245, v128
  v_rcp_f32 v128, v128
  v_cvt_pk_bf16_f32 v34, v36, v37
  v_cvt_pk_bf16_f32 v35, v38, v39
  v_mfma_f32_16x16x32_bf16 v[68:71], a[76:79], v[152:155], v[68:71]
  ds_write_b32 v253, v245 offset:0
  v_mfma_f32_16x16x32_bf16 v[68:71], a[40:43], v[172:175], v[68:71]
  buffer_load_dwordx4 v182, s[52:55], s72 offen offset:2048 lds
  v_mfma_f32_16x16x32_bf16 v[68:71], a[44:47], v[176:179], v[68:71]
  v_mfma_f32_16x16x32_bf16 v[8:11], a[80:83], v[132:135], v[8:11]
  s_waitcnt lgkmcnt(0)
  s_barrier
  v_mfma_f32_16x16x32_bf16 v[8:11], a[84:87], v[136:139], v[8:11]
  ds_read_b64_tr_b16 a[64:65], v236 offset:16384
  ds_read_b128 v[108:111], v244 offset:0
  buffer_load_dwordx4 v183, s[52:55], s72 offen offset:3072 lds
  v_mul_f32 v64, v128, v64
  v_mul_f32 v65, v128, v65
  v_mfma_f32_16x16x32_bf16 v[8:11], a[48:51], v[156:159], v[8:11]
  buffer_store_dwordx4 v[0:3], v248, s[48:51], s73 offen offset:0 sc1
  v_mul_f32 v66, v128, v66
  v_mul_f32 v67, v128, v67
  v_mfma_f32_16x16x32_bf16 v[8:11], a[52:55], v[160:163], v[8:11]
  ds_read_b64_tr_b16 a[66:67], v236 offset:20480
  v_cvt_pk_bf16_f32 v64, v64, v65
  v_cvt_pk_bf16_f32 v65, v66, v67
  v_mfma_f32_16x16x32_bf16 v[12:15], a[88:91], v[132:135], v[12:15]
  v_mul_f32 v68, v128, v68
  v_mul_f32 v69, v128, v69
  v_mfma_f32_16x16x32_bf16 v[12:15], a[92:95], v[136:139], v[12:15]
  ds_read_b64_tr_b16 a[68:69], v236 offset:24576
  v_mul_f32 v70, v128, v70
  v_mul_f32 v71, v128, v71
  v_mfma_f32_16x16x32_bf16 v[12:15], a[56:59], v[156:159], v[12:15]
  v_cvt_pk_bf16_f32 v66, v68, v69
  v_cvt_pk_bf16_f32 v67, v70, v71
  v_mfma_f32_16x16x32_bf16 v[12:15], a[60:63], v[160:163], v[12:15]
  ds_read_b64_tr_b16 a[70:71], v236 offset:28672
  v_mfma_f32_16x16x32_bf16 v[40:43], a[80:83], v[140:143], v[40:43]
  buffer_store_dwordx4 v[32:35], v248, s[48:51], s73 offen offset:1024 sc1
  s_waitcnt lgkmcnt(3)
  v_add_f32 v108, v108, v109
  v_add_f32 v110, v110, v111
  v_mfma_f32_16x16x32_bf16 v[40:43], a[84:87], v[144:147], v[40:43]
  ds_read_b64_tr_b16 a[72:73], v237 offset:16384
  v_mul_f32 v8, v120, v8
  v_mul_f32 v9, v120, v9
  v_mfma_f32_16x16x32_bf16 v[40:43], a[48:51], v[164:167], v[40:43]
  v_mul_f32 v10, v120, v10
  v_mul_f32 v11, v120, v11
  v_mfma_f32_16x16x32_bf16 v[40:43], a[52:55], v[168:171], v[40:43]
  ds_read_b64_tr_b16 a[74:75], v237 offset:20480
  v_cvt_pk_bf16_f32 v8, v8, v9
  v_cvt_pk_bf16_f32 v9, v10, v11
  v_mfma_f32_16x16x32_bf16 v[44:47], a[88:91], v[140:143], v[44:47]
  v_mul_f32 v12, v120, v12
  v_mul_f32 v13, v120, v13
  v_mfma_f32_16x16x32_bf16 v[44:47], a[92:95], v[144:147], v[44:47]
  ds_read_b64_tr_b16 a[76:77], v237 offset:24576
  v_mul_f32 v14, v120, v14
  v_mul_f32 v15, v120, v15
  v_mfma_f32_16x16x32_bf16 v[44:47], a[56:59], v[164:167], v[44:47]
  buffer_store_dwordx4 v[64:67], v248, s[48:51], s73 offen offset:2048 sc1
  s_add_u32 s73, s73, 12288
  v_cvt_pk_bf16_f32 v10, v12, v13
  v_cvt_pk_bf16_f32 v11, v14, v15
  v_mfma_f32_16x16x32_bf16 v[44:47], a[60:63], v[168:171], v[44:47]
  ds_read_b64_tr_b16 a[78:79], v237 offset:28672
  v_mfma_f32_16x16x32_bf16 v[72:75], a[80:83], v[148:151], v[72:75]
  v_add_f32 v108, v108, v110
  v_mov_b32 v109, 0x4e800000
  v_mfma_f32_16x16x32_bf16 v[72:75], a[84:87], v[152:155], v[72:75]
  v_mul_f32 v40, v124, v40
  v_mul_f32 v41, v124, v41
  v_mfma_f32_16x16x32_bf16 v[72:75], a[48:51], v[172:175], v[72:75]
  v_mul_f32 v42, v124, v42
  v_mul_f32 v43, v124, v43
  v_mfma_f32_16x16x32_bf16 v[72:75], a[52:55], v[176:179], v[72:75]
  v_cvt_pk_bf16_f32 v40, v40, v41
  v_cvt_pk_bf16_f32 v41, v42, v43
  v_mfma_f32_16x16x32_bf16 v[76:79], a[88:91], v[148:151], v[76:79]
  buffer_store_dwordx4 v[8:11], v248, s[48:51], s73 offen offset:0 sc1
  v_mul_f32 v44, v124, v44
  v_mul_f32 v45, v124, v45
  v_mfma_f32_16x16x32_bf16 v[76:79], a[92:95], v[152:155], v[76:79]
  v_mul_f32 v46, v124, v46
  v_mul_f32 v47, v124, v47
  v_mfma_f32_16x16x32_bf16 v[76:79], a[56:59], v[172:175], v[76:79]
  v_cvt_pk_bf16_f32 v42, v44, v45
  v_cvt_pk_bf16_f32 v43, v46, v47
  v_mfma_f32_16x16x32_bf16 v[76:79], a[60:63], v[176:179], v[76:79]
  v_mfma_f32_16x16x32_bf16 v[16:19], a[96:99], v[132:135], v[16:19]
  v_cmp_nle_f32 vcc, v108, v109
  v_mfma_f32_16x16x32_bf16 v[16:19], a[100:103], v[136:139], v[16:19]
  ds_read_b64_tr_b16 a[80:81], v238 offset:16384
  v_mul_f32 v72, v128, v72
  v_mul_f32 v73, v128, v73
  s_waitcnt lgkmcnt(7)
  v_mfma_f32_16x16x32_bf16 v[16:19], a[64:67], v[156:159], v[16:19]
  buffer_store_dwordx4 v[40:43], v248, s[48:51], s73 offen offset:1024 sc1
  v_mul_f32 v74, v128, v74
  v_mul_f32 v75, v128, v75
  s_waitcnt lgkmcnt(5)
  v_mfma_f32_16x16x32_bf16 v[16:19], a[68:71], v[160:163], v[16:19]
  ds_read_b64_tr_b16 a[82:83], v238 offset:20480
  v_cvt_pk_bf16_f32 v72, v72, v73
  v_cvt_pk_bf16_f32 v73, v74, v75
  v_mfma_f32_16x16x32_bf16 v[20:23], a[104:107], v[132:135], v[20:23]
  v_mul_f32 v76, v128, v76
  v_mul_f32 v77, v128, v77
  v_mfma_f32_16x16x32_bf16 v[20:23], a[108:111], v[136:139], v[20:23]
  ds_read_b64_tr_b16 a[84:85], v238 offset:24576
  v_mul_f32 v78, v128, v78
  v_mul_f32 v79, v128, v79
  s_waitcnt lgkmcnt(5)
  v_mfma_f32_16x16x32_bf16 v[20:23], a[72:75], v[156:159], v[20:23]
  v_cvt_pk_bf16_f32 v74, v76, v77
  v_cvt_pk_bf16_f32 v75, v78, v79
  s_waitcnt lgkmcnt(3)
  v_mfma_f32_16x16x32_bf16 v[20:23], a[76:79], v[160:163], v[20:23]
  ds_read_b64_tr_b16 a[86:87], v238 offset:28672
  v_mfma_f32_16x16x32_bf16 v[48:51], a[96:99], v[140:143], v[48:51]
  buffer_store_dwordx4 v[72:75], v248, s[48:51], s73 offen offset:2048 sc1
  s_add_u32 s73, s73, 12288
  v_mfma_f32_16x16x32_bf16 v[48:51], a[100:103], v[144:147], v[48:51]
  ds_read_b64_tr_b16 a[88:89], v239 offset:16384
  v_mul_f32 v16, v120, v16
  v_mul_f32 v17, v120, v17
  v_mfma_f32_16x16x32_bf16 v[48:51], a[64:67], v[164:167], v[48:51]
  v_mul_f32 v18, v120, v18
  v_mul_f32 v19, v120, v19
  v_mfma_f32_16x16x32_bf16 v[48:51], a[68:71], v[168:171], v[48:51]
  ds_read_b64_tr_b16 a[90:91], v239 offset:20480
  v_cvt_pk_bf16_f32 v16, v16, v17
  v_cvt_pk_bf16_f32 v17, v18, v19
  v_mfma_f32_16x16x32_bf16 v[52:55], a[104:107], v[140:143], v[52:55]
  v_mul_f32 v20, v120, v20
  v_mul_f32 v21, v120, v21
  v_mfma_f32_16x16x32_bf16 v[52:55], a[108:111], v[144:147], v[52:55]
  ds_read_b64_tr_b16 a[92:93], v239 offset:24576
  v_mul_f32 v22, v120, v22
  v_mul_f32 v23, v120, v23
  v_mfma_f32_16x16x32_bf16 v[52:55], a[72:75], v[164:167], v[52:55]
  v_cvt_pk_bf16_f32 v18, v20, v21
  v_cvt_pk_bf16_f32 v19, v22, v23
  v_mfma_f32_16x16x32_bf16 v[52:55], a[76:79], v[168:171], v[52:55]
  ds_read_b64_tr_b16 a[94:95], v239 offset:28672
  buffer_store_dwordx4 v[16:19], v248, s[48:51], s73 offen offset:0 sc1
  v_mfma_f32_16x16x32_bf16 v[80:83], a[96:99], v[148:151], v[80:83]
  v_mfma_f32_16x16x32_bf16 v[80:83], a[100:103], v[152:155], v[80:83]
  v_mul_f32 v48, v124, v48
  v_mul_f32 v49, v124, v49
  v_mfma_f32_16x16x32_bf16 v[80:83], a[64:67], v[172:175], v[80:83]
  v_mul_f32 v50, v124, v50
  v_mul_f32 v51, v124, v51
  v_mfma_f32_16x16x32_bf16 v[80:83], a[68:71], v[176:179], v[80:83]
  v_cvt_pk_bf16_f32 v48, v48, v49
  v_cvt_pk_bf16_f32 v49, v50, v51
  v_mfma_f32_16x16x32_bf16 v[84:87], a[104:107], v[148:151], v[84:87]
  v_mul_f32 v52, v124, v52
  v_mul_f32 v53, v124, v53
  v_mfma_f32_16x16x32_bf16 v[84:87], a[108:111], v[152:155], v[84:87]
  v_mul_f32 v54, v124, v54
  v_mul_f32 v55, v124, v55
  v_mfma_f32_16x16x32_bf16 v[84:87], a[72:75], v[172:175], v[84:87]
  v_cvt_pk_bf16_f32 v50, v52, v53
  v_cvt_pk_bf16_f32 v51, v54, v55
  v_mfma_f32_16x16x32_bf16 v[84:87], a[76:79], v[176:179], v[84:87]
  buffer_store_dwordx4 v[48:51], v248, s[48:51], s73 offen offset:1024 sc1
  v_mfma_f32_16x16x32_bf16 v[24:27], a[112:115], v[132:135], v[24:27]
  v_mfma_f32_16x16x32_bf16 v[24:27], a[116:119], v[136:139], v[24:27]
  v_mul_f32 v80, v128, v80
  v_mul_f32 v81, v128, v81
  s_waitcnt lgkmcnt(6)
  v_mfma_f32_16x16x32_bf16 v[24:27], a[80:83], v[156:159], v[24:27]
  v_mul_f32 v82, v128, v82
  v_mul_f32 v83, v128, v83
  s_waitcnt lgkmcnt(4)
  v_mfma_f32_16x16x32_bf16 v[24:27], a[84:87], v[160:163], v[24:27]
  v_cvt_pk_bf16_f32 v80, v80, v81
  v_cvt_pk_bf16_f32 v81, v82, v83
  v_mfma_f32_16x16x32_bf16 v[28:31], a[120:123], v[132:135], v[28:31]
  v_mul_f32 v84, v128, v84
  v_mul_f32 v85, v128, v85
  v_mfma_f32_16x16x32_bf16 v[28:31], a[124:127], v[136:139], v[28:31]
  v_mul_f32 v86, v128, v86
  v_mul_f32 v87, v128, v87
  s_waitcnt lgkmcnt(2)
  v_mfma_f32_16x16x32_bf16 v[28:31], a[88:91], v[156:159], v[28:31]
  v_cvt_pk_bf16_f32 v82, v84, v85
  v_cvt_pk_bf16_f32 v83, v86, v87
  s_waitcnt lgkmcnt(0)
  v_mfma_f32_16x16x32_bf16 v[28:31], a[92:95], v[160:163], v[28:31]
  buffer_store_dwordx4 v[80:83], v248, s[48:51], s73 offen offset:2048 sc1
  s_add_u32 s73, s73, 12288
  v_mfma_f32_16x16x32_bf16 v[56:59], a[112:115], v[140:143], v[56:59]
  v_mfma_f32_16x16x32_bf16 v[56:59], a[116:119], v[144:147], v[56:59]
  v_mul_f32 v24, v120, v24
  v_mul_f32 v25, v120, v25
  v_mfma_f32_16x16x32_bf16 v[56:59], a[80:83], v[164:167], v[56:59]
  v_mul_f32 v26, v120, v26
  v_mul_f32 v27, v120, v27
  v_mfma_f32_16x16x32_bf16 v[56:59], a[84:87], v[168:171], v[56:59]
  v_cvt_pk_bf16_f32 v24, v24, v25
  v_cvt_pk_bf16_f32 v25, v26, v27
  v_mfma_f32_16x16x32_bf16 v[60:63], a[120:123], v[140:143], v[60:63]
  v_mul_f32 v28, v120, v28
  v_mul_f32 v29, v120, v29
  v_mfma_f32_16x16x32_bf16 v[60:63], a[124:127], v[144:147], v[60:63]
  v_mul_f32 v30, v120, v30
  v_mul_f32 v31, v120, v31
  v_mfma_f32_16x16x32_bf16 v[60:63], a[88:91], v[164:167], v[60:63]
  v_cvt_pk_bf16_f32 v26, v28, v29
  v_cvt_pk_bf16_f32 v27, v30, v31
  v_mfma_f32_16x16x32_bf16 v[60:63], a[92:95], v[168:171], v[60:63]
  buffer_store_dwordx4 v[24:27], v248, s[48:51], s73 offen offset:0 sc1
  v_mfma_f32_16x16x32_bf16 v[88:91], a[112:115], v[148:151], v[88:91]
  v_mfma_f32_16x16x32_bf16 v[88:91], a[116:119], v[152:155], v[88:91]
  v_mul_f32 v56, v124, v56
  v_mul_f32 v57, v124, v57
  v_mfma_f32_16x16x32_bf16 v[88:91], a[80:83], v[172:175], v[88:91]
  v_mul_f32 v58, v124, v58
  v_mul_f32 v59, v124, v59
  s_waitcnt lgkmcnt(0)
  s_barrier
  v_mfma_f32_16x16x32_bf16 v[88:91], a[84:87], v[176:179], v[88:91]
  v_cvt_pk_bf16_f32 v56, v56, v57
  v_cvt_pk_bf16_f32 v57, v58, v59
  v_mfma_f32_16x16x32_bf16 v[92:95], a[120:123], v[148:151], v[92:95]
  v_mul_f32 v60, v124, v60
  v_mul_f32 v61, v124, v61
  v_mfma_f32_16x16x32_bf16 v[92:95], a[124:127], v[152:155], v[92:95]
  v_mul_f32 v62, v124, v62
  v_mul_f32 v63, v124, v63
  v_mfma_f32_16x16x32_bf16 v[92:95], a[88:91], v[172:175], v[92:95]
  v_cvt_pk_bf16_f32 v58, v60, v61
  v_cvt_pk_bf16_f32 v59, v62, v63
  v_mfma_f32_16x16x32_bf16 v[92:95], a[92:95], v[176:179], v[92:95]
  buffer_store_dwordx4 v[56:59], v248, s[48:51], s73 offen offset:1024 sc1
  v_mul_f32 v88, v128, v88
  v_mul_f32 v89, v128, v89
  v_mul_f32 v90, v128, v90
  v_mul_f32 v91, v128, v91
  v_cvt_pk_bf16_f32 v88, v88, v89
  v_cvt_pk_bf16_f32 v89, v90, v91
  s_nop 0
  v_mul_f32 v92, v128, v92
  v_mul_f32 v93, v128, v93
  v_mul_f32 v94, v128, v94
  v_mul_f32 v95, v128, v95
  v_cvt_pk_bf16_f32 v90, v92, v93
  v_cvt_pk_bf16_f32 v91, v94, v95
  buffer_store_dwordx4 v[88:91], v248, s[48:51], s73 offen offset:2048 sc1
  s_cbranch_vccz OKL%=
  s_cmp_lg_u32 s67, 0
  s_cbranch_scc1 OKL%=
  s_mov_b32 s67, 1
  s_waitcnt vmcnt(0) lgkmcnt(0)
  s_barrier
  s_branch RESTART%=
PC0_%=:
  s_sleep 1
  s_memrealtime s[82:83]
  s_waitcnt lgkmcnt(0)
  s_sub_u32 s84, s82, s85
  s_sub_u32 s84, s84, 14
  s_cmp_lt_u32 s84, s94
  s_cbranch_scc1 PC0_%=
  s_branch PC0_%=e
PC1_%=:
  s_sleep 1
  s_memrealtime s[82:83]
  s_waitcnt lgkmcnt(0)
  s_sub_u32 s84, s82, s85
  s_sub_u32 s84, s84, 14
  s_cmp_lt_u32 s84, s94
  s_cbranch_scc1 PC1_%=
  s_branch PC1_%=e
PC2_%=:
  s_sleep 1
  s_memrealtime s[82:83]
  s_waitcnt lgkmcnt(0)
  s_sub_u32 s84, s82, s85
  s_sub_u32 s84, s84, 14
  s_cmp_lt_u32 s84, s94
  s_cbranch_scc1 PC2_%=
  s_branch PC2_%=e
PC3_%=:
  s_sleep 1
  s_memrealtime s[82:83]
  s_waitcnt lgkmcnt(0)
  s_sub_u32 s84, s82, s85
  s_sub_u32 s84, s84, 14
  s_cmp_lt_u32 s84, s94
  s_cbranch_scc1 PC3_%=
  s_branch PC3_%=e
PC4_%=:
  s_sleep 1
  s_memrealtime s[82:83]
  s_waitcnt lgkmcnt(0)
  s_sub_u32 s84, s82, s85
  s_sub_u32 s84, s84, 14
  s_cmp_lt_u32 s84, s94
  s_cbranch_scc1 PC4_%=
  s_branch PC4_%=e
PC5_%=:
  s_sleep 1
  s_memrealtime s[82:83]
  s_waitcnt lgkmcnt(0)
  s_sub_u32 s84, s82, s85
  s_sub_u32 s84, s84, 14
  s_cmp_lt_u32 s84, s94
  s_cbranch_scc1 PC5_%=
  s_branch PC5_%=e
PC6_%=:
  s_sleep 1
  s_memrealtime s[82:83]
  s_waitcnt lgkmcnt(0)
  s_sub_u32 s84, s82, s85
  s_sub_u32 s84, s84, 14
  s_cmp_lt_u32 s84, s94
  s_cbranch_scc1 PC6_%=
  s_branch PC6_%=e
PC7_%=:
  s_sleep 1
  s_memrealtime s[82:83]
  s_waitcnt lgkmcnt(0)
  s_sub_u32 s84, s82, s85
  s_sub_u32 s84, s84, 14
  s_cmp_lt_u32 s84, s94
  s_cbranch_scc1 PC7_%=
  s_branch PC7_%=e
PC8_%=:
  s_sleep 1
  s_memrealtime s[82:83]
  s_waitcnt lgkmcnt(0)
  s_sub_u32 s84, s82, s85
  s_sub_u32 s84, s84, 14
  s_cmp_lt_u32 s84, s94
  s_cbranch_scc1 PC8_%=
  s_branch PC8_%=e
PC9_%=:
  s_sleep 1
  s_memrealtime s[82:83]
  s_waitcnt lgkmcnt(0)
  s_sub_u32 s84, s82, s85
  s_sub_u32 s84, s84, 14
  s_cmp_lt_u32 s84, s94
  s_cbranch_scc1 PC9_%=
  s_branch PC9_%=e
PC10_%=:
  s_sleep 1
  s_memrealtime s[82:83]
  s_waitcnt lgkmcnt(0)
  s_sub_u32 s84, s82, s85
  s_sub_u32 s84, s84, 14
  s_cmp_lt_u32 s84, s94
  s_cbranch_scc1 PC10_%=
  s_branch PC10_%=e
PC11_%=:
  s_sleep 1
  s_memrealtime s[82:83]
  s_waitcnt lgkmcnt(0)
  s_sub_u32 s84, s82, s85
  s_sub_u32 s84, s84, 14
  s_cmp_lt_u32 s84, s94
  s_cbranch_scc1 PC11_%=
  s_branch PC11_%=e
PC12_%=:
  s_sleep 1
  s_memrealtime s[82:83]
  s_waitcnt lgkmcnt(0)
  s_sub_u32 s84, s82, s85
  s_sub_u32 s84, s84, 14
  s_cmp_lt_u32 s84, s94
  s_cbranch_scc1 PC12_%=
  s_branch PC12_%=e
PC13_%=:
  s_sleep 1
  s_memrealtime s[82:83]
  s_waitcnt lgkmcnt(0)
  s_sub_u32 s84, s82, s85
  s_sub_u32 s84, s84, 14
  s_cmp_lt_u32 s84, s94
  s_cbranch_scc1 PC13_%=
  s_branch PC13_%=e
PC14_%=:
  s_sleep 1
  s_memrealtime s[82:83]
  s_waitcnt lgkmcnt(0)
  s_sub_u32 s84, s82, s85
  s_sub_u32 s84, s84, 14
  s_cmp_lt_u32 s84, s94
  s_cbranch_scc1 PC14_%=
  s_branch PC14_%=e
PC15_%=:
  s_sleep 1
  s_memrealtime s[82:83]
  s_waitcnt lgkmcnt(0)
  s_sub_u32 s84, s82, s85
  s_sub_u32 s84, s84, 14
  s_cmp_lt_u32 s84, s94
  s_cbranch_scc1 PC15_%=
  s_branch PC15_%=e
PC16_%=:
  s_sleep 1
  s_memrealtime s[82:83]
  s_waitcnt lgkmcnt(0)
  s_sub_u32 s84, s82, s85
  s_sub_u32 s84, s84, 14
  s_cmp_lt_u32 s84, s94
  s_cbranch_scc1 PC16_%=
  s_branch PC16_%=e
PC17_%=:
  s_sleep 1
  s_memrealtime s[82:83]
  s_waitcnt lgkmcnt(0)
  s_sub_u32 s84, s82, s85
  s_sub_u32 s84, s84, 14
  s_cmp_lt_u32 s84, s94
  s_cbranch_scc1 PC17_%=
  s_branch PC17_%=e
PC18_%=:
  s_sleep 1
  s_memrealtime s[82:83]
  s_waitcnt lgkmcnt(0)
  s_sub_u32 s84, s82, s85
  s_sub_u32 s84, s84, 14
  s_cmp_lt_u32 s84, s94
  s_cbranch_scc1 PC18_%=
  s_branch PC18_%=e
PC19_%=:
  s_sleep 1
  s_memrealtime s[82:83]
  s_waitcnt lgkmcnt(0)
  s_sub_u32 s84, s82, s85
  s_sub_u32 s84, s84, 14
  s_cmp_lt_u32 s84, s94
  s_cbranch_scc1 PC19_%=
  s_branch PC19_%=e
PC20_%=:
  s_sleep 1
  s_memrealtime s[82:83]
  s_waitcnt lgkmcnt(0)
  s_sub_u32 s84, s82, s85
  s_sub_u32 s84, s84, 14
  s_cmp_lt_u32 s84, s94
  s_cbranch_scc1 PC20_%=
  s_branch PC20_%=e
PC21_%=:
  s_sleep 1
  s_memrealtime s[82:83]
  s_waitcnt lgkmcnt(0)
  s_sub_u32 s84, s82, s85
  s_sub_u32 s84, s84, 14
  s_cmp_lt_u32 s84, s94
  s_cbranch_scc1 PC21_%=
  s_branch PC21_%=e
PC22_%=:
  s_sleep 1
  s_memrealtime s[82:83]
  s_waitcnt lgkmcnt(0)
  s_sub_u32 s84, s82, s85
  s_sub_u32 s84, s84, 14
  s_cmp_lt_u32 s84, s94
  s_cbranch_scc1 PC22_%=
  s_branch PC22_%=e
PC23_%=:
  s_sleep 1
  s_memrealtime s[82:83]
  s_waitcnt lgkmcnt(0)
  s_sub_u32 s84, s82, s85
  s_sub_u32 s84, s84, 14
  s_cmp_lt_u32 s84, s94
  s_cbranch_scc1 PC23_%=
  s_branch PC23_%=e
PC24_%=:
  s_sleep 1
  s_memrealtime s[82:83]
  s_waitcnt lgkmcnt(0)
  s_sub_u32 s84, s82, s85
  s_sub_u32 s84, s84, 14
  s_cmp_lt_u32 s84, s94
  s_cbranch_scc1 PC24_%=
  s_branch PC24_%=e
PC25_%=:
  s_sleep 1
  s_memrealtime s[82:83]
  s_waitcnt lgkmcnt(0)
  s_sub_u32 s84, s82, s85
  s_sub_u32 s84, s84, 14
  s_cmp_lt_u32 s84, s94
  s_cbranch_scc1 PC25_%=
  s_branch PC25_%=e
PC26_%=:
  s_sleep 1
  s_memrealtime s[82:83]
  s_waitcnt lgkmcnt(0)
  s_sub_u32 s84, s82, s85
  s_sub_u32 s84, s84, 14
  s_cmp_lt_u32 s84, s94
  s_cbranch_scc1 PC26_%=
  s_branch PC26_%=e
PC27_%=:
  s_sleep 1
  s_memrealtime s[82:83]
  s_waitcnt lgkmcnt(0)
  s_sub_u32 s84, s82, s85
  s_sub_u32 s84, s84, 14
  s_cmp_lt_u32 s84, s94
  s_cbranch_scc1 PC27_%=
  s_branch PC27_%=e
PC28_%=:
  s_sleep 1
  s_memrealtime s[82:83]
  s_waitcnt lgkmcnt(0)
  s_sub_u32 s84, s82, s85
  s_sub_u32 s84, s84, 14
  s_cmp_lt_u32 s84, s94
  s_cbranch_scc1 PC28_%=
  s_branch PC28_%=e
OKL%=:
  s_add_u32 s72, s93, 19
  s_and_b32 s72, s72, 31
  s_mul_i32 s72, s72, 40960
  s_add_u32 m0, s62, 90112
  s_nop 0
  buffer_load_dwordx4 v180, s[52:55], s72 offen offset:0 lds
  buffer_load_dwordx4 v181, s[52:55], s72 offen offset:1024 lds
  buffer_load_dwordx4 v182, s[52:55], s72 offen offset:2048 lds
  buffer_load_dwordx4 v183, s[52:55], s72 offen offset:3072 lds
  s_add_u32 s72, s93, 20
  s_and_b32 s72, s72, 31
  s_mul_i32 s72, s72, 40960
  s_add_u32 m0, s62, 16384
  s_nop 0
  buffer_load_dwordx4 v180, s[52:55], s72 offen offset:0 lds
  buffer_load_dwordx4 v181, s[52:55], s72 offen offset:1024 lds
  buffer_load_dwordx4 v182, s[52:55], s72 offen offset:2048 lds
  buffer_load_dwordx4 v183, s[52:55], s72 offen offset:3072 lds
  s_waitcnt vmcnt(20)
  ds_read_b64_tr_b16 a[32:33], v240 offset:32768
  ds_read_b64_tr_b16 a[34:35], v240 offset:34816
  ds_read_b64_tr_b16 a[36:37], v241 offset:32768
  ds_read_b64_tr_b16 a[38:39], v241 offset:34816
  ds_read_b64_tr_b16 a[40:41], v242 offset:32768
  ds_read_b64_tr_b16 a[42:43], v242 offset:34816
  ds_read_b64_tr_b16 a[44:45], v243 offset:32768
  ds_read_b64_tr_b16 a[46:47], v243 offset:34816
  ds_read_b64_tr_b16 a[48:49], v228 offset:16384
  ds_read_b64_tr_b16 a[50:51], v228 offset:18432
  ds_read_b64_tr_b16 a[52:53], v229 offset:16384
  ds_read_b64_tr_b16 a[54:55], v229 offset:18432
  ds_read_b64_tr_b16 a[56:57], v230 offset:16384
  ds_read_b64_tr_b16 a[58:59], v230 offset:18432
  ds_read_b64_tr_b16 a[60:61], v231 offset:16384
  ds_read_b64_tr_b16 a[62:63], v231 offset:18432
  ds_read_b64_tr_b16 a[64:65], v240 offset:0
  ds_read_b64_tr_b16 a[66:67], v240 offset:2048
  ds_read_b64_tr_b16 a[68:69], v241 offset:0
  ds_read_b64_tr_b16 a[70:71], v241 offset:2048
  ds_read_b64_tr_b16 a[72:73], v242 offset:0
  ds_read_b64_tr_b16 a[74:75], v242 offset:2048
  ds_read_b64_tr_b16 a[76:77], v243 offset:0
  ds_read_b64_tr_b16 a[78:79], v243 offset:2048
  s_waitcnt lgkmcnt(8)
  s_add_u32 s72, s93, 21
  s_and_b32 s72, s72, 31
  s_mul_i32 s72, s72, 40960
  s_add_u32 m0, s62, 32768
  s_nop 0
  buffer_load_dwordx4 v180, s[52:55], s72 offen offset:0 lds
  buffer_load_dwordx4 v181, s[52:55], s72 offen offset:1024 lds
  buffer_load_dwordx4 v182, s[52:55], s72 offen offset:2048 lds
  buffer_load_dwordx4 v183, s[52:55], s72 offen offset:3072 lds
  s_add_u32 s72, s93, 22
  s_and_b32 s72, s72, 31
  s_mul_i32 s72, s72, 40960
  s_add_u32 m0, s62, 139264
  s_nop 0
  buffer_load_dwordx4 v180, s[52:55], s72 offen offset:0 lds
  buffer_load_dwordx4 v181, s[52:55], s72 offen offset:1024 lds
  buffer_load_dwordx4 v182, s[52:55], s72 offen offset:2048 lds
  buffer_load_dwordx4 v183, s[52:55], s72 offen offset:3072 lds
  ds_read_b64_tr_b16 a[80:81], v184 offset:16384
  ds_read_b64_tr_b16 a[82:83], v184 offset:18432
  ds_read_b64_tr_b16 a[84:85], v185 offset:16384
  ds_read_b64_tr_b16 a[86:87], v185 offset:18432
  ds_read_b64_tr_b16 a[88:89], v186 offset:16384
  ds_read_b64_tr_b16 a[90:91], v186 offset:18432
  ds_read_b64_tr_b16 a[92:93], v187 offset:16384
  ds_read_b64_tr_b16 a[94:95], v187 offset:18432
  ds_read_b64_tr_b16 a[96:97], v184 offset:0
  ds_read_b64_tr_b16 a[98:99], v184 offset:2048
  ds_read_b64_tr_b16 a[100:101], v185 offset:0
  ds_read_b64_tr_b16 a[102:103], v185 offset:2048
  ds_read_b64_tr_b16 a[104:105], v186 offset:0
  ds_read_b64_tr_b16 a[106:107], v186 offset:2048
  ds_read_b64_tr_b16 a[108:109], v187 offset:0
  ds_read_b64_tr_b16 a[110:111], v187 offset:2048
  ds_read_b64_tr_b16 a[112:113], v228 offset:0
  ds_read_b64_tr_b16 a[114:115], v228 offset:2048
  ds_read_b64_tr_b16 a[116:117], v229 offset:0
  ds_read_b64_tr_b16 a[118:119], v229 offset:2048
  ds_read_b64_tr_b16 a[120:121], v230 offset:0
  ds_read_b64_tr_b16 a[122:123], v230 offset:2048
  ds_read_b64_tr_b16 a[124:125], v231 offset:0
  ds_read_b64_tr_b16 a[126:127], v231 offset:2048
  ds_read_b64_tr_b16 v[132:133], v184 offset:57344
  ds_read_b64_tr_b16 v[134:135], v184 offset:59392
  ds_read_b64_tr_b16 v[136:137], v185 offset:57344
  ds_read_b64_tr_b16 v[138:139], v185 offset:59392
  ds_read_b64_tr_b16 v[140:141], v186 offset:57344
  ds_read_b64_tr_b16 v[142:143], v186 offset:59392
  ds_read_b64_tr_b16 v[144:145], v187 offset:57344
  ds_read_b64_tr_b16 v[146:147], v187 offset:59392
  s_lshr_b32 s69, s92, 2
  s_and_b32 s70, s92, 3
  s_mov_b64 s[40:41], s[22:23]
  s_mov_b32 s42, 0
  s_mul_i32 s43, s69, 1572864
  s_add_u32 s42, s42, s43
  s_mul_i32 s43, s70, 4096
  s_add_u32 s42, s42, s43
  s_mul_i32 s43, s64, 393216
  s_add_u32 s42, s42, s43
  s_add_u32 s40, s40, s42
  s_addc_u32 s41, s41, 0
  s_and_b32 s41, s41, 0xffff
  s_mov_b32 s42, 385024
  s_mov_b32 s43, 0x20000
  s_waitcnt vmcnt(16) lgkmcnt(0)
  buffer_store_dword v252, v250, s[56:59], 0 offen offset:0 sc1
  s_cmp_lg_u32 s63, 0
  s_cbranch_scc1 FIN%=
  s_add_u32 s72, s93, 23
  s_and_b32 s72, s72, 31
  s_mul_i32 s72, s72, 40960
  s_add_u32 m0, s62, 0
  s_nop 0
  buffer_load_dwordx4 v180, s[52:55], s72 offen offset:0 lds
  buffer_load_dwordx4 v181, s[52:55], s72 offen offset:1024 lds
  buffer_load_dwordx4 v182, s[52:55], s72 offen offset:2048 lds
  buffer_load_dwordx4 v183, s[52:55], s72 offen offset:3072 lds
  s_add_u32 s72, s93, 24
  s_and_b32 s72, s72, 31
  s_mul_i32 s72, s72, 40960
  s_add_u32 m0, s62, 65536
  s_nop 0
  buffer_load_dwordx4 v180, s[52:55], s72 offen offset:0 lds
  buffer_load_dwordx4 v181, s[52:55], s72 offen offset:1024 lds
  buffer_load_dwordx4 v182, s[52:55], s72 offen offset:2048 lds
  buffer_load_dwordx4 v183, s[52:55], s72 offen offset:3072 lds
  s_add_u32 s72, s93, 25
  s_and_b32 s72, s72, 31
  s_mul_i32 s72, s72, 40960
  s_add_u32 m0, s62, 49152
  s_nop 0
  buffer_load_dwordx4 v180, s[52:55], s72 offen offset:0 lds
  buffer_load_dwordx4 v181, s[52:55], s72 offen offset:1024 lds
  buffer_load_dwordx4 v182, s[52:55], s72 offen offset:2048 lds
  buffer_load_dwordx4 v183, s[52:55], s72 offen offset:3072 lds
  s_waitcnt vmcnt(17)
  ds_read_b64_tr_b16 v[148:149], v184 offset:40960
  ds_read_b64_tr_b16 v[150:151], v184 offset:43008
  ds_read_b64_tr_b16 v[152:153], v185 offset:40960
  ds_read_b64_tr_b16 v[154:155], v185 offset:43008
  ds_read_b64_tr_b16 v[156:157], v186 offset:40960
  ds_read_b64_tr_b16 v[158:159], v186 offset:43008
  ds_read_b64_tr_b16 v[160:161], v187 offset:40960
  ds_read_b64_tr_b16 v[162:163], v187 offset:43008
  ds_read_b64_tr_b16 v[164:165], v240 offset:16384
  ds_read_b64_tr_b16 v[166:167], v240 offset:18432
  ds_read_b64_tr_b16 v[168:169], v241 offset:16384
  ds_read_b64_tr_b16 v[170:171], v241 offset:18432
  ds_read_b64_tr_b16 v[172:173], v242 offset:16384
  ds_read_b64_tr_b16 v[174:175], v242 offset:18432
  ds_read_b64_tr_b16 v[176:177], v243 offset:16384
  ds_read_b64_tr_b16 v[178:179], v243 offset:18432
  ds_read_b64_tr_b16 v[96:97], v240 offset:32768
  ds_read_b64_tr_b16 v[98:99], v240 offset:34816
  ds_read_b64_tr_b16 v[100:101], v241 offset:32768
  ds_read_b64_tr_b16 v[102:103], v241 offset:34816
  ds_read_b64_tr_b16 v[104:105], v242 offset:32768
  ds_read_b64_tr_b16 v[106:107], v242 offset:34816
  ds_read_b64_tr_b16 v[108:109], v243 offset:32768
  ds_read_b64_tr_b16 v[110:111], v243 offset:34816
  s_add_u32 s72, s93, 26
  s_and_b32 s72, s72, 31
  s_mul_i32 s72, s72, 40960
  s_add_u32 m0, s62, 122880
  s_nop 0
  buffer_load_dwordx4 v180, s[52:55], s72 offen offset:0 lds
  buffer_load_dwordx4 v181, s[52:55], s72 offen offset:1024 lds
  buffer_load_dwordx4 v182, s[52:55], s72 offen offset:2048 lds
  buffer_load_dwordx4 v183, s[52:55], s72 offen offset:3072 lds
  s_add_u32 s72, s93, 27
  s_and_b32 s72, s72, 31
  s_mul_i32 s72, s72, 40960
  s_add_u32 m0, s62, 106496
  s_nop 0
  buffer_load_dwordx4 v180, s[52:55], s72 offen offset:0 lds
  buffer_load_dwordx4 v181, s[52:55], s72 offen offset:1024 lds
  buffer_load_dwordx4 v182, s[52:55], s72 offen offset:2048 lds
  buffer_load_dwordx4 v183, s[52:55], s72 offen offset:3072 lds
  s_waitcnt lgkmcnt(0)
  s_add_u32 s72, s93, 28
  s_and_b32 s72, s72, 31
  s_mul_i32 s72, s72, 40960
  s_add_u32 m0, s62, 90112
  s_nop 0
  buffer_load_dwordx4 v180, s[52:55], s72 offen offset:0 lds
  buffer_load_dwordx4 v181, s[52:55], s72 offen offset:1024 lds
  buffer_load_dwordx4 v182, s[52:55], s72 offen offset:2048 lds
  buffer_load_dwordx4 v183, s[52:55], s72 offen offset:3072 lds
  buffer_load_dword v251, v249, s[56:59], 0 offen offset:0 sc1
  s_add_u32 s72, s93, 29
  s_and_b32 s72, s72, 31
  s_mul_i32 s72, s72, 40960
  s_add_u32 m0, s62, 16384
  s_nop 0
  buffer_load_dwordx4 v180, s[52:55], s72 offen offset:0 lds
  buffer_load_dwordx4 v181, s[52:55], s72 offen offset:1024 lds
  buffer_load_dwordx4 v182, s[52:55], s72 offen offset:2048 lds
  buffer_load_dwordx4 v183, s[52:55], s72 offen offset:3072 lds
  s_add_u32 s72, s93, 30
  s_and_b32 s72, s72, 31
  s_mul_i32 s72, s72, 40960
  s_add_u32 m0, s62, 32768
  s_nop 0
  buffer_load_dwordx4 v180, s[52:55], s72 offen offset:0 lds
  buffer_load_dwordx4 v181, s[52:55], s72 offen offset:1024 lds
  buffer_load_dwordx4 v182, s[52:55], s72 offen offset:2048 lds
  buffer_load_dwordx4 v183, s[52:55], s72 offen offset:3072 lds
  s_waitcnt vmcnt(8)
  v_cmp_ne_u32 vcc, v251, v247
  s_nop 1
  s_and_b64 vcc, vcc, 0xffff
  s_cbranch_scc0 PO0e%=
  s_mov_b32 s78, 0
PO0%=:
  s_sleep 2
  buffer_load_dword v251, v249, s[56:59], 0 offen offset:0 sc1
  s_waitcnt vmcnt(0)
  v_cmp_ne_u32 vcc, v251, v247
  s_nop 1
  s_and_b64 vcc, vcc, 0xffff
  s_cbranch_scc0 PO0e%=
  s_add_u32 s78, s78, 1
  s_cmp_lt_u32 s78, 20000
  s_cbranch_scc1 PO0%=
PO0e%=:
  s_add_u32 s73, s93, 0
  s_and_b32 s73, s73, 31
  s_mul_i32 s73, s73, 12288
  buffer_load_dwordx4 v[112:115], v248, s[40:43], s73 offen offset:0 sc1
  buffer_load_dwordx4 v[116:119], v248, s[40:43], s73 offen offset:1024 sc1
  buffer_load_dwordx4 v[120:123], v248, s[40:43], s73 offen offset:2048 sc1
  buffer_load_dwordx4 v[124:127], v248, s[40:43], s73 offen offset:3072 sc1
  s_add_u32 s73, s93, 1
  s_and_b32 s73, s73, 31
  s_mul_i32 s73, s73, 12288
  buffer_load_dwordx4 v[128:131], v248, s[40:43], s73 offen offset:0 sc1
  buffer_load_dwordx4 v[224:227], v248, s[40:43], s73 offen offset:1024 sc1
  buffer_load_dwordx4 a[240:243], v248, s[40:43], s73 offen offset:2048 sc1
  buffer_load_dwordx4 v[232:235], v248, s[40:43], s73 offen offset:3072 sc1
  s_add_u32 s73, s93, 2
  s_and_b32 s73, s73, 31
  s_mul_i32 s73, s73, 12288
  buffer_load_dwordx4 v[236:239], v248, s[40:43], s73 offen offset:0 sc1
  buffer_load_dwordx4 v[64:67], v248, s[40:43], s73 offen offset:1024 sc1
  buffer_load_dwordx4 v[68:71], v248, s[40:43], s73 offen offset:2048 sc1
  buffer_load_dwordx4 v[72:75], v248, s[40:43], s73 offen offset:3072 sc1
  s_add_u32 s73, s93, 3
  s_and_b32 s73, s73, 31
  s_mul_i32 s73, s73, 12288
  buffer_load_dwordx4 v[76:79], v248, s[40:43], s73 offen offset:0 sc1
  buffer_load_dwordx4 v[80:83], v248, s[40:43], s73 offen offset:1024 sc1
  buffer_load_dwordx4 v[84:87], v248, s[40:43], s73 offen offset:2048 sc1
  buffer_load_dwordx4 v[88:91], v248, s[40:43], s73 offen offset:3072 sc1
  s_add_u32 s73, s93, 4
  s_and_b32 s73, s73, 31
  s_mul_i32 s73, s73, 12288
  buffer_load_dwordx4 v[92:95], v248, s[40:43], s73 offen offset:0 sc1
  v_lshrrev_b32 v247, 5, v246
  v_lshlrev_b32 v247, 5, v247
  v_xor_b32 v247, v247, v248
  v_add_u32 v253, s62, v247
  v_xor_b32 v249, 64, v253
  v_xor_b32 v251, 128, v253
  v_xor_b32 v252, 192, v253
  v_lshrrev_b32 v245, 3, v246
  v_and_b32 v247, 7, v246
  v_mul_u32_u24 v250, 1280, v245
  v_lshl_add_u32 v250, v247, 4, v250
  v_lshlrev_b32 v244, 1, v247
  v_xor_b32 v244, v244, v245
  v_lshlrev_b32 v244, 4, v244
  v_and_b32 v245, 1, v247
  v_lshl_add_u32 v244, v245, 9, v244
  v_lshrrev_b32 v247, 1, v247
  v_lshl_add_u32 v244, v247, 14, v244
  s_lshl_b32 s68, s64, 10
  v_add_u32 v244, s68, v244
  v_xor_b32 v245, 128, v244
  s_mov_b64 s[48:49], s[24:25]
  s_mov_b32 s50, 0
  s_mul_i32 s51, s31, 81920
  s_add_u32 s50, s50, s51
  s_mul_i32 s51, s79, 128
  s_add_u32 s50, s50, s51
  s_add_u32 s48, s48, s50
  s_addc_u32 s49, s49, 0
  s_and_b32 s49, s49, 0xffff
  s_mov_b32 s50, 80768
  s_mov_b32 s51, 0x20000
  s_mul_i32 s68, s64, 20480
  s_add_u32 s69, s68, 10240
  s_waitcnt vmcnt(9)
  v_mfma_f32_16x16x32_bf16 v[0:3], a[176:179], v[112:115], 0
  v_mfma_f32_16x16x32_bf16 v[0:3], a[192:195], v[128:131], v[0:3]
  v_mfma_f32_16x16x32_bf16 v[4:7], a[176:179], v[116:119], 0
  v_mfma_f32_16x16x32_bf16 v[4:7], a[192:195], v[224:227], v[4:7]
  v_mfma_f32_16x16x32_bf16 v[8:11], a[176:179], v[120:123], 0
  v_mfma_f32_16x16x32_bf16 v[8:11], a[192:195], a[240:243], v[8:11]
  v_mfma_f32_16x16x32_bf16 v[12:15], a[176:179], v[124:127], 0
  v_mfma_f32_16x16x32_bf16 v[12:15], a[192:195], v[232:235], v[12:15]
  v_mfma_f32_16x16x32_bf16 v[16:19], a[180:183], v[112:115], 0
  v_mfma_f32_16x16x32_bf16 v[16:19], a[196:199], v[128:131], v[16:19]
  buffer_load_dwordx4 a[176:179], v248, s[40:43], s73 offen offset:1024 sc1
  v_mfma_f32_16x16x32_bf16 v[20:23], a[180:183], v[116:119], 0
  v_mfma_f32_16x16x32_bf16 v[20:23], a[196:199], v[224:227], v[20:23]
  buffer_load_dwordx4 a[192:195], v248, s[40:43], s73 offen offset:2048 sc1
  v_mfma_f32_16x16x32_bf16 v[24:27], a[180:183], v[120:123], 0
  v_mfma_f32_16x16x32_bf16 v[24:27], a[196:199], a[240:243], v[24:27]
  v_mfma_f32_16x16x32_bf16 v[28:31], a[180:183], v[124:127], 0
  v_mfma_f32_16x16x32_bf16 v[28:31], a[196:199], v[232:235], v[28:31]
  v_mfma_f32_16x16x32_bf16 v[32:35], a[184:187], v[112:115], 0
  v_mfma_f32_16x16x32_bf16 v[32:35], a[200:203], v[128:131], v[32:35]
  buffer_load_dwordx4 a[180:183], v248, s[40:43], s73 offen offset:3072 sc1
  v_mfma_f32_16x16x32_bf16 v[36:39], a[184:187], v[116:119], 0
  v_mfma_f32_16x16x32_bf16 v[36:39], a[200:203], v[224:227], v[36:39]
  s_add_u32 s73, s93, 5
  s_and_b32 s73, s73, 31
  s_mul_i32 s73, s73, 12288
  buffer_load_dwordx4 a[196:199], v248, s[40:43], s73 offen offset:0 sc1
  v_mfma_f32_16x16x32_bf16 v[40:43], a[184:187], v[120:123], 0
  v_mfma_f32_16x16x32_bf16 v[40:43], a[200:203], a[240:243], v[40:43]
  v_mfma_f32_16x16x32_bf16 v[44:47], a[184:187], v[124:127], 0
  v_mfma_f32_16x16x32_bf16 v[44:47], a[200:203], v[232:235], v[44:47]
  v_mfma_f32_16x16x32_bf16 v[48:51], a[188:191], v[112:115], 0
  v_mfma_f32_16x16x32_bf16 v[48:51], a[204:207], v[128:131], v[48:51]
  buffer_load_dwordx4 a[184:187], v248, s[40:43], s73 offen offset:1024 sc1
  v_mfma_f32_16x16x32_bf16 v[52:55], a[188:191], v[116:119], 0
  v_mfma_f32_16x16x32_bf16 v[52:55], a[204:207], v[224:227], v[52:55]
  buffer_load_dwordx4 a[200:203], v248, s[40:43], s73 offen offset:2048 sc1
  v_mfma_f32_16x16x32_bf16 v[56:59], a[188:191], v[120:123], 0
  v_mfma_f32_16x16x32_bf16 v[56:59], a[204:207], a[240:243], v[56:59]
  buffer_load_dwordx4 v[112:115], v248, s[40:43], s73 offen offset:3072 sc1
  v_mfma_f32_16x16x32_bf16 v[60:63], a[188:191], v[124:127], 0
  v_mfma_f32_16x16x32_bf16 v[60:63], a[204:207], v[232:235], v[60:63]
  s_add_u32 s73, s93, 6
  s_and_b32 s73, s73, 31
  s_mul_i32 s73, s73, 12288
  buffer_load_dwordx4 v[128:131], v248, s[40:43], s73 offen offset:0 sc1
  s_nop 0
  s_waitcnt vmcnt(9)
  v_mfma_f32_16x16x32_bf16 v[0:3], a[208:211], v[236:239], v[0:3]
  v_mfma_f32_16x16x32_bf16 v[0:3], a[224:227], v[76:79], v[0:3]
  buffer_load_dwordx4 v[116:119], v248, s[40:43], s73 offen offset:1024 sc1
  v_mfma_f32_16x16x32_bf16 v[4:7], a[208:211], v[64:67], v[4:7]
  v_mfma_f32_16x16x32_bf16 v[4:7], a[224:227], v[80:83], v[4:7]
  buffer_load_dwordx4 v[224:227], v248, s[40:43], s73 offen offset:2048 sc1
  v_mfma_f32_16x16x32_bf16 v[8:11], a[208:211], v[68:71], v[8:11]
  v_mfma_f32_16x16x32_bf16 v[8:11], a[224:227], v[84:87], v[8:11]
  buffer_load_dwordx4 v[120:123], v248, s[40:43], s73 offen offset:3072 sc1
  v_mfma_f32_16x16x32_bf16 v[12:15], a[208:211], v[72:75], v[12:15]
  v_mfma_f32_16x16x32_bf16 v[12:15], a[224:227], v[88:91], v[12:15]
  s_add_u32 s73, s93, 7
  s_and_b32 s73, s73, 31
  s_mul_i32 s73, s73, 12288
  buffer_load_dwordx4 a[240:243], v248, s[40:43], s73 offen offset:0 sc1
  v_mfma_f32_16x16x32_bf16 v[16:19], a[212:215], v[236:239], v[16:19]
  v_mfma_f32_16x16x32_bf16 v[16:19], a[228:231], v[76:79], v[16:19]
  buffer_load_dwordx4 v[124:127], v248, s[40:43], s73 offen offset:1024 sc1
  v_mfma_f32_16x16x32_bf16 v[20:23], a[212:215], v[64:67], v[20:23]
  v_mfma_f32_16x16x32_bf16 v[20:23], a[228:231], v[80:83], v[20:23]
  buffer_load_dwordx4 a[188:191], v248, s[40:43], s73 offen offset:2048 sc1
  v_mfma_f32_16x16x32_bf16 v[24:27], a[212:215], v[68:71], v[24:27]
  v_mfma_f32_16x16x32_bf16 v[24:27], a[228:231], v[84:87], v[24:27]
  buffer_load_dwordx4 v[232:235], v248, s[40:43], s73 offen offset:3072 sc1
  v_mfma_f32_16x16x32_bf16 v[28:31], a[212:215], v[72:75], v[28:31]
  v_mfma_f32_16x16x32_bf16 v[28:31], a[228:231], v[88:91], v[28:31]
  s_add_u32 s73, s93, 8
  s_and_b32 s73, s73, 31
  s_mul_i32 s73, s73, 12288
  buffer_load_dwordx4 a[204:207], v248, s[40:43], s73 offen offset:0 sc1
  v_mfma_f32_16x16x32_bf16 v[32:35], a[216:219], v[236:239], v[32:35]
  v_mfma_f32_16x16x32_bf16 v[32:35], a[232:235], v[76:79], v[32:35]
  buffer_load_dwordx4 a[208:211], v248, s[40:43], s73 offen offset:1024 sc1
  v_mfma_f32_16x16x32_bf16 v[36:39], a[216:219], v[64:67], v[36:39]
  v_mfma_f32_16x16x32_bf16 v[36:39], a[232:235], v[80:83], v[36:39]
  buffer_load_dwordx4 a[224:227], v248, s[40:43], s73 offen offset:2048 sc1
  v_mfma_f32_16x16x32_bf16 v[40:43], a[216:219], v[68:71], v[40:43]
  v_mfma_f32_16x16x32_bf16 v[40:43], a[232:235], v[84:87], v[40:43]
  buffer_load_dwordx4 a[212:215], v248, s[40:43], s73 offen offset:3072 sc1
  v_mfma_f32_16x16x32_bf16 v[44:47], a[216:219], v[72:75], v[44:47]
  v_mfma_f32_16x16x32_bf16 v[44:47], a[232:235], v[88:91], v[44:47]
  s_add_u32 s73, s93, 9
  s_and_b32 s73, s73, 31
  s_mul_i32 s73, s73, 12288
  buffer_load_dwordx4 a[228:231], v248, s[40:43], s73 offen offset:0 sc1
  v_mfma_f32_16x16x32_bf16 v[48:51], a[220:223], v[236:239], v[48:51]
  v_mfma_f32_16x16x32_bf16 v[48:51], a[236:239], v[76:79], v[48:51]
  buffer_load_dwordx4 a[216:219], v248, s[40:43], s73 offen offset:1024 sc1
  v_mfma_f32_16x16x32_bf16 v[52:55], a[220:223], v[64:67], v[52:55]
  v_mfma_f32_16x16x32_bf16 v[52:55], a[236:239], v[80:83], v[52:55]
  v_mfma_f32_16x16x32_bf16 v[56:59], a[220:223], v[68:71], v[56:59]
  v_mfma_f32_16x16x32_bf16 v[56:59], a[236:239], v[84:87], v[56:59]
  v_mfma_f32_16x16x32_bf16 v[60:63], a[220:223], v[72:75], v[60:63]
  v_mfma_f32_16x16x32_bf16 v[60:63], a[236:239], v[88:91], v[60:63]
  s_nop 0
  s_waitcnt vmcnt(14)
  v_mfma_f32_16x16x32_bf16 v[0:3], a[244:247], v[92:95], v[0:3]
  v_mfma_f32_16x16x32_bf16 v[0:3], v[192:195], a[196:199], v[0:3]
  buffer_load_dwordx4 a[232:235], v248, s[40:43], s73 offen offset:2048 sc1
  v_mfma_f32_16x16x32_bf16 v[4:7], a[244:247], a[176:179], v[4:7]
  v_mfma_f32_16x16x32_bf16 v[4:7], v[192:195], a[184:187], v[4:7]
  buffer_load_dwordx4 v[236:239], v248, s[40:43], s73 offen offset:3072 sc1
  v_mfma_f32_16x16x32_bf16 v[8:11], a[244:247], a[192:195], v[8:11]
  v_mfma_f32_16x16x32_bf16 v[8:11], v[192:195], a[200:203], v[8:11]
  s_add_u32 s73, s93, 10
  s_and_b32 s73, s73, 31
  s_mul_i32 s73, s73, 12288
  buffer_load_dwordx4 v[76:79], v248, s[40:43], s73 offen offset:0 sc1
  v_mfma_f32_16x16x32_bf16 v[12:15], a[244:247], a[180:183], v[12:15]
  v_mfma_f32_16x16x32_bf16 v[12:15], v[192:195], v[112:115], v[12:15]
  buffer_load_dwordx4 v[64:67], v248, s[40:43], s73 offen offset:1024 sc1
  v_mfma_f32_16x16x32_bf16 v[16:19], a[248:251], v[92:95], v[16:19]
  s_waitcnt vmcnt(62)
  v_mfma_f32_16x16x32_bf16 v[16:19], v[196:199], a[196:199], v[16:19]
  ds_read_b64_tr_b16 v[80:81], v228 offset:16384
  v_mfma_f32_16x16x32_bf16 v[20:23], a[248:251], a[176:179], v[20:23]
  ds_read_b64_tr_b16 v[82:83], v228 offset:18432
  v_mfma_f32_16x16x32_bf16 v[20:23], v[196:199], a[184:187], v[20:23]
  ds_read_b64_tr_b16 v[68:69], v229 offset:16384
  v_mfma_f32_16x16x32_bf16 v[24:27], a[248:251], a[192:195], v[24:27]
  ds_read_b64_tr_b16 v[70:71], v229 offset:18432
  v_mfma_f32_16x16x32_bf16 v[24:27], v[196:199], a[200:203], v[24:27]
  ds_read_b64_tr_b16 v[84:85], v230 offset:16384
  v_mfma_f32_16x16x32_bf16 v[28:31], a[248:251], a[180:183], v[28:31]
  ds_read_b64_tr_b16 v[86:87], v230 offset:18432
  v_mfma_f32_16x16x32_bf16 v[28:31], v[196:199], v[112:115], v[28:31]
  ds_read_b64_tr_b16 v[72:73], v231 offset:16384
  v_mfma_f32_16x16x32_bf16 v[32:35], a[252:255], v[92:95], v[32:35]
  ds_read_b64_tr_b16 v[74:75], v231 offset:18432
  v_mfma_f32_16x16x32_bf16 v[32:35], v[200:203], a[196:199], v[32:35]
  buffer_load_dwordx4 a[220:223], v248, s[40:43], s73 offen offset:2048 sc1
  v_mfma_f32_16x16x32_bf16 v[36:39], a[252:255], a[176:179], v[36:39]
  v_mfma_f32_16x16x32_bf16 v[36:39], v[200:203], a[184:187], v[36:39]
  buffer_load_dwordx4 v[88:91], v248, s[40:43], s73 offen offset:3072 sc1
  v_mfma_f32_16x16x32_bf16 v[40:43], a[252:255], a[192:195], v[40:43]
  v_mfma_f32_16x16x32_bf16 v[40:43], v[200:203], a[200:203], v[40:43]
  s_add_u32 s73, s93, 11
  s_and_b32 s73, s73, 31
  s_mul_i32 s73, s73, 12288
  buffer_load_dwordx4 a[236:239], v248, s[40:43], s73 offen offset:0 sc1
  v_mfma_f32_16x16x32_bf16 v[44:47], a[252:255], a[180:183], v[44:47]
  v_mfma_f32_16x16x32_bf16 v[44:47], v[200:203], v[112:115], v[44:47]
  buffer_load_dwordx4 a[244:247], v248, s[40:43], s73 offen offset:1024 sc1
  v_mfma_f32_16x16x32_bf16 v[48:51], v[188:191], v[92:95], v[48:51]
  v_mfma_f32_16x16x32_bf16 v[48:51], v[204:207], a[196:199], v[48:51]
  v_mfma_f32_16x16x32_bf16 v[52:55], v[188:191], a[176:179], v[52:55]
  v_mfma_f32_16x16x32_bf16 v[52:55], v[204:207], a[184:187], v[52:55]
  v_mfma_f32_16x16x32_bf16 v[56:59], v[188:191], a[192:195], v[56:59]
  v_mfma_f32_16x16x32_bf16 v[56:59], v[204:207], a[200:203], v[56:59]
  v_mfma_f32_16x16x32_bf16 v[60:63], v[188:191], a[180:183], v[60:63]
  v_mfma_f32_16x16x32_bf16 v[60:63], v[204:207], v[112:115], v[60:63]
  s_nop 0
  s_waitcnt vmcnt(14) lgkmcnt(0)
  v_mfma_f32_16x16x32_bf16 v[0:3], v[208:211], v[128:131], v[0:3]
  v_mfma_f32_16x16x32_bf16 v[0:3], a[0:3], a[240:243], v[0:3]
  buffer_load_dwordx4 v[192:195], v248, s[40:43], s73 offen offset:2048 sc1
  v_mfma_f32_16x16x32_bf16 v[4:7], v[208:211], v[116:119], v[4:7]
  s_add_u32 s72, s93, 31
  s_and_b32 s72, s72, 31
  s_mul_i32 s72, s72, 40960
  s_add_u32 m0, s62, 139264
  s_nop 0
  buffer_load_dwordx4 v180, s[52:55], s72 offen offset:0 lds
  v_mfma_f32_16x16x32_bf16 v[4:7], a[0:3], v[124:127], v[4:7]
  buffer_load_dwordx4 a[248:251], v248, s[40:43], s73 offen offset:3072 sc1
  v_mfma_f32_16x16x32_bf16 v[8:11], v[208:211], v[224:227], v[8:11]
  v_mfma_f32_16x16x32_bf16 v[8:11], a[0:3], a[188:191], v[8:11]
  s_add_u32 s73, s93, 12
  s_and_b32 s73, s73, 31
  s_mul_i32 s73, s73, 12288
  buffer_load_dwordx4 v[196:199], v248, s[40:43], s73 offen offset:0 sc1
  v_mfma_f32_16x16x32_bf16 v[12:15], v[208:211], v[120:123], v[12:15]
  v_mfma_f32_16x16x32_bf16 v[12:15], a[0:3], v[232:235], v[12:15]
  buffer_load_dwordx4 a[252:255], v248, s[40:43], s73 offen offset:1024 sc1
  v_mfma_f32_16x16x32_bf16 v[16:19], v[212:215], v[128:131], v[16:19]
  v_mfma_f32_16x16x32_bf16 v[16:19], a[4:7], a[240:243], v[16:19]
  buffer_load_dwordx4 v[200:203], v248, s[40:43], s73 offen offset:2048 sc1
  v_mfma_f32_16x16x32_bf16 v[20:23], v[212:215], v[116:119], v[20:23]
  buffer_load_dwordx4 v181, s[52:55], s72 offen offset:1024 lds
  v_mfma_f32_16x16x32_bf16 v[20:23], a[4:7], v[124:127], v[20:23]
  buffer_load_dwordx4 v[92:95], v248, s[40:43], s73 offen offset:3072 sc1
  v_mfma_f32_16x16x32_bf16 v[24:27], v[212:215], v[224:227], v[24:27]
  v_mfma_f32_16x16x32_bf16 v[24:27], a[4:7], a[188:191], v[24:27]
  s_add_u32 s73, s93, 13
  s_and_b32 s73, s73, 31
  s_mul_i32 s73, s73, 12288
  buffer_load_dwordx4 a[196:199], v248, s[40:43], s73 offen offset:0 sc1
  v_mfma_f32_16x16x32_bf16 v[28:31], v[212:215], v[120:123], v[28:31]
  v_mfma_f32_16x16x32_bf16 v[28:31], a[4:7], v[232:235], v[28:31]
  buffer_load_dwordx4 a[176:179], v248, s[40:43], s73 offen offset:1024 sc1
  v_mfma_f32_16x16x32_bf16 v[32:35], v[216:219], v[128:131], v[32:35]
  v_mfma_f32_16x16x32_bf16 v[32:35], a[8:11], a[240:243], v[32:35]
  v_mfma_f32_16x16x32_bf16 v[36:39], v[216:219], v[116:119], v[36:39]
  buffer_load_dwordx4 v182, s[52:55], s72 offen offset:2048 lds
  v_mfma_f32_16x16x32_bf16 v[36:39], a[8:11], v[124:127], v[36:39]
  v_mfma_f32_16x16x32_bf16 v[40:43], v[216:219], v[224:227], v[40:43]
  v_mfma_f32_16x16x32_bf16 v[40:43], a[8:11], a[188:191], v[40:43]
  v_mfma_f32_16x16x32_bf16 v[44:47], v[216:219], v[120:123], v[44:47]
  v_mfma_f32_16x16x32_bf16 v[44:47], a[8:11], v[232:235], v[44:47]
  v_mfma_f32_16x16x32_bf16 v[48:51], v[220:223], v[128:131], v[48:51]
  v_mfma_f32_16x16x32_bf16 v[48:51], a[12:15], a[240:243], v[48:51]
  v_mfma_f32_16x16x32_bf16 v[52:55], v[220:223], v[116:119], v[52:55]
  buffer_load_dwordx4 v183, s[52:55], s72 offen offset:3072 lds
  v_mfma_f32_16x16x32_bf16 v[52:55], a[12:15], v[124:127], v[52:55]
  v_mfma_f32_16x16x32_bf16 v[56:59], v[220:223], v[224:227], v[56:59]
  v_mfma_f32_16x16x32_bf16 v[56:59], a[12:15], a[188:191], v[56:59]
  v_mfma_f32_16x16x32_bf16 v[60:63], v[220:223], v[120:123], v[60:63]
  v_mfma_f32_16x16x32_bf16 v[60:63], a[12:15], v[232:235], v[60:63]
  s_nop 0
  s_waitcnt vmcnt(18)
  v_mfma_f32_16x16x32_bf16 v[0:3], a[16:19], a[204:207], v[0:3]
  v_mfma_f32_16x16x32_bf16 v[0:3], a[128:131], a[228:231], v[0:3]
  buffer_load_dwordx4 a[184:187], v248, s[40:43], s73 offen offset:2048 sc1
  v_mfma_f32_16x16x32_bf16 v[4:7], a[16:19], a[208:211], v[4:7]
  v_mfma_f32_16x16x32_bf16 v[4:7], a[128:131], a[216:219], v[4:7]
  buffer_load_dwordx4 a[192:195], v248, s[40:43], s73 offen offset:3072 sc1
  v_mfma_f32_16x16x32_bf16 v[8:11], a[16:19], a[224:227], v[8:11]
  v_mfma_f32_16x16x32_bf16 v[8:11], a[128:131], a[232:235], v[8:11]
  s_add_u32 s73, s93, 14
  s_and_b32 s73, s73, 31
  s_mul_i32 s73, s73, 12288
  buffer_load_dwordx4 a[200:203], v248, s[40:43], s73 offen offset:0 sc1
  v_mfma_f32_16x16x32_bf16 v[12:15], a[16:19], a[212:215], v[12:15]
  v_mfma_f32_16x16x32_bf16 v[12:15], a[128:131], v[236:239], v[12:15]
  buffer_load_dwordx4 a[180:183], v248, s[40:43], s73 offen offset:1024 sc1
  v_mfma_f32_16x16x32_bf16 v[16:19], a[20:23], a[204:207], v[16:19]
  v_mfma_f32_16x16x32_bf16 v[16:19], a[132:135], a[228:231], v[16:19]
  buffer_load_dwordx4 v[188:191], v248, s[40:43], s73 offen offset:2048 sc1
  v_mfma_f32_16x16x32_bf16 v[20:23], a[20:23], a[208:211], v[20:23]
  v_mfma_f32_16x16x32_bf16 v[20:23], a[132:135], a[216:219], v[20:23]
  buffer_load_dwordx4 v[112:115], v248, s[40:43], s73 offen offset:3072 sc1
  v_mfma_f32_16x16x32_bf16 v[24:27], a[20:23], a[224:227], v[24:27]
  v_mfma_f32_16x16x32_bf16 v[24:27], a[132:135], a[232:235], v[24:27]
  s_add_u32 s73, s93, 15
  s_and_b32 s73, s73, 31
  s_mul_i32 s73, s73, 12288
  buffer_load_dwordx4 v[204:207], v248, s[40:43], s73 offen offset:0 sc1
  v_mfma_f32_16x16x32_bf16 v[28:31], a[20:23], a[212:215], v[28:31]
  v_mfma_f32_16x16x32_bf16 v[28:31], a[132:135], v[236:239], v[28:31]
  buffer_load_dwordx4 v[208:211], v248, s[40:43], s73 offen offset:1024 sc1
  v_mfma_f32_16x16x32_bf16 v[32:35], a[24:27], a[204:207], v[32:35]
  v_mfma_f32_16x16x32_bf16 v[32:35], a[136:139], a[228:231], v[32:35]
  v_mfma_f32_16x16x32_bf16 v[36:39], a[24:27], a[208:211], v[36:39]
  v_mfma_f32_16x16x32_bf16 v[36:39], a[136:139], a[216:219], v[36:39]
  v_mfma_f32_16x16x32_bf16 v[40:43], a[24:27], a[224:227], v[40:43]
  v_mfma_f32_16x16x32_bf16 v[40:43], a[136:139], a[232:235], v[40:43]
  v_mfma_f32_16x16x32_bf16 v[44:47], a[24:27], a[212:215], v[44:47]
  v_mfma_f32_16x16x32_bf16 v[44:47], a[136:139], v[236:239], v[44:47]
  v_mfma_f32_16x16x32_bf16 v[48:51], a[28:31], a[204:207], v[48:51]
  v_mfma_f32_16x16x32_bf16 v[48:51], a[140:143], a[228:231], v[48:51]
  v_mfma_f32_16x16x32_bf16 v[52:55], a[28:31], a[208:211], v[52:55]
  v_mfma_f32_16x16x32_bf16 v[52:55], a[140:143], a[216:219], v[52:55]
  v_mfma_f32_16x16x32_bf16 v[56:59], a[28:31], a[224:227], v[56:59]
  v_mfma_f32_16x16x32_bf16 v[56:59], a[140:143], a[232:235], v[56:59]
  v_mfma_f32_16x16x32_bf16 v[60:63], a[28:31], a[212:215], v[60:63]
  v_mfma_f32_16x16x32_bf16 v[60:63], a[140:143], v[236:239], v[60:63]
  s_nop 0
  s_waitcnt vmcnt(17)
  v_mfma_f32_16x16x32_bf16 v[0:3], a[144:147], v[76:79], v[0:3]
  v_mfma_f32_16x16x32_bf16 v[0:3], a[160:163], a[236:239], v[0:3]
  buffer_load_dwordx4 a[0:3], v248, s[40:43], s73 offen offset:2048 sc1
  v_mfma_f32_16x16x32_bf16 v[4:7], a[144:147], v[64:67], v[4:7]
  v_mfma_f32_16x16x32_bf16 v[4:7], a[160:163], a[244:247], v[4:7]
  buffer_load_dwordx4 v[212:215], v248, s[40:43], s73 offen offset:3072 sc1
  v_mfma_f32_16x16x32_bf16 v[8:11], a[144:147], a[220:223], v[8:11]
  v_mfma_f32_16x16x32_bf16 v[8:11], a[160:163], v[192:195], v[8:11]
  s_add_u32 s73, s93, 16
  s_and_b32 s73, s73, 31
  s_mul_i32 s73, s73, 12288
  buffer_load_dwordx4 a[4:7], v248, s[40:43], s73 offen offset:0 sc1
  v_mfma_f32_16x16x32_bf16 v[12:15], a[144:147], v[88:91], v[12:15]
  v_mfma_f32_16x16x32_bf16 v[12:15], a[160:163], a[248:251], v[12:15]
  buffer_load_dwordx4 v[216:219], v248, s[40:43], s73 offen offset:1024 sc1
  v_mfma_f32_16x16x32_bf16 v[16:19], a[148:151], v[76:79], v[16:19]
  v_mfma_f32_16x16x32_bf16 v[16:19], a[164:167], a[236:239], v[16:19]
  buffer_load_dwordx4 a[8:11], v248, s[40:43], s73 offen offset:2048 sc1
  v_mfma_f32_16x16x32_bf16 v[20:23], a[148:151], v[64:67], v[20:23]
  v_mfma_f32_16x16x32_bf16 v[20:23], a[164:167], a[244:247], v[20:23]
  buffer_load_dwordx4 v[128:131], v248, s[40:43], s73 offen offset:3072 sc1
  v_mfma_f32_16x16x32_bf16 v[24:27], a[148:151], a[220:223], v[24:27]
  v_mfma_f32_16x16x32_bf16 v[24:27], a[164:167], v[192:195], v[24:27]
  s_add_u32 s73, s93, 17
  s_and_b32 s73, s73, 31
  s_mul_i32 s73, s73, 12288
  buffer_load_dwordx4 a[240:243], v248, s[40:43], s73 offen offset:0 sc1
  v_mfma_f32_16x16x32_bf16 v[28:31], a[148:151], v[88:91], v[28:31]
  v_mfma_f32_16x16x32_bf16 v[28:31], a[164:167], a[248:251], v[28:31]
  buffer_load_dwordx4 v[116:119], v248, s[40:43], s73 offen offset:1024 sc1
  v_mfma_f32_16x16x32_bf16 v[32:35], a[152:155], v[76:79], v[32:35]
  v_mfma_f32_16x16x32_bf16 v[32:35], a[168:171], a[236:239], v[32:35]
  v_mfma_f32_16x16x32_bf16 v[36:39], a[152:155], v[64:67], v[36:39]
  v_mfma_f32_16x16x32_bf16 v[36:39], a[168:171], a[244:247], v[36:39]
  v_mfma_f32_16x16x32_bf16 v[40:43], a[152:155], a[220:223], v[40:43]
  v_mfma_f32_16x16x32_bf16 v[40:43], a[168:171], v[192:195], v[40:43]
  v_mfma_f32_16x16x32_bf16 v[44:47], a[152:155], v[88:91], v[44:47]
  v_mfma_f32_16x16x32_bf16 v[44:47], a[168:171], a[248:251], v[44:47]
  v_mfma_f32_16x16x32_bf16 v[48:51], a[156:159], v[76:79], v[48:51]
  v_mfma_f32_16x16x32_bf16 v[48:51], a[172:175], a[236:239], v[48:51]
  v_mfma_f32_16x16x32_bf16 v[52:55], a[156:159], v[64:67], v[52:55]
  v_mfma_f32_16x16x32_bf16 v[52:55], a[172:175], a[244:247], v[52:55]
  v_mfma_f32_16x16x32_bf16 v[56:59], a[156:159], a[220:223], v[56:59]
  v_mfma_f32_16x16x32_bf16 v[56:59], a[172:175], v[192:195], v[56:59]
  v_mfma_f32_16x16x32_bf16 v[60:63], a[156:159], v[88:91], v[60:63]
  v_mfma_f32_16x16x32_bf16 v[60:63], a[172:175], a[248:251], v[60:63]
  s_nop 0
  s_waitcnt vmcnt(14)
  v_mfma_f32_16x16x32_bf16 v[0:3], a[32:35], v[196:199], v[0:3]
  v_mfma_f32_16x16x32_bf16 v[0:3], a[48:51], a[196:199], v[0:3]
  buffer_load_dwordx4 v[124:127], v248, s[40:43], s73 offen offset:2048 sc1
  v_mfma_f32_16x16x32_bf16 v[4:7], a[32:35], a[252:255], v[4:7]
  v_mfma_f32_16x16x32_bf16 v[4:7], a[48:51], a[176:179], v[4:7]
  buffer_load_dwordx4 v[224:227], v248, s[40:43], s73 offen offset:3072 sc1
  v_mfma_f32_16x16x32_bf16 v[8:11], a[32:35], v[200:203], v[8:11]
  v_mfma_f32_16x16x32_bf16 v[8:11], a[48:51], a[184:187], v[8:11]
  s_add_u32 s73, s93, 18
  s_and_b32 s73, s73, 31
  s_mul_i32 s73, s73, 12288
  buffer_load_dwordx4 a[188:191], v248, s[40:43], s73 offen offset:0 sc1
  v_mfma_f32_16x16x32_bf16 v[12:15], a[32:35], v[92:95], v[12:15]
  v_mfma_f32_16x16x32_bf16 v[12:15], a[48:51], a[192:195], v[12:15]
  buffer_load_dwordx4 v[120:123], v248, s[40:43], s73 offen offset:1024 sc1
  v_mfma_f32_16x16x32_bf16 v[16:19], a[36:39], v[196:199], v[16:19]
  v_mfma_f32_16x16x32_bf16 v[16:19], a[52:55], a[196:199], v[16:19]
  buffer_load_dwordx4 v[220:223], v248, s[40:43], s73 offen offset:2048 sc1
  v_mfma_f32_16x16x32_bf16 v[20:23], a[36:39], a[252:255], v[20:23]
  v_mfma_f32_16x16x32_bf16 v[20:23], a[52:55], a[176:179], v[20:23]
  buffer_load_dwordx4 v[232:235], v248, s[40:43], s73 offen offset:3072 sc1
  v_mfma_f32_16x16x32_bf16 v[24:27], a[36:39], v[200:203], v[24:27]
  v_mfma_f32_16x16x32_bf16 v[24:27], a[52:55], a[184:187], v[24:27]
  s_add_u32 s73, s93, 19
  s_and_b32 s73, s73, 31
  s_mul_i32 s73, s73, 12288
  buffer_load_dwordx4 a[12:15], v248, s[40:43], s73 offen offset:0 sc1
  v_mfma_f32_16x16x32_bf16 v[28:31], a[36:39], v[92:95], v[28:31]
  v_mfma_f32_16x16x32_bf16 v[28:31], a[52:55], a[192:195], v[28:31]
  buffer_load_dwordx4 a[16:19], v248, s[40:43], s73 offen offset:1024 sc1
  v_mfma_f32_16x16x32_bf16 v[32:35], a[40:43], v[196:199], v[32:35]
  v_mfma_f32_16x16x32_bf16 v[32:35], a[56:59], a[196:199], v[32:35]
  v_mfma_f32_16x16x32_bf16 v[36:39], a[40:43], a[252:255], v[36:39]
  v_mfma_f32_16x16x32_bf16 v[36:39], a[56:59], a[176:179], v[36:39]
  v_mfma_f32_16x16x32_bf16 v[40:43], a[40:43], v[200:203], v[40:43]
  v_mfma_f32_16x16x32_bf16 v[40:43], a[56:59], a[184:187], v[40:43]
  v_mfma_f32_16x16x32_bf16 v[44:47], a[40:43], v[92:95], v[44:47]
  v_mfma_f32_16x16x32_bf16 v[44:47], a[56:59], a[192:195], v[44:47]
  v_mfma_f32_16x16x32_bf16 v[48:51], a[44:47], v[196:199], v[48:51]
  v_mfma_f32_16x16x32_bf16 v[48:51], a[60:63], a[196:199], v[48:51]
  v_mfma_f32_16x16x32_bf16 v[52:55], a[44:47], a[252:255], v[52:55]
  v_mfma_f32_16x16x32_bf16 v[52:55], a[60:63], a[176:179], v[52:55]
  v_mfma_f32_16x16x32_bf16 v[56:59], a[44:47], v[200:203], v[56:59]
  v_mfma_f32_16x16x32_bf16 v[56:59], a[60:63], a[184:187], v[56:59]
  v_mfma_f32_16x16x32_bf16 v[60:63], a[44:47], v[92:95], v[60:63]
  v_mfma_f32_16x16x32_bf16 v[60:63], a[60:63], a[192:195], v[60:63]
  s_nop 0
  s_waitcnt vmcnt(14)
  v_mfma_f32_16x16x32_bf16 v[0:3], a[64:67], a[200:203], v[0:3]
  v_mfma_f32_16x16x32_bf16 v[0:3], a[80:83], v[204:207], v[0:3]
  buffer_load_dwordx4 a[128:131], v248, s[40:43], s73 offen offset:2048 sc1
  v_mfma_f32_16x16x32_bf16 v[4:7], a[64:67], a[180:183], v[4:7]
  v_mfma_f32_16x16x32_bf16 v[4:7], a[80:83], v[208:211], v[4:7]
  buffer_load_dwordx4 a[20:23], v248, s[40:43], s73 offen offset:3072 sc1
  v_mfma_f32_16x16x32_bf16 v[8:11], a[64:67], v[188:191], v[8:11]
  v_mfma_f32_16x16x32_bf16 v[8:11], a[80:83], a[0:3], v[8:11]
  s_add_u32 s73, s93, 20
  s_and_b32 s73, s73, 31
  s_mul_i32 s73, s73, 12288
  buffer_load_dwordx4 a[132:135], v248, s[40:43], s73 offen offset:0 sc1
  v_mfma_f32_16x16x32_bf16 v[12:15], a[64:67], v[112:115], v[12:15]
  v_mfma_f32_16x16x32_bf16 v[12:15], a[80:83], v[212:215], v[12:15]
  buffer_load_dwordx4 a[24:27], v248, s[40:43], s73 offen offset:1024 sc1
  v_mfma_f32_16x16x32_bf16 v[16:19], a[68:71], a[200:203], v[16:19]
  v_mfma_f32_16x16x32_bf16 v[16:19], a[84:87], v[204:207], v[16:19]
  buffer_load_dwordx4 a[136:139], v248, s[40:43], s73 offen offset:2048 sc1
  v_mfma_f32_16x16x32_bf16 v[20:23], a[68:71], a[180:183], v[20:23]
  v_mfma_f32_16x16x32_bf16 v[20:23], a[84:87], v[208:211], v[20:23]
  buffer_load_dwordx4 a[204:207], v248, s[40:43], s73 offen offset:3072 sc1
  v_mfma_f32_16x16x32_bf16 v[24:27], a[68:71], v[188:191], v[24:27]
  v_mfma_f32_16x16x32_bf16 v[24:27], a[84:87], a[0:3], v[24:27]
  s_add_u32 s73, s93, 21
  s_and_b32 s73, s73, 31
  s_mul_i32 s73, s73, 12288
  buffer_load_dwordx4 a[228:231], v248, s[40:43], s73 offen offset:0 sc1
  v_mfma_f32_16x16x32_bf16 v[28:31], a[68:71], v[112:115], v[28:31]
  v_mfma_f32_16x16x32_bf16 v[28:31], a[84:87], v[212:215], v[28:31]
  buffer_load_dwordx4 a[208:211], v248, s[40:43], s73 offen offset:1024 sc1
  v_mfma_f32_16x16x32_bf16 v[32:35], a[72:75], a[200:203], v[32:35]
  v_mfma_f32_16x16x32_bf16 v[32:35], a[88:91], v[204:207], v[32:35]
  v_mfma_f32_16x16x32_bf16 v[36:39], a[72:75], a[180:183], v[36:39]
  v_mfma_f32_16x16x32_bf16 v[36:39], a[88:91], v[208:211], v[36:39]
  v_mfma_f32_16x16x32_bf16 v[40:43], a[72:75], v[188:191], v[40:43]
  v_mfma_f32_16x16x32_bf16 v[40:43], a[88:91], a[0:3], v[40:43]
  v_mfma_f32_16x16x32_bf16 v[44:47], a[72:75], v[112:115], v[44:47]
  v_mfma_f32_16x16x32_bf16 v[44:47], a[88:91], v[212:215], v[44:47]
  v_mfma_f32_16x16x32_bf16 v[48:51], a[76:79], a[200:203], v[48:51]
  v_mfma_f32_16x16x32_bf16 v[48:51], a[92:95], v[204:207], v[48:51]
  v_mfma_f32_16x16x32_bf16 v[52:55], a[76:79], a[180:183], v[52:55]
  v_mfma_f32_16x16x32_bf16 v[52:55], a[92:95], v[208:211], v[52:55]
  v_mfma_f32_16x16x32_bf16 v[56:59], a[76:79], v[188:191], v[56:59]
  v_mfma_f32_16x16x32_bf16 v[56:59], a[92:95], a[0:3], v[56:59]
  v_mfma_f32_16x16x32_bf16 v[60:63], a[76:79], v[112:115], v[60:63]
  v_mfma_f32_16x16x32_bf16 v[60:63], a[92:95], v[212:215], v[60:63]
  s_nop 0
  s_waitcnt vmcnt(14)
  v_mfma_f32_16x16x32_bf16 v[0:3], a[96:99], a[4:7], v[0:3]
  v_mfma_f32_16x16x32_bf16 v[0:3], a[112:115], a[240:243], v[0:3]
  buffer_load_dwordx4 a[216:219], v248, s[40:43], s73 offen offset:2048 sc1
  v_mfma_f32_16x16x32_bf16 v[4:7], a[96:99], v[216:219], v[4:7]
  v_mfma_f32_16x16x32_bf16 v[4:7], a[112:115], v[116:119], v[4:7]
  buffer_load_dwordx4 a[224:227], v248, s[40:43], s73 offen offset:3072 sc1
  v_mfma_f32_16x16x32_bf16 v[8:11], a[96:99], a[8:11], v[8:11]
  v_mfma_f32_16x16x32_bf16 v[8:11], a[112:115], v[124:127], v[8:11]
  s_add_u32 s73, s93, 22
  s_and_b32 s73, s73, 31
  s_mul_i32 s73, s73, 12288
  buffer_load_dwordx4 a[232:235], v248, s[40:43], s73 offen offset:0 sc1
  v_mfma_f32_16x16x32_bf16 v[12:15], a[96:99], v[128:131], v[12:15]
  v_mfma_f32_16x16x32_bf16 v[12:15], a[112:115], v[224:227], v[12:15]
  buffer_load_dwordx4 a[212:215], v248, s[40:43], s73 offen offset:1024 sc1
  v_mfma_f32_16x16x32_bf16 v[16:19], a[100:103], a[4:7], v[16:19]
  v_mfma_f32_16x16x32_bf16 v[16:19], a[116:119], a[240:243], v[16:19]
  buffer_load_dwordx4 a[28:31], v248, s[40:43], s73 offen offset:2048 sc1
  v_mfma_f32_16x16x32_bf16 v[20:23], a[100:103], v[216:219], v[20:23]
  v_mfma_f32_16x16x32_bf16 v[20:23], a[116:119], v[116:119], v[20:23]
  buffer_load_dwordx4 v[236:239], v248, s[40:43], s73 offen offset:3072 sc1
  v_mfma_f32_16x16x32_bf16 v[24:27], a[100:103], a[8:11], v[24:27]
  v_mfma_f32_16x16x32_bf16 v[24:27], a[116:119], v[124:127], v[24:27]
  s_add_u32 s73, s93, 23
  s_and_b32 s73, s73, 31
  s_mul_i32 s73, s73, 12288
  buffer_load_dwordx4 a[140:143], v248, s[40:43], s73 offen offset:0 sc1
  v_mfma_f32_16x16x32_bf16 v[28:31], a[100:103], v[128:131], v[28:31]
  v_mfma_f32_16x16x32_bf16 v[28:31], a[116:119], v[224:227], v[28:31]
  buffer_load_dwordx4 a[144:147], v248, s[40:43], s73 offen offset:1024 sc1
  v_mfma_f32_16x16x32_bf16 v[32:35], a[104:107], a[4:7], v[32:35]
  v_mfma_f32_16x16x32_bf16 v[32:35], a[120:123], a[240:243], v[32:35]
  v_mfma_f32_16x16x32_bf16 v[36:39], a[104:107], v[216:219], v[36:39]
  v_mfma_f32_16x16x32_bf16 v[36:39], a[120:123], v[116:119], v[36:39]
  v_mfma_f32_16x16x32_bf16 v[40:43], a[104:107], a[8:11], v[40:43]
  v_mfma_f32_16x16x32_bf16 v[40:43], a[120:123], v[124:127], v[40:43]
  v_mfma_f32_16x16x32_bf16 v[44:47], a[104:107], v[128:131], v[44:47]
  v_mfma_f32_16x16x32_bf16 v[44:47], a[120:123], v[224:227], v[44:47]
  v_mfma_f32_16x16x32_bf16 v[48:51], a[108:111], a[4:7], v[48:51]
  v_mfma_f32_16x16x32_bf16 v[48:51], a[124:127], a[240:243], v[48:51]
  v_mfma_f32_16x16x32_bf16 v[52:55], a[108:111], v[216:219], v[52:55]
  v_mfma_f32_16x16x32_bf16 v[52:55], a[124:127], v[116:119], v[52:55]
  v_mfma_f32_16x16x32_bf16 v[56:59], a[108:111], a[8:11], v[56:59]
  v_mfma_f32_16x16x32_bf16 v[56:59], a[124:127], v[124:127], v[56:59]
  v_mfma_f32_16x16x32_bf16 v[60:63], a[108:111], v[128:131], v[60:63]
  v_mfma_f32_16x16x32_bf16 v[60:63], a[124:127], v[224:227], v[60:63]
  s_nop 0
  s_waitcnt vmcnt(14)
  v_mfma_f32_16x16x32_bf16 v[0:3], v[132:135], a[188:191], v[0:3]
  s_waitcnt vmcnt(62)
  v_mfma_f32_16x16x32_bf16 v[0:3], v[148:151], a[12:15], v[0:3]
  ds_read_b64_tr_b16 a[160:161], v240 offset:0
  v_mfma_f32_16x16x32_bf16 v[4:7], v[132:135], v[120:123], v[4:7]
  ds_read_b64_tr_b16 a[162:163], v240 offset:2048
  v_mfma_f32_16x16x32_bf16 v[4:7], v[148:151], a[16:19], v[4:7]
  ds_read_b64_tr_b16 a[148:149], v241 offset:0
  v_mfma_f32_16x16x32_bf16 v[8:11], v[132:135], v[220:223], v[8:11]
  ds_read_b64_tr_b16 a[150:151], v241 offset:2048
  v_mfma_f32_16x16x32_bf16 v[8:11], v[148:151], a[128:131], v[8:11]
  ds_read_b64_tr_b16 a[164:165], v242 offset:0
  v_mfma_f32_16x16x32_bf16 v[12:15], v[132:135], v[232:235], v[12:15]
  ds_read_b64_tr_b16 a[166:167], v242 offset:2048
  v_mfma_f32_16x16x32_bf16 v[12:15], v[148:151], a[20:23], v[12:15]
  ds_read_b64_tr_b16 a[152:153], v243 offset:0
  v_mfma_f32_16x16x32_bf16 v[16:19], v[136:139], a[188:191], v[16:19]
  ds_read_b64_tr_b16 a[154:155], v243 offset:2048
  v_mfma_f32_16x16x32_bf16 v[16:19], v[152:155], a[12:15], v[16:19]
  buffer_load_dwordx4 a[168:171], v248, s[40:43], s73 offen offset:2048 sc1
  v_mfma_f32_16x16x32_bf16 v[20:23], v[136:139], v[120:123], v[20:23]
  v_mfma_f32_16x16x32_bf16 v[20:23], v[152:155], a[16:19], v[20:23]
  buffer_load_dwordx4 v[76:79], v248, s[40:43], s73 offen offset:3072 sc1
  v_mfma_f32_16x16x32_bf16 v[24:27], v[136:139], v[220:223], v[24:27]
  v_mfma_f32_16x16x32_bf16 v[24:27], v[152:155], a[128:131], v[24:27]
  s_add_u32 s73, s93, 24
  s_and_b32 s73, s73, 31
  s_mul_i32 s73, s73, 12288
  buffer_load_dwordx4 a[236:239], v248, s[40:43], s73 offen offset:0 sc1
  v_mfma_f32_16x16x32_bf16 v[28:31], v[136:139], v[232:235], v[28:31]
  v_mfma_f32_16x16x32_bf16 v[28:31], v[152:155], a[20:23], v[28:31]
  buffer_load_dwordx4 v[64:67], v248, s[40:43], s73 offen offset:1024 sc1
  v_mfma_f32_16x16x32_bf16 v[32:35], v[140:143], a[188:191], v[32:35]
  v_mfma_f32_16x16x32_bf16 v[32:35], v[156:159], a[12:15], v[32:35]
  buffer_load_dwordx4 a[244:247], v248, s[40:43], s73 offen offset:2048 sc1
  v_mfma_f32_16x16x32_bf16 v[36:39], v[140:143], v[120:123], v[36:39]
  v_mfma_f32_16x16x32_bf16 v[36:39], v[156:159], a[16:19], v[36:39]
  buffer_load_dwordx4 a[220:223], v248, s[40:43], s73 offen offset:3072 sc1
  v_mfma_f32_16x16x32_bf16 v[40:43], v[140:143], v[220:223], v[40:43]
  v_mfma_f32_16x16x32_bf16 v[40:43], v[156:159], a[128:131], v[40:43]
  s_add_u32 s73, s93, 25
  s_and_b32 s73, s73, 31
  s_mul_i32 s73, s73, 12288
  buffer_load_dwordx4 v[192:195], v248, s[40:43], s73 offen offset:0 sc1
  v_mfma_f32_16x16x32_bf16 v[44:47], v[140:143], v[232:235], v[44:47]
  v_mfma_f32_16x16x32_bf16 v[44:47], v[156:159], a[20:23], v[44:47]
  buffer_load_dwordx4 v[88:91], v248, s[40:43], s73 offen offset:1024 sc1
  v_mfma_f32_16x16x32_bf16 v[48:51], v[144:147], a[188:191], v[48:51]
  v_mfma_f32_16x16x32_bf16 v[48:51], v[160:163], a[12:15], v[48:51]
  v_mfma_f32_16x16x32_bf16 v[52:55], v[144:147], v[120:123], v[52:55]
  v_mfma_f32_16x16x32_bf16 v[52:55], v[160:163], a[16:19], v[52:55]
  v_mfma_f32_16x16x32_bf16 v[56:59], v[144:147], v[220:223], v[56:59]
  v_mfma_f32_16x16x32_bf16 v[56:59], v[160:163], a[128:131], v[56:59]
  v_mfma_f32_16x16x32_bf16 v[60:63], v[144:147], v[232:235], v[60:63]
  v_mfma_f32_16x16x32_bf16 v[60:63], v[160:163], a[20:23], v[60:63]
  s_nop 0
  s_waitcnt vmcnt(14)
  v_mfma_f32_16x16x32_bf16 v[0:3], v[164:167], a[132:135], v[0:3]
  s_waitcnt vmcnt(62)
  v_mfma_f32_16x16x32_bf16 v[0:3], v[96:99], a[228:231], v[0:3]
  ds_read_b64_tr_b16 a[156:157], v184 offset:16384
  v_mfma_f32_16x16x32_bf16 v[4:7], v[164:167], a[24:27], v[4:7]
  ds_read_b64_tr_b16 a[158:159], v184 offset:18432
  v_mfma_f32_16x16x32_bf16 v[4:7], v[96:99], a[208:211], v[4:7]
  ds_read_b64_tr_b16 a[248:249], v185 offset:16384
  v_mfma_f32_16x16x32_bf16 v[8:11], v[164:167], a[136:139], v[8:11]
  ds_read_b64_tr_b16 a[250:251], v185 offset:18432
  v_mfma_f32_16x16x32_bf16 v[8:11], v[96:99], a[216:219], v[8:11]
  ds_read_b64_tr_b16 a[172:173], v186 offset:16384
  v_mfma_f32_16x16x32_bf16 v[12:15], v[164:167], a[204:207], v[12:15]
  ds_read_b64_tr_b16 a[174:175], v186 offset:18432
  v_mfma_f32_16x16x32_bf16 v[12:15], v[96:99], a[224:227], v[12:15]
  ds_read_b64_tr_b16 a[32:33], v187 offset:16384
  v_mfma_f32_16x16x32_bf16 v[16:19], v[168:171], a[132:135], v[16:19]
  ds_read_b64_tr_b16 a[34:35], v187 offset:18432
  v_mfma_f32_16x16x32_bf16 v[16:19], v[100:103], a[228:231], v[16:19]
  s_waitcnt vmcnt(62)
  v_mfma_f32_16x16x32_bf16 v[20:23], v[168:171], a[24:27], v[20:23]
  ds_read_b64_tr_b16 a[48:49], v184 offset:0
  v_mfma_f32_16x16x32_bf16 v[20:23], v[100:103], a[208:211], v[20:23]
  ds_read_b64_tr_b16 a[50:51], v184 offset:2048
  v_mfma_f32_16x16x32_bf16 v[24:27], v[168:171], a[136:139], v[24:27]
  ds_read_b64_tr_b16 a[36:37], v185 offset:0
  v_mfma_f32_16x16x32_bf16 v[24:27], v[100:103], a[216:219], v[24:27]
  ds_read_b64_tr_b16 a[38:39], v185 offset:2048
  v_mfma_f32_16x16x32_bf16 v[28:31], v[168:171], a[204:207], v[28:31]
  ds_read_b64_tr_b16 a[52:53], v186 offset:0
  v_mfma_f32_16x16x32_bf16 v[28:31], v[100:103], a[224:227], v[28:31]
  ds_read_b64_tr_b16 a[54:55], v186 offset:2048
  v_mfma_f32_16x16x32_bf16 v[32:35], v[172:175], a[132:135], v[32:35]
  ds_read_b64_tr_b16 a[40:41], v187 offset:0
  v_mfma_f32_16x16x32_bf16 v[32:35], v[104:107], a[228:231], v[32:35]
  ds_read_b64_tr_b16 a[42:43], v187 offset:2048
  v_mfma_f32_16x16x32_bf16 v[36:39], v[172:175], a[24:27], v[36:39]
  v_mfma_f32_16x16x32_bf16 v[36:39], v[104:107], a[208:211], v[36:39]
  buffer_load_dwordx4 a[56:59], v248, s[40:43], s73 offen offset:2048 sc1
  v_mfma_f32_16x16x32_bf16 v[40:43], v[172:175], a[136:139], v[40:43]
  v_mfma_f32_16x16x32_bf16 v[40:43], v[104:107], a[216:219], v[40:43]
  buffer_load_dwordx4 v[196:199], v248, s[40:43], s73 offen offset:3072 sc1
  v_mfma_f32_16x16x32_bf16 v[44:47], v[172:175], a[204:207], v[44:47]
  v_mfma_f32_16x16x32_bf16 v[44:47], v[104:107], a[224:227], v[44:47]
  s_add_u32 s73, s93, 26
  s_and_b32 s73, s73, 31
  s_mul_i32 s73, s73, 12288
  buffer_load_dwordx4 a[196:199], v248, s[40:43], s73 offen offset:0 sc1
  v_mfma_f32_16x16x32_bf16 v[48:51], v[176:179], a[132:135], v[48:51]
  v_mfma_f32_16x16x32_bf16 v[48:51], v[108:111], a[228:231], v[48:51]
  buffer_load_dwordx4 a[252:255], v248, s[40:43], s73 offen offset:1024 sc1
  v_mfma_f32_16x16x32_bf16 v[52:55], v[176:179], a[24:27], v[52:55]
  v_mfma_f32_16x16x32_bf16 v[52:55], v[108:111], a[208:211], v[52:55]
  buffer_load_dwordx4 a[176:179], v248, s[40:43], s73 offen offset:2048 sc1
  v_mfma_f32_16x16x32_bf16 v[56:59], v[176:179], a[136:139], v[56:59]
  v_mfma_f32_16x16x32_bf16 v[56:59], v[108:111], a[216:219], v[56:59]
  buffer_load_dwordx4 v[200:203], v248, s[40:43], s73 offen offset:3072 sc1
  v_mfma_f32_16x16x32_bf16 v[60:63], v[176:179], a[204:207], v[60:63]
  v_mfma_f32_16x16x32_bf16 v[60:63], v[108:111], a[224:227], v[60:63]
  s_add_u32 s73, s93, 27
  s_and_b32 s73, s73, 31
  s_mul_i32 s73, s73, 12288
  buffer_load_dwordx4 a[184:187], v248, s[40:43], s73 offen offset:0 sc1
  s_nop 0
  s_waitcnt vmcnt(13)
  v_mfma_f32_16x16x32_bf16 v[0:3], v[80:83], a[232:235], v[0:3]
  s_waitcnt vmcnt(62)
  s_waitcnt lgkmcnt(14)
  v_mfma_f32_16x16x32_bf16 v[0:3], a[160:163], a[140:143], v[0:3]
  ds_read_b64_tr_b16 v[92:93], v228 offset:0
  v_mfma_f32_16x16x32_bf16 v[4:7], v[80:83], a[212:215], v[4:7]
  ds_read_b64_tr_b16 v[94:95], v228 offset:2048
  v_mfma_f32_16x16x32_bf16 v[4:7], a[160:163], a[144:147], v[4:7]
  ds_read_b64_tr_b16 a[44:45], v229 offset:0
  v_mfma_f32_16x16x32_bf16 v[8:11], v[80:83], a[28:31], v[8:11]
  ds_read_b64_tr_b16 a[46:47], v229 offset:2048
  v_mfma_f32_16x16x32_bf16 v[8:11], a[160:163], a[168:171], v[8:11]
  ds_read_b64_tr_b16 a[192:193], v230 offset:0
  v_mfma_f32_16x16x32_bf16 v[12:15], v[80:83], v[236:239], v[12:15]
  ds_read_b64_tr_b16 a[194:195], v230 offset:2048
  v_mfma_f32_16x16x32_bf16 v[12:15], a[160:163], v[76:79], v[12:15]
  ds_read_b64_tr_b16 a[60:61], v231 offset:0
  v_mfma_f32_16x16x32_bf16 v[16:19], v[68:71], a[232:235], v[16:19]
  ds_read_b64_tr_b16 a[62:63], v231 offset:2048
  v_mfma_f32_16x16x32_bf16 v[16:19], a[148:151], a[140:143], v[16:19]
  s_waitcnt vmcnt(62)
  v_mfma_f32_16x16x32_bf16 v[20:23], v[68:71], a[212:215], v[20:23]
  ds_read_b64_tr_b16 a[64:65], v184 offset:57344
  v_mfma_f32_16x16x32_bf16 v[20:23], a[148:151], a[144:147], v[20:23]
  ds_read_b64_tr_b16 a[66:67], v184 offset:59392
  v_mfma_f32_16x16x32_bf16 v[24:27], v[68:71], a[28:31], v[24:27]
  ds_read_b64_tr_b16 a[80:81], v185 offset:57344
  v_mfma_f32_16x16x32_bf16 v[24:27], a[148:151], a[168:171], v[24:27]
  ds_read_b64_tr_b16 a[82:83], v185 offset:59392
  v_mfma_f32_16x16x32_bf16 v[28:31], v[68:71], v[236:239], v[28:31]
  ds_read_b64_tr_b16 a[68:69], v186 offset:57344
  v_mfma_f32_16x16x32_bf16 v[28:31], a[148:151], v[76:79], v[28:31]
  ds_read_b64_tr_b16 a[70:71], v186 offset:59392
  v_mfma_f32_16x16x32_bf16 v[32:35], v[84:87], a[232:235], v[32:35]
  ds_read_b64_tr_b16 a[84:85], v187 offset:57344
  v_mfma_f32_16x16x32_bf16 v[32:35], a[164:167], a[140:143], v[32:35]
  ds_read_b64_tr_b16 a[86:87], v187 offset:59392
  v_mfma_f32_16x16x32_bf16 v[36:39], v[84:87], a[212:215], v[36:39]
  v_mfma_f32_16x16x32_bf16 v[36:39], a[164:167], a[144:147], v[36:39]
  buffer_load_dwordx4 a[72:75], v248, s[40:43], s73 offen offset:1024 sc1
  v_mfma_f32_16x16x32_bf16 v[40:43], v[84:87], a[28:31], v[40:43]
  v_mfma_f32_16x16x32_bf16 v[40:43], a[164:167], a[168:171], v[40:43]
  buffer_load_dwordx4 a[88:91], v248, s[40:43], s73 offen offset:2048 sc1
  v_mfma_f32_16x16x32_bf16 v[44:47], v[84:87], v[236:239], v[44:47]
  v_mfma_f32_16x16x32_bf16 v[44:47], a[164:167], v[76:79], v[44:47]
  buffer_load_dwordx4 a[200:203], v248, s[40:43], s73 offen offset:3072 sc1
  v_mfma_f32_16x16x32_bf16 v[48:51], v[72:75], a[232:235], v[48:51]
  v_mfma_f32_16x16x32_bf16 v[48:51], a[152:155], a[140:143], v[48:51]
  s_add_u32 s73, s93, 28
  s_and_b32 s73, s73, 31
  s_mul_i32 s73, s73, 12288
  buffer_load_dwordx4 v[204:207], v248, s[40:43], s73 offen offset:0 sc1
  v_mfma_f32_16x16x32_bf16 v[52:55], v[72:75], a[212:215], v[52:55]
  v_mfma_f32_16x16x32_bf16 v[52:55], a[152:155], a[144:147], v[52:55]
  buffer_load_dwordx4 a[180:183], v248, s[40:43], s73 offen offset:1024 sc1
  v_mfma_f32_16x16x32_bf16 v[56:59], v[72:75], a[28:31], v[56:59]
  v_mfma_f32_16x16x32_bf16 v[56:59], a[152:155], a[168:171], v[56:59]
  buffer_load_dwordx4 v[208:211], v248, s[40:43], s73 offen offset:2048 sc1
  v_mfma_f32_16x16x32_bf16 v[60:63], v[72:75], v[236:239], v[60:63]
  v_mfma_f32_16x16x32_bf16 v[60:63], a[152:155], v[76:79], v[60:63]
  buffer_load_dwordx4 v[188:191], v248, s[40:43], s73 offen offset:3072 sc1
  s_nop 0
  s_waitcnt vmcnt(12)
  v_mfma_f32_16x16x32_bf16 v[0:3], a[156:159], a[236:239], v[0:3]
  s_waitcnt vmcnt(62)
  s_waitcnt lgkmcnt(14)
  v_mfma_f32_16x16x32_bf16 v[0:3], a[48:51], v[192:195], v[0:3]
  ds_read_b64_tr_b16 a[0:1], v184 offset:40960
  v_mfma_f32_16x16x32_bf16 v[4:7], a[156:159], v[64:67], v[4:7]
  ds_read_b64_tr_b16 a[2:3], v184 offset:43008
  v_mfma_f32_16x16x32_bf16 v[4:7], a[48:51], v[88:91], v[4:7]
  ds_read_b64_tr_b16 v[112:113], v185 offset:40960
  v_mfma_f32_16x16x32_bf16 v[8:11], a[156:159], a[244:247], v[8:11]
  ds_read_b64_tr_b16 v[114:115], v185 offset:43008
  v_mfma_f32_16x16x32_bf16 v[8:11], a[48:51], a[56:59], v[8:11]
  ds_read_b64_tr_b16 a[76:77], v186 offset:40960
  v_mfma_f32_16x16x32_bf16 v[12:15], a[156:159], a[220:223], v[12:15]
  ds_read_b64_tr_b16 a[78:79], v186 offset:43008
  v_mfma_f32_16x16x32_bf16 v[12:15], a[48:51], v[196:199], v[12:15]
  ds_read_b64_tr_b16 v[212:213], v187 offset:40960
  v_mfma_f32_16x16x32_bf16 v[16:19], a[248:251], a[236:239], v[16:19]
  ds_read_b64_tr_b16 v[214:215], v187 offset:43008
  v_mfma_f32_16x16x32_bf16 v[16:19], a[36:39], v[192:195], v[16:19]
  s_waitcnt vmcnt(62)
  v_mfma_f32_16x16x32_bf16 v[20:23], a[248:251], v[64:67], v[20:23]
  ds_read_b64_tr_b16 a[92:93], v240 offset:16384
  v_mfma_f32_16x16x32_bf16 v[20:23], a[36:39], v[88:91], v[20:23]
  ds_read_b64_tr_b16 a[94:95], v240 offset:18432
  v_mfma_f32_16x16x32_bf16 v[24:27], a[248:251], a[244:247], v[24:27]
  ds_read_b64_tr_b16 a[96:97], v241 offset:16384
  v_mfma_f32_16x16x32_bf16 v[24:27], a[36:39], a[56:59], v[24:27]
  ds_read_b64_tr_b16 a[98:99], v241 offset:18432
  v_mfma_f32_16x16x32_bf16 v[28:31], a[248:251], a[220:223], v[28:31]
  ds_read_b64_tr_b16 a[112:113], v242 offset:16384
  v_mfma_f32_16x16x32_bf16 v[28:31], a[36:39], v[196:199], v[28:31]
  ds_read_b64_tr_b16 a[114:115], v242 offset:18432
  v_mfma_f32_16x16x32_bf16 v[32:35], a[172:175], a[236:239], v[32:35]
  ds_read_b64_tr_b16 a[100:101], v243 offset:16384
  v_mfma_f32_16x16x32_bf16 v[32:35], a[52:55], v[192:195], v[32:35]
  ds_read_b64_tr_b16 a[102:103], v243 offset:18432
  v_mfma_f32_16x16x32_bf16 v[36:39], a[172:175], v[64:67], v[36:39]
  v_mfma_f32_16x16x32_bf16 v[36:39], a[52:55], v[88:91], v[36:39]
  s_add_u32 s73, s93, 29
  s_and_b32 s73, s73, 31
  s_mul_i32 s73, s73, 12288
  buffer_load_dwordx4 a[116:119], v248, s[40:43], s73 offen offset:0 sc1
  v_mfma_f32_16x16x32_bf16 v[40:43], a[172:175], a[244:247], v[40:43]
  v_mfma_f32_16x16x32_bf16 v[40:43], a[52:55], a[56:59], v[40:43]
  buffer_load_dwordx4 a[104:107], v248, s[40:43], s73 offen offset:1024 sc1
  v_mfma_f32_16x16x32_bf16 v[44:47], a[172:175], a[220:223], v[44:47]
  v_mfma_f32_16x16x32_bf16 v[44:47], a[52:55], v[196:199], v[44:47]
  buffer_load_dwordx4 a[120:123], v248, s[40:43], s73 offen offset:2048 sc1
  v_mfma_f32_16x16x32_bf16 v[48:51], a[32:35], a[236:239], v[48:51]
  v_mfma_f32_16x16x32_bf16 v[48:51], a[40:43], v[192:195], v[48:51]
  buffer_load_dwordx4 a[4:7], v248, s[40:43], s73 offen offset:3072 sc1
  v_mfma_f32_16x16x32_bf16 v[52:55], a[32:35], v[64:67], v[52:55]
  v_mfma_f32_16x16x32_bf16 v[52:55], a[40:43], v[88:91], v[52:55]
  s_add_u32 s73, s93, 30
  s_and_b32 s73, s73, 31
  s_mul_i32 s73, s73, 12288
  buffer_load_dwordx4 a[240:243], v248, s[40:43], s73 offen offset:0 sc1
  v_mfma_f32_16x16x32_bf16 v[56:59], a[32:35], a[244:247], v[56:59]
  v_mfma_f32_16x16x32_bf16 v[56:59], a[40:43], a[56:59], v[56:59]
  buffer_load_dwordx4 v[216:219], v248, s[40:43], s73 offen offset:1024 sc1
  v_mfma_f32_16x16x32_bf16 v[60:63], a[32:35], a[220:223], v[60:63]
  v_mfma_f32_16x16x32_bf16 v[60:63], a[40:43], v[196:199], v[60:63]
  buffer_load_dwordx4 v[116:119], v248, s[40:43], s73 offen offset:2048 sc1
  s_nop 0
  s_waitcnt vmcnt(11)
  v_mfma_f32_16x16x32_bf16 v[0:3], v[92:95], a[196:199], v[0:3]
  s_waitcnt vmcnt(62)
  s_waitcnt lgkmcnt(14)
  v_mfma_f32_16x16x32_bf16 v[0:3], a[64:67], a[184:187], v[0:3]
  ds_read_b64_tr_b16 a[8:9], v240 offset:32768
  v_mfma_f32_16x16x32_bf16 v[4:7], v[92:95], a[252:255], v[4:7]
  ds_read_b64_tr_b16 a[10:11], v240 offset:34816
  v_mfma_f32_16x16x32_bf16 v[4:7], a[64:67], a[72:75], v[4:7]
  ds_read_b64_tr_b16 v[124:125], v241 offset:32768
  v_mfma_f32_16x16x32_bf16 v[8:11], v[92:95], a[176:179], v[8:11]
  ds_read_b64_tr_b16 v[126:127], v241 offset:34816
  v_mfma_f32_16x16x32_bf16 v[8:11], a[64:67], a[88:91], v[8:11]
  ds_read_b64_tr_b16 v[128:129], v242 offset:32768
  v_mfma_f32_16x16x32_bf16 v[12:15], v[92:95], v[200:203], v[12:15]
  ds_read_b64_tr_b16 v[130:131], v242 offset:34816
  v_mfma_f32_16x16x32_bf16 v[12:15], a[64:67], a[200:203], v[12:15]
  ds_read_b64_tr_b16 a[108:109], v243 offset:32768
  v_mfma_f32_16x16x32_bf16 v[16:19], a[44:47], a[196:199], v[16:19]
  ds_read_b64_tr_b16 a[110:111], v243 offset:34816
  v_mfma_f32_16x16x32_bf16 v[16:19], a[80:83], a[184:187], v[16:19]
  s_waitcnt vmcnt(62)
  v_mfma_f32_16x16x32_bf16 v[20:23], a[44:47], a[252:255], v[20:23]
  ds_read_b64_tr_b16 v[224:225], v228 offset:16384
  v_mfma_f32_16x16x32_bf16 v[20:23], a[80:83], a[72:75], v[20:23]
  ds_read_b64_tr_b16 v[226:227], v228 offset:18432
  v_mfma_f32_16x16x32_bf16 v[24:27], a[44:47], a[176:179], v[24:27]
  ds_read_b64_tr_b16 a[124:125], v229 offset:16384
  v_mfma_f32_16x16x32_bf16 v[24:27], a[80:83], a[88:91], v[24:27]
  ds_read_b64_tr_b16 a[126:127], v229 offset:18432
  v_mfma_f32_16x16x32_bf16 v[28:31], a[44:47], v[200:203], v[28:31]
  ds_read_b64_tr_b16 v[132:133], v230 offset:16384
  v_mfma_f32_16x16x32_bf16 v[28:31], a[80:83], a[200:203], v[28:31]
  ds_read_b64_tr_b16 v[134:135], v230 offset:18432
  v_mfma_f32_16x16x32_bf16 v[32:35], a[192:195], a[196:199], v[32:35]
  ds_read_b64_tr_b16 v[148:149], v231 offset:16384
  v_mfma_f32_16x16x32_bf16 v[32:35], a[68:71], a[184:187], v[32:35]
  ds_read_b64_tr_b16 v[150:151], v231 offset:18432
  v_mfma_f32_16x16x32_bf16 v[36:39], a[192:195], a[252:255], v[36:39]
  v_mfma_f32_16x16x32_bf16 v[36:39], a[68:71], a[72:75], v[36:39]
  buffer_load_dwordx4 v[136:139], v248, s[40:43], s73 offen offset:3072 sc1
  v_mfma_f32_16x16x32_bf16 v[40:43], a[192:195], a[176:179], v[40:43]
  v_mfma_f32_16x16x32_bf16 v[40:43], a[68:71], a[88:91], v[40:43]
  s_add_u32 s73, s93, 31
  s_and_b32 s73, s73, 31
  s_mul_i32 s73, s73, 12288
  buffer_load_dwordx4 v[152:155], v248, s[40:43], s73 offen offset:0 sc1
  v_mfma_f32_16x16x32_bf16 v[44:47], a[192:195], v[200:203], v[44:47]
  v_mfma_f32_16x16x32_bf16 v[44:47], a[68:71], a[200:203], v[44:47]
  buffer_load_dwordx4 v[140:143], v248, s[40:43], s73 offen offset:1024 sc1
  v_mfma_f32_16x16x32_bf16 v[48:51], a[60:63], a[196:199], v[48:51]
  v_mfma_f32_16x16x32_bf16 v[48:51], a[84:87], a[184:187], v[48:51]
  buffer_load_dwordx4 v[156:159], v248, s[40:43], s73 offen offset:2048 sc1
  v_mfma_f32_16x16x32_bf16 v[52:55], a[60:63], a[252:255], v[52:55]
  v_mfma_f32_16x16x32_bf16 v[52:55], a[84:87], a[72:75], v[52:55]
  buffer_load_dwordx4 a[188:191], v248, s[40:43], s73 offen offset:3072 sc1
  v_mfma_f32_16x16x32_bf16 v[56:59], a[60:63], a[176:179], v[56:59]
  v_mfma_f32_16x16x32_bf16 v[56:59], a[84:87], a[88:91], v[56:59]
  v_mfma_f32_16x16x32_bf16 v[60:63], a[60:63], v[200:203], v[60:63]
  v_mfma_f32_16x16x32_bf16 v[60:63], a[84:87], a[200:203], v[60:63]
  s_nop 0
  s_waitcnt vmcnt(8)
  v_mfma_f32_16x16x32_bf16 v[0:3], a[0:3], v[204:207], v[0:3]
  s_waitcnt lgkmcnt(14)
  v_mfma_f32_16x16x32_bf16 v[0:3], a[92:95], a[116:119], v[0:3]
  v_mfma_f32_16x16x32_bf16 v[4:7], a[0:3], a[180:183], v[4:7]
  v_mfma_f32_16x16x32_bf16 v[4:7], a[92:95], a[104:107], v[4:7]
  v_mfma_f32_16x16x32_bf16 v[8:11], a[0:3], v[208:211], v[8:11]
  v_mfma_f32_16x16x32_bf16 v[8:11], a[92:95], a[120:123], v[8:11]
  v_mfma_f32_16x16x32_bf16 v[12:15], a[0:3], v[188:191], v[12:15]
  v_mfma_f32_16x16x32_bf16 v[12:15], a[92:95], a[4:7], v[12:15]
  v_mfma_f32_16x16x32_bf16 v[16:19], v[112:115], v[204:207], v[16:19]
  v_mfma_f32_16x16x32_bf16 v[16:19], a[96:99], a[116:119], v[16:19]
  v_mfma_f32_16x16x32_bf16 v[20:23], v[112:115], a[180:183], v[20:23]
  v_mfma_f32_16x16x32_bf16 v[20:23], a[96:99], a[104:107], v[20:23]
  v_mfma_f32_16x16x32_bf16 v[24:27], v[112:115], v[208:211], v[24:27]
  v_mfma_f32_16x16x32_bf16 v[24:27], a[96:99], a[120:123], v[24:27]
  v_mfma_f32_16x16x32_bf16 v[28:31], v[112:115], v[188:191], v[28:31]
  v_mfma_f32_16x16x32_bf16 v[28:31], a[96:99], a[4:7], v[28:31]
  v_mfma_f32_16x16x32_bf16 v[32:35], a[76:79], v[204:207], v[32:35]
  v_mfma_f32_16x16x32_bf16 v[32:35], a[112:115], a[116:119], v[32:35]
  v_mfma_f32_16x16x32_bf16 v[36:39], a[76:79], a[180:183], v[36:39]
  v_mfma_f32_16x16x32_bf16 v[36:39], a[112:115], a[104:107], v[36:39]
  v_mfma_f32_16x16x32_bf16 v[40:43], a[76:79], v[208:211], v[40:43]
  v_mfma_f32_16x16x32_bf16 v[40:43], a[112:115], a[120:123], v[40:43]
  v_mfma_f32_16x16x32_bf16 v[44:47], a[76:79], v[188:191], v[44:47]
  v_mfma_f32_16x16x32_bf16 v[44:47], a[112:115], a[4:7], v[44:47]
  v_mfma_f32_16x16x32_bf16 v[48:51], v[212:215], v[204:207], v[48:51]
  v_mfma_f32_16x16x32_bf16 v[48:51], a[100:103], a[116:119], v[48:51]
  v_mfma_f32_16x16x32_bf16 v[52:55], v[212:215], a[180:183], v[52:55]
  v_mfma_f32_16x16x32_bf16 v[52:55], a[100:103], a[104:107], v[52:55]
  v_mfma_f32_16x16x32_bf16 v[56:59], v[212:215], v[208:211], v[56:59]
  v_mfma_f32_16x16x32_bf16 v[56:59], a[100:103], a[120:123], v[56:59]
  v_mfma_f32_16x16x32_bf16 v[60:63], v[212:215], v[188:191], v[60:63]
  v_mfma_f32_16x16x32_bf16 v[60:63], a[100:103], a[4:7], v[60:63]
  s_nop 0
  s_waitcnt vmcnt(0)
  v_mfma_f32_16x16x32_bf16 v[0:3], a[8:11], a[240:243], v[0:3]
  s_waitcnt lgkmcnt(6)
  v_mfma_f32_16x16x32_bf16 v[0:3], v[224:227], v[152:155], v[0:3]
  v_mfma_f32_16x16x32_bf16 v[4:7], a[8:11], v[216:219], v[4:7]
  v_mfma_f32_16x16x32_bf16 v[4:7], v[224:227], v[140:143], v[4:7]
  v_mfma_f32_16x16x32_bf16 v[8:11], a[8:11], v[116:119], v[8:11]
  v_mfma_f32_16x16x32_bf16 v[8:11], v[224:227], v[156:159], v[8:11]
  v_mfma_f32_16x16x32_bf16 v[12:15], a[8:11], v[136:139], v[12:15]
  v_mfma_f32_16x16x32_bf16 v[12:15], v[224:227], a[188:191], v[12:15]
  v_mfma_f32_16x16x32_bf16 v[16:19], v[124:127], a[240:243], v[16:19]
  s_waitcnt lgkmcnt(4)
  v_mfma_f32_16x16x32_bf16 v[16:19], a[124:127], v[152:155], v[16:19]
  ds_write_b128 v253, v[0:3] offset:0
  v_mfma_f32_16x16x32_bf16 v[20:23], v[124:127], v[216:219], v[20:23]
  v_mfma_f32_16x16x32_bf16 v[20:23], a[124:127], v[140:143], v[20:23]
  ds_write_b128 v253, v[4:7] offset:1024
  v_mfma_f32_16x16x32_bf16 v[24:27], v[124:127], v[116:119], v[24:27]
  v_mfma_f32_16x16x32_bf16 v[24:27], a[124:127], v[156:159], v[24:27]
  ds_write_b128 v253, v[8:11] offset:2048
  v_mfma_f32_16x16x32_bf16 v[28:31], v[124:127], v[136:139], v[28:31]
  v_mfma_f32_16x16x32_bf16 v[28:31], a[124:127], a[188:191], v[28:31]
  ds_write_b128 v253, v[12:15] offset:3072
  v_mfma_f32_16x16x32_bf16 v[32:35], v[128:131], a[240:243], v[32:35]
  s_waitcnt lgkmcnt(6)
  v_mfma_f32_16x16x32_bf16 v[32:35], v[132:135], v[152:155], v[32:35]
  ds_write_b128 v249, v[16:19] offset:16384
  v_mfma_f32_16x16x32_bf16 v[36:39], v[128:131], v[216:219], v[36:39]
  v_mfma_f32_16x16x32_bf16 v[36:39], v[132:135], v[140:143], v[36:39]
  ds_write_b128 v249, v[20:23] offset:17408
  v_mfma_f32_16x16x32_bf16 v[40:43], v[128:131], v[116:119], v[40:43]
  v_mfma_f32_16x16x32_bf16 v[40:43], v[132:135], v[156:159], v[40:43]
  ds_write_b128 v249, v[24:27] offset:18432
  v_mfma_f32_16x16x32_bf16 v[44:47], v[128:131], v[136:139], v[44:47]
  v_mfma_f32_16x16x32_bf16 v[44:47], v[132:135], a[188:191], v[44:47]
  ds_write_b128 v249, v[28:31] offset:19456
  v_mfma_f32_16x16x32_bf16 v[48:51], a[108:111], a[240:243], v[48:51]
  s_waitcnt lgkmcnt(8)
  v_mfma_f32_16x16x32_bf16 v[48:51], v[148:151], v[152:155], v[48:51]
  ds_write_b128 v251, v[32:35] offset:32768
  v_mfma_f32_16x16x32_bf16 v[52:55], a[108:111], v[216:219], v[52:55]
  v_mfma_f32_16x16x32_bf16 v[52:55], v[148:151], v[140:143], v[52:55]
  ds_write_b128 v251, v[36:39] offset:33792
  v_mfma_f32_16x16x32_bf16 v[56:59], a[108:111], v[116:119], v[56:59]
  v_mfma_f32_16x16x32_bf16 v[56:59], v[148:151], v[156:159], v[56:59]
  ds_write_b128 v251, v[40:43] offset:34816
  v_mfma_f32_16x16x32_bf16 v[60:63], a[108:111], v[136:139], v[60:63]
  v_mfma_f32_16x16x32_bf16 v[60:63], v[148:151], a[188:191], v[60:63]
  ds_write_b128 v251, v[44:47] offset:35840
  s_nop 0
  ds_write_b128 v252, v[48:51] offset:49152
  ds_write_b128 v252, v[52:55] offset:50176
  s_nop 0
  ds_write_b128 v252, v[56:59] offset:51200
  s_nop 1
  ds_write_b128 v252, v[60:63] offset:52224
  s_waitcnt lgkmcnt(0)
  s_barrier
  ds_read_b128 v[64:67], v244 offset:0
  ds_read_b128 v[68:71], v244 offset:4096
  ds_read_b128 v[72:75], v244 offset:8192
  ds_read_b128 v[76:79], v244 offset:12288
  ds_read_b128 v[80:83], v244 offset:256
  ds_read_b128 v[84:87], v244 offset:4352
  ds_read_b128 v[88:91], v244 offset:8448
  ds_read_b128 v[92:95], v244 offset:12544
  ds_read_b128 v[96:99], v245 offset:0
  ds_read_b128 v[100:103], v245 offset:4096
  ds_read_b128 v[104:107], v245 offset:8192
  ds_read_b128 v[108:111], v245 offset:12288
  ds_read_b128 v[112:115], v245 offset:256
  ds_read_b128 v[116:119], v245 offset:4352
  ds_read_b128 v[120:123], v245 offset:8448
  ds_read_b128 v[124:127], v245 offset:12544
  s_waitcnt lgkmcnt(14)
  v_pk_add_f32 v[64:65], v[64:65], v[68:69]
  s_waitcnt lgkmcnt(12)
  v_pk_add_f32 v[72:73], v[72:73], v[76:77]
  v_pk_add_f32 v[66:67], v[66:67], v[70:71]
  v_pk_add_f32 v[74:75], v[74:75], v[78:79]
  v_pk_add_f32 v[64:65], v[64:65], v[72:73]
  v_pk_add_f32 v[66:67], v[66:67], v[74:75]
  v_cvt_pk_bf16_f32 v0, v64, v65
  v_cvt_pk_bf16_f32 v1, v66, v67
  s_waitcnt lgkmcnt(10)
  v_pk_add_f32 v[80:81], v[80:81], v[84:85]
  s_waitcnt lgkmcnt(8)
  v_pk_add_f32 v[88:89], v[88:89], v[92:93]
  v_pk_add_f32 v[82:83], v[82:83], v[86:87]
  v_pk_add_f32 v[90:91], v[90:91], v[94:95]
  v_pk_add_f32 v[80:81], v[80:81], v[88:89]
  v_pk_add_f32 v[82:83], v[82:83], v[90:91]
  v_cvt_pk_bf16_f32 v2, v80, v81
  v_cvt_pk_bf16_f32 v3, v82, v83
  buffer_store_dwordx4 v[0:3], v250, s[48:51], s68 offen offset:0 sc0 sc1
  s_waitcnt lgkmcnt(6)
  v_pk_add_f32 v[96:97], v[96:97], v[100:101]
  s_waitcnt lgkmcnt(4)
  v_pk_add_f32 v[104:105], v[104:105], v[108:109]
  v_pk_add_f32 v[98:99], v[98:99], v[102:103]
  v_pk_add_f32 v[106:107], v[106:107], v[110:111]
  v_pk_add_f32 v[96:97], v[96:97], v[104:105]
  v_pk_add_f32 v[98:99], v[98:99], v[106:107]
  v_cvt_pk_bf16_f32 v32, v96, v97
  v_cvt_pk_bf16_f32 v33, v98, v99
  s_waitcnt lgkmcnt(2)
  v_pk_add_f32 v[112:113], v[112:113], v[116:117]
  s_waitcnt lgkmcnt(0)
  v_pk_add_f32 v[120:121], v[120:121], v[124:125]
  v_pk_add_f32 v[114:115], v[114:115], v[118:119]
  v_pk_add_f32 v[122:123], v[122:123], v[126:127]
  v_pk_add_f32 v[112:113], v[112:113], v[120:121]
  v_pk_add_f32 v[114:115], v[114:115], v[122:123]
  v_cvt_pk_bf16_f32 v34, v112, v113
  v_cvt_pk_bf16_f32 v35, v114, v115
  buffer_store_dwordx4 v[32:35], v250, s[48:51], s69 offen offset:0 sc0 sc1
FIN%=:
CEND%=:
)ASM" :: "s"(__builtin_amdgcn_kernarg_segment_ptr()), "s"(blockIdx.x), "s"(wid) : CLOB);
}
extern "C" void kernel_launch(void* const* d_in, const int* in_sizes, int n_in, void* d_out, int out_size, void* d_ws, size_t ws_size, hipStream_t stream) {
  (void)hipFuncSetAttribute((const void*)mha_k, hipFuncAttributeMaxDynamicSharedMemorySize, 163840);
  hipLaunchKernelGGL(mha_k, dim3(256), dim3(256), 163840, stream, (const char*)d_in[0], (const char*)d_in[1], (const char*)d_in[2], (const char*)d_in[3], (const char*)d_in[4], (char*)d_ws, (char*)d_out);
}
```
